```python
import torch

def _softmax(x, axis=-1):
    return torch.softmax(x, dim=axis)

def _gelu(x):
    return torch.nn.functional.gelu(x, approximate='tanh')

def _silu(x):
    return torch.nn.functional.silu(x)

def _sigmoid(x):
    return torch.sigmoid(x)

def _layernorm(x, axis=-1, eps=1e-05):
    m = x.mean(dim=axis, keepdim=True)
    v = x.var(dim=axis, keepdim=True, unbiased=False)
    return (x - m) / torch.sqrt(v + eps)

def _scatter_add(tgt, idx, src, axis=0):
    return tgt.clone().index_add_(axis, idx, src)

def _segment_sum(src, seg, n):
    out = torch.zeros((n,) + src.shape[1:], dtype=src.dtype)
    return out.index_add_(0, seg, src)

def _pad(x, pw):
    flat = tuple((v for pair in reversed(pw) for v in pair))
    return torch.nn.functional.pad(x, flat)
kernel_name = 'sequential_preLN__memory_aug_recurrent_mem_tr__mixer_s_mlp__prompt'

def setup_inputs():

    _w = lambda a: (lambda t: t.to(torch.bfloat16) if t.is_floating_point() else t)(torch.as_tensor(a))

    def _A_setup():
        B, S, Dm = (1, 2048, 1024)
        H, Dh = (32, 128)
        M, seg = (8, 32)
        seg = min(seg, S)
        S = S // seg * seg
        return {'x': _w((lambda *s: torch.randn(*s, dtype=torch.bfloat16) * 0.05)(B, S, Dm)), 'mem0': _w((lambda *s: torch.randn(*s, dtype=torch.bfloat16) * 0.2)(B, M, Dm)), 'Wq': _w((lambda *s: torch.randn(*s, dtype=torch.bfloat16) * 1.6)(Dm, H, Dh) * Dm ** (-0.5)), 'Wk': _w((lambda *s: torch.randn(*s, dtype=torch.bfloat16) * 1.6)(Dm, H, Dh) * Dm ** (-0.5)), 'Wv': _w((lambda *s: torch.randn(*s, dtype=torch.bfloat16) * 0.2)(Dm, H, Dh) * Dm ** (-0.5)), 'Wo': _w((lambda *s: torch.randn(*s, dtype=torch.bfloat16) * 0.2)(H, Dh, Dm) * (H * Dh) ** (-0.5)), 'seg_len': seg}
    _A_in = _A_setup()

    def _B_setup():
        B, S, D, F = (1, 2048, 1024, 3584)
        return {'x': _w((lambda *s: torch.randn(*s, dtype=torch.bfloat16) * 0.05)(B, S, D)), 'w1': _w((lambda *s: torch.randn(*s, dtype=torch.bfloat16) * 0.8)(D, D) * D ** (-0.5)), 'w2': _w((lambda *s: torch.randn(*s, dtype=torch.bfloat16) * 0.8)(D, D) * D ** (-0.5)), 'w3': _w((lambda *s: torch.randn(*s, dtype=torch.bfloat16) * 0.8)(D, F) * D ** (-0.5)), 'w4': _w((lambda *s: torch.randn(*s, dtype=torch.bfloat16) * 0.8)(F, D) * F ** (-0.5))}
    _B_in = _B_setup()
    return {'A_x': _A_in['x'], 'A_mem0': _A_in['mem0'], 'A_Wq': _A_in['Wq'], 'A_Wk': _A_in['Wk'], 'A_Wv': _A_in['Wv'], 'A_Wo': _A_in['Wo'], 'A_seg_len': _A_in['seg_len'], 'B_x': _B_in['x'], 'B_w1': _B_in['w1'], 'B_w2': _B_in['w2'], 'B_w3': _B_in['w3'], 'B_w4': _B_in['w4']}

def reference(A_x, A_mem0, A_Wq, A_Wk, A_Wv, A_Wo, A_seg_len, B_x, B_w1, B_w2, B_w3, B_w4):

    def _A_ref(x, mem0, Wq, Wk, Wv, Wo, seg_len):
        B, S, Dm = x.shape
        H, Dh = (Wq.shape[1], Wq.shape[2])
        M = mem0.shape[1]
        scale = Dh ** (-0.5)
        n_seg = S // seg_len
        L = M + seg_len
        idx = torch.arange(L)
        mask = (idx[:, None] < M) | (idx[:, None] >= idx[None, :])
        mem = mem0
        outs = []
        for s in range(n_seg):
            xs = x[:, s * seg_len:(s + 1) * seg_len, :].clone()
            h = torch.concatenate([mem, xs], axis=1)
            q = torch.einsum('btd,dhe->bhte', h, Wq)
            k = torch.einsum('btd,dhe->bhte', h, Wk)
            v = torch.einsum('btd,dhe->bhte', h, Wv)
            scores = torch.einsum('bhqe,bhke->bhqk', q, k) * scale
            scores = torch.where(mask[None, None], scores, -1000000000.0)
            p = _softmax(scores, axis=-1)
            o = torch.einsum('bhqk,bhke->bhqe', p, v)
            o = torch.einsum('bhte,hed->btd', o, Wo)
            mem = o[:, :M, :].clone()
            outs.append(o[:, M:, :])
        return torch.concatenate(outs, axis=1)

    def _B_ref(x, w1, w2, w3, w4):
        B, S, D = x.shape
        h = _gelu(_layernorm(x, axis=-1) @ w1)
        c = D // 4
        g0 = _pad(h[:, :, 0 * c:1 * c], ((0, 0), (1, 0), (0, 0)))[:, :S, :]
        g1 = _pad(h[:, :, 1 * c:2 * c], ((0, 0), (0, 1), (0, 0)))[:, 1:, :]
        g2 = _pad(h[:, :, 2 * c:3 * c], ((0, 0), (2, 0), (0, 0)))[:, :S, :]
        g3 = _pad(h[:, :, 3 * c:], ((0, 0), (0, 2), (0, 0)))[:, 2:, :]
        hs = torch.concatenate([g0, g1, g2, g3], axis=-1) @ w2
        x1 = x + hs
        h2 = _gelu(_layernorm(x1, axis=-1) @ w3) @ w4
        return x1 + h2
    x = A_x
    h = x + _A_ref(_layernorm(x, axis=-1), A_mem0, A_Wq, A_Wk, A_Wv, A_Wo, A_seg_len)
    h2 = h + _B_ref(_layernorm(h, axis=-1), B_w1, B_w2, B_w3, B_w4)
    return h2
```

```cpp
#include <hip/hip_runtime.h>
#include <stdint.h>
typedef __bf16 bf16;
typedef __bf16 bf16x2 __attribute__((ext_vector_type(2)));
typedef __bf16 bf16x4 __attribute__((ext_vector_type(4)));
typedef __bf16 bf16x8 __attribute__((ext_vector_type(8)));
typedef __bf16 bf16x16 __attribute__((ext_vector_type(16)));
typedef float f32x2 __attribute__((ext_vector_type(2)));
typedef float f32x4 __attribute__((ext_vector_type(4)));
typedef unsigned u32x2 __attribute__((ext_vector_type(2)));
typedef unsigned u32x4 __attribute__((ext_vector_type(4)));
typedef unsigned u32x8 __attribute__((ext_vector_type(8)));
#define LAS __attribute__((address_space(3)))
#define GAS __attribute__((address_space(1)))
#define DEV __device__ __forceinline__
constexpr unsigned MB = 1u << 20;
#define LAY 1
constexpr unsigned WSB = 242 * MB;
constexpr unsigned OFF_UX = 0, OFF_RING = 8192, OFF_KM = 16 * MB + 8192, OFF_P = 20 * MB + 8192, OFF_Q = LAY & 1 ? 160 * MB + 8192 : 128 * MB, OFF_K = LAY & 2 ? 192 * MB + 8192 : 160 * MB, OFF_H3 = 192 * MB, OFF_G = 224 * MB,
                   OFF_H = OFF_G + 8192, OFF_V = 128 * MB + (LAY & 4 ? 0 : 8192), OFF_X1 = 232 * MB, OFF_CNT = 240 * MB, OFF_ST2 = 241 * MB, OFF_ST1 = OFF_ST2 + 8192;
constexpr unsigned OFF_XN = 246 * MB, OFF_DBG = 252 * MB;
constexpr unsigned OFF_CAL = 126 * MB, OFF_HB = OFF_CNT + 524288, N_CAL = 64;
constexpr unsigned C_H = 0, C_RDY = 64, C_G = 128, C_Q = 192, C_X1 = 256, C_P = 320, C_H3 = 384, C_PROG = 448, C_DONE = 452, C_V = 456, C_TK = 488, C_END = 512;
template <unsigned OFF> __device__ __forceinline__ unsigned rowoff(unsigned tb) {
  return OFF == OFF_H3 ? tb * 524288 + (tb & 4) * 2048 : OFF == OFF_H ? tb * 131072 - (tb & 16) * 512 : tb * 131072 + (tb & 16) * 512;
}
__device__ __forceinline__ unsigned coloff(unsigned nb) { return (nb >> 2) * 16384 + (nb & 3) * 2048; }
__device__ __forceinline__ unsigned qoff(unsigned seg, unsigned h) { return (OFF_Q ^ (seg & 4) * 2048) + h * 16384 + seg * 524288; }
__device__ __forceinline__ unsigned koff(unsigned seg, unsigned h) { return (OFF_K ^ (seg & 4) * 2048) + h * 16384 + seg * 524288; }
__device__ __forceinline__ unsigned ws_align(const char* ws) { return -(unsigned)(uint64_t)ws & (2 * MB - 1); }
DEV unsigned pk2(float a, float b) { f32x2 f = {a, b}; return __builtin_bit_cast(unsigned, __builtin_convertvector(f, bf16x2)); }
DEV float bf_lo(unsigned u) { return __builtin_bit_cast(float, u << 16); }
DEV float bf_hi(unsigned u) { return __builtin_bit_cast(float, u & 0xffff0000u); }
DEV unsigned f2u(float f) { return __builtin_bit_cast(unsigned, f); }
DEV float u2f(unsigned u) { return __builtin_bit_cast(float, u); }
DEV u32x2 tr_read(unsigned addr) { return __builtin_bit_cast(u32x2, __builtin_amdgcn_ds_read_tr16_b64_v4bf16((LAS bf16x4*)addr)); }
DEV u32x4 lds_r128(unsigned addr) { return *(LAS u32x4*)addr; }
DEV void lds_w128(unsigned addr, u32x4 v) { *(LAS u32x4*)addr = v; }
DEV void lds_bar() { asm volatile("s_waitcnt lgkmcnt(0)\n\ts_barrier" ::: "memory"); }
DEV f32x4 smf(u32x4 a, u32x8 b, f32x4 c, int idx) { return __builtin_amdgcn_smfmac_f32_16x16x64_bf16(__builtin_bit_cast(bf16x8, a), __builtin_bit_cast(bf16x16, b), c, idx, 0, 0); }
DEV f32x4 mfma16(u32x4 a, u32x4 b, f32x4 c) { return __builtin_amdgcn_mfma_f32_16x16x32_bf16(__builtin_bit_cast(bf16x8, a), __builtin_bit_cast(bf16x8, b), c, 0, 0, 0); }
DEV float fold32(float a, float b) { auto r = __builtin_amdgcn_permlane32_swap(f2u(a), f2u(b), false, false); return u2f(r[0]) + u2f(r[1]); }
DEV unsigned umax3(unsigned a, unsigned b, unsigned c) { return max(max(a, b), c); }
DEV unsigned vmax4(unsigned m, u32x4 v) { return umax3(umax3(m, v.x, v.y), v.z, v.w); }
DEV u32x8 cat8(u32x4 a, u32x4 b) { return __builtin_shufflevector(a, b, 0, 1, 2, 3, 4, 5, 6, 7); }
DEV u32x4 cat4(u32x2 a, u32x2 b) { return __builtin_shufflevector(a, b, 0, 1, 2, 3); }
constexpr unsigned BS=16384, RDY_T=208, FLAG_S=62;
#define REC_ASM \
  "s_cmp_eq_u32 s100, 0\n" \
  "s_cbranch_scc1 RB_start_%=\n" \
  "v_mov_b32 v18, v118\n" \
  "v_xor_b32 v121, 0x20, v120\n" \
  "v_xor_b32 v122, 0x8000, v120\n" \
  "v_xor_b32 v123, 0x8020, v120\n" \
  "v_xor_b32 v124, 0x10000, v120\n" \
  "v_xor_b32 v125, 0x10020, v120\n" \
  "v_xor_b32 v126, 0x18000, v120\n" \
  "v_xor_b32 v127, 0x18020, v120\n" \
  "v_xor_b32 v118, 0x80, v116\n" \
  "v_xor_b32 v119, 0x80, v117\n" \
  "s_lshl_b32 s76, s60, 21\n" \
  "s_lshl_b32 s96, s61, 8\n" \
  "s_add_u32 s76, s76, s96\n" \
  "s_lshl_b32 s77, s61, 18\n" \
  "s_lshl_b32 s96, s60, 9\n" \
  "s_add_u32 s77, s77, s96\n" \
  "s_lshl_b32 s78, s60, 15\n" \
  "s_lshr_b32 s96, s35, 17\n" \
  "s_and_b32 s96, s96, 0xf80\n" \
  "s_add_u32 s96, s96, 252182528\n" \
  "buffer_load_dword v17, off, s[40:43], s96 sc1\n" \
  "v_min_u32 v229, 1, v6\n" \
  "v_lshlrev_b32 v229, 30, v229\n" \
  "v_mov_b64 v[236:237], -1\n" \
  "v_mov_b64 v[238:239], -1\n" \
  "s_add_u32 m0, s78, 0\n" \
  "s_add_u32 s96, s76, 0\n" \
  "buffer_load_dwordx4 v116, s[52:55], s96 offen lds\n" \
  "s_add_u32 m0, s78, 1024\n" \
  "s_add_u32 s96, s76, 32768\n" \
  "buffer_load_dwordx4 v116, s[52:55], s96 offen lds\n" \
  "s_add_u32 m0, s78, 2048\n" \
  "s_add_u32 s96, s76, 65536\n" \
  "buffer_load_dwordx4 v118, s[52:55], s96 offen lds\n" \
  "s_add_u32 m0, s78, 3072\n" \
  "s_add_u32 s96, s76, 98304\n" \
  "buffer_load_dwordx4 v118, s[52:55], s96 offen lds\n" \
  "s_add_u32 m0, s78, 4096\n" \
  "s_add_u32 s96, s76, 131072\n" \
  "buffer_load_dwordx4 v116, s[52:55], s96 offen lds\n" \
  "s_add_u32 m0, s78, 5120\n" \
  "s_add_u32 s96, s76, 163840\n" \
  "buffer_load_dwordx4 v116, s[52:55], s96 offen lds\n" \
  "s_add_u32 m0, s78, 6144\n" \
  "s_add_u32 s96, s76, 196608\n" \
  "buffer_load_dwordx4 v118, s[52:55], s96 offen lds\n" \
  "s_add_u32 m0, s78, 7168\n" \
  "s_add_u32 s96, s76, 229376\n" \
  "buffer_load_dwordx4 v118, s[52:55], s96 offen lds\n" \
  "s_add_u32 m0, s78, 8192\n" \
  "s_add_u32 s96, s76, 262144\n" \
  "buffer_load_dwordx4 v116, s[52:55], s96 offen lds\n" \
  "s_add_u32 m0, s78, 9216\n" \
  "s_add_u32 s96, s76, 294912\n" \
  "buffer_load_dwordx4 v116, s[52:55], s96 offen lds\n" \
  "s_add_u32 m0, s78, 10240\n" \
  "s_add_u32 s96, s76, 327680\n" \
  "buffer_load_dwordx4 v118, s[52:55], s96 offen lds\n" \
  "s_add_u32 m0, s78, 11264\n" \
  "s_add_u32 s96, s76, 360448\n" \
  "buffer_load_dwordx4 v118, s[52:55], s96 offen lds\n" \
  "s_add_u32 m0, s78, 12288\n" \
  "s_add_u32 s96, s76, 393216\n" \
  "buffer_load_dwordx4 v116, s[52:55], s96 offen lds\n" \
  "s_add_u32 m0, s78, 13312\n" \
  "s_add_u32 s96, s76, 425984\n" \
  "buffer_load_dwordx4 v116, s[52:55], s96 offen lds\n" \
  "s_add_u32 m0, s78, 14336\n" \
  "s_add_u32 s96, s76, 458752\n" \
  "buffer_load_dwordx4 v118, s[52:55], s96 offen lds\n" \
  "s_add_u32 m0, s78, 15360\n" \
  "s_add_u32 s96, s76, 491520\n" \
  "buffer_load_dwordx4 v118, s[52:55], s96 offen lds\n" \
  "s_add_u32 m0, s78, 16384\n" \
  "s_add_u32 s96, s76, 524288\n" \
  "buffer_load_dwordx4 v116, s[52:55], s96 offen lds\n" \
  "s_add_u32 m0, s78, 17408\n" \
  "s_add_u32 s96, s76, 557056\n" \
  "buffer_load_dwordx4 v116, s[52:55], s96 offen lds\n" \
  "s_add_u32 m0, s78, 18432\n" \
  "s_add_u32 s96, s76, 589824\n" \
  "buffer_load_dwordx4 v118, s[52:55], s96 offen lds\n" \
  "s_add_u32 m0, s78, 19456\n" \
  "s_add_u32 s96, s76, 622592\n" \
  "buffer_load_dwordx4 v118, s[52:55], s96 offen lds\n" \
  "s_add_u32 m0, s78, 20480\n" \
  "s_add_u32 s96, s76, 655360\n" \
  "buffer_load_dwordx4 v116, s[52:55], s96 offen lds\n" \
  "s_add_u32 m0, s78, 21504\n" \
  "s_add_u32 s96, s76, 688128\n" \
  "buffer_load_dwordx4 v116, s[52:55], s96 offen lds\n" \
  "s_add_u32 m0, s78, 22528\n" \
  "s_add_u32 s96, s76, 720896\n" \
  "buffer_load_dwordx4 v118, s[52:55], s96 offen lds\n" \
  "s_add_u32 m0, s78, 23552\n" \
  "s_add_u32 s96, s76, 753664\n" \
  "buffer_load_dwordx4 v118, s[52:55], s96 offen lds\n" \
  "s_waitcnt vmcnt(16)\n" \
  "s_barrier\n" \
  "s_add_u32 m0, s78, 24576\n" \
  "s_add_u32 s96, s76, 786432\n" \
  "buffer_load_dwordx4 v116, s[52:55], s96 offen lds\n" \
  "s_add_u32 m0, s78, 25600\n" \
  "s_add_u32 s96, s76, 819200\n" \
  "buffer_load_dwordx4 v116, s[52:55], s96 offen lds\n" \
  "s_add_u32 m0, s78, 26624\n" \
  "s_add_u32 s96, s76, 851968\n" \
  "buffer_load_dwordx4 v118, s[52:55], s96 offen lds\n" \
  "s_add_u32 m0, s78, 27648\n" \
  "s_add_u32 s96, s76, 884736\n" \
  "buffer_load_dwordx4 v118, s[52:55], s96 offen lds\n" \
  "s_add_u32 m0, s78, 28672\n" \
  "s_add_u32 s96, s76, 917504\n" \
  "buffer_load_dwordx4 v116, s[52:55], s96 offen lds\n" \
  "s_add_u32 m0, s78, 29696\n" \
  "s_add_u32 s96, s76, 950272\n" \
  "buffer_load_dwordx4 v116, s[52:55], s96 offen lds\n" \
  "s_add_u32 m0, s78, 30720\n" \
  "s_add_u32 s96, s76, 983040\n" \
  "buffer_load_dwordx4 v118, s[52:55], s96 offen lds\n" \
  "s_add_u32 m0, s78, 31744\n" \
  "s_add_u32 s96, s76, 1015808\n" \
  "buffer_load_dwordx4 v118, s[52:55], s96 offen lds\n" \
  "ds_read_b64_tr_b16 a[0:1], v120 offset:0\n" \
  "ds_read_b64_tr_b16 a[2:3], v120 offset:1024\n" \
  "ds_read_b64_tr_b16 a[8:9], v121 offset:0\n" \
  "ds_read_b64_tr_b16 a[10:11], v121 offset:1024\n" \
  "ds_read_b64_tr_b16 a[96:97], v122 offset:0\n" \
  "ds_read_b64_tr_b16 a[98:99], v122 offset:1024\n" \
  "ds_read_b64_tr_b16 a[104:105], v123 offset:0\n" \
  "ds_read_b64_tr_b16 a[106:107], v123 offset:1024\n" \
  "ds_read_b64_tr_b16 a[144:145], v124 offset:0\n" \
  "ds_read_b64_tr_b16 a[146:147], v124 offset:1024\n" \
  "ds_read_b64_tr_b16 a[152:153], v125 offset:0\n" \
  "ds_read_b64_tr_b16 a[154:155], v125 offset:1024\n" \
  "ds_read_b64_tr_b16 a[192:193], v126 offset:0\n" \
  "ds_read_b64_tr_b16 a[194:195], v126 offset:1024\n" \
  "ds_read_b64_tr_b16 a[200:201], v127 offset:0\n" \
  "ds_read_b64_tr_b16 a[202:203], v127 offset:1024\n" \
  "s_waitcnt lgkmcnt(0)\n" \
  "s_waitcnt vmcnt(16)\n" \
  "s_barrier\n" \
  "s_add_u32 m0, s78, 0\n" \
  "s_add_u32 s96, s76, 1048576\n" \
  "buffer_load_dwordx4 v116, s[52:55], s96 offen lds\n" \
  "s_add_u32 m0, s78, 1024\n" \
  "s_add_u32 s96, s76, 1081344\n" \
  "buffer_load_dwordx4 v116, s[52:55], s96 offen lds\n" \
  "s_add_u32 m0, s78, 2048\n" \
  "s_add_u32 s96, s76, 1114112\n" \
  "buffer_load_dwordx4 v118, s[52:55], s96 offen lds\n" \
  "s_add_u32 m0, s78, 3072\n" \
  "s_add_u32 s96, s76, 1146880\n" \
  "buffer_load_dwordx4 v118, s[52:55], s96 offen lds\n" \
  "s_add_u32 m0, s78, 4096\n" \
  "s_add_u32 s96, s76, 1179648\n" \
  "buffer_load_dwordx4 v116, s[52:55], s96 offen lds\n" \
  "s_add_u32 m0, s78, 5120\n" \
  "s_add_u32 s96, s76, 1212416\n" \
  "buffer_load_dwordx4 v116, s[52:55], s96 offen lds\n" \
  "s_add_u32 m0, s78, 6144\n" \
  "s_add_u32 s96, s76, 1245184\n" \
  "buffer_load_dwordx4 v118, s[52:55], s96 offen lds\n" \
  "s_add_u32 m0, s78, 7168\n" \
  "s_add_u32 s96, s76, 1277952\n" \
  "buffer_load_dwordx4 v118, s[52:55], s96 offen lds\n" \
  "ds_read_b64_tr_b16 a[4:5], v120 offset:8192\n" \
  "ds_read_b64_tr_b16 a[6:7], v120 offset:9216\n" \
  "ds_read_b64_tr_b16 a[12:13], v121 offset:8192\n" \
  "ds_read_b64_tr_b16 a[14:15], v121 offset:9216\n" \
  "ds_read_b64_tr_b16 a[100:101], v122 offset:8192\n" \
  "ds_read_b64_tr_b16 a[102:103], v122 offset:9216\n" \
  "ds_read_b64_tr_b16 a[108:109], v123 offset:8192\n" \
  "ds_read_b64_tr_b16 a[110:111], v123 offset:9216\n" \
  "ds_read_b64_tr_b16 a[148:149], v124 offset:8192\n" \
  "ds_read_b64_tr_b16 a[150:151], v124 offset:9216\n" \
  "ds_read_b64_tr_b16 a[156:157], v125 offset:8192\n" \
  "ds_read_b64_tr_b16 a[158:159], v125 offset:9216\n" \
  "ds_read_b64_tr_b16 a[196:197], v126 offset:8192\n" \
  "ds_read_b64_tr_b16 a[198:199], v126 offset:9216\n" \
  "ds_read_b64_tr_b16 a[204:205], v127 offset:8192\n" \
  "ds_read_b64_tr_b16 a[206:207], v127 offset:9216\n" \
  "s_waitcnt lgkmcnt(0)\n" \
  "s_waitcnt vmcnt(16)\n" \
  "s_barrier\n" \
  "s_add_u32 m0, s78, 8192\n" \
  "s_add_u32 s96, s76, 1310720\n" \
  "buffer_load_dwordx4 v116, s[52:55], s96 offen lds\n" \
  "s_add_u32 m0, s78, 9216\n" \
  "s_add_u32 s96, s76, 1343488\n" \
  "buffer_load_dwordx4 v116, s[52:55], s96 offen lds\n" \
  "s_add_u32 m0, s78, 10240\n" \
  "s_add_u32 s96, s76, 1376256\n" \
  "buffer_load_dwordx4 v118, s[52:55], s96 offen lds\n" \
  "s_add_u32 m0, s78, 11264\n" \
  "s_add_u32 s96, s76, 1409024\n" \
  "buffer_load_dwordx4 v118, s[52:55], s96 offen lds\n" \
  "s_add_u32 m0, s78, 12288\n" \
  "s_add_u32 s96, s76, 1441792\n" \
  "buffer_load_dwordx4 v116, s[52:55], s96 offen lds\n" \
  "s_add_u32 m0, s78, 13312\n" \
  "s_add_u32 s96, s76, 1474560\n" \
  "buffer_load_dwordx4 v116, s[52:55], s96 offen lds\n" \
  "s_add_u32 m0, s78, 14336\n" \
  "s_add_u32 s96, s76, 1507328\n" \
  "buffer_load_dwordx4 v118, s[52:55], s96 offen lds\n" \
  "s_add_u32 m0, s78, 15360\n" \
  "s_add_u32 s96, s76, 1540096\n" \
  "buffer_load_dwordx4 v118, s[52:55], s96 offen lds\n" \
  "ds_read_b64_tr_b16 v[20:21], v120 offset:16384\n" \
  "ds_read_b64_tr_b16 v[22:23], v120 offset:17408\n" \
  "ds_read_b64_tr_b16 v[28:29], v121 offset:16384\n" \
  "ds_read_b64_tr_b16 v[30:31], v121 offset:17408\n" \
  "ds_read_b64_tr_b16 a[112:113], v122 offset:16384\n" \
  "ds_read_b64_tr_b16 a[114:115], v122 offset:17408\n" \
  "ds_read_b64_tr_b16 a[120:121], v123 offset:16384\n" \
  "ds_read_b64_tr_b16 a[122:123], v123 offset:17408\n" \
  "ds_read_b64_tr_b16 a[160:161], v124 offset:16384\n" \
  "ds_read_b64_tr_b16 a[162:163], v124 offset:17408\n" \
  "ds_read_b64_tr_b16 a[168:169], v125 offset:16384\n" \
  "ds_read_b64_tr_b16 a[170:171], v125 offset:17408\n" \
  "ds_read_b64_tr_b16 a[208:209], v126 offset:16384\n" \
  "ds_read_b64_tr_b16 a[210:211], v126 offset:17408\n" \
  "ds_read_b64_tr_b16 a[216:217], v127 offset:16384\n" \
  "ds_read_b64_tr_b16 a[218:219], v127 offset:17408\n" \
  "s_waitcnt lgkmcnt(0)\n" \
  "s_waitcnt vmcnt(16)\n" \
  "s_barrier\n" \
  "s_add_u32 m0, s78, 16384\n" \
  "s_add_u32 s96, s76, 1572864\n" \
  "buffer_load_dwordx4 v116, s[52:55], s96 offen lds\n" \
  "s_add_u32 m0, s78, 17408\n" \
  "s_add_u32 s96, s76, 1605632\n" \
  "buffer_load_dwordx4 v116, s[52:55], s96 offen lds\n" \
  "s_add_u32 m0, s78, 18432\n" \
  "s_add_u32 s96, s76, 1638400\n" \
  "buffer_load_dwordx4 v118, s[52:55], s96 offen lds\n" \
  "s_add_u32 m0, s78, 19456\n" \
  "s_add_u32 s96, s76, 1671168\n" \
  "buffer_load_dwordx4 v118, s[52:55], s96 offen lds\n" \
  "s_add_u32 m0, s78, 20480\n" \
  "s_add_u32 s96, s76, 1703936\n" \
  "buffer_load_dwordx4 v116, s[52:55], s96 offen lds\n" \
  "s_add_u32 m0, s78, 21504\n" \
  "s_add_u32 s96, s76, 1736704\n" \
  "buffer_load_dwordx4 v116, s[52:55], s96 offen lds\n" \
  "s_add_u32 m0, s78, 22528\n" \
  "s_add_u32 s96, s76, 1769472\n" \
  "buffer_load_dwordx4 v118, s[52:55], s96 offen lds\n" \
  "s_add_u32 m0, s78, 23552\n" \
  "s_add_u32 s96, s76, 1802240\n" \
  "buffer_load_dwordx4 v118, s[52:55], s96 offen lds\n" \
  "ds_read_b64_tr_b16 v[24:25], v120 offset:24576\n" \
  "ds_read_b64_tr_b16 v[26:27], v120 offset:25600\n" \
  "ds_read_b64_tr_b16 v[32:33], v121 offset:24576\n" \
  "ds_read_b64_tr_b16 v[34:35], v121 offset:25600\n" \
  "ds_read_b64_tr_b16 a[116:117], v122 offset:24576\n" \
  "ds_read_b64_tr_b16 a[118:119], v122 offset:25600\n" \
  "ds_read_b64_tr_b16 a[124:125], v123 offset:24576\n" \
  "ds_read_b64_tr_b16 a[126:127], v123 offset:25600\n" \
  "ds_read_b64_tr_b16 a[164:165], v124 offset:24576\n" \
  "ds_read_b64_tr_b16 a[166:167], v124 offset:25600\n" \
  "ds_read_b64_tr_b16 a[172:173], v125 offset:24576\n" \
  "ds_read_b64_tr_b16 a[174:175], v125 offset:25600\n" \
  "ds_read_b64_tr_b16 a[212:213], v126 offset:24576\n" \
  "ds_read_b64_tr_b16 a[214:215], v126 offset:25600\n" \
  "ds_read_b64_tr_b16 a[220:221], v127 offset:24576\n" \
  "ds_read_b64_tr_b16 a[222:223], v127 offset:25600\n" \
  "s_waitcnt lgkmcnt(0)\n" \
  "s_waitcnt vmcnt(16)\n" \
  "s_barrier\n" \
  "s_add_u32 m0, s78, 24576\n" \
  "s_add_u32 s96, s76, 1835008\n" \
  "buffer_load_dwordx4 v116, s[52:55], s96 offen lds\n" \
  "s_add_u32 m0, s78, 25600\n" \
  "s_add_u32 s96, s76, 1867776\n" \
  "buffer_load_dwordx4 v116, s[52:55], s96 offen lds\n" \
  "s_add_u32 m0, s78, 26624\n" \
  "s_add_u32 s96, s76, 1900544\n" \
  "buffer_load_dwordx4 v118, s[52:55], s96 offen lds\n" \
  "s_add_u32 m0, s78, 27648\n" \
  "s_add_u32 s96, s76, 1933312\n" \
  "buffer_load_dwordx4 v118, s[52:55], s96 offen lds\n" \
  "s_add_u32 m0, s78, 28672\n" \
  "s_add_u32 s96, s76, 1966080\n" \
  "buffer_load_dwordx4 v116, s[52:55], s96 offen lds\n" \
  "s_add_u32 m0, s78, 29696\n" \
  "s_add_u32 s96, s76, 1998848\n" \
  "buffer_load_dwordx4 v116, s[52:55], s96 offen lds\n" \
  "s_add_u32 m0, s78, 30720\n" \
  "s_add_u32 s96, s76, 2031616\n" \
  "buffer_load_dwordx4 v118, s[52:55], s96 offen lds\n" \
  "s_add_u32 m0, s78, 31744\n" \
  "s_add_u32 s96, s76, 2064384\n" \
  "buffer_load_dwordx4 v118, s[52:55], s96 offen lds\n" \
  "ds_read_b64_tr_b16 a[48:49], v120 offset:0\n" \
  "ds_read_b64_tr_b16 a[50:51], v120 offset:1024\n" \
  "ds_read_b64_tr_b16 a[56:57], v121 offset:0\n" \
  "ds_read_b64_tr_b16 a[58:59], v121 offset:1024\n" \
  "ds_read_b64_tr_b16 v[36:37], v122 offset:0\n" \
  "ds_read_b64_tr_b16 v[38:39], v122 offset:1024\n" \
  "ds_read_b64_tr_b16 v[44:45], v123 offset:0\n" \
  "ds_read_b64_tr_b16 v[46:47], v123 offset:1024\n" \
  "ds_read_b64_tr_b16 v[52:53], v124 offset:0\n" \
  "ds_read_b64_tr_b16 v[54:55], v124 offset:1024\n" \
  "ds_read_b64_tr_b16 v[60:61], v125 offset:0\n" \
  "ds_read_b64_tr_b16 v[62:63], v125 offset:1024\n" \
  "ds_read_b64_tr_b16 v[68:69], v126 offset:0\n" \
  "ds_read_b64_tr_b16 v[70:71], v126 offset:1024\n" \
  "ds_read_b64_tr_b16 v[76:77], v127 offset:0\n" \
  "ds_read_b64_tr_b16 v[78:79], v127 offset:1024\n" \
  "s_waitcnt lgkmcnt(0)\n" \
  "s_waitcnt vmcnt(16)\n" \
  "s_barrier\n" \
  "s_add_u32 m0, s78, 0\n" \
  "s_add_u32 s96, s76, 524288\n" \
  "buffer_load_dwordx4 v116, s[56:59], s96 offen lds\n" \
  "s_add_u32 m0, s78, 1024\n" \
  "s_add_u32 s96, s76, 557056\n" \
  "buffer_load_dwordx4 v116, s[56:59], s96 offen lds\n" \
  "s_add_u32 m0, s78, 2048\n" \
  "s_add_u32 s96, s76, 589824\n" \
  "buffer_load_dwordx4 v118, s[56:59], s96 offen lds\n" \
  "s_add_u32 m0, s78, 3072\n" \
  "s_add_u32 s96, s76, 622592\n" \
  "buffer_load_dwordx4 v118, s[56:59], s96 offen lds\n" \
  "s_add_u32 m0, s78, 4096\n" \
  "s_add_u32 s96, s76, 655360\n" \
  "buffer_load_dwordx4 v116, s[56:59], s96 offen lds\n" \
  "s_add_u32 m0, s78, 5120\n" \
  "s_add_u32 s96, s76, 688128\n" \
  "buffer_load_dwordx4 v116, s[56:59], s96 offen lds\n" \
  "s_add_u32 m0, s78, 6144\n" \
  "s_add_u32 s96, s76, 720896\n" \
  "buffer_load_dwordx4 v118, s[56:59], s96 offen lds\n" \
  "s_add_u32 m0, s78, 7168\n" \
  "s_add_u32 s96, s76, 753664\n" \
  "buffer_load_dwordx4 v118, s[56:59], s96 offen lds\n" \
  "ds_read_b64_tr_b16 a[52:53], v120 offset:8192\n" \
  "ds_read_b64_tr_b16 a[54:55], v120 offset:9216\n" \
  "ds_read_b64_tr_b16 a[60:61], v121 offset:8192\n" \
  "ds_read_b64_tr_b16 a[62:63], v121 offset:9216\n" \
  "ds_read_b64_tr_b16 v[40:41], v122 offset:8192\n" \
  "ds_read_b64_tr_b16 v[42:43], v122 offset:9216\n" \
  "ds_read_b64_tr_b16 v[48:49], v123 offset:8192\n" \
  "ds_read_b64_tr_b16 v[50:51], v123 offset:9216\n" \
  "ds_read_b64_tr_b16 v[56:57], v124 offset:8192\n" \
  "ds_read_b64_tr_b16 v[58:59], v124 offset:9216\n" \
  "ds_read_b64_tr_b16 v[64:65], v125 offset:8192\n" \
  "ds_read_b64_tr_b16 v[66:67], v125 offset:9216\n" \
  "ds_read_b64_tr_b16 v[72:73], v126 offset:8192\n" \
  "ds_read_b64_tr_b16 v[74:75], v126 offset:9216\n" \
  "ds_read_b64_tr_b16 v[80:81], v127 offset:8192\n" \
  "ds_read_b64_tr_b16 v[82:83], v127 offset:9216\n" \
  "s_waitcnt lgkmcnt(0)\n" \
  "s_waitcnt vmcnt(16)\n" \
  "s_barrier\n" \
  "s_add_u32 m0, s78, 8192\n" \
  "s_add_u32 s96, s76, 786432\n" \
  "buffer_load_dwordx4 v116, s[56:59], s96 offen lds\n" \
  "s_add_u32 m0, s78, 9216\n" \
  "s_add_u32 s96, s76, 819200\n" \
  "buffer_load_dwordx4 v116, s[56:59], s96 offen lds\n" \
  "s_add_u32 m0, s78, 10240\n" \
  "s_add_u32 s96, s76, 851968\n" \
  "buffer_load_dwordx4 v118, s[56:59], s96 offen lds\n" \
  "s_add_u32 m0, s78, 11264\n" \
  "s_add_u32 s96, s76, 884736\n" \
  "buffer_load_dwordx4 v118, s[56:59], s96 offen lds\n" \
  "s_add_u32 m0, s78, 12288\n" \
  "s_add_u32 s96, s76, 917504\n" \
  "buffer_load_dwordx4 v116, s[56:59], s96 offen lds\n" \
  "s_add_u32 m0, s78, 13312\n" \
  "s_add_u32 s96, s76, 950272\n" \
  "buffer_load_dwordx4 v116, s[56:59], s96 offen lds\n" \
  "s_add_u32 m0, s78, 14336\n" \
  "s_add_u32 s96, s76, 983040\n" \
  "buffer_load_dwordx4 v118, s[56:59], s96 offen lds\n" \
  "s_add_u32 m0, s78, 15360\n" \
  "s_add_u32 s96, s76, 1015808\n" \
  "buffer_load_dwordx4 v118, s[56:59], s96 offen lds\n" \
  "ds_read_b64_tr_b16 v[84:85], v120 offset:16384\n" \
  "ds_read_b64_tr_b16 v[86:87], v120 offset:17408\n" \
  "ds_read_b64_tr_b16 v[92:93], v121 offset:16384\n" \
  "ds_read_b64_tr_b16 v[94:95], v121 offset:17408\n" \
  "ds_read_b64_tr_b16 a[128:129], v122 offset:16384\n" \
  "ds_read_b64_tr_b16 a[130:131], v122 offset:17408\n" \
  "ds_read_b64_tr_b16 a[136:137], v123 offset:16384\n" \
  "ds_read_b64_tr_b16 a[138:139], v123 offset:17408\n" \
  "ds_read_b64_tr_b16 a[176:177], v124 offset:16384\n" \
  "ds_read_b64_tr_b16 a[178:179], v124 offset:17408\n" \
  "ds_read_b64_tr_b16 a[184:185], v125 offset:16384\n" \
  "ds_read_b64_tr_b16 a[186:187], v125 offset:17408\n" \
  "ds_read_b64_tr_b16 a[224:225], v126 offset:16384\n" \
  "ds_read_b64_tr_b16 a[226:227], v126 offset:17408\n" \
  "ds_read_b64_tr_b16 a[232:233], v127 offset:16384\n" \
  "ds_read_b64_tr_b16 a[234:235], v127 offset:17408\n" \
  "s_waitcnt lgkmcnt(0)\n" \
  "s_waitcnt vmcnt(16)\n" \
  "s_barrier\n" \
  "s_lshr_b32 s96, s35, 17\n" \
  "s_and_b32 s96, s96, 0xf80\n" \
  "s_add_u32 s96, s96, 252182528\n" \
  "buffer_load_dword v228, off, s[40:43], s96 sc1\n" \
  "buffer_load_dword v19, v19, s[88:91], 0 offen\n" \
  "s_add_u32 m0, s78, 16384\n" \
  "s_add_u32 s96, s76, 1048576\n" \
  "buffer_load_dwordx4 v116, s[56:59], s96 offen lds\n" \
  "s_add_u32 m0, s78, 17408\n" \
  "s_add_u32 s96, s76, 1081344\n" \
  "buffer_load_dwordx4 v116, s[56:59], s96 offen lds\n" \
  "s_add_u32 m0, s78, 18432\n" \
  "s_add_u32 s96, s76, 1114112\n" \
  "buffer_load_dwordx4 v118, s[56:59], s96 offen lds\n" \
  "s_add_u32 m0, s78, 19456\n" \
  "s_add_u32 s96, s76, 1146880\n" \
  "buffer_load_dwordx4 v118, s[56:59], s96 offen lds\n" \
  "s_add_u32 m0, s78, 20480\n" \
  "s_add_u32 s96, s76, 1179648\n" \
  "buffer_load_dwordx4 v116, s[56:59], s96 offen lds\n" \
  "s_add_u32 m0, s78, 21504\n" \
  "s_add_u32 s96, s76, 1212416\n" \
  "buffer_load_dwordx4 v116, s[56:59], s96 offen lds\n" \
  "s_add_u32 m0, s78, 22528\n" \
  "s_add_u32 s96, s76, 1245184\n" \
  "buffer_load_dwordx4 v118, s[56:59], s96 offen lds\n" \
  "s_add_u32 m0, s78, 23552\n" \
  "s_add_u32 s96, s76, 1277952\n" \
  "buffer_load_dwordx4 v118, s[56:59], s96 offen lds\n" \
  "ds_read_b64_tr_b16 v[88:89], v120 offset:24576\n" \
  "ds_read_b64_tr_b16 v[90:91], v120 offset:25600\n" \
  "ds_read_b64_tr_b16 v[96:97], v121 offset:24576\n" \
  "ds_read_b64_tr_b16 v[98:99], v121 offset:25600\n" \
  "ds_read_b64_tr_b16 a[132:133], v122 offset:24576\n" \
  "ds_read_b64_tr_b16 a[134:135], v122 offset:25600\n" \
  "ds_read_b64_tr_b16 a[140:141], v123 offset:24576\n" \
  "ds_read_b64_tr_b16 a[142:143], v123 offset:25600\n" \
  "ds_read_b64_tr_b16 a[180:181], v124 offset:24576\n" \
  "ds_read_b64_tr_b16 a[182:183], v124 offset:25600\n" \
  "ds_read_b64_tr_b16 a[188:189], v125 offset:24576\n" \
  "ds_read_b64_tr_b16 a[190:191], v125 offset:25600\n" \
  "ds_read_b64_tr_b16 a[228:229], v126 offset:24576\n" \
  "ds_read_b64_tr_b16 a[230:231], v126 offset:25600\n" \
  "ds_read_b64_tr_b16 a[236:237], v127 offset:24576\n" \
  "ds_read_b64_tr_b16 a[238:239], v127 offset:25600\n" \
  "s_waitcnt lgkmcnt(0)\n" \
  "s_waitcnt vmcnt(18)\n" \
  "s_barrier\n" \
  "s_add_u32 m0, s78, 24576\n" \
  "s_add_u32 s96, s76, 1310720\n" \
  "buffer_load_dwordx4 v116, s[56:59], s96 offen lds\n" \
  "s_add_u32 m0, s78, 25600\n" \
  "s_add_u32 s96, s76, 1343488\n" \
  "buffer_load_dwordx4 v116, s[56:59], s96 offen lds\n" \
  "s_add_u32 m0, s78, 26624\n" \
  "s_add_u32 s96, s76, 1376256\n" \
  "buffer_load_dwordx4 v118, s[56:59], s96 offen lds\n" \
  "s_add_u32 m0, s78, 27648\n" \
  "s_add_u32 s96, s76, 1409024\n" \
  "buffer_load_dwordx4 v118, s[56:59], s96 offen lds\n" \
  "s_add_u32 m0, s78, 28672\n" \
  "s_add_u32 s96, s76, 1441792\n" \
  "buffer_load_dwordx4 v116, s[56:59], s96 offen lds\n" \
  "s_add_u32 m0, s78, 29696\n" \
  "s_add_u32 s96, s76, 1474560\n" \
  "buffer_load_dwordx4 v116, s[56:59], s96 offen lds\n" \
  "s_add_u32 m0, s78, 30720\n" \
  "s_add_u32 s96, s76, 1507328\n" \
  "buffer_load_dwordx4 v118, s[56:59], s96 offen lds\n" \
  "s_add_u32 m0, s78, 31744\n" \
  "s_add_u32 s96, s76, 1540096\n" \
  "buffer_load_dwordx4 v118, s[56:59], s96 offen lds\n" \
  "ds_read_b64_tr_b16 a[32:33], v120 offset:0\n" \
  "ds_read_b64_tr_b16 a[34:35], v120 offset:1024\n" \
  "ds_read_b64_tr_b16 a[40:41], v121 offset:0\n" \
  "ds_read_b64_tr_b16 a[42:43], v121 offset:1024\n" \
  "ds_read_b64_tr_b16 v[240:241], v122 offset:0\n" \
  "ds_read_b64_tr_b16 v[242:243], v122 offset:1024\n" \
  "ds_read_b64_tr_b16 v[248:249], v123 offset:0\n" \
  "ds_read_b64_tr_b16 v[250:251], v123 offset:1024\n" \
  "ds_read_b64_tr_b16 v[128:129], v124 offset:0\n" \
  "ds_read_b64_tr_b16 v[130:131], v124 offset:1024\n" \
  "ds_read_b64_tr_b16 v[136:137], v125 offset:0\n" \
  "ds_read_b64_tr_b16 v[138:139], v125 offset:1024\n" \
  "ds_read_b64_tr_b16 v[192:193], v126 offset:0\n" \
  "ds_read_b64_tr_b16 v[194:195], v126 offset:1024\n" \
  "ds_read_b64_tr_b16 v[200:201], v127 offset:0\n" \
  "ds_read_b64_tr_b16 v[202:203], v127 offset:1024\n" \
  "s_waitcnt lgkmcnt(0)\n" \
  "s_waitcnt vmcnt(18)\n" \
  "s_barrier\n" \
  "s_add_u32 m0, s78, 0\n" \
  "s_add_u32 s96, s76, 1572864\n" \
  "buffer_load_dwordx4 v116, s[56:59], s96 offen lds\n" \
  "s_add_u32 m0, s78, 1024\n" \
  "s_add_u32 s96, s76, 1605632\n" \
  "buffer_load_dwordx4 v116, s[56:59], s96 offen lds\n" \
  "s_add_u32 m0, s78, 2048\n" \
  "s_add_u32 s96, s76, 1638400\n" \
  "buffer_load_dwordx4 v118, s[56:59], s96 offen lds\n" \
  "s_add_u32 m0, s78, 3072\n" \
  "s_add_u32 s96, s76, 1671168\n" \
  "buffer_load_dwordx4 v118, s[56:59], s96 offen lds\n" \
  "s_add_u32 m0, s78, 4096\n" \
  "s_add_u32 s96, s76, 1703936\n" \
  "buffer_load_dwordx4 v116, s[56:59], s96 offen lds\n" \
  "s_add_u32 m0, s78, 5120\n" \
  "s_add_u32 s96, s76, 1736704\n" \
  "buffer_load_dwordx4 v116, s[56:59], s96 offen lds\n" \
  "s_add_u32 m0, s78, 6144\n" \
  "s_add_u32 s96, s76, 1769472\n" \
  "buffer_load_dwordx4 v118, s[56:59], s96 offen lds\n" \
  "s_add_u32 m0, s78, 7168\n" \
  "s_add_u32 s96, s76, 1802240\n" \
  "buffer_load_dwordx4 v118, s[56:59], s96 offen lds\n" \
  "ds_read_b64_tr_b16 a[36:37], v120 offset:8192\n" \
  "ds_read_b64_tr_b16 a[38:39], v120 offset:9216\n" \
  "ds_read_b64_tr_b16 a[44:45], v121 offset:8192\n" \
  "ds_read_b64_tr_b16 a[46:47], v121 offset:9216\n" \
  "ds_read_b64_tr_b16 v[244:245], v122 offset:8192\n" \
  "ds_read_b64_tr_b16 v[246:247], v122 offset:9216\n" \
  "ds_read_b64_tr_b16 v[252:253], v123 offset:8192\n" \
  "ds_read_b64_tr_b16 v[254:255], v123 offset:9216\n" \
  "ds_read_b64_tr_b16 v[132:133], v124 offset:8192\n" \
  "ds_read_b64_tr_b16 v[134:135], v124 offset:9216\n" \
  "ds_read_b64_tr_b16 v[140:141], v125 offset:8192\n" \
  "ds_read_b64_tr_b16 v[142:143], v125 offset:9216\n" \
  "ds_read_b64_tr_b16 v[196:197], v126 offset:8192\n" \
  "ds_read_b64_tr_b16 v[198:199], v126 offset:9216\n" \
  "ds_read_b64_tr_b16 v[204:205], v127 offset:8192\n" \
  "ds_read_b64_tr_b16 v[206:207], v127 offset:9216\n" \
  "s_waitcnt lgkmcnt(0)\n" \
  "s_waitcnt vmcnt(16)\n" \
  "s_barrier\n" \
  "s_and_b32 s96, s35, 34\n" \
  "s_cbranch_scc1 RA_ye_%=\n" \
  "RA_yc_%=:\n" \
  "v_readfirstlane_b32 s96, v17\n" \
  "v_readfirstlane_b32 s97, v228\n" \
  "s_cmp_eq_u32 s96, s97\n" \
  "s_cbranch_scc1 RA_ys_%=\n" \
  "s_xor_b32 s96, s97, s35\n" \
  "s_and_b32 s96, s96, 28\n" \
  "s_cbranch_scc1 RA_yb_%=\n" \
  "RA_ye_%=:\n" \
  "v_mbcnt_lo_u32_b32 v17, -1, 0\n" \
  "v_mbcnt_hi_u32_b32 v17, -1, v17\n" \
  "v_lshlrev_b32 v17, 2, v17\n" \
  "s_lshr_b32 s96, s35, 16\n" \
  "s_and_b32 s96, s96, 0x1f00\n" \
  "s_add_u32 s96, s96, 252190720\n" \
  "buffer_load_dword v228, v17, s[40:43], s96 offen sc1\n" \
  "s_add_u32 m0, s78, 8192\n" \
  "s_add_u32 s96, s76, 1835008\n" \
  "buffer_load_dwordx4 v116, s[56:59], s96 offen lds\n" \
  "s_add_u32 m0, s78, 9216\n" \
  "s_add_u32 s96, s76, 1867776\n" \
  "buffer_load_dwordx4 v116, s[56:59], s96 offen lds\n" \
  "s_add_u32 m0, s78, 10240\n" \
  "s_add_u32 s96, s76, 1900544\n" \
  "buffer_load_dwordx4 v118, s[56:59], s96 offen lds\n" \
  "s_add_u32 m0, s78, 11264\n" \
  "s_add_u32 s96, s76, 1933312\n" \
  "buffer_load_dwordx4 v118, s[56:59], s96 offen lds\n" \
  "s_add_u32 m0, s78, 12288\n" \
  "s_add_u32 s96, s76, 1966080\n" \
  "buffer_load_dwordx4 v116, s[56:59], s96 offen lds\n" \
  "s_add_u32 m0, s78, 13312\n" \
  "s_add_u32 s96, s76, 1998848\n" \
  "buffer_load_dwordx4 v116, s[56:59], s96 offen lds\n" \
  "s_add_u32 m0, s78, 14336\n" \
  "s_add_u32 s96, s76, 2031616\n" \
  "buffer_load_dwordx4 v118, s[56:59], s96 offen lds\n" \
  "s_add_u32 m0, s78, 15360\n" \
  "s_add_u32 s96, s76, 2064384\n" \
  "buffer_load_dwordx4 v118, s[56:59], s96 offen lds\n" \
  "ds_read_b64_tr_b16 a[64:65], v120 offset:16384\n" \
  "ds_read_b64_tr_b16 a[66:67], v120 offset:17408\n" \
  "ds_read_b64_tr_b16 a[72:73], v121 offset:16384\n" \
  "ds_read_b64_tr_b16 a[74:75], v121 offset:17408\n" \
  "ds_read_b64_tr_b16 v[208:209], v122 offset:16384\n" \
  "ds_read_b64_tr_b16 v[210:211], v122 offset:17408\n" \
  "ds_read_b64_tr_b16 v[216:217], v123 offset:16384\n" \
  "ds_read_b64_tr_b16 v[218:219], v123 offset:17408\n" \
  "ds_read_b64_tr_b16 v[100:101], v124 offset:16384\n" \
  "ds_read_b64_tr_b16 v[102:103], v124 offset:17408\n" \
  "ds_read_b64_tr_b16 v[108:109], v125 offset:16384\n" \
  "ds_read_b64_tr_b16 v[110:111], v125 offset:17408\n" \
  "ds_read_b64_tr_b16 v[160:161], v126 offset:16384\n" \
  "ds_read_b64_tr_b16 v[162:163], v126 offset:17408\n" \
  "ds_read_b64_tr_b16 v[168:169], v127 offset:16384\n" \
  "ds_read_b64_tr_b16 v[170:171], v127 offset:17408\n" \
  "s_waitcnt lgkmcnt(0)\n" \
  "s_waitcnt vmcnt(17)\n" \
  "s_barrier\n" \
  "s_add_u32 m0, s78, 16384\n" \
  "s_add_u32 s96, s76, 0\n" \
  "buffer_load_dwordx4 v116, s[56:59], s96 offen lds\n" \
  "s_add_u32 m0, s78, 17408\n" \
  "s_add_u32 s96, s76, 32768\n" \
  "buffer_load_dwordx4 v116, s[56:59], s96 offen lds\n" \
  "s_add_u32 m0, s78, 18432\n" \
  "s_add_u32 s96, s76, 65536\n" \
  "buffer_load_dwordx4 v118, s[56:59], s96 offen lds\n" \
  "s_add_u32 m0, s78, 19456\n" \
  "s_add_u32 s96, s76, 98304\n" \
  "buffer_load_dwordx4 v118, s[56:59], s96 offen lds\n" \
  "s_add_u32 m0, s78, 20480\n" \
  "s_add_u32 s96, s76, 131072\n" \
  "buffer_load_dwordx4 v116, s[56:59], s96 offen lds\n" \
  "s_add_u32 m0, s78, 21504\n" \
  "s_add_u32 s96, s76, 163840\n" \
  "buffer_load_dwordx4 v116, s[56:59], s96 offen lds\n" \
  "s_add_u32 m0, s78, 22528\n" \
  "s_add_u32 s96, s76, 196608\n" \
  "buffer_load_dwordx4 v118, s[56:59], s96 offen lds\n" \
  "s_add_u32 m0, s78, 23552\n" \
  "s_add_u32 s96, s76, 229376\n" \
  "buffer_load_dwordx4 v118, s[56:59], s96 offen lds\n" \
  "ds_read_b64_tr_b16 a[68:69], v120 offset:24576\n" \
  "ds_read_b64_tr_b16 a[70:71], v120 offset:25600\n" \
  "ds_read_b64_tr_b16 a[76:77], v121 offset:24576\n" \
  "ds_read_b64_tr_b16 a[78:79], v121 offset:25600\n" \
  "ds_read_b64_tr_b16 v[212:213], v122 offset:24576\n" \
  "ds_read_b64_tr_b16 v[214:215], v122 offset:25600\n" \
  "ds_read_b64_tr_b16 v[220:221], v123 offset:24576\n" \
  "ds_read_b64_tr_b16 v[222:223], v123 offset:25600\n" \
  "ds_read_b64_tr_b16 v[104:105], v124 offset:24576\n" \
  "ds_read_b64_tr_b16 v[106:107], v124 offset:25600\n" \
  "ds_read_b64_tr_b16 v[112:113], v125 offset:24576\n" \
  "ds_read_b64_tr_b16 v[114:115], v125 offset:25600\n" \
  "ds_read_b64_tr_b16 v[164:165], v126 offset:24576\n" \
  "ds_read_b64_tr_b16 v[166:167], v126 offset:25600\n" \
  "ds_read_b64_tr_b16 v[172:173], v127 offset:24576\n" \
  "ds_read_b64_tr_b16 v[174:175], v127 offset:25600\n" \
  "s_waitcnt lgkmcnt(0)\n" \
  "s_waitcnt vmcnt(17)\n" \
  "s_barrier\n" \
  "s_add_u32 m0, s78, 24576\n" \
  "s_add_u32 s96, s76, 262144\n" \
  "buffer_load_dwordx4 v116, s[56:59], s96 offen lds\n" \
  "s_add_u32 m0, s78, 25600\n" \
  "s_add_u32 s96, s76, 294912\n" \
  "buffer_load_dwordx4 v116, s[56:59], s96 offen lds\n" \
  "s_add_u32 m0, s78, 26624\n" \
  "s_add_u32 s96, s76, 327680\n" \
  "buffer_load_dwordx4 v118, s[56:59], s96 offen lds\n" \
  "s_add_u32 m0, s78, 27648\n" \
  "s_add_u32 s96, s76, 360448\n" \
  "buffer_load_dwordx4 v118, s[56:59], s96 offen lds\n" \
  "s_add_u32 m0, s78, 28672\n" \
  "s_add_u32 s96, s76, 393216\n" \
  "buffer_load_dwordx4 v116, s[56:59], s96 offen lds\n" \
  "s_add_u32 m0, s78, 29696\n" \
  "s_add_u32 s96, s76, 425984\n" \
  "buffer_load_dwordx4 v116, s[56:59], s96 offen lds\n" \
  "s_add_u32 m0, s78, 30720\n" \
  "s_add_u32 s96, s76, 458752\n" \
  "buffer_load_dwordx4 v118, s[56:59], s96 offen lds\n" \
  "s_add_u32 m0, s78, 31744\n" \
  "s_add_u32 s96, s76, 491520\n" \
  "buffer_load_dwordx4 v118, s[56:59], s96 offen lds\n" \
  "ds_read_b64_tr_b16 a[80:81], v120 offset:0\n" \
  "ds_read_b64_tr_b16 a[82:83], v120 offset:1024\n" \
  "ds_read_b64_tr_b16 a[88:89], v121 offset:0\n" \
  "ds_read_b64_tr_b16 a[90:91], v121 offset:1024\n" \
  "ds_read_b64_tr_b16 v[224:225], v122 offset:0\n" \
  "ds_read_b64_tr_b16 v[226:227], v122 offset:1024\n" \
  "ds_read_b64_tr_b16 v[232:233], v123 offset:0\n" \
  "ds_read_b64_tr_b16 v[234:235], v123 offset:1024\n" \
  "ds_read_b64_tr_b16 v[144:145], v124 offset:0\n" \
  "ds_read_b64_tr_b16 v[146:147], v124 offset:1024\n" \
  "ds_read_b64_tr_b16 v[152:153], v125 offset:0\n" \
  "ds_read_b64_tr_b16 v[154:155], v125 offset:1024\n" \
  "ds_read_b64_tr_b16 v[176:177], v126 offset:0\n" \
  "ds_read_b64_tr_b16 v[178:179], v126 offset:1024\n" \
  "ds_read_b64_tr_b16 v[184:185], v127 offset:0\n" \
  "ds_read_b64_tr_b16 v[186:187], v127 offset:1024\n" \
  "s_waitcnt lgkmcnt(0)\n" \
  "s_waitcnt vmcnt(16)\n" \
  "s_barrier\n" \
  "s_bitcmp1_b32 s35, 1\n" \
  "s_cbranch_scc1 RA_yd_%=\n" \
  "RA_yf_%=:\n" \
  "v_cmp_gt_i32 vcc, 0, v228\n" \
  "s_bcnt1_i32_b64 s97, vcc\n" \
  "v_and_b32 v228, 0x10001, v228\n" \
  "v_cmp_ne_u32 vcc, 0x10001, v228\n" \
  "s_cbranch_vccnz RA_yt_%=\n" \
  "RA_yg_%=:\n" \
  "s_lshr_b32 s96, s35, 17\n" \
  "s_and_b32 s96, s96, 0xf80\n" \
  "s_add_u32 s96, s96, 252186624\n" \
  "v_mov_b32 v228, 1\n" \
  "buffer_atomic_add v228, v229, s[40:43], s96 offen\n" \
  "s_cmp_gt_u32 s97, 32\n" \
  "s_cselect_b32 s96, 0x200000, 0\n" \
  "s_or_b32 s35, s35, s96\n" \
  "s_or_b32 s35, s35, 2\n" \
  "s_add_u32 s40, s40, s96\n" \
  "s_addc_u32 s41, s41, 0\n" \
  "s_add_u32 s36, s36, s96\n" \
  "s_addc_u32 s37, s37, 0\n" \
  "RA_yd_%=:\n" \
  "s_add_u32 s96, s74, 0\n" \
  "buffer_store_dwordx4 v[236:239], v1, s[36:39], s96 offen sc1\n" \
  "buffer_store_dwordx4 v[236:239], v1, s[36:39], s96 offen offset:1024 sc1\n" \
  "s_add_u32 s96, s74, 1048576\n" \
  "buffer_store_dwordx4 v[236:239], v1, s[36:39], s96 offen sc1\n" \
  "buffer_store_dwordx4 v[236:239], v1, s[36:39], s96 offen offset:1024 sc1\n" \
  "s_add_u32 s96, s74, 4194304\n" \
  "buffer_store_dwordx4 v[236:239], v1, s[36:39], s96 offen sc1\n" \
  "buffer_store_dwordx4 v[236:239], v1, s[36:39], s96 offen offset:1024 sc1\n" \
  "s_add_u32 s96, s74, 5242880\n" \
  "buffer_store_dwordx4 v[236:239], v1, s[36:39], s96 offen sc1\n" \
  "buffer_store_dwordx4 v[236:239], v1, s[36:39], s96 offen offset:1024 sc1\n" \
  "s_add_u32 s96, s74, 8388608\n" \
  "buffer_store_dwordx4 v[236:239], v1, s[36:39], s96 offen sc1\n" \
  "buffer_store_dwordx4 v[236:239], v1, s[36:39], s96 offen offset:1024 sc1\n" \
  "s_add_u32 s96, s74, 9437184\n" \
  "buffer_store_dwordx4 v[236:239], v1, s[36:39], s96 offen sc1\n" \
  "buffer_store_dwordx4 v[236:239], v1, s[36:39], s96 offen offset:1024 sc1\n" \
  "v_lshlrev_b32 v228, 3, v1\n" \
  "s_getpc_b64 s[96:97]\n" \
  "RA_pc_%=:\n" \
  "s_add_u32 s96, s96, RA_top_%=-RA_pc_%=\n" \
  "s_addc_u32 s97, s97, 0\n" \
  "v_min_u32 v228, RA_end_%=-RA_top_%=, v228\n" \
  "s_mov_b32 m0, 0x27000\n" \
  "s_nop 0\n" \
  "global_load_lds_dword v228, s[96:97]\n" \
  "ds_write_b128 v6, v[20:23] offset:0\n" \
  "ds_write_b128 v6, v[24:27] offset:1024\n" \
  "ds_write_b128 v6, v[28:31] offset:2048\n" \
  "ds_write_b128 v6, v[32:35] offset:3072\n" \
  "ds_write_b128 v6, v[84:87] offset:4096\n" \
  "ds_write_b128 v6, v[88:91] offset:5120\n" \
  "ds_write_b128 v6, v[92:95] offset:6144\n" \
  "ds_write_b128 v6, v[96:99] offset:7168\n" \
  "s_waitcnt lgkmcnt(0)\n" \
  "ds_read_b64_tr_b16 a[84:85], v120 offset:8192\n" \
  "ds_read_b64_tr_b16 a[86:87], v120 offset:9216\n" \
  "ds_read_b64_tr_b16 a[92:93], v121 offset:8192\n" \
  "ds_read_b64_tr_b16 a[94:95], v121 offset:9216\n" \
  "ds_read_b64_tr_b16 v[228:229], v122 offset:8192\n" \
  "ds_read_b64_tr_b16 v[230:231], v122 offset:9216\n" \
  "ds_read_b64_tr_b16 v[236:237], v123 offset:8192\n" \
  "ds_read_b64_tr_b16 v[238:239], v123 offset:9216\n" \
  "ds_read_b64_tr_b16 v[148:149], v124 offset:8192\n" \
  "ds_read_b64_tr_b16 v[150:151], v124 offset:9216\n" \
  "ds_read_b64_tr_b16 v[156:157], v125 offset:8192\n" \
  "ds_read_b64_tr_b16 v[158:159], v125 offset:9216\n" \
  "ds_read_b64_tr_b16 v[180:181], v126 offset:8192\n" \
  "ds_read_b64_tr_b16 v[182:183], v126 offset:9216\n" \
  "ds_read_b64_tr_b16 v[188:189], v127 offset:8192\n" \
  "ds_read_b64_tr_b16 v[190:191], v127 offset:9216\n" \
  "s_waitcnt lgkmcnt(0)\n" \
  "s_waitcnt vmcnt(21)\n" \
  "s_barrier\n" \
  "ds_write_b128 v6, v[36:39] offset:8192\n" \
  "ds_write_b128 v6, v[40:43] offset:9216\n" \
  "ds_write_b128 v6, v[44:47] offset:10240\n" \
  "ds_write_b128 v6, v[48:51] offset:11264\n" \
  "ds_write_b128 v6, v[52:55] offset:12288\n" \
  "ds_write_b128 v6, v[56:59] offset:13312\n" \
  "ds_write_b128 v6, v[60:63] offset:14336\n" \
  "ds_write_b128 v6, v[64:67] offset:15360\n" \
  "s_waitcnt lgkmcnt(0)\n" \
  "ds_read_b64_tr_b16 a[16:17], v120 offset:16384\n" \
  "ds_read_b64_tr_b16 a[18:19], v120 offset:17408\n" \
  "ds_read_b64_tr_b16 a[24:25], v121 offset:16384\n" \
  "ds_read_b64_tr_b16 a[26:27], v121 offset:17408\n" \
  "ds_read_b64_tr_b16 v[20:21], v122 offset:16384\n" \
  "ds_read_b64_tr_b16 v[22:23], v122 offset:17408\n" \
  "ds_read_b64_tr_b16 v[28:29], v123 offset:16384\n" \
  "ds_read_b64_tr_b16 v[30:31], v123 offset:17408\n" \
  "ds_read_b64_tr_b16 a[240:241], v124 offset:16384\n" \
  "ds_read_b64_tr_b16 a[242:243], v124 offset:17408\n" \
  "ds_read_b64_tr_b16 a[248:249], v125 offset:16384\n" \
  "ds_read_b64_tr_b16 a[250:251], v125 offset:17408\n" \
  "ds_read_b64_tr_b16 v[84:85], v126 offset:16384\n" \
  "ds_read_b64_tr_b16 v[86:87], v126 offset:17408\n" \
  "ds_read_b64_tr_b16 v[92:93], v127 offset:16384\n" \
  "ds_read_b64_tr_b16 v[94:95], v127 offset:17408\n" \
  "s_waitcnt lgkmcnt(0)\n" \
  "s_waitcnt vmcnt(13)\n" \
  "s_barrier\n" \
  "ds_write_b128 v6, v[68:71] offset:16384\n" \
  "ds_write_b128 v6, v[72:75] offset:17408\n" \
  "ds_write_b128 v6, v[76:79] offset:18432\n" \
  "ds_write_b128 v6, v[80:83] offset:19456\n" \
  "ds_write_b128 v6, v[100:103] offset:20480\n" \
  "ds_write_b128 v6, v[104:107] offset:21504\n" \
  "ds_write_b128 v6, v[108:111] offset:22528\n" \
  "ds_write_b128 v6, v[112:115] offset:23552\n" \
  "s_waitcnt lgkmcnt(0)\n" \
  "ds_read_b64_tr_b16 a[20:21], v120 offset:24576\n" \
  "ds_read_b64_tr_b16 a[22:23], v120 offset:25600\n" \
  "ds_read_b64_tr_b16 a[28:29], v121 offset:24576\n" \
  "ds_read_b64_tr_b16 a[30:31], v121 offset:25600\n" \
  "ds_read_b64_tr_b16 v[24:25], v122 offset:24576\n" \
  "ds_read_b64_tr_b16 v[26:27], v122 offset:25600\n" \
  "ds_read_b64_tr_b16 v[32:33], v123 offset:24576\n" \
  "ds_read_b64_tr_b16 v[34:35], v123 offset:25600\n" \
  "ds_read_b64_tr_b16 a[244:245], v124 offset:24576\n" \
  "ds_read_b64_tr_b16 a[246:247], v124 offset:25600\n" \
  "ds_read_b64_tr_b16 a[252:253], v125 offset:24576\n" \
  "ds_read_b64_tr_b16 a[254:255], v125 offset:25600\n" \
  "ds_read_b64_tr_b16 v[88:89], v126 offset:24576\n" \
  "ds_read_b64_tr_b16 v[90:91], v126 offset:25600\n" \
  "ds_read_b64_tr_b16 v[96:97], v127 offset:24576\n" \
  "ds_read_b64_tr_b16 v[98:99], v127 offset:25600\n" \
  "s_waitcnt lgkmcnt(0)\n" \
  "s_waitcnt vmcnt(0)\n" \
  "s_barrier\n" \
  "s_mov_b32 s47, 0\n" \
  "s_mov_b32 s50, 4194304\n" \
  "s_mov_b32 s51, 8388608\n" \
  "s_mov_b32 s65, 12582912\n" \
  "s_add_u32 s67, s65, 98304\n" \
  "s_add_u32 s66, s65, 196608\n" \
  "v_min_u32 v19, -2, v19\n" \
  "s_cmp_eq_u32 s60, 0\n" \
  "s_cbranch_scc0 RA_g2_%=\n" \
  "s_add_u32 s96, s65, s71\n" \
  "buffer_store_dword v19, v10, s[36:39], s96 offen sc1\n" \
  "RA_g2_%=:\n" \
  "ds_write_b128 v6, v[84:87] offset:24576\n" \
  "ds_write_b128 v6, v[88:91] offset:25600\n" \
  "ds_write_b128 v6, v[92:95] offset:26624\n" \
  "ds_write_b128 v6, v[96:99] offset:27648\n" \
  "ds_write_b128 v6, v[20:23] offset:28672\n" \
  "ds_write_b128 v6, v[24:27] offset:29696\n" \
  "ds_write_b128 v6, v[28:31] offset:30720\n" \
  "ds_write_b128 v6, v[32:35] offset:31744\n" \
  "s_waitcnt lgkmcnt(0)\n" \
  "s_mov_b32 s64, 0\n" \
  "s_sub_u32 s84, s48, 4194304\n" \
  "s_mov_b32 s89, s86\n" \
  "s_mov_b32 s46, s45\n" \
  "s_cmp_lt_u32 s60, 2\n" \
  "s_cbranch_scc1 RA_g1_%=\n" \
  "RA_g0_%=:\n" \
  "buffer_load_dword v17, off, s[40:43], s45 offset:64 sc1\n" \
  "s_waitcnt vmcnt(0)\n" \
  "v_readfirstlane_b32 s96, v17\n" \
  "s_cmp_ge_u32 s96, 64\n" \
  "s_cbranch_scc1 RA_g1_%=\n" \
  "s_sleep 8\n" \
  "s_sub_u32 s63, s63, 1\n" \
  "s_cbranch_scc0 RA_g0_%=\n" \
  "s_mov_b32 s63, 0\n" \
  "RA_g1_%=:\n" \
  "s_cmp_lt_u32 s60, 2\n" \
  "s_cbranch_scc1 RA_kd843_%=\n" \
  "s_add_u32 m0, s44, 0\n" \
  "s_add_u32 s96, s89, 0\n" \
  "buffer_load_dwordx4 v1, s[40:43], s96 offen sc1 lds\n" \
  "s_add_u32 m0, s44, 1024\n" \
  "s_add_u32 s96, s89, 1024\n" \
  "buffer_load_dwordx4 v1, s[40:43], s96 offen sc1 lds\n" \
  "s_add_u32 m0, s44, 8192\n" \
  "s_add_u32 s96, s89, 2048\n" \
  "buffer_load_dwordx4 v1, s[40:43], s96 offen sc1 lds\n" \
  "s_add_u32 m0, s44, 9216\n" \
  "s_add_u32 s96, s89, 3072\n" \
  "buffer_load_dwordx4 v1, s[40:43], s96 offen sc1 lds\n" \
  "RA_kd843_%=:\n" \
  "v_mov_b32 v17, 0\n" \
  "v_mov_b64 v[36:37], 0\n" \
  "v_mov_b64 v[38:39], 0\n" \
  "v_mov_b64 v[40:41], 0\n" \
  "v_mov_b64 v[42:43], 0\n" \
  "v_mov_b64 v[44:45], 0\n" \
  "v_mov_b64 v[46:47], 0\n" \
  "v_mov_b64 v[48:49], 0\n" \
  "v_mov_b64 v[50:51], 0\n" \
  "ds_read_b128 v[52:55], v6 offset:0\n" \
  "ds_read_b128 v[56:59], v6 offset:1024\n" \
  "ds_read_b128 v[60:63], v6 offset:2048\n" \
  "ds_read_b128 v[64:67], v6 offset:3072\n" \
  "s_waitcnt lgkmcnt(0)\n" \
  "RA_top_%=:\n" \
  "s_add_u32 s46, s46, 128\n" \
  "s_add_u32 s84, s84, 4194304\n" \
  "s_cmp_eq_u32 s84, s49\n" \
  "s_cselect_b32 s84, s48, s84\n" \
  "s_add_u32 s76, s65, s70\n" \
  "s_add_u32 s77, s67, s71\n" \
  "s_add_u32 s78, s66, s94\n" \
  "s_add_u32 s81, s65, s73\n" \
  "s_add_u32 s82, s81, 4096\n" \
  "s_add_u32 s83, s47, s74\n" \
  "s_add_u32 s85, s84, 4096\n" \
  "v_mov_b64 v[124:125], -1\n" \
  "v_mov_b64 v[126:127], -1\n" \
  "s_add_u32 s79, s65, s72\n" \
  "s_add_u32 s88, s64, 1\n" \
  "s_and_b32 s89, s88, 63\n" \
  "s_lshl_b32 s89, s89, 8\n" \
  "s_and_b32 s88, s88, 4\n" \
  "s_add_u32 s89, s89, s88\n" \
  "s_lshl_b32 s89, s89, 11\n" \
  "s_xor_b32 s89, s89, s86\n" \
  "s_and_b32 s88, s64, 15\n" \
  "s_lshl_b32 s88, s88, 17\n" \
  "s_add_u32 s88, s88, s87\n" \
  "s_waitcnt lgkmcnt(0)\n" \
  "v_cmp_gt_u32 vcc, 208, v17\n" \
  "v_mov_b32 v17, s95\n" \
  "s_cbranch_vccnz RA_r0_%=\n" \
  "s_mov_b32 s96, s68\n" \
  ".p2align 3\n" \
  "RA_d1_%=:\n" \
  "s_sub_u32 s96, s96, 1\n" \
  "s_cbranch_scc0 RA_d1_%=\n" \
  "RA_p1_%=:\n" \
  "buffer_load_dwordx4 v[20:23], v1, s[36:39], s76 offen sc1\n" \
  "buffer_load_dwordx4 v[24:27], v1, s[36:39], s76 offen offset:1024 sc1\n" \
  "buffer_load_dwordx4 v[28:31], v1, s[36:39], s76 offen offset:2048 sc1\n" \
  "buffer_load_dwordx4 v[32:35], v1, s[36:39], s76 offen offset:3072 sc1\n" \
  "RA_p1b_%=:\n" \
  "s_waitcnt vmcnt(3)\n" \
  "ds_write_b128 v2, v[20:23] offset:0\n" \
  "v_smfmac_f32_16x16x64_bf16 v[36:39], v[20:23], a[0:7], v0\n" \
  "v_max3_u32 v18, v20, v21, v22\n" \
  "v_max_u32 v18, v18, v23\n" \
  "v_smfmac_f32_16x16x64_bf16 v[40:43], v[20:23], a[8:15], v0\n" \
  "v_smfmac_f32_16x16x64_bf16 v[44:47], v[20:23], a[16:23], v0\n" \
  "v_smfmac_f32_16x16x64_bf16 v[48:51], v[20:23], a[24:31], v0\n" \
  "s_waitcnt vmcnt(2)\n" \
  "ds_write_b128 v2, v[24:27] offset:1024\n" \
  "v_max3_u32 v18, v18, v24, v25\n" \
  "v_max3_u32 v18, v18, v26, v27\n" \
  "s_waitcnt vmcnt(1)\n" \
  "ds_write_b128 v2, v[28:31] offset:2048\n" \
  "v_max3_u32 v18, v18, v28, v29\n" \
  "v_max3_u32 v18, v18, v30, v31\n" \
  "s_waitcnt vmcnt(0)\n" \
  "ds_write_b128 v2, v[32:35] offset:3072\n" \
  "v_max3_u32 v18, v18, v32, v33\n" \
  "v_max3_u32 v18, v18, v34, v35\n" \
  "v_cmp_eq_u32 vcc, -1, v18\n" \
  "s_cbranch_vccnz RA_r1_%=\n" \
  "RA_k1_%=:\n" \
  "v_smfmac_f32_16x16x64_bf16 v[36:39], v[24:27], v[52:59], v0\n" \
  "ds_read_b128 v[52:55], v6 offset:4096\n" \
  "ds_read_b128 v[56:59], v6 offset:5120\n" \
  "v_smfmac_f32_16x16x64_bf16 v[40:43], v[24:27], v[60:67], v0\n" \
  "ds_read_b128 v[60:63], v6 offset:6144\n" \
  "ds_read_b128 v[64:67], v6 offset:7168\n" \
  "v_smfmac_f32_16x16x64_bf16 v[44:47], v[24:27], a[32:39], v0\n" \
  "s_waitcnt lgkmcnt(4)\n" \
  "s_barrier\n" \
  "ds_read_b128 v[20:23], v3 offset:0\n" \
  "v_smfmac_f32_16x16x64_bf16 v[48:51], v[24:27], a[40:47], v0\n" \
  "ds_read_b128 v[68:71], v4 offset:0\n" \
  "v_smfmac_f32_16x16x64_bf16 v[36:39], v[28:31], a[48:55], v0\n" \
  "ds_read_b128 v[24:27], v3 offset:1024\n" \
  "buffer_store_dwordx4 v[124:127], v7, s[36:39], s78 offen sc1\n" \
  "v_smfmac_f32_16x16x64_bf16 v[40:43], v[28:31], a[56:63], v0\n" \
  "ds_read_b128 v[72:75], v4 offset:1024\n" \
  "v_smfmac_f32_16x16x64_bf16 v[44:47], v[28:31], a[64:71], v0\n" \
  "ds_read_b128 v[76:79], v4 offset:2048\n" \
  "v_smfmac_f32_16x16x64_bf16 v[48:51], v[28:31], a[72:79], v0\n" \
  "s_waitcnt lgkmcnt(7)\n" \
  "ds_read_b128 v[28:31], v3 offset:2048\n" \
  "buffer_load_dwordx4 v[84:87], v1, s[40:43], s84 offen sc1\n" \
  "v_smfmac_f32_16x16x64_bf16 v[36:39], v[32:35], v[52:59], v0\n" \
  "s_waitcnt lgkmcnt(6)\n" \
  "ds_read_b128 v[52:55], v6 offset:8192\n" \
  "ds_read_b128 v[56:59], v6 offset:9216\n" \
  "v_smfmac_f32_16x16x64_bf16 v[40:43], v[32:35], v[60:67], v0\n" \
  "ds_read_b128 v[60:63], v6 offset:10240\n" \
  "ds_read_b128 v[64:67], v6 offset:11264\n" \
  "v_smfmac_f32_16x16x64_bf16 v[44:47], v[32:35], a[80:87], v0\n" \
  "ds_read_b128 v[80:83], v4 offset:3072\n" \
  "v_smfmac_f32_16x16x64_bf16 v[48:51], v[32:35], a[88:95], v0\n" \
  "s_waitcnt lgkmcnt(10)\n" \
  "ds_read_b128 v[32:35], v3 offset:3072\n" \
  "buffer_load_dwordx4 v[88:91], v1, s[40:43], s84 offen offset:1024 sc1\n" \
  "v_smfmac_f32_16x16x64_bf16 v[36:39], v[20:23], a[96:103], v0\n" \
  "ds_read_b128 v[116:119], v5 offset:2048\n" \
  "v_smfmac_f32_16x16x64_bf16 v[40:43], v[20:23], a[104:111], v0\n" \
  "s_waitcnt lgkmcnt(10)\n" \
  "ds_read_b128 v[20:23], v5 offset:0\n" \
  "v_smfmac_f32_16x16x64_bf16 v[36:39], v[24:27], a[112:119], v0\n" \
  "ds_read_b128 v[120:123], v5 offset:3072\n" \
  "v_smfmac_f32_16x16x64_bf16 v[40:43], v[24:27], a[120:127], v0\n" \
  "s_waitcnt lgkmcnt(7)\n" \
  "ds_read_b128 v[24:27], v5 offset:1024\n" \
  "buffer_load_dwordx4 v[92:95], v1, s[40:43], s84 offen offset:2048 sc1\n" \
  "v_smfmac_f32_16x16x64_bf16 v[36:39], v[28:31], v[52:59], v0\n" \
  "s_waitcnt lgkmcnt(6)\n" \
  "ds_read_b128 v[52:55], v6 offset:12288\n" \
  "ds_read_b128 v[56:59], v6 offset:13312\n" \
  "v_smfmac_f32_16x16x64_bf16 v[40:43], v[28:31], v[60:67], v0\n" \
  "s_waitcnt lgkmcnt(6)\n" \
  "ds_read_b128 v[60:63], v6 offset:14336\n" \
  "ds_read_b128 v[64:67], v6 offset:15360\n" \
  "v_smfmac_f32_16x16x64_bf16 v[36:39], v[32:35], a[128:135], v0\n" \
  "v_smfmac_f32_16x16x64_bf16 v[40:43], v[32:35], a[136:143], v0\n" \
  "buffer_load_dwordx4 v[96:99], v1, s[40:43], s84 offen offset:3072 sc1\n" \
  "v_smfmac_f32_16x16x64_bf16 v[36:39], v[68:71], a[144:151], v0\n" \
  "v_smfmac_f32_16x16x64_bf16 v[40:43], v[68:71], a[152:159], v0\n" \
  "v_smfmac_f32_16x16x64_bf16 v[36:39], v[72:75], a[160:167], v0\n" \
  "v_smfmac_f32_16x16x64_bf16 v[40:43], v[72:75], a[168:175], v0\n" \
  "s_waitcnt lgkmcnt(2)\n" \
  "buffer_load_dwordx4 v[100:103], v1, s[40:43], s85 offen sc1\n" \
  "v_smfmac_f32_16x16x64_bf16 v[36:39], v[76:79], v[52:59], v0\n" \
  "s_waitcnt lgkmcnt(0)\n" \
  "ds_read_b128 v[52:55], v6 offset:16384\n" \
  "ds_read_b128 v[56:59], v6 offset:17408\n" \
  "v_smfmac_f32_16x16x64_bf16 v[40:43], v[76:79], v[60:67], v0\n" \
  "ds_read_b128 v[60:63], v6 offset:18432\n" \
  "ds_read_b128 v[64:67], v6 offset:19456\n" \
  "v_smfmac_f32_16x16x64_bf16 v[36:39], v[80:83], a[176:183], v0\n" \
  "v_smfmac_f32_16x16x64_bf16 v[40:43], v[80:83], a[184:191], v0\n" \
  "buffer_load_dwordx4 v[104:107], v1, s[40:43], s85 offen offset:1024 sc1\n" \
  "v_smfmac_f32_16x16x64_bf16 v[36:39], v[20:23], a[192:199], v0\n" \
  "v_smfmac_f32_16x16x64_bf16 v[40:43], v[20:23], a[200:207], v0\n" \
  "v_smfmac_f32_16x16x64_bf16 v[36:39], v[24:27], a[208:215], v0\n" \
  "v_smfmac_f32_16x16x64_bf16 v[40:43], v[24:27], a[216:223], v0\n" \
  "s_waitcnt lgkmcnt(2)\n" \
  "buffer_load_dwordx4 v[108:111], v1, s[40:43], s85 offen offset:2048 sc1\n" \
  "v_smfmac_f32_16x16x64_bf16 v[36:39], v[116:119], v[52:59], v0\n" \
  "s_waitcnt lgkmcnt(0)\n" \
  "ds_read_b128 v[52:55], v6 offset:20480\n" \
  "ds_read_b128 v[56:59], v6 offset:21504\n" \
  "v_smfmac_f32_16x16x64_bf16 v[40:43], v[116:119], v[60:67], v0\n" \
  "ds_read_b128 v[60:63], v6 offset:22528\n" \
  "ds_read_b128 v[64:67], v6 offset:23552\n" \
  "v_smfmac_f32_16x16x64_bf16 v[36:39], v[120:123], a[224:231], v0\n" \
  "v_smfmac_f32_16x16x64_bf16 v[40:43], v[120:123], a[232:239], v0\n" \
  "buffer_load_dwordx4 v[112:115], v1, s[40:43], s85 offen offset:3072 sc1\n" \
  "v_smfmac_f32_16x16x64_bf16 v[44:47], v[68:71], a[240:247], v0\n" \
  "v_smfmac_f32_16x16x64_bf16 v[48:51], v[68:71], a[248:255], v0\n" \
  "buffer_load_dword v17, v17, s[40:43], s46 offen offset:128 sc1\n" \
  "v_smfmac_f32_16x16x64_bf16 v[44:47], v[72:75], v[128:135], v0\n" \
  "s_nop 2\n" \
  "v_permlane32_swap_b32 v36, v40\n" \
  "v_smfmac_f32_16x16x64_bf16 v[48:51], v[72:75], v[136:143], v0\n" \
  "s_waitcnt lgkmcnt(2)\n" \
  "v_permlane32_swap_b32 v37, v41\n" \
  "v_smfmac_f32_16x16x64_bf16 v[44:47], v[76:79], v[52:59], v0\n" \
  "s_waitcnt lgkmcnt(0)\n" \
  "ds_read_b128 v[52:55], v6 offset:24576\n" \
  "v_permlane32_swap_b32 v38, v42\n" \
  "v_smfmac_f32_16x16x64_bf16 v[48:51], v[76:79], v[60:67], v0\n" \
  "ds_read_b128 v[56:59], v6 offset:25600\n" \
  "v_permlane32_swap_b32 v39, v43\n" \
  "v_smfmac_f32_16x16x64_bf16 v[44:47], v[80:83], v[144:151], v0\n" \
  "ds_read_b128 v[60:63], v6 offset:26624\n" \
  "v_add_f32 v36, v36, v40\n" \
  "v_smfmac_f32_16x16x64_bf16 v[48:51], v[80:83], v[152:159], v0\n" \
  "ds_read_b128 v[64:67], v6 offset:27648\n" \
  "v_add_f32 v37, v37, v41\n" \
  "v_smfmac_f32_16x16x64_bf16 v[44:47], v[116:119], v[160:167], v0\n" \
  "v_add_f32 v38, v38, v42\n" \
  "v_smfmac_f32_16x16x64_bf16 v[48:51], v[116:119], v[168:175], v0\n" \
  "v_add_f32 v39, v39, v43\n" \
  "v_smfmac_f32_16x16x64_bf16 v[44:47], v[120:123], v[176:183], v0\n" \
  "v_cvt_pk_bf16_f32 v40, v36, v37\n" \
  "v_smfmac_f32_16x16x64_bf16 v[48:51], v[120:123], v[184:191], v0\n" \
  "s_waitcnt lgkmcnt(2)\n" \
  "v_cvt_pk_bf16_f32 v41, v38, v39\n" \
  "v_smfmac_f32_16x16x64_bf16 v[44:47], v[20:23], v[52:59], v0\n" \
  "s_waitcnt lgkmcnt(0)\n" \
  "ds_write_b64 v13, v[40:41]\n" \
  "ds_read_b128 v[52:55], v6 offset:28672\n" \
  "v_smfmac_f32_16x16x64_bf16 v[48:51], v[20:23], v[60:67], v0\n" \
  "ds_read_b128 v[56:59], v6 offset:29696\n" \
  "ds_read_b128 v[20:23], v3 offset:0\n" \
  "v_smfmac_f32_16x16x64_bf16 v[44:47], v[24:27], v[192:199], v0\n" \
  "ds_read_b128 v[60:63], v6 offset:30720\n" \
  "v_smfmac_f32_16x16x64_bf16 v[48:51], v[24:27], v[200:207], v0\n" \
  "ds_read_b128 v[64:67], v6 offset:31744\n" \
  "ds_read_b128 v[24:27], v3 offset:1024\n" \
  "s_cmp_eq_u32 s60, 0\n" \
  "s_waitcnt lgkmcnt(6)\n" \
  "s_barrier\n" \
  "s_cbranch_scc0 RA_t1_%=\n" \
  "v_smfmac_f32_16x16x64_bf16 v[44:47], v[28:31], v[208:215], v0\n" \
  "ds_read_b64_tr_b16 v[68:69], v14 offset:0\n" \
  "ds_read_b64_tr_b16 v[70:71], v14 offset:256\n" \
  "v_smfmac_f32_16x16x64_bf16 v[48:51], v[28:31], v[216:223], v0\n" \
  "ds_read_b128 v[36:39], v2 offset:4096\n" \
  "ds_read_b128 v[40:43], v2 offset:20480\n" \
  "v_smfmac_f32_16x16x64_bf16 v[44:47], v[32:35], v[224:231], v0\n" \
  "ds_read_b64_tr_b16 v[72:73], v14 offset:8192\n" \
  "ds_read_b64_tr_b16 v[74:75], v14 offset:8448\n" \
  "v_smfmac_f32_16x16x64_bf16 v[48:51], v[32:35], v[232:239], v0\n" \
  "s_waitcnt lgkmcnt(9)\n" \
  "ds_read_b128 v[28:31], v2 offset:5120\n" \
  "ds_read_b128 v[32:35], v2 offset:21504\n" \
  "v_smfmac_f32_16x16x64_bf16 v[44:47], v[20:23], v[52:59], v0\n" \
  "s_waitcnt lgkmcnt(9)\n" \
  "ds_read_b64_tr_b16 v[76:77], v14 offset:16384\n" \
  "ds_read_b64_tr_b16 v[78:79], v14 offset:16640\n" \
  "v_smfmac_f32_16x16x64_bf16 v[48:51], v[20:23], v[60:67], v0\n" \
  "s_waitcnt lgkmcnt(10)\n" \
  "ds_read_b128 v[52:55], v2 offset:12288\n" \
  "ds_read_b128 v[56:59], v2 offset:28672\n" \
  "v_smfmac_f32_16x16x64_bf16 v[44:47], v[24:27], v[240:247], v0\n" \
  "ds_read_b64_tr_b16 v[80:81], v14 offset:24576\n" \
  "ds_read_b64_tr_b16 v[82:83], v14 offset:24832\n" \
  "v_smfmac_f32_16x16x64_bf16 v[48:51], v[24:27], v[248:255], v0\n" \
  "s_waitcnt lgkmcnt(11)\n" \
  "ds_read_b128 v[60:63], v2 offset:13312\n" \
  "ds_read_b128 v[64:67], v2 offset:29696\n" \
  "v_mfma_f32_16x16x32_bf16 v[116:119], v[36:39], v[68:71], 0\n" \
  "s_waitcnt lgkmcnt(12)\n" \
  "v_mfma_f32_16x16x32_bf16 v[120:123], v[40:43], v[68:71], 0\n" \
  "s_waitcnt lgkmcnt(9)\n" \
  "s_nop 0\n" \
  "v_permlane32_swap_b32 v44, v48\n" \
  "v_permlane32_swap_b32 v45, v49\n" \
  "v_permlane32_swap_b32 v46, v50\n" \
  "v_permlane32_swap_b32 v47, v51\n" \
  "v_add_f32 v44, v44, v48\n" \
  "v_add_f32 v45, v45, v49\n" \
  "v_add_f32 v46, v46, v50\n" \
  "v_add_f32 v47, v47, v51\n" \
  "v_cvt_pk_bf16_f32 v48, v44, v45\n" \
  "v_cvt_pk_bf16_f32 v49, v46, v47\n" \
  "ds_write_b64 v13, v[48:49] offset:512\n" \
  "v_mfma_f32_16x16x32_bf16 v[116:119], v[28:31], v[72:75], v[116:119]\n" \
  "s_waitcnt lgkmcnt(9)\n" \
  "s_mov_b32 s96, 0x3e0293ee\n" \
  "v_mfma_f32_16x16x32_bf16 v[120:123], v[32:35], v[72:75], v[120:123]\n" \
  "s_waitcnt lgkmcnt(6)\n" \
  "v_mfma_f32_16x16x32_bf16 v[116:119], v[52:55], v[76:79], v[116:119]\n" \
  "s_waitcnt lgkmcnt(5)\n" \
  "v_mfma_f32_16x16x32_bf16 v[120:123], v[56:59], v[76:79], v[120:123]\n" \
  "s_waitcnt lgkmcnt(2)\n" \
  "s_waitcnt lgkmcnt(0)\n" \
  "s_barrier\n" \
  "ds_read_b64_tr_b16 v[20:21], v14 offset:512\n" \
  "ds_read_b64_tr_b16 v[22:23], v14 offset:768\n" \
  "v_mfma_f32_16x16x32_bf16 v[116:119], v[60:63], v[80:83], v[116:119]\n" \
  "ds_read_b64_tr_b16 v[24:25], v14 offset:8704\n" \
  "ds_read_b64_tr_b16 v[26:27], v14 offset:8960\n" \
  "v_mfma_f32_16x16x32_bf16 v[120:123], v[64:67], v[80:83], v[120:123]\n" \
  "ds_read_b64_tr_b16 v[28:29], v14 offset:16896\n" \
  "ds_read_b64_tr_b16 v[30:31], v14 offset:17152\n" \
  "ds_read_b64_tr_b16 v[32:33], v14 offset:25088\n" \
  "ds_read_b64_tr_b16 v[34:35], v14 offset:25344\n" \
  "s_nop 0\n" \
  "v_max3_f32 v36, v116, v117, v118\n" \
  "s_nop 1\n" \
  "v_max3_f32 v37, v120, v121, v122\n" \
  "v_max3_f32 v36, v36, v119, v123\n" \
  "v_max_f32 v39, v36, v37\n" \
  "v_max_f32 v37, v36, v37\n" \
  "v_mul_f32 v54, s96, v37\n" \
  "v_pk_fma_f32 v[116:117], v[116:117], s[96:97], v[54:55] op_sel_hi:[1,0,0] neg_lo:[0,0,1] neg_hi:[0,0,1]\n" \
  "v_pk_fma_f32 v[118:119], v[118:119], s[96:97], v[54:55] op_sel_hi:[1,0,0] neg_lo:[0,0,1] neg_hi:[0,0,1]\n" \
  "v_pk_fma_f32 v[120:121], v[120:121], s[96:97], v[54:55] op_sel_hi:[1,0,0] neg_lo:[0,0,1] neg_hi:[0,0,1]\n" \
  "v_pk_fma_f32 v[122:123], v[122:123], s[96:97], v[54:55] op_sel_hi:[1,0,0] neg_lo:[0,0,1] neg_hi:[0,0,1]\n" \
  "s_waitcnt lgkmcnt(6)\n" \
  "v_mfma_f32_16x16x32_bf16 v[124:127], v[20:23], v[68:71], 0\n" \
  "s_waitcnt lgkmcnt(4)\n" \
  "v_permlane32_swap_b32 v39, v37\n" \
  "v_mfma_f32_16x16x32_bf16 v[124:127], v[24:27], v[72:75], v[124:127]\n" \
  "s_waitcnt lgkmcnt(2)\n" \
  "v_max_f32 v36, v39, v37\n" \
  "v_max_f32 v37, v39, v37\n" \
  "v_mfma_f32_16x16x32_bf16 v[124:127], v[28:31], v[76:79], v[124:127]\n" \
  "s_waitcnt lgkmcnt(0)\n" \
  "v_exp_f32 v116, v116\n" \
  "v_mfma_f32_16x16x32_bf16 v[124:127], v[32:35], v[80:83], v[124:127]\n" \
  "v_exp_f32 v117, v117\n" \
  "v_permlane16_swap_b32 v36, v37\n" \
  "v_exp_f32 v118, v118\n" \
  "v_exp_f32 v119, v119\n" \
  "v_exp_f32 v120, v120\n" \
  "v_exp_f32 v121, v121\n" \
  "v_exp_f32 v122, v122\n" \
  "v_exp_f32 v123, v123\n" \
  "v_max_f32 v36, v36, v37\n" \
  "v_max3_f32 v37, v124, v125, v36\n" \
  "v_max3_f32 v39, v126, v127, v37\n" \
  "v_max3_f32 v37, v126, v127, v37\n" \
  "v_pk_add_f32 v[40:41], v[116:117], v[118:119]\n" \
  "v_pk_add_f32 v[42:43], v[120:121], v[122:123]\n" \
  "v_permlane16_swap_b32 v39, v37\n" \
  "v_pk_add_f32 v[40:41], v[40:41], v[42:43]\n" \
  "v_max_f32 v37, v39, v37\n" \
  "v_mul_f32 v42, s96, v37\n" \
  "v_pk_fma_f32 v[124:125], v[124:125], s[96:97], v[42:43] op_sel_hi:[1,0,0] neg_lo:[0,0,1] neg_hi:[0,0,1]\n" \
  "v_pk_fma_f32 v[126:127], v[126:127], s[96:97], v[42:43] op_sel_hi:[1,0,0] neg_lo:[0,0,1] neg_hi:[0,0,1]\n" \
  "v_sub_f32 v39, v54, v42\n" \
  "v_exp_f32 v124, v124\n" \
  "v_exp_f32 v125, v125\n" \
  "v_exp_f32 v126, v126\n" \
  "v_exp_f32 v127, v127\n" \
  "v_exp_f32 v39, v39\n" \
  "v_add_f32 v40, v40, v41\n" \
  "v_pk_add_f32 v[42:43], v[124:125], v[126:127]\n" \
  "v_mul_f32 v40, v40, v39\n" \
  "v_add_f32 v36, v42, v43\n" \
  "v_fma_f32 v38, v36, 0.5, v40\n" \
  "v_fma_f32 v37, v36, 0.5, v40\n" \
  "s_nop 1\n" \
  "v_permlane32_swap_b32 v38, v37\n" \
  "v_add_f32 v36, v38, v37\n" \
  "v_add_f32 v37, v38, v37\n" \
  "s_nop 1\n" \
  "v_permlane16_swap_b32 v36, v37\n" \
  "v_add_f32 v36, v36, v37\n" \
  "v_rcp_f32 v36, v36\n" \
  "s_nop 0\n" \
  "v_mul_f32 v42, v39, v36\n" \
  "v_pk_mul_f32 v[116:117], v[116:117], v[42:43] op_sel_hi:[1,0]\n" \
  "v_pk_mul_f32 v[118:119], v[118:119], v[42:43] op_sel_hi:[1,0]\n" \
  "v_pk_mul_f32 v[120:121], v[120:121], v[42:43] op_sel_hi:[1,0]\n" \
  "v_pk_mul_f32 v[122:123], v[122:123], v[42:43] op_sel_hi:[1,0]\n" \
  "v_cvt_pk_bf16_f32 v44, v116, v117\n" \
  "v_cvt_pk_bf16_f32 v45, v118, v119\n" \
  "v_cvt_pk_bf16_f32 v46, v120, v121\n" \
  "v_cvt_pk_bf16_f32 v47, v122, v123\n" \
  "buffer_store_dwordx4 v[44:47], v15, s[36:39], s79 offen sc1\n" \
  "v_pk_mul_f32 v[124:125], v[124:125], v[36:37] op_sel_hi:[1,0]\n" \
  "v_pk_mul_f32 v[126:127], v[126:127], v[36:37] op_sel_hi:[1,0]\n" \
  "v_cvt_pk_bf16_f32 v48, v124, v125\n" \
  "v_cvt_pk_bf16_f32 v49, v126, v127\n" \
  "buffer_store_dwordx2 v[48:49], v16, s[36:39], s79 offen sc1\n" \
  "s_branch RA_ns_%=\n" \
  "RA_t1_%=:\n" \
  "v_smfmac_f32_16x16x64_bf16 v[44:47], v[28:31], v[208:215], v0\n" \
  "v_smfmac_f32_16x16x64_bf16 v[48:51], v[28:31], v[216:223], v0\n" \
  "v_smfmac_f32_16x16x64_bf16 v[44:47], v[32:35], v[224:231], v0\n" \
  "v_smfmac_f32_16x16x64_bf16 v[48:51], v[32:35], v[232:239], v0\n" \
  "s_waitcnt lgkmcnt(3)\n" \
  "v_smfmac_f32_16x16x64_bf16 v[44:47], v[20:23], v[52:59], v0\n" \
  "s_waitcnt lgkmcnt(1)\n" \
  "v_smfmac_f32_16x16x64_bf16 v[48:51], v[20:23], v[60:67], v0\n" \
  "s_waitcnt lgkmcnt(0)\n" \
  "v_smfmac_f32_16x16x64_bf16 v[44:47], v[24:27], v[240:247], v0\n" \
  "v_smfmac_f32_16x16x64_bf16 v[48:51], v[24:27], v[248:255], v0\n" \
  "s_cmp_eq_u32 s60, 1\n" \
  "s_nop 6\n" \
  "v_permlane32_swap_b32 v44, v48\n" \
  "v_permlane32_swap_b32 v45, v49\n" \
  "v_permlane32_swap_b32 v46, v50\n" \
  "v_permlane32_swap_b32 v47, v51\n" \
  "v_add_f32 v44, v44, v48\n" \
  "v_add_f32 v45, v45, v49\n" \
  "v_add_f32 v46, v46, v50\n" \
  "v_add_f32 v47, v47, v51\n" \
  "v_cvt_pk_bf16_f32 v48, v44, v45\n" \
  "v_cvt_pk_bf16_f32 v49, v46, v47\n" \
  "ds_write_b64 v13, v[48:49] offset:512\n" \
  "s_waitcnt lgkmcnt(0)\n" \
  "s_barrier\n" \
  "s_cbranch_scc0 RA_t2_%=\n" \
  "ds_read_b64_tr_b16 v[20:21], v14 offset:512\n" \
  "ds_read_b64_tr_b16 v[22:23], v14 offset:768\n" \
  "ds_read_b64_tr_b16 v[24:25], v14 offset:8704\n" \
  "ds_read_b64_tr_b16 v[26:27], v14 offset:8960\n" \
  "ds_read_b64_tr_b16 v[28:29], v14 offset:16896\n" \
  "ds_read_b64_tr_b16 v[30:31], v14 offset:17152\n" \
  "ds_read_b64_tr_b16 v[32:33], v14 offset:25088\n" \
  "ds_read_b64_tr_b16 v[34:35], v14 offset:25344\n" \
  "s_waitcnt lgkmcnt(0)\n" \
  "buffer_store_dwordx4 v[20:23], v15, s[40:43], s88 offen sc1\n" \
  "buffer_store_dwordx4 v[24:27], v15, s[40:43], s88 offen offset:64 sc1\n" \
  "buffer_store_dwordx4 v[28:31], v15, s[40:43], s88 offen offset:128 sc1\n" \
  "buffer_store_dwordx4 v[32:35], v15, s[40:43], s88 offen offset:192 sc1\n" \
  "s_cmp_ge_u32 s64, 62\n" \
  "s_cbranch_scc0 RA_t2_%=\n" \
  "s_waitcnt vmcnt(0)\n" \
  "s_lshl_b32 s96, s62, 7\n" \
  "s_add_u32 s96, s96, s45\n" \
  "s_add_u32 s97, s64, 200\n" \
  "s_mov_b64 exec, 1\n" \
  "v_mov_b32 v18, s97\n" \
  "buffer_store_dword v18, off, s[40:43], s96 offset:192 sc1\n" \
  "s_mov_b64 exec, -1\n" \
  "RA_t2_%=:\n" \
  "s_cmp_lt_u32 s60, 2\n" \
  "s_cbranch_scc1 RA_kd1123_%=\n" \
  "s_add_u32 m0, s44, 0\n" \
  "s_add_u32 s96, s89, 0\n" \
  "buffer_load_dwordx4 v1, s[40:43], s96 offen sc1 lds\n" \
  "s_add_u32 m0, s44, 1024\n" \
  "s_add_u32 s96, s89, 1024\n" \
  "buffer_load_dwordx4 v1, s[40:43], s96 offen sc1 lds\n" \
  "s_add_u32 m0, s44, 8192\n" \
  "s_add_u32 s96, s89, 2048\n" \
  "buffer_load_dwordx4 v1, s[40:43], s96 offen sc1 lds\n" \
  "s_add_u32 m0, s44, 9216\n" \
  "s_add_u32 s96, s89, 3072\n" \
  "buffer_load_dwordx4 v1, s[40:43], s96 offen sc1 lds\n" \
  "RA_kd1123_%=:\n" \
  "RA_ns_%=:\n" \
  "s_nop 1\n" \
  "s_cmp_eq_u32 s64, 63\n" \
  "s_cbranch_scc1 RA_end_%=\n" \
  "v_mov_b64 v[116:117], 0\n" \
  "v_mov_b64 v[118:119], 0\n" \
  "v_mov_b64 v[36:37], 0\n" \
  "v_mov_b64 v[38:39], 0\n" \
  "v_mov_b64 v[40:41], 0\n" \
  "v_mov_b64 v[42:43], 0\n" \
  "v_mov_b64 v[44:45], 0\n" \
  "v_mov_b64 v[46:47], 0\n" \
  "v_mov_b64 v[48:49], 0\n" \
  "v_mov_b64 v[50:51], 0\n" \
  "ds_read_b128 v[52:55], v6 offset:0\n" \
  "ds_read_b128 v[56:59], v6 offset:1024\n" \
  "ds_read_b128 v[60:63], v6 offset:2048\n" \
  "ds_read_b128 v[64:67], v6 offset:3072\n" \
  "s_waitcnt lgkmcnt(0)\n" \
  "s_mov_b32 s96, s69\n" \
  ".p2align 3\n" \
  "RA_d2_%=:\n" \
  "s_sub_u32 s96, s96, 1\n" \
  "s_cbranch_scc0 RA_d2_%=\n" \
  "buffer_load_dwordx4 v[68:71], v1, s[36:39], s83 offen sc1\n" \
  "buffer_load_dwordx4 v[72:75], v1, s[36:39], s83 offen offset:1024 sc1\n" \
  "s_mov_b32 s96, s101\n" \
  ".p2align 3\n" \
  "RA_d3_%=:\n" \
  "s_sub_u32 s96, s96, 1\n" \
  "s_cbranch_scc0 RA_d3_%=\n" \
  "RA_p2_%=:\n" \
  "buffer_load_dwordx4 v[20:23], v8, s[36:39], s81 offen sc1\n" \
  "buffer_load_dwordx4 v[24:27], v8, s[36:39], s81 offen offset:2048 sc1\n" \
  "buffer_load_dwordx4 v[28:31], v8, s[36:39], s82 offen sc1\n" \
  "buffer_load_dwordx4 v[32:35], v8, s[36:39], s82 offen offset:2048 sc1\n" \
  "buffer_load_dwordx2 v[76:77], v9, s[36:39], s81 offen sc1\n" \
  "buffer_load_dwordx2 v[78:79], v9, s[36:39], s81 offen offset:1024 sc1\n" \
  "s_waitcnt vmcnt(6)\n" \
  "ds_write2_b32 v11, v17, v17 offset0:128 offset1:160\n" \
  "v_max3_u32 v18, v68, v69, v70\n" \
  "v_max_u32 v18, v18, v71\n" \
  "v_max3_u32 v18, v18, v72, v73\n" \
  "v_max3_u32 v18, v18, v74, v75\n" \
  "s_waitcnt vmcnt(5)\n" \
  "v_smfmac_f32_16x16x64_bf16 v[116:119], v[20:23], v[84:91], v0\n" \
  "v_max3_u32 v18, v18, v20, v21\n" \
  "v_max3_u32 v18, v18, v22, v23\n" \
  "s_waitcnt vmcnt(4)\n" \
  "v_smfmac_f32_16x16x64_bf16 v[116:119], v[24:27], v[92:99], v0\n" \
  "v_max3_u32 v18, v18, v24, v25\n" \
  "v_max3_u32 v18, v18, v26, v27\n" \
  "s_waitcnt vmcnt(3)\n" \
  "v_smfmac_f32_16x16x64_bf16 v[116:119], v[28:31], v[100:107], v0\n" \
  "v_max3_u32 v18, v18, v28, v29\n" \
  "v_max3_u32 v18, v18, v30, v31\n" \
  "s_waitcnt vmcnt(2)\n" \
  "v_smfmac_f32_16x16x64_bf16 v[116:119], v[32:35], v[108:115], v0\n" \
  "v_max3_u32 v18, v18, v32, v33\n" \
  "v_max3_u32 v18, v18, v34, v35\n" \
  "s_waitcnt vmcnt(0)\n" \
  "v_smfmac_f32_16x16x64_bf16 v[116:119], v[76:79], v[68:75], v0\n" \
  "v_max3_u32 v18, v18, v76, v77\n" \
  "v_max3_u32 v18, v18, v78, v79\n" \
  "v_cmp_eq_u32 vcc, -1, v18\n" \
  "s_cbranch_vccnz RA_r2_%=\n" \
  "RA_k2_%=:\n" \
  "s_cmp_eq_u32 s60, 0\n" \
  "s_nop 2\n" \
  "v_permlane32_swap_b32 v116, v117\n" \
  "v_permlane32_swap_b32 v118, v119\n" \
  "v_add_f32 v116, v116, v117\n" \
  "v_add_f32 v118, v118, v119\n" \
  "ds_write2_b32 v11, v116, v118 offset1:32\n" \
  "s_waitcnt lgkmcnt(0)\n" \
  "s_barrier\n" \
  "s_cbranch_scc0 RA_c1_%=\n" \
  "ds_read_b64 v[76:77], v12 offset:0\n" \
  "ds_read_b64 v[78:79], v12 offset:8192\n" \
  "ds_read_b64 v[80:81], v12 offset:16384\n" \
  "ds_read_b64 v[82:83], v12 offset:24576\n" \
  "s_waitcnt lgkmcnt(2)\n" \
  "v_add_f32 v76, v76, v78\n" \
  "v_add_f32 v77, v77, v79\n" \
  "s_waitcnt lgkmcnt(1)\n" \
  "v_add_f32 v76, v76, v80\n" \
  "v_add_f32 v77, v77, v81\n" \
  "s_waitcnt lgkmcnt(0)\n" \
  "v_add_f32 v76, v76, v82\n" \
  "v_add_f32 v77, v77, v83\n" \
  "v_cvt_pk_bf16_f32 v116, v76, v77\n" \
  "buffer_store_dword v116, v10, s[36:39], s77 offen sc1\n" \
  "RA_c1_%=:\n" \
  "ds_read_b32 v17, v12 offset:25088\n" \
  "s_mov_b32 s96, s66\n" \
  "s_mov_b32 s66, s65\n" \
  "s_mov_b32 s65, s67\n" \
  "s_mov_b32 s67, s96\n" \
  "s_add_u32 s64, s64, 1\n" \
  "s_xor_b32 s96, s47, 0x100000\n" \
  "s_mov_b32 s47, s50\n" \
  "s_mov_b32 s50, s51\n" \
  "s_mov_b32 s51, s96\n" \
  "s_branch RA_top_%=\n" \
  "RA_r0_%=:\n" \
  "s_mov_b32 s96, s68\n" \
  ".p2align 3\n" \
  "RA_d0_%=:\n" \
  "s_sub_u32 s96, s96, 1\n" \
  "s_cbranch_scc0 RA_d0_%=\n" \
  "buffer_load_dword v18, v17, s[40:43], s46 offen sc1\n" \
  "buffer_load_dwordx4 v[20:23], v1, s[36:39], s76 offen sc1\n" \
  "buffer_load_dwordx4 v[24:27], v1, s[36:39], s76 offen offset:1024 sc1\n" \
  "buffer_load_dwordx4 v[28:31], v1, s[36:39], s76 offen offset:2048 sc1\n" \
  "buffer_load_dwordx4 v[32:35], v1, s[36:39], s76 offen offset:3072 sc1\n" \
  "s_cmp_eq_u32 s60, 3\n" \
  "s_cbranch_scc0 RA_r0c_%=\n" \
  "s_waitcnt vmcnt(4)\n" \
  "RA_r0b_%=:\n" \
  "v_cmp_gt_u32 vcc, 208, v18\n" \
  "s_cbranch_vccz RA_r0c_%=\n" \
  "s_sleep 2\n" \
  "buffer_load_dword v18, v17, s[40:43], s46 offen sc1\n" \
  "s_waitcnt vmcnt(0)\n" \
  "s_sub_u32 s63, s63, 1\n" \
  "s_cbranch_scc0 RA_r0b_%=\n" \
  "s_mov_b32 s63, 0\n" \
  "RA_r0c_%=:\n" \
  "s_barrier\n" \
  "s_branch RA_p1b_%=\n" \
  "RA_r1_%=:\n" \
  "s_nop 7\n" \
  "s_sub_u32 s63, s63, 1\n" \
  "s_cbranch_scc1 RA_r1d_%=\n" \
  "v_mov_b64 v[36:37], 0\n" \
  "v_mov_b64 v[38:39], 0\n" \
  "v_mov_b64 v[40:41], 0\n" \
  "v_mov_b64 v[42:43], 0\n" \
  "v_mov_b64 v[44:45], 0\n" \
  "v_mov_b64 v[46:47], 0\n" \
  "v_mov_b64 v[48:49], 0\n" \
  "v_mov_b64 v[50:51], 0\n" \
  "s_branch RA_p1_%=\n" \
  "RA_r1d_%=:\n" \
  "s_mov_b32 s63, 0\n" \
  "s_branch RA_k1_%=\n" \
  "RA_r2_%=:\n" \
  "s_nop 7\n" \
  "s_sub_u32 s63, s63, 1\n" \
  "s_cbranch_scc1 RA_r2d_%=\n" \
  "v_mov_b64 v[116:117], 0\n" \
  "v_mov_b64 v[118:119], 0\n" \
  "buffer_load_dwordx4 v[68:71], v1, s[36:39], s83 offen sc1\n" \
  "buffer_load_dwordx4 v[72:75], v1, s[36:39], s83 offen offset:1024 sc1\n" \
  "s_branch RA_p2_%=\n" \
  "RA_r2d_%=:\n" \
  "s_mov_b32 s63, 0\n" \
  "s_branch RA_k2_%=\n" \
  "RA_ys_%=:\n" \
  "s_sub_u32 s63, s63, 1\n" \
  "s_cbranch_scc1 RA_ysx_%=\n" \
  "s_sleep 2\n" \
  "s_lshr_b32 s96, s35, 17\n" \
  "s_and_b32 s96, s96, 0xf80\n" \
  "s_add_u32 s96, s96, 252182528\n" \
  "buffer_load_dword v228, off, s[40:43], s96 sc1\n" \
  "s_waitcnt vmcnt(0)\n" \
  "s_branch RA_yc_%=\n" \
  "RA_ysx_%=:\n" \
  "s_mov_b32 s63, 0\n" \
  "s_branch RA_ye_%=\n" \
  "RA_yt_%=:\n" \
  "s_sub_u32 s63, s63, 1\n" \
  "s_cbranch_scc1 RA_ytx_%=\n" \
  "s_sleep 2\n" \
  "s_lshr_b32 s96, s35, 16\n" \
  "s_and_b32 s96, s96, 0x1f00\n" \
  "s_add_u32 s96, s96, 252190720\n" \
  "buffer_load_dword v228, v17, s[40:43], s96 offen sc1\n" \
  "s_waitcnt vmcnt(0)\n" \
  "s_branch RA_yf_%=\n" \
  "RA_ytx_%=:\n" \
  "s_mov_b32 s63, 0\n" \
  "s_branch RA_yg_%=\n" \
  "RA_yb_%=:\n" \
  "s_and_b32 s97, s97, 28\n" \
  "s_lshl_b32 s97, s97, 6\n" \
  "s_or_b32 s35, s35, s97\n" \
  "s_or_b32 s35, s35, 1\n" \
  "s_waitcnt vmcnt(0) lgkmcnt(0)\n" \
  "s_branch RA_end_%=\n" \
  "RA_end_%=:\n" \
  "s_waitcnt vmcnt(0) lgkmcnt(0)\n" \
  "s_branch RB_fin_%=\n" \
  "RB_start_%=:\n" \
  "v_mov_b32 v18, v118\n" \
  "v_xor_b32 v121, 0x20, v120\n" \
  "v_xor_b32 v122, 0x8000, v120\n" \
  "v_xor_b32 v123, 0x8020, v120\n" \
  "v_xor_b32 v124, 0x10000, v120\n" \
  "v_xor_b32 v125, 0x10020, v120\n" \
  "v_xor_b32 v126, 0x18000, v120\n" \
  "v_xor_b32 v127, 0x18020, v120\n" \
  "v_xor_b32 v118, 0x80, v116\n" \
  "v_xor_b32 v119, 0x80, v117\n" \
  "s_lshl_b32 s76, s60, 21\n" \
  "s_lshl_b32 s96, s61, 8\n" \
  "s_add_u32 s76, s76, s96\n" \
  "s_lshl_b32 s77, s61, 18\n" \
  "s_lshl_b32 s96, s60, 9\n" \
  "s_add_u32 s77, s77, s96\n" \
  "s_lshl_b32 s78, s60, 15\n" \
  "s_lshr_b32 s96, s35, 17\n" \
  "s_and_b32 s96, s96, 0xf80\n" \
  "s_add_u32 s96, s96, 252182528\n" \
  "buffer_load_dword v17, off, s[40:43], s96 sc1\n" \
  "v_min_u32 v245, 1, v6\n" \
  "v_lshlrev_b32 v245, 30, v245\n" \
  "v_mov_b64 v[240:241], -1\n" \
  "v_mov_b64 v[242:243], -1\n" \
  "s_add_u32 m0, s78, 0\n" \
  "s_add_u32 s96, s76, 0\n" \
  "buffer_load_dwordx4 v116, s[52:55], s96 offen lds\n" \
  "s_add_u32 m0, s78, 1024\n" \
  "s_add_u32 s96, s76, 32768\n" \
  "buffer_load_dwordx4 v116, s[52:55], s96 offen lds\n" \
  "s_add_u32 m0, s78, 2048\n" \
  "s_add_u32 s96, s76, 65536\n" \
  "buffer_load_dwordx4 v118, s[52:55], s96 offen lds\n" \
  "s_add_u32 m0, s78, 3072\n" \
  "s_add_u32 s96, s76, 98304\n" \
  "buffer_load_dwordx4 v118, s[52:55], s96 offen lds\n" \
  "s_add_u32 m0, s78, 4096\n" \
  "s_add_u32 s96, s76, 131072\n" \
  "buffer_load_dwordx4 v116, s[52:55], s96 offen lds\n" \
  "s_add_u32 m0, s78, 5120\n" \
  "s_add_u32 s96, s76, 163840\n" \
  "buffer_load_dwordx4 v116, s[52:55], s96 offen lds\n" \
  "s_add_u32 m0, s78, 6144\n" \
  "s_add_u32 s96, s76, 196608\n" \
  "buffer_load_dwordx4 v118, s[52:55], s96 offen lds\n" \
  "s_add_u32 m0, s78, 7168\n" \
  "s_add_u32 s96, s76, 229376\n" \
  "buffer_load_dwordx4 v118, s[52:55], s96 offen lds\n" \
  "s_add_u32 m0, s78, 8192\n" \
  "s_add_u32 s96, s76, 262144\n" \
  "buffer_load_dwordx4 v116, s[52:55], s96 offen lds\n" \
  "s_add_u32 m0, s78, 9216\n" \
  "s_add_u32 s96, s76, 294912\n" \
  "buffer_load_dwordx4 v116, s[52:55], s96 offen lds\n" \
  "s_add_u32 m0, s78, 10240\n" \
  "s_add_u32 s96, s76, 327680\n" \
  "buffer_load_dwordx4 v118, s[52:55], s96 offen lds\n" \
  "s_add_u32 m0, s78, 11264\n" \
  "s_add_u32 s96, s76, 360448\n" \
  "buffer_load_dwordx4 v118, s[52:55], s96 offen lds\n" \
  "s_add_u32 m0, s78, 12288\n" \
  "s_add_u32 s96, s76, 393216\n" \
  "buffer_load_dwordx4 v116, s[52:55], s96 offen lds\n" \
  "s_add_u32 m0, s78, 13312\n" \
  "s_add_u32 s96, s76, 425984\n" \
  "buffer_load_dwordx4 v116, s[52:55], s96 offen lds\n" \
  "s_add_u32 m0, s78, 14336\n" \
  "s_add_u32 s96, s76, 458752\n" \
  "buffer_load_dwordx4 v118, s[52:55], s96 offen lds\n" \
  "s_add_u32 m0, s78, 15360\n" \
  "s_add_u32 s96, s76, 491520\n" \
  "buffer_load_dwordx4 v118, s[52:55], s96 offen lds\n" \
  "s_add_u32 m0, s78, 16384\n" \
  "s_add_u32 s96, s76, 524288\n" \
  "buffer_load_dwordx4 v116, s[52:55], s96 offen lds\n" \
  "s_add_u32 m0, s78, 17408\n" \
  "s_add_u32 s96, s76, 557056\n" \
  "buffer_load_dwordx4 v116, s[52:55], s96 offen lds\n" \
  "s_add_u32 m0, s78, 18432\n" \
  "s_add_u32 s96, s76, 589824\n" \
  "buffer_load_dwordx4 v118, s[52:55], s96 offen lds\n" \
  "s_add_u32 m0, s78, 19456\n" \
  "s_add_u32 s96, s76, 622592\n" \
  "buffer_load_dwordx4 v118, s[52:55], s96 offen lds\n" \
  "s_add_u32 m0, s78, 20480\n" \
  "s_add_u32 s96, s76, 655360\n" \
  "buffer_load_dwordx4 v116, s[52:55], s96 offen lds\n" \
  "s_add_u32 m0, s78, 21504\n" \
  "s_add_u32 s96, s76, 688128\n" \
  "buffer_load_dwordx4 v116, s[52:55], s96 offen lds\n" \
  "s_add_u32 m0, s78, 22528\n" \
  "s_add_u32 s96, s76, 720896\n" \
  "buffer_load_dwordx4 v118, s[52:55], s96 offen lds\n" \
  "s_add_u32 m0, s78, 23552\n" \
  "s_add_u32 s96, s76, 753664\n" \
  "buffer_load_dwordx4 v118, s[52:55], s96 offen lds\n" \
  "s_waitcnt vmcnt(16)\n" \
  "s_barrier\n" \
  "s_add_u32 m0, s78, 24576\n" \
  "s_add_u32 s96, s76, 786432\n" \
  "buffer_load_dwordx4 v116, s[52:55], s96 offen lds\n" \
  "s_add_u32 m0, s78, 25600\n" \
  "s_add_u32 s96, s76, 819200\n" \
  "buffer_load_dwordx4 v116, s[52:55], s96 offen lds\n" \
  "s_add_u32 m0, s78, 26624\n" \
  "s_add_u32 s96, s76, 851968\n" \
  "buffer_load_dwordx4 v118, s[52:55], s96 offen lds\n" \
  "s_add_u32 m0, s78, 27648\n" \
  "s_add_u32 s96, s76, 884736\n" \
  "buffer_load_dwordx4 v118, s[52:55], s96 offen lds\n" \
  "s_add_u32 m0, s78, 28672\n" \
  "s_add_u32 s96, s76, 917504\n" \
  "buffer_load_dwordx4 v116, s[52:55], s96 offen lds\n" \
  "s_add_u32 m0, s78, 29696\n" \
  "s_add_u32 s96, s76, 950272\n" \
  "buffer_load_dwordx4 v116, s[52:55], s96 offen lds\n" \
  "s_add_u32 m0, s78, 30720\n" \
  "s_add_u32 s96, s76, 983040\n" \
  "buffer_load_dwordx4 v118, s[52:55], s96 offen lds\n" \
  "s_add_u32 m0, s78, 31744\n" \
  "s_add_u32 s96, s76, 1015808\n" \
  "buffer_load_dwordx4 v118, s[52:55], s96 offen lds\n" \
  "ds_read_b64_tr_b16 a[0:1], v120 offset:0\n" \
  "ds_read_b64_tr_b16 a[2:3], v120 offset:1024\n" \
  "ds_read_b64_tr_b16 a[8:9], v121 offset:0\n" \
  "ds_read_b64_tr_b16 a[10:11], v121 offset:1024\n" \
  "ds_read_b64_tr_b16 a[48:49], v122 offset:0\n" \
  "ds_read_b64_tr_b16 a[50:51], v122 offset:1024\n" \
  "ds_read_b64_tr_b16 a[56:57], v123 offset:0\n" \
  "ds_read_b64_tr_b16 a[58:59], v123 offset:1024\n" \
  "ds_read_b64_tr_b16 a[96:97], v124 offset:0\n" \
  "ds_read_b64_tr_b16 a[98:99], v124 offset:1024\n" \
  "ds_read_b64_tr_b16 a[104:105], v125 offset:0\n" \
  "ds_read_b64_tr_b16 a[106:107], v125 offset:1024\n" \
  "ds_read_b64_tr_b16 a[144:145], v126 offset:0\n" \
  "ds_read_b64_tr_b16 a[146:147], v126 offset:1024\n" \
  "ds_read_b64_tr_b16 a[152:153], v127 offset:0\n" \
  "ds_read_b64_tr_b16 a[154:155], v127 offset:1024\n" \
  "s_waitcnt lgkmcnt(0)\n" \
  "s_waitcnt vmcnt(16)\n" \
  "s_barrier\n" \
  "s_add_u32 m0, s78, 0\n" \
  "s_add_u32 s96, s76, 1048576\n" \
  "buffer_load_dwordx4 v116, s[52:55], s96 offen lds\n" \
  "s_add_u32 m0, s78, 1024\n" \
  "s_add_u32 s96, s76, 1081344\n" \
  "buffer_load_dwordx4 v116, s[52:55], s96 offen lds\n" \
  "s_add_u32 m0, s78, 2048\n" \
  "s_add_u32 s96, s76, 1114112\n" \
  "buffer_load_dwordx4 v118, s[52:55], s96 offen lds\n" \
  "s_add_u32 m0, s78, 3072\n" \
  "s_add_u32 s96, s76, 1146880\n" \
  "buffer_load_dwordx4 v118, s[52:55], s96 offen lds\n" \
  "s_add_u32 m0, s78, 4096\n" \
  "s_add_u32 s96, s76, 1179648\n" \
  "buffer_load_dwordx4 v116, s[52:55], s96 offen lds\n" \
  "s_add_u32 m0, s78, 5120\n" \
  "s_add_u32 s96, s76, 1212416\n" \
  "buffer_load_dwordx4 v116, s[52:55], s96 offen lds\n" \
  "s_add_u32 m0, s78, 6144\n" \
  "s_add_u32 s96, s76, 1245184\n" \
  "buffer_load_dwordx4 v118, s[52:55], s96 offen lds\n" \
  "s_add_u32 m0, s78, 7168\n" \
  "s_add_u32 s96, s76, 1277952\n" \
  "buffer_load_dwordx4 v118, s[52:55], s96 offen lds\n" \
  "ds_read_b64_tr_b16 a[4:5], v120 offset:8192\n" \
  "ds_read_b64_tr_b16 a[6:7], v120 offset:9216\n" \
  "ds_read_b64_tr_b16 a[12:13], v121 offset:8192\n" \
  "ds_read_b64_tr_b16 a[14:15], v121 offset:9216\n" \
  "ds_read_b64_tr_b16 a[52:53], v122 offset:8192\n" \
  "ds_read_b64_tr_b16 a[54:55], v122 offset:9216\n" \
  "ds_read_b64_tr_b16 a[60:61], v123 offset:8192\n" \
  "ds_read_b64_tr_b16 a[62:63], v123 offset:9216\n" \
  "ds_read_b64_tr_b16 a[100:101], v124 offset:8192\n" \
  "ds_read_b64_tr_b16 a[102:103], v124 offset:9216\n" \
  "ds_read_b64_tr_b16 a[108:109], v125 offset:8192\n" \
  "ds_read_b64_tr_b16 a[110:111], v125 offset:9216\n" \
  "ds_read_b64_tr_b16 a[148:149], v126 offset:8192\n" \
  "ds_read_b64_tr_b16 a[150:151], v126 offset:9216\n" \
  "ds_read_b64_tr_b16 a[156:157], v127 offset:8192\n" \
  "ds_read_b64_tr_b16 a[158:159], v127 offset:9216\n" \
  "s_waitcnt lgkmcnt(0)\n" \
  "s_waitcnt vmcnt(16)\n" \
  "s_barrier\n" \
  "s_add_u32 m0, s78, 8192\n" \
  "s_add_u32 s96, s76, 1310720\n" \
  "buffer_load_dwordx4 v116, s[52:55], s96 offen lds\n" \
  "s_add_u32 m0, s78, 9216\n" \
  "s_add_u32 s96, s76, 1343488\n" \
  "buffer_load_dwordx4 v116, s[52:55], s96 offen lds\n" \
  "s_add_u32 m0, s78, 10240\n" \
  "s_add_u32 s96, s76, 1376256\n" \
  "buffer_load_dwordx4 v118, s[52:55], s96 offen lds\n" \
  "s_add_u32 m0, s78, 11264\n" \
  "s_add_u32 s96, s76, 1409024\n" \
  "buffer_load_dwordx4 v118, s[52:55], s96 offen lds\n" \
  "s_add_u32 m0, s78, 12288\n" \
  "s_add_u32 s96, s76, 1441792\n" \
  "buffer_load_dwordx4 v116, s[52:55], s96 offen lds\n" \
  "s_add_u32 m0, s78, 13312\n" \
  "s_add_u32 s96, s76, 1474560\n" \
  "buffer_load_dwordx4 v116, s[52:55], s96 offen lds\n" \
  "s_add_u32 m0, s78, 14336\n" \
  "s_add_u32 s96, s76, 1507328\n" \
  "buffer_load_dwordx4 v118, s[52:55], s96 offen lds\n" \
  "s_add_u32 m0, s78, 15360\n" \
  "s_add_u32 s96, s76, 1540096\n" \
  "buffer_load_dwordx4 v118, s[52:55], s96 offen lds\n" \
  "ds_read_b64_tr_b16 a[16:17], v120 offset:16384\n" \
  "ds_read_b64_tr_b16 a[18:19], v120 offset:17408\n" \
  "ds_read_b64_tr_b16 a[24:25], v121 offset:16384\n" \
  "ds_read_b64_tr_b16 a[26:27], v121 offset:17408\n" \
  "ds_read_b64_tr_b16 a[64:65], v122 offset:16384\n" \
  "ds_read_b64_tr_b16 a[66:67], v122 offset:17408\n" \
  "ds_read_b64_tr_b16 a[72:73], v123 offset:16384\n" \
  "ds_read_b64_tr_b16 a[74:75], v123 offset:17408\n" \
  "ds_read_b64_tr_b16 a[112:113], v124 offset:16384\n" \
  "ds_read_b64_tr_b16 a[114:115], v124 offset:17408\n" \
  "ds_read_b64_tr_b16 a[120:121], v125 offset:16384\n" \
  "ds_read_b64_tr_b16 a[122:123], v125 offset:17408\n" \
  "ds_read_b64_tr_b16 a[160:161], v126 offset:16384\n" \
  "ds_read_b64_tr_b16 a[162:163], v126 offset:17408\n" \
  "ds_read_b64_tr_b16 a[168:169], v127 offset:16384\n" \
  "ds_read_b64_tr_b16 a[170:171], v127 offset:17408\n" \
  "s_waitcnt lgkmcnt(0)\n" \
  "s_waitcnt vmcnt(16)\n" \
  "s_barrier\n" \
  "s_add_u32 m0, s78, 16384\n" \
  "s_add_u32 s96, s76, 1572864\n" \
  "buffer_load_dwordx4 v116, s[52:55], s96 offen lds\n" \
  "s_add_u32 m0, s78, 17408\n" \
  "s_add_u32 s96, s76, 1605632\n" \
  "buffer_load_dwordx4 v116, s[52:55], s96 offen lds\n" \
  "s_add_u32 m0, s78, 18432\n" \
  "s_add_u32 s96, s76, 1638400\n" \
  "buffer_load_dwordx4 v118, s[52:55], s96 offen lds\n" \
  "s_add_u32 m0, s78, 19456\n" \
  "s_add_u32 s96, s76, 1671168\n" \
  "buffer_load_dwordx4 v118, s[52:55], s96 offen lds\n" \
  "s_add_u32 m0, s78, 20480\n" \
  "s_add_u32 s96, s76, 1703936\n" \
  "buffer_load_dwordx4 v116, s[52:55], s96 offen lds\n" \
  "s_add_u32 m0, s78, 21504\n" \
  "s_add_u32 s96, s76, 1736704\n" \
  "buffer_load_dwordx4 v116, s[52:55], s96 offen lds\n" \
  "s_add_u32 m0, s78, 22528\n" \
  "s_add_u32 s96, s76, 1769472\n" \
  "buffer_load_dwordx4 v118, s[52:55], s96 offen lds\n" \
  "s_add_u32 m0, s78, 23552\n" \
  "s_add_u32 s96, s76, 1802240\n" \
  "buffer_load_dwordx4 v118, s[52:55], s96 offen lds\n" \
  "ds_read_b64_tr_b16 a[20:21], v120 offset:24576\n" \
  "ds_read_b64_tr_b16 a[22:23], v120 offset:25600\n" \
  "ds_read_b64_tr_b16 a[28:29], v121 offset:24576\n" \
  "ds_read_b64_tr_b16 a[30:31], v121 offset:25600\n" \
  "ds_read_b64_tr_b16 a[68:69], v122 offset:24576\n" \
  "ds_read_b64_tr_b16 a[70:71], v122 offset:25600\n" \
  "ds_read_b64_tr_b16 a[76:77], v123 offset:24576\n" \
  "ds_read_b64_tr_b16 a[78:79], v123 offset:25600\n" \
  "ds_read_b64_tr_b16 a[116:117], v124 offset:24576\n" \
  "ds_read_b64_tr_b16 a[118:119], v124 offset:25600\n" \
  "ds_read_b64_tr_b16 a[124:125], v125 offset:24576\n" \
  "ds_read_b64_tr_b16 a[126:127], v125 offset:25600\n" \
  "ds_read_b64_tr_b16 a[164:165], v126 offset:24576\n" \
  "ds_read_b64_tr_b16 a[166:167], v126 offset:25600\n" \
  "ds_read_b64_tr_b16 a[172:173], v127 offset:24576\n" \
  "ds_read_b64_tr_b16 a[174:175], v127 offset:25600\n" \
  "s_waitcnt lgkmcnt(0)\n" \
  "s_waitcnt vmcnt(16)\n" \
  "s_barrier\n" \
  "s_add_u32 m0, s78, 24576\n" \
  "s_add_u32 s96, s76, 1835008\n" \
  "buffer_load_dwordx4 v116, s[52:55], s96 offen lds\n" \
  "s_add_u32 m0, s78, 25600\n" \
  "s_add_u32 s96, s76, 1867776\n" \
  "buffer_load_dwordx4 v116, s[52:55], s96 offen lds\n" \
  "s_add_u32 m0, s78, 26624\n" \
  "s_add_u32 s96, s76, 1900544\n" \
  "buffer_load_dwordx4 v118, s[52:55], s96 offen lds\n" \
  "s_add_u32 m0, s78, 27648\n" \
  "s_add_u32 s96, s76, 1933312\n" \
  "buffer_load_dwordx4 v118, s[52:55], s96 offen lds\n" \
  "s_add_u32 m0, s78, 28672\n" \
  "s_add_u32 s96, s76, 1966080\n" \
  "buffer_load_dwordx4 v116, s[52:55], s96 offen lds\n" \
  "s_add_u32 m0, s78, 29696\n" \
  "s_add_u32 s96, s76, 1998848\n" \
  "buffer_load_dwordx4 v116, s[52:55], s96 offen lds\n" \
  "s_add_u32 m0, s78, 30720\n" \
  "s_add_u32 s96, s76, 2031616\n" \
  "buffer_load_dwordx4 v118, s[52:55], s96 offen lds\n" \
  "s_add_u32 m0, s78, 31744\n" \
  "s_add_u32 s96, s76, 2064384\n" \
  "buffer_load_dwordx4 v118, s[52:55], s96 offen lds\n" \
  "ds_read_b64_tr_b16 a[32:33], v120 offset:0\n" \
  "ds_read_b64_tr_b16 a[34:35], v120 offset:1024\n" \
  "ds_read_b64_tr_b16 a[40:41], v121 offset:0\n" \
  "ds_read_b64_tr_b16 a[42:43], v121 offset:1024\n" \
  "ds_read_b64_tr_b16 a[80:81], v122 offset:0\n" \
  "ds_read_b64_tr_b16 a[82:83], v122 offset:1024\n" \
  "ds_read_b64_tr_b16 a[88:89], v123 offset:0\n" \
  "ds_read_b64_tr_b16 a[90:91], v123 offset:1024\n" \
  "ds_read_b64_tr_b16 a[128:129], v124 offset:0\n" \
  "ds_read_b64_tr_b16 a[130:131], v124 offset:1024\n" \
  "ds_read_b64_tr_b16 a[136:137], v125 offset:0\n" \
  "ds_read_b64_tr_b16 a[138:139], v125 offset:1024\n" \
  "ds_read_b64_tr_b16 a[176:177], v126 offset:0\n" \
  "ds_read_b64_tr_b16 a[178:179], v126 offset:1024\n" \
  "ds_read_b64_tr_b16 a[184:185], v127 offset:0\n" \
  "ds_read_b64_tr_b16 a[186:187], v127 offset:1024\n" \
  "s_waitcnt lgkmcnt(0)\n" \
  "s_waitcnt vmcnt(16)\n" \
  "s_barrier\n" \
  "s_add_u32 m0, s78, 0\n" \
  "s_add_u32 s96, s77, 0\n" \
  "buffer_load_dwordx4 v117, s[56:59], s96 offen lds\n" \
  "s_add_u32 m0, s78, 1024\n" \
  "s_add_u32 s96, s77, 8192\n" \
  "buffer_load_dwordx4 v117, s[56:59], s96 offen lds\n" \
  "s_add_u32 m0, s78, 2048\n" \
  "s_add_u32 s96, s77, 16384\n" \
  "buffer_load_dwordx4 v117, s[56:59], s96 offen lds\n" \
  "s_add_u32 m0, s78, 3072\n" \
  "s_add_u32 s96, s77, 24576\n" \
  "buffer_load_dwordx4 v117, s[56:59], s96 offen lds\n" \
  "s_add_u32 m0, s78, 4096\n" \
  "s_add_u32 s96, s77, 32768\n" \
  "buffer_load_dwordx4 v119, s[56:59], s96 offen lds\n" \
  "s_add_u32 m0, s78, 5120\n" \
  "s_add_u32 s96, s77, 40960\n" \
  "buffer_load_dwordx4 v119, s[56:59], s96 offen lds\n" \
  "s_add_u32 m0, s78, 6144\n" \
  "s_add_u32 s96, s77, 49152\n" \
  "buffer_load_dwordx4 v119, s[56:59], s96 offen lds\n" \
  "s_add_u32 m0, s78, 7168\n" \
  "s_add_u32 s96, s77, 57344\n" \
  "buffer_load_dwordx4 v119, s[56:59], s96 offen lds\n" \
  "ds_read_b64_tr_b16 a[36:37], v120 offset:8192\n" \
  "ds_read_b64_tr_b16 a[38:39], v120 offset:9216\n" \
  "ds_read_b64_tr_b16 a[44:45], v121 offset:8192\n" \
  "ds_read_b64_tr_b16 a[46:47], v121 offset:9216\n" \
  "ds_read_b64_tr_b16 a[84:85], v122 offset:8192\n" \
  "ds_read_b64_tr_b16 a[86:87], v122 offset:9216\n" \
  "ds_read_b64_tr_b16 a[92:93], v123 offset:8192\n" \
  "ds_read_b64_tr_b16 a[94:95], v123 offset:9216\n" \
  "ds_read_b64_tr_b16 a[132:133], v124 offset:8192\n" \
  "ds_read_b64_tr_b16 a[134:135], v124 offset:9216\n" \
  "ds_read_b64_tr_b16 a[140:141], v125 offset:8192\n" \
  "ds_read_b64_tr_b16 a[142:143], v125 offset:9216\n" \
  "ds_read_b64_tr_b16 a[180:181], v126 offset:8192\n" \
  "ds_read_b64_tr_b16 a[182:183], v126 offset:9216\n" \
  "ds_read_b64_tr_b16 a[188:189], v127 offset:8192\n" \
  "ds_read_b64_tr_b16 a[190:191], v127 offset:9216\n" \
  "s_waitcnt lgkmcnt(0)\n" \
  "s_waitcnt vmcnt(16)\n" \
  "s_barrier\n" \
  "s_add_u32 m0, s78, 8192\n" \
  "s_add_u32 s96, s77, 65536\n" \
  "buffer_load_dwordx4 v117, s[56:59], s96 offen lds\n" \
  "s_add_u32 m0, s78, 9216\n" \
  "s_add_u32 s96, s77, 73728\n" \
  "buffer_load_dwordx4 v117, s[56:59], s96 offen lds\n" \
  "s_add_u32 m0, s78, 10240\n" \
  "s_add_u32 s96, s77, 81920\n" \
  "buffer_load_dwordx4 v117, s[56:59], s96 offen lds\n" \
  "s_add_u32 m0, s78, 11264\n" \
  "s_add_u32 s96, s77, 90112\n" \
  "buffer_load_dwordx4 v117, s[56:59], s96 offen lds\n" \
  "s_add_u32 m0, s78, 12288\n" \
  "s_add_u32 s96, s77, 98304\n" \
  "buffer_load_dwordx4 v119, s[56:59], s96 offen lds\n" \
  "s_add_u32 m0, s78, 13312\n" \
  "s_add_u32 s96, s77, 106496\n" \
  "buffer_load_dwordx4 v119, s[56:59], s96 offen lds\n" \
  "s_add_u32 m0, s78, 14336\n" \
  "s_add_u32 s96, s77, 114688\n" \
  "buffer_load_dwordx4 v119, s[56:59], s96 offen lds\n" \
  "s_add_u32 m0, s78, 15360\n" \
  "s_add_u32 s96, s77, 122880\n" \
  "buffer_load_dwordx4 v119, s[56:59], s96 offen lds\n" \
  "ds_read_b64_tr_b16 v[20:21], v120 offset:16384\n" \
  "ds_read_b64_tr_b16 v[22:23], v120 offset:17408\n" \
  "ds_read_b64_tr_b16 v[28:29], v121 offset:16384\n" \
  "ds_read_b64_tr_b16 v[30:31], v121 offset:17408\n" \
  "ds_read_b64_tr_b16 v[36:37], v122 offset:16384\n" \
  "ds_read_b64_tr_b16 v[38:39], v122 offset:17408\n" \
  "ds_read_b64_tr_b16 v[44:45], v123 offset:16384\n" \
  "ds_read_b64_tr_b16 v[46:47], v123 offset:17408\n" \
  "ds_read_b64_tr_b16 v[52:53], v124 offset:16384\n" \
  "ds_read_b64_tr_b16 v[54:55], v124 offset:17408\n" \
  "ds_read_b64_tr_b16 v[60:61], v125 offset:16384\n" \
  "ds_read_b64_tr_b16 v[62:63], v125 offset:17408\n" \
  "ds_read_b64_tr_b16 v[68:69], v126 offset:16384\n" \
  "ds_read_b64_tr_b16 v[70:71], v126 offset:17408\n" \
  "ds_read_b64_tr_b16 v[76:77], v127 offset:16384\n" \
  "ds_read_b64_tr_b16 v[78:79], v127 offset:17408\n" \
  "s_waitcnt lgkmcnt(0)\n" \
  "s_waitcnt vmcnt(16)\n" \
  "s_barrier\n" \
  "s_lshr_b32 s96, s35, 17\n" \
  "s_and_b32 s96, s96, 0xf80\n" \
  "s_add_u32 s96, s96, 252182528\n" \
  "buffer_load_dword v244, off, s[40:43], s96 sc1\n" \
  "buffer_load_dword v19, v19, s[88:91], 0 offen\n" \
  "s_add_u32 m0, s78, 16384\n" \
  "s_add_u32 s96, s77, 256\n" \
  "buffer_load_dwordx4 v117, s[56:59], s96 offen lds\n" \
  "s_add_u32 m0, s78, 17408\n" \
  "s_add_u32 s96, s77, 8448\n" \
  "buffer_load_dwordx4 v117, s[56:59], s96 offen lds\n" \
  "s_add_u32 m0, s78, 18432\n" \
  "s_add_u32 s96, s77, 16640\n" \
  "buffer_load_dwordx4 v117, s[56:59], s96 offen lds\n" \
  "s_add_u32 m0, s78, 19456\n" \
  "s_add_u32 s96, s77, 24832\n" \
  "buffer_load_dwordx4 v117, s[56:59], s96 offen lds\n" \
  "s_add_u32 m0, s78, 20480\n" \
  "s_add_u32 s96, s77, 33024\n" \
  "buffer_load_dwordx4 v119, s[56:59], s96 offen lds\n" \
  "s_add_u32 m0, s78, 21504\n" \
  "s_add_u32 s96, s77, 41216\n" \
  "buffer_load_dwordx4 v119, s[56:59], s96 offen lds\n" \
  "s_add_u32 m0, s78, 22528\n" \
  "s_add_u32 s96, s77, 49408\n" \
  "buffer_load_dwordx4 v119, s[56:59], s96 offen lds\n" \
  "s_add_u32 m0, s78, 23552\n" \
  "s_add_u32 s96, s77, 57600\n" \
  "buffer_load_dwordx4 v119, s[56:59], s96 offen lds\n" \
  "ds_read_b64_tr_b16 v[24:25], v120 offset:24576\n" \
  "ds_read_b64_tr_b16 v[26:27], v120 offset:25600\n" \
  "ds_read_b64_tr_b16 v[32:33], v121 offset:24576\n" \
  "ds_read_b64_tr_b16 v[34:35], v121 offset:25600\n" \
  "ds_read_b64_tr_b16 v[40:41], v122 offset:24576\n" \
  "ds_read_b64_tr_b16 v[42:43], v122 offset:25600\n" \
  "ds_read_b64_tr_b16 v[48:49], v123 offset:24576\n" \
  "ds_read_b64_tr_b16 v[50:51], v123 offset:25600\n" \
  "ds_read_b64_tr_b16 v[56:57], v124 offset:24576\n" \
  "ds_read_b64_tr_b16 v[58:59], v124 offset:25600\n" \
  "ds_read_b64_tr_b16 v[64:65], v125 offset:24576\n" \
  "ds_read_b64_tr_b16 v[66:67], v125 offset:25600\n" \
  "ds_read_b64_tr_b16 v[72:73], v126 offset:24576\n" \
  "ds_read_b64_tr_b16 v[74:75], v126 offset:25600\n" \
  "ds_read_b64_tr_b16 v[80:81], v127 offset:24576\n" \
  "ds_read_b64_tr_b16 v[82:83], v127 offset:25600\n" \
  "s_waitcnt lgkmcnt(0)\n" \
  "s_waitcnt vmcnt(18)\n" \
  "s_barrier\n" \
  "s_add_u32 m0, s78, 24576\n" \
  "s_add_u32 s96, s77, 65792\n" \
  "buffer_load_dwordx4 v117, s[56:59], s96 offen lds\n" \
  "s_add_u32 m0, s78, 25600\n" \
  "s_add_u32 s96, s77, 73984\n" \
  "buffer_load_dwordx4 v117, s[56:59], s96 offen lds\n" \
  "s_add_u32 m0, s78, 26624\n" \
  "s_add_u32 s96, s77, 82176\n" \
  "buffer_load_dwordx4 v117, s[56:59], s96 offen lds\n" \
  "s_add_u32 m0, s78, 27648\n" \
  "s_add_u32 s96, s77, 90368\n" \
  "buffer_load_dwordx4 v117, s[56:59], s96 offen lds\n" \
  "s_add_u32 m0, s78, 28672\n" \
  "s_add_u32 s96, s77, 98560\n" \
  "buffer_load_dwordx4 v119, s[56:59], s96 offen lds\n" \
  "s_add_u32 m0, s78, 29696\n" \
  "s_add_u32 s96, s77, 106752\n" \
  "buffer_load_dwordx4 v119, s[56:59], s96 offen lds\n" \
  "s_add_u32 m0, s78, 30720\n" \
  "s_add_u32 s96, s77, 114944\n" \
  "buffer_load_dwordx4 v119, s[56:59], s96 offen lds\n" \
  "s_add_u32 m0, s78, 31744\n" \
  "s_add_u32 s96, s77, 123136\n" \
  "buffer_load_dwordx4 v119, s[56:59], s96 offen lds\n" \
  "v_xor_b32 v120, 0, v18\n" \
  "v_xor_b32 v121, 64, v18\n" \
  "v_xor_b32 v122, 128, v18\n" \
  "v_xor_b32 v123, 192, v18\n" \
  "ds_read_b64_tr_b16 a[192:193], v120 offset:0\n" \
  "ds_read_b64_tr_b16 a[194:195], v120 offset:512\n" \
  "ds_read_b64_tr_b16 a[196:197], v120 offset:2048\n" \
  "ds_read_b64_tr_b16 a[198:199], v120 offset:2560\n" \
  "ds_read_b64_tr_b16 a[208:209], v121 offset:0\n" \
  "ds_read_b64_tr_b16 a[210:211], v121 offset:512\n" \
  "ds_read_b64_tr_b16 a[212:213], v121 offset:2048\n" \
  "ds_read_b64_tr_b16 a[214:215], v121 offset:2560\n" \
  "ds_read_b64_tr_b16 a[240:241], v122 offset:0\n" \
  "ds_read_b64_tr_b16 a[242:243], v122 offset:512\n" \
  "ds_read_b64_tr_b16 a[244:245], v122 offset:2048\n" \
  "ds_read_b64_tr_b16 a[246:247], v122 offset:2560\n" \
  "ds_read_b64_tr_b16 v[128:129], v123 offset:0\n" \
  "ds_read_b64_tr_b16 v[130:131], v123 offset:512\n" \
  "ds_read_b64_tr_b16 v[132:133], v123 offset:2048\n" \
  "ds_read_b64_tr_b16 v[134:135], v123 offset:2560\n" \
  "s_waitcnt lgkmcnt(0)\n" \
  "s_waitcnt vmcnt(18)\n" \
  "s_barrier\n" \
  "s_add_u32 m0, s78, 0\n" \
  "s_add_u32 s96, s77, 131072\n" \
  "buffer_load_dwordx4 v117, s[56:59], s96 offen lds\n" \
  "s_add_u32 m0, s78, 1024\n" \
  "s_add_u32 s96, s77, 139264\n" \
  "buffer_load_dwordx4 v117, s[56:59], s96 offen lds\n" \
  "s_add_u32 m0, s78, 2048\n" \
  "s_add_u32 s96, s77, 147456\n" \
  "buffer_load_dwordx4 v117, s[56:59], s96 offen lds\n" \
  "s_add_u32 m0, s78, 3072\n" \
  "s_add_u32 s96, s77, 155648\n" \
  "buffer_load_dwordx4 v117, s[56:59], s96 offen lds\n" \
  "s_add_u32 m0, s78, 4096\n" \
  "s_add_u32 s96, s77, 163840\n" \
  "buffer_load_dwordx4 v119, s[56:59], s96 offen lds\n" \
  "s_add_u32 m0, s78, 5120\n" \
  "s_add_u32 s96, s77, 172032\n" \
  "buffer_load_dwordx4 v119, s[56:59], s96 offen lds\n" \
  "s_add_u32 m0, s78, 6144\n" \
  "s_add_u32 s96, s77, 180224\n" \
  "buffer_load_dwordx4 v119, s[56:59], s96 offen lds\n" \
  "s_add_u32 m0, s78, 7168\n" \
  "s_add_u32 s96, s77, 188416\n" \
  "buffer_load_dwordx4 v119, s[56:59], s96 offen lds\n" \
  "ds_read_b64_tr_b16 a[200:201], v120 offset:8192\n" \
  "ds_read_b64_tr_b16 a[202:203], v120 offset:8704\n" \
  "ds_read_b64_tr_b16 a[204:205], v120 offset:10240\n" \
  "ds_read_b64_tr_b16 a[206:207], v120 offset:10752\n" \
  "ds_read_b64_tr_b16 a[216:217], v121 offset:8192\n" \
  "ds_read_b64_tr_b16 a[218:219], v121 offset:8704\n" \
  "ds_read_b64_tr_b16 a[220:221], v121 offset:10240\n" \
  "ds_read_b64_tr_b16 a[222:223], v121 offset:10752\n" \
  "ds_read_b64_tr_b16 a[248:249], v122 offset:8192\n" \
  "ds_read_b64_tr_b16 a[250:251], v122 offset:8704\n" \
  "ds_read_b64_tr_b16 a[252:253], v122 offset:10240\n" \
  "ds_read_b64_tr_b16 a[254:255], v122 offset:10752\n" \
  "ds_read_b64_tr_b16 v[136:137], v123 offset:8192\n" \
  "ds_read_b64_tr_b16 v[138:139], v123 offset:8704\n" \
  "ds_read_b64_tr_b16 v[140:141], v123 offset:10240\n" \
  "ds_read_b64_tr_b16 v[142:143], v123 offset:10752\n" \
  "s_waitcnt lgkmcnt(0)\n" \
  "s_waitcnt vmcnt(16)\n" \
  "s_barrier\n" \
  "s_and_b32 s96, s35, 34\n" \
  "s_cbranch_scc1 RB_ye_%=\n" \
  "RB_yc_%=:\n" \
  "v_readfirstlane_b32 s96, v17\n" \
  "v_readfirstlane_b32 s97, v244\n" \
  "s_cmp_eq_u32 s96, s97\n" \
  "s_cbranch_scc1 RB_ys_%=\n" \
  "s_xor_b32 s96, s97, s35\n" \
  "s_and_b32 s96, s96, 28\n" \
  "s_cbranch_scc1 RB_yb_%=\n" \
  "RB_ye_%=:\n" \
  "v_mbcnt_lo_u32_b32 v17, -1, 0\n" \
  "v_mbcnt_hi_u32_b32 v17, -1, v17\n" \
  "v_lshlrev_b32 v17, 2, v17\n" \
  "s_lshr_b32 s96, s35, 16\n" \
  "s_and_b32 s96, s96, 0x1f00\n" \
  "s_add_u32 s96, s96, 252190720\n" \
  "buffer_load_dword v244, v17, s[40:43], s96 offen sc1\n" \
  "s_add_u32 m0, s78, 8192\n" \
  "s_add_u32 s96, s77, 196608\n" \
  "buffer_load_dwordx4 v117, s[56:59], s96 offen lds\n" \
  "s_add_u32 m0, s78, 9216\n" \
  "s_add_u32 s96, s77, 204800\n" \
  "buffer_load_dwordx4 v117, s[56:59], s96 offen lds\n" \
  "s_add_u32 m0, s78, 10240\n" \
  "s_add_u32 s96, s77, 212992\n" \
  "buffer_load_dwordx4 v117, s[56:59], s96 offen lds\n" \
  "s_add_u32 m0, s78, 11264\n" \
  "s_add_u32 s96, s77, 221184\n" \
  "buffer_load_dwordx4 v117, s[56:59], s96 offen lds\n" \
  "s_add_u32 m0, s78, 12288\n" \
  "s_add_u32 s96, s77, 229376\n" \
  "buffer_load_dwordx4 v119, s[56:59], s96 offen lds\n" \
  "s_add_u32 m0, s78, 13312\n" \
  "s_add_u32 s96, s77, 237568\n" \
  "buffer_load_dwordx4 v119, s[56:59], s96 offen lds\n" \
  "s_add_u32 m0, s78, 14336\n" \
  "s_add_u32 s96, s77, 245760\n" \
  "buffer_load_dwordx4 v119, s[56:59], s96 offen lds\n" \
  "s_add_u32 m0, s78, 15360\n" \
  "s_add_u32 s96, s77, 253952\n" \
  "buffer_load_dwordx4 v119, s[56:59], s96 offen lds\n" \
  "ds_read_b64_tr_b16 v[160:161], v120 offset:16384\n" \
  "ds_read_b64_tr_b16 v[162:163], v120 offset:16896\n" \
  "ds_read_b64_tr_b16 v[164:165], v120 offset:18432\n" \
  "ds_read_b64_tr_b16 v[166:167], v120 offset:18944\n" \
  "ds_read_b64_tr_b16 v[176:177], v121 offset:16384\n" \
  "ds_read_b64_tr_b16 v[178:179], v121 offset:16896\n" \
  "ds_read_b64_tr_b16 v[180:181], v121 offset:18432\n" \
  "ds_read_b64_tr_b16 v[182:183], v121 offset:18944\n" \
  "ds_read_b64_tr_b16 v[208:209], v122 offset:16384\n" \
  "ds_read_b64_tr_b16 v[210:211], v122 offset:16896\n" \
  "ds_read_b64_tr_b16 v[212:213], v122 offset:18432\n" \
  "ds_read_b64_tr_b16 v[214:215], v122 offset:18944\n" \
  "ds_read_b64_tr_b16 v[224:225], v123 offset:16384\n" \
  "ds_read_b64_tr_b16 v[226:227], v123 offset:16896\n" \
  "ds_read_b64_tr_b16 v[228:229], v123 offset:18432\n" \
  "ds_read_b64_tr_b16 v[230:231], v123 offset:18944\n" \
  "s_waitcnt lgkmcnt(0)\n" \
  "s_waitcnt vmcnt(17)\n" \
  "s_barrier\n" \
  "s_add_u32 m0, s78, 16384\n" \
  "s_add_u32 s96, s77, 131328\n" \
  "buffer_load_dwordx4 v117, s[56:59], s96 offen lds\n" \
  "s_add_u32 m0, s78, 17408\n" \
  "s_add_u32 s96, s77, 139520\n" \
  "buffer_load_dwordx4 v117, s[56:59], s96 offen lds\n" \
  "s_add_u32 m0, s78, 18432\n" \
  "s_add_u32 s96, s77, 147712\n" \
  "buffer_load_dwordx4 v117, s[56:59], s96 offen lds\n" \
  "s_add_u32 m0, s78, 19456\n" \
  "s_add_u32 s96, s77, 155904\n" \
  "buffer_load_dwordx4 v117, s[56:59], s96 offen lds\n" \
  "s_add_u32 m0, s78, 20480\n" \
  "s_add_u32 s96, s77, 164096\n" \
  "buffer_load_dwordx4 v119, s[56:59], s96 offen lds\n" \
  "s_add_u32 m0, s78, 21504\n" \
  "s_add_u32 s96, s77, 172288\n" \
  "buffer_load_dwordx4 v119, s[56:59], s96 offen lds\n" \
  "s_add_u32 m0, s78, 22528\n" \
  "s_add_u32 s96, s77, 180480\n" \
  "buffer_load_dwordx4 v119, s[56:59], s96 offen lds\n" \
  "s_add_u32 m0, s78, 23552\n" \
  "s_add_u32 s96, s77, 188672\n" \
  "buffer_load_dwordx4 v119, s[56:59], s96 offen lds\n" \
  "ds_read_b64_tr_b16 v[168:169], v120 offset:24576\n" \
  "ds_read_b64_tr_b16 v[170:171], v120 offset:25088\n" \
  "ds_read_b64_tr_b16 v[172:173], v120 offset:26624\n" \
  "ds_read_b64_tr_b16 v[174:175], v120 offset:27136\n" \
  "ds_read_b64_tr_b16 v[184:185], v121 offset:24576\n" \
  "ds_read_b64_tr_b16 v[186:187], v121 offset:25088\n" \
  "ds_read_b64_tr_b16 v[188:189], v121 offset:26624\n" \
  "ds_read_b64_tr_b16 v[190:191], v121 offset:27136\n" \
  "ds_read_b64_tr_b16 v[216:217], v122 offset:24576\n" \
  "ds_read_b64_tr_b16 v[218:219], v122 offset:25088\n" \
  "ds_read_b64_tr_b16 v[220:221], v122 offset:26624\n" \
  "ds_read_b64_tr_b16 v[222:223], v122 offset:27136\n" \
  "ds_read_b64_tr_b16 v[232:233], v123 offset:24576\n" \
  "ds_read_b64_tr_b16 v[234:235], v123 offset:25088\n" \
  "ds_read_b64_tr_b16 v[236:237], v123 offset:26624\n" \
  "ds_read_b64_tr_b16 v[238:239], v123 offset:27136\n" \
  "s_waitcnt lgkmcnt(0)\n" \
  "s_waitcnt vmcnt(17)\n" \
  "s_barrier\n" \
  "s_add_u32 m0, s78, 24576\n" \
  "s_add_u32 s96, s77, 196864\n" \
  "buffer_load_dwordx4 v117, s[56:59], s96 offen lds\n" \
  "s_add_u32 m0, s78, 25600\n" \
  "s_add_u32 s96, s77, 205056\n" \
  "buffer_load_dwordx4 v117, s[56:59], s96 offen lds\n" \
  "s_add_u32 m0, s78, 26624\n" \
  "s_add_u32 s96, s77, 213248\n" \
  "buffer_load_dwordx4 v117, s[56:59], s96 offen lds\n" \
  "s_add_u32 m0, s78, 27648\n" \
  "s_add_u32 s96, s77, 221440\n" \
  "buffer_load_dwordx4 v117, s[56:59], s96 offen lds\n" \
  "s_add_u32 m0, s78, 28672\n" \
  "s_add_u32 s96, s77, 229632\n" \
  "buffer_load_dwordx4 v119, s[56:59], s96 offen lds\n" \
  "s_add_u32 m0, s78, 29696\n" \
  "s_add_u32 s96, s77, 237824\n" \
  "buffer_load_dwordx4 v119, s[56:59], s96 offen lds\n" \
  "s_add_u32 m0, s78, 30720\n" \
  "s_add_u32 s96, s77, 246016\n" \
  "buffer_load_dwordx4 v119, s[56:59], s96 offen lds\n" \
  "s_add_u32 m0, s78, 31744\n" \
  "s_add_u32 s96, s77, 254208\n" \
  "buffer_load_dwordx4 v119, s[56:59], s96 offen lds\n" \
  "ds_read_b64_tr_b16 a[224:225], v120 offset:0\n" \
  "ds_read_b64_tr_b16 a[226:227], v120 offset:512\n" \
  "ds_read_b64_tr_b16 a[228:229], v120 offset:2048\n" \
  "ds_read_b64_tr_b16 a[230:231], v120 offset:2560\n" \
  "ds_read_b64_tr_b16 v[84:85], v121 offset:0\n" \
  "ds_read_b64_tr_b16 v[86:87], v121 offset:512\n" \
  "ds_read_b64_tr_b16 v[88:89], v121 offset:2048\n" \
  "ds_read_b64_tr_b16 v[90:91], v121 offset:2560\n" \
  "ds_read_b64_tr_b16 v[144:145], v122 offset:0\n" \
  "ds_read_b64_tr_b16 v[146:147], v122 offset:512\n" \
  "ds_read_b64_tr_b16 v[148:149], v122 offset:2048\n" \
  "ds_read_b64_tr_b16 v[150:151], v122 offset:2560\n" \
  "ds_read_b64_tr_b16 v[100:101], v123 offset:0\n" \
  "ds_read_b64_tr_b16 v[102:103], v123 offset:512\n" \
  "ds_read_b64_tr_b16 v[104:105], v123 offset:2048\n" \
  "ds_read_b64_tr_b16 v[106:107], v123 offset:2560\n" \
  "s_waitcnt lgkmcnt(0)\n" \
  "s_waitcnt vmcnt(16)\n" \
  "s_barrier\n" \
  "s_bitcmp1_b32 s35, 1\n" \
  "s_cbranch_scc1 RB_yd_%=\n" \
  "RB_yf_%=:\n" \
  "v_cmp_gt_i32 vcc, 0, v244\n" \
  "s_bcnt1_i32_b64 s97, vcc\n" \
  "v_and_b32 v244, 0x10001, v244\n" \
  "v_cmp_ne_u32 vcc, 0x10001, v244\n" \
  "s_cbranch_vccnz RB_yt_%=\n" \
  "RB_yg_%=:\n" \
  "s_lshr_b32 s96, s35, 17\n" \
  "s_and_b32 s96, s96, 0xf80\n" \
  "s_add_u32 s96, s96, 252186624\n" \
  "v_mov_b32 v244, 1\n" \
  "buffer_atomic_add v244, v245, s[40:43], s96 offen\n" \
  "s_cmp_gt_u32 s97, 32\n" \
  "s_cselect_b32 s96, 0x200000, 0\n" \
  "s_or_b32 s35, s35, s96\n" \
  "s_or_b32 s35, s35, 2\n" \
  "s_add_u32 s40, s40, s96\n" \
  "s_addc_u32 s41, s41, 0\n" \
  "s_add_u32 s36, s36, s96\n" \
  "s_addc_u32 s37, s37, 0\n" \
  "RB_yd_%=:\n" \
  "s_add_u32 s96, s74, 0\n" \
  "buffer_store_dwordx4 v[240:243], v1, s[36:39], s96 offen sc1\n" \
  "buffer_store_dwordx4 v[240:243], v1, s[36:39], s96 offen offset:1024 sc1\n" \
  "s_add_u32 s96, s74, 1048576\n" \
  "buffer_store_dwordx4 v[240:243], v1, s[36:39], s96 offen sc1\n" \
  "buffer_store_dwordx4 v[240:243], v1, s[36:39], s96 offen offset:1024 sc1\n" \
  "s_add_u32 s96, s74, 4194304\n" \
  "buffer_store_dwordx4 v[240:243], v1, s[36:39], s96 offen sc1\n" \
  "buffer_store_dwordx4 v[240:243], v1, s[36:39], s96 offen offset:1024 sc1\n" \
  "s_add_u32 s96, s74, 5242880\n" \
  "buffer_store_dwordx4 v[240:243], v1, s[36:39], s96 offen sc1\n" \
  "buffer_store_dwordx4 v[240:243], v1, s[36:39], s96 offen offset:1024 sc1\n" \
  "s_add_u32 s96, s74, 8388608\n" \
  "buffer_store_dwordx4 v[240:243], v1, s[36:39], s96 offen sc1\n" \
  "buffer_store_dwordx4 v[240:243], v1, s[36:39], s96 offen offset:1024 sc1\n" \
  "s_add_u32 s96, s74, 9437184\n" \
  "buffer_store_dwordx4 v[240:243], v1, s[36:39], s96 offen sc1\n" \
  "buffer_store_dwordx4 v[240:243], v1, s[36:39], s96 offen offset:1024 sc1\n" \
  "v_lshlrev_b32 v244, 3, v1\n" \
  "s_getpc_b64 s[96:97]\n" \
  "RB_pc_%=:\n" \
  "s_add_u32 s96, s96, RB_top_%=-RB_pc_%=\n" \
  "s_addc_u32 s97, s97, 0\n" \
  "v_min_u32 v244, RB_end_%=-RB_top_%=, v244\n" \
  "s_mov_b32 m0, 0x27000\n" \
  "s_nop 0\n" \
  "global_load_lds_dword v244, s[96:97]\n" \
  "ds_write_b128 v6, v[20:23] offset:0\n" \
  "ds_write_b128 v6, v[24:27] offset:1024\n" \
  "ds_write_b128 v6, v[28:31] offset:2048\n" \
  "ds_write_b128 v6, v[32:35] offset:3072\n" \
  "ds_write_b128 v6, v[36:39] offset:4096\n" \
  "ds_write_b128 v6, v[40:43] offset:5120\n" \
  "ds_write_b128 v6, v[44:47] offset:6144\n" \
  "ds_write_b128 v6, v[48:51] offset:7168\n" \
  "s_waitcnt lgkmcnt(0)\n" \
  "ds_read_b64_tr_b16 a[232:233], v120 offset:8192\n" \
  "ds_read_b64_tr_b16 a[234:235], v120 offset:8704\n" \
  "ds_read_b64_tr_b16 a[236:237], v120 offset:10240\n" \
  "ds_read_b64_tr_b16 a[238:239], v120 offset:10752\n" \
  "ds_read_b64_tr_b16 v[92:93], v121 offset:8192\n" \
  "ds_read_b64_tr_b16 v[94:95], v121 offset:8704\n" \
  "ds_read_b64_tr_b16 v[96:97], v121 offset:10240\n" \
  "ds_read_b64_tr_b16 v[98:99], v121 offset:10752\n" \
  "ds_read_b64_tr_b16 v[152:153], v122 offset:8192\n" \
  "ds_read_b64_tr_b16 v[154:155], v122 offset:8704\n" \
  "ds_read_b64_tr_b16 v[156:157], v122 offset:10240\n" \
  "ds_read_b64_tr_b16 v[158:159], v122 offset:10752\n" \
  "ds_read_b64_tr_b16 v[108:109], v123 offset:8192\n" \
  "ds_read_b64_tr_b16 v[110:111], v123 offset:8704\n" \
  "ds_read_b64_tr_b16 v[112:113], v123 offset:10240\n" \
  "ds_read_b64_tr_b16 v[114:115], v123 offset:10752\n" \
  "s_waitcnt lgkmcnt(0)\n" \
  "s_waitcnt vmcnt(21)\n" \
  "s_barrier\n" \
  "ds_write_b128 v6, v[52:55] offset:8192\n" \
  "ds_write_b128 v6, v[56:59] offset:9216\n" \
  "ds_write_b128 v6, v[60:63] offset:10240\n" \
  "ds_write_b128 v6, v[64:67] offset:11264\n" \
  "ds_write_b128 v6, v[68:71] offset:12288\n" \
  "ds_write_b128 v6, v[72:75] offset:13312\n" \
  "ds_write_b128 v6, v[76:79] offset:14336\n" \
  "ds_write_b128 v6, v[80:83] offset:15360\n" \
  "s_waitcnt lgkmcnt(0)\n" \
  "ds_read_b64_tr_b16 v[192:193], v120 offset:16384\n" \
  "ds_read_b64_tr_b16 v[194:195], v120 offset:16896\n" \
  "ds_read_b64_tr_b16 v[196:197], v120 offset:18432\n" \
  "ds_read_b64_tr_b16 v[198:199], v120 offset:18944\n" \
  "ds_read_b64_tr_b16 v[20:21], v121 offset:16384\n" \
  "ds_read_b64_tr_b16 v[22:23], v121 offset:16896\n" \
  "ds_read_b64_tr_b16 v[24:25], v121 offset:18432\n" \
  "ds_read_b64_tr_b16 v[26:27], v121 offset:18944\n" \
  "ds_read_b64_tr_b16 v[240:241], v122 offset:16384\n" \
  "ds_read_b64_tr_b16 v[242:243], v122 offset:16896\n" \
  "ds_read_b64_tr_b16 v[244:245], v122 offset:18432\n" \
  "ds_read_b64_tr_b16 v[246:247], v122 offset:18944\n" \
  "ds_read_b64_tr_b16 v[36:37], v123 offset:16384\n" \
  "ds_read_b64_tr_b16 v[38:39], v123 offset:16896\n" \
  "ds_read_b64_tr_b16 v[40:41], v123 offset:18432\n" \
  "ds_read_b64_tr_b16 v[42:43], v123 offset:18944\n" \
  "s_waitcnt lgkmcnt(0)\n" \
  "s_waitcnt vmcnt(13)\n" \
  "s_barrier\n" \
  "ds_write_b128 v6, v[84:87] offset:16384\n" \
  "ds_write_b128 v6, v[88:91] offset:17408\n" \
  "ds_write_b128 v6, v[92:95] offset:18432\n" \
  "ds_write_b128 v6, v[96:99] offset:19456\n" \
  "ds_write_b128 v6, v[100:103] offset:20480\n" \
  "ds_write_b128 v6, v[104:107] offset:21504\n" \
  "ds_write_b128 v6, v[108:111] offset:22528\n" \
  "ds_write_b128 v6, v[112:115] offset:23552\n" \
  "s_waitcnt lgkmcnt(0)\n" \
  "ds_read_b64_tr_b16 v[200:201], v120 offset:24576\n" \
  "ds_read_b64_tr_b16 v[202:203], v120 offset:25088\n" \
  "ds_read_b64_tr_b16 v[204:205], v120 offset:26624\n" \
  "ds_read_b64_tr_b16 v[206:207], v120 offset:27136\n" \
  "ds_read_b64_tr_b16 v[28:29], v121 offset:24576\n" \
  "ds_read_b64_tr_b16 v[30:31], v121 offset:25088\n" \
  "ds_read_b64_tr_b16 v[32:33], v121 offset:26624\n" \
  "ds_read_b64_tr_b16 v[34:35], v121 offset:27136\n" \
  "ds_read_b64_tr_b16 v[248:249], v122 offset:24576\n" \
  "ds_read_b64_tr_b16 v[250:251], v122 offset:25088\n" \
  "ds_read_b64_tr_b16 v[252:253], v122 offset:26624\n" \
  "ds_read_b64_tr_b16 v[254:255], v122 offset:27136\n" \
  "ds_read_b64_tr_b16 v[44:45], v123 offset:24576\n" \
  "ds_read_b64_tr_b16 v[46:47], v123 offset:25088\n" \
  "ds_read_b64_tr_b16 v[48:49], v123 offset:26624\n" \
  "ds_read_b64_tr_b16 v[50:51], v123 offset:27136\n" \
  "s_waitcnt lgkmcnt(0)\n" \
  "s_waitcnt vmcnt(0)\n" \
  "s_barrier\n" \
  "s_mov_b32 s47, 0\n" \
  "s_mov_b32 s50, 4194304\n" \
  "s_mov_b32 s51, 8388608\n" \
  "s_mov_b32 s65, 12582912\n" \
  "s_add_u32 s67, s65, 98304\n" \
  "s_add_u32 s66, s65, 196608\n" \
  "v_min_u32 v19, -2, v19\n" \
  "s_cmp_eq_u32 s60, 0\n" \
  "s_cbranch_scc0 RB_g2_%=\n" \
  "s_add_u32 s96, s65, s71\n" \
  "buffer_store_dword v19, v10, s[36:39], s96 offen sc1\n" \
  "RB_g2_%=:\n" \
  "ds_write_b128 v6, v[20:23] offset:24576\n" \
  "ds_write_b128 v6, v[24:27] offset:25600\n" \
  "ds_write_b128 v6, v[28:31] offset:26624\n" \
  "ds_write_b128 v6, v[32:35] offset:27648\n" \
  "ds_write_b128 v6, v[36:39] offset:28672\n" \
  "ds_write_b128 v6, v[40:43] offset:29696\n" \
  "ds_write_b128 v6, v[44:47] offset:30720\n" \
  "ds_write_b128 v6, v[48:51] offset:31744\n" \
  "s_waitcnt lgkmcnt(0)\n" \
  "s_mov_b32 s64, 0\n" \
  "s_sub_u32 s84, s48, 4194304\n" \
  "s_mov_b32 s89, s86\n" \
  "s_mov_b32 s46, s45\n" \
  "v_mov_b32 v17, 0\n" \
  "v_mov_b64 v[36:37], 0\n" \
  "v_mov_b64 v[38:39], 0\n" \
  "v_mov_b64 v[40:41], 0\n" \
  "v_mov_b64 v[42:43], 0\n" \
  "v_mov_b64 v[44:45], 0\n" \
  "v_mov_b64 v[46:47], 0\n" \
  "v_mov_b64 v[48:49], 0\n" \
  "v_mov_b64 v[50:51], 0\n" \
  "ds_read_b128 v[52:55], v6 offset:0\n" \
  "ds_read_b128 v[56:59], v6 offset:1024\n" \
  "ds_read_b128 v[60:63], v6 offset:2048\n" \
  "ds_read_b128 v[64:67], v6 offset:3072\n" \
  "s_waitcnt lgkmcnt(0)\n" \
  "RB_top_%=:\n" \
  "v_mov_b64 v[116:117], 0\n" \
  "v_mov_b64 v[118:119], 0\n" \
  "v_mov_b64 v[120:121], 0\n" \
  "v_mov_b64 v[122:123], 0\n" \
  "s_add_u32 s46, s46, 128\n" \
  "s_add_u32 s84, s84, 4194304\n" \
  "s_cmp_eq_u32 s84, s49\n" \
  "s_cselect_b32 s84, s48, s84\n" \
  "v_mov_b32 v19, s64\n" \
  "s_add_u32 s76, s65, s70\n" \
  "s_add_u32 s77, s67, s71\n" \
  "s_add_u32 s78, s66, s94\n" \
  "s_add_u32 s81, s65, s73\n" \
  "s_add_u32 s82, s81, 4096\n" \
  "s_add_u32 s83, s47, s74\n" \
  "s_add_u32 s85, s84, 4096\n" \
  "v_mov_b64 v[124:125], -1\n" \
  "v_mov_b64 v[126:127], -1\n" \
  "s_add_u32 s88, s47, s75\n" \
  "s_add_u32 s89, s88, 65536\n" \
  "s_add_u32 s90, s88, 131072\n" \
  "s_add_u32 s91, s88, 196608\n" \
  "s_waitcnt lgkmcnt(0)\n" \
  "v_cmp_gt_u32 vcc, 208, v17\n" \
  "v_mov_b32 v17, s95\n" \
  "s_cbranch_vccnz RB_r0_%=\n" \
  "s_mov_b32 s96, s68\n" \
  ".p2align 3\n" \
  "RB_d1_%=:\n" \
  "s_sub_u32 s96, s96, 1\n" \
  "s_cbranch_scc0 RB_d1_%=\n" \
  "RB_p1_%=:\n" \
  "buffer_load_dwordx4 v[20:23], v1, s[36:39], s76 offen sc1\n" \
  "buffer_load_dwordx4 v[24:27], v1, s[36:39], s76 offen offset:1024 sc1\n" \
  "buffer_load_dwordx4 v[28:31], v1, s[36:39], s76 offen offset:2048 sc1\n" \
  "buffer_load_dwordx4 v[32:35], v1, s[36:39], s76 offen offset:3072 sc1\n" \
  "RB_p1b_%=:\n" \
  "s_waitcnt vmcnt(3)\n" \
  "ds_write_b128 v2, v[20:23] offset:0\n" \
  "v_smfmac_f32_16x16x64_bf16 v[116:119], v[20:23], a[0:7], v0\n" \
  "v_max3_u32 v18, v20, v21, v22\n" \
  "v_max_u32 v18, v18, v23\n" \
  "v_smfmac_f32_16x16x64_bf16 v[120:123], v[20:23], a[8:15], v0\n" \
  "s_waitcnt vmcnt(2)\n" \
  "ds_write_b128 v2, v[24:27] offset:1024\n" \
  "v_max3_u32 v18, v18, v24, v25\n" \
  "v_max3_u32 v18, v18, v26, v27\n" \
  "s_waitcnt vmcnt(1)\n" \
  "ds_write_b128 v2, v[28:31] offset:2048\n" \
  "v_max3_u32 v18, v18, v28, v29\n" \
  "v_max3_u32 v18, v18, v30, v31\n" \
  "s_waitcnt vmcnt(0)\n" \
  "ds_write_b128 v2, v[32:35] offset:3072\n" \
  "v_max3_u32 v18, v18, v32, v33\n" \
  "v_max3_u32 v18, v18, v34, v35\n" \
  "v_cmp_eq_u32 vcc, -1, v18\n" \
  "s_cbranch_vccnz RB_r1_%=\n" \
  "RB_k1_%=:\n" \
  "v_smfmac_f32_16x16x64_bf16 v[116:119], v[24:27], a[16:23], v0\n" \
  "v_smfmac_f32_16x16x64_bf16 v[120:123], v[24:27], a[24:31], v0\n" \
  "s_waitcnt lgkmcnt(0)\n" \
  "s_barrier\n" \
  "ds_read_b128 v[68:71], v3 offset:0\n" \
  "ds_read_b128 v[72:75], v3 offset:1024\n" \
  "v_smfmac_f32_16x16x64_bf16 v[116:119], v[28:31], a[32:39], v0\n" \
  "ds_read_b128 v[76:79], v3 offset:2048\n" \
  "ds_read_b128 v[80:83], v3 offset:3072\n" \
  "v_smfmac_f32_16x16x64_bf16 v[120:123], v[28:31], a[40:47], v0\n" \
  "ds_read_b128 v[20:23], v4 offset:0\n" \
  "ds_read_b128 v[24:27], v4 offset:1024\n" \
  "v_smfmac_f32_16x16x64_bf16 v[116:119], v[32:35], v[52:59], v0\n" \
  "ds_read_b128 v[52:55], v6 offset:4096\n" \
  "ds_read_b128 v[56:59], v6 offset:5120\n" \
  "v_smfmac_f32_16x16x64_bf16 v[120:123], v[32:35], v[60:67], v0\n" \
  "s_waitcnt lgkmcnt(7)\n" \
  "ds_read_b128 v[60:63], v6 offset:6144\n" \
  "ds_read_b128 v[64:67], v6 offset:7168\n" \
  "v_smfmac_f32_16x16x64_bf16 v[116:119], v[68:71], a[48:55], v0\n" \
  "ds_read_b128 v[28:31], v4 offset:2048\n" \
  "buffer_store_dwordx4 v[124:127], v7, s[36:39], s78 offen sc1\n" \
  "v_smfmac_f32_16x16x64_bf16 v[120:123], v[68:71], a[56:63], v0\n" \
  "s_waitcnt lgkmcnt(9)\n" \
  "ds_read_b128 v[32:35], v4 offset:3072\n" \
  "buffer_store_dword v19, v16, s[40:43], 0 offen sc1\n" \
  "v_smfmac_f32_16x16x64_bf16 v[116:119], v[72:75], a[64:71], v0\n" \
  "ds_read_b128 v[68:71], v5 offset:0\n" \
  "v_smfmac_f32_16x16x64_bf16 v[120:123], v[72:75], a[72:79], v0\n" \
  "s_waitcnt lgkmcnt(10)\n" \
  "buffer_load_dwordx4 v[84:87], v1, s[40:43], s84 offen sc1\n" \
  "v_smfmac_f32_16x16x64_bf16 v[116:119], v[76:79], a[80:87], v0\n" \
  "ds_read_b128 v[72:75], v5 offset:1024\n" \
  "v_smfmac_f32_16x16x64_bf16 v[120:123], v[76:79], a[88:95], v0\n" \
  "s_waitcnt lgkmcnt(6)\n" \
  "buffer_load_dwordx4 v[88:91], v1, s[40:43], s84 offen offset:1024 sc1\n" \
  "v_smfmac_f32_16x16x64_bf16 v[116:119], v[80:83], v[52:59], v0\n" \
  "s_waitcnt lgkmcnt(4)\n" \
  "ds_read_b128 v[52:55], v6 offset:8192\n" \
  "ds_read_b128 v[56:59], v6 offset:9216\n" \
  "v_smfmac_f32_16x16x64_bf16 v[120:123], v[80:83], v[60:67], v0\n" \
  "ds_read_b128 v[60:63], v6 offset:10240\n" \
  "ds_read_b128 v[64:67], v6 offset:11264\n" \
  "v_smfmac_f32_16x16x64_bf16 v[116:119], v[20:23], a[96:103], v0\n" \
  "ds_read_b128 v[76:79], v5 offset:2048\n" \
  "v_smfmac_f32_16x16x64_bf16 v[120:123], v[20:23], a[104:111], v0\n" \
  "ds_read_b128 v[80:83], v5 offset:3072\n" \
  "buffer_load_dwordx4 v[92:95], v1, s[40:43], s84 offen offset:2048 sc1\n" \
  "v_smfmac_f32_16x16x64_bf16 v[116:119], v[24:27], a[112:119], v0\n" \
  "v_smfmac_f32_16x16x64_bf16 v[120:123], v[24:27], a[120:127], v0\n" \
  "s_waitcnt lgkmcnt(9)\n" \
  "buffer_load_dwordx4 v[96:99], v1, s[40:43], s84 offen offset:3072 sc1\n" \
  "v_mov_b64 v[20:21], 0\n" \
  "v_smfmac_f32_16x16x64_bf16 v[116:119], v[28:31], a[128:135], v0\n" \
  "v_mov_b64 v[22:23], 0\n" \
  "v_smfmac_f32_16x16x64_bf16 v[120:123], v[28:31], a[136:143], v0\n" \
  "s_waitcnt lgkmcnt(4)\n" \
  "buffer_load_dwordx4 v[100:103], v1, s[40:43], s85 offen sc1\n" \
  "v_mov_b64 v[24:25], 0\n" \
  "v_smfmac_f32_16x16x64_bf16 v[116:119], v[32:35], v[52:59], v0\n" \
  "s_waitcnt lgkmcnt(2)\n" \
  "ds_read_b128 v[52:55], v6 offset:12288\n" \
  "ds_read_b128 v[56:59], v6 offset:13312\n" \
  "v_smfmac_f32_16x16x64_bf16 v[120:123], v[32:35], v[60:67], v0\n" \
  "ds_read_b128 v[60:63], v6 offset:14336\n" \
  "ds_read_b128 v[64:67], v6 offset:15360\n" \
  "v_smfmac_f32_16x16x64_bf16 v[116:119], v[68:71], a[144:151], v0\n" \
  "buffer_load_dword v17, v17, s[40:43], s46 offen offset:128 sc1\n" \
  "v_mov_b64 v[26:27], 0\n" \
  "v_smfmac_f32_16x16x64_bf16 v[120:123], v[68:71], a[152:159], v0\n" \
  "buffer_load_dwordx4 v[104:107], v1, s[40:43], s85 offen offset:1024 sc1\n" \
  "v_mov_b64 v[28:29], 0\n" \
  "v_smfmac_f32_16x16x64_bf16 v[116:119], v[72:75], a[160:167], v0\n" \
  "v_mov_b64 v[30:31], 0\n" \
  "v_smfmac_f32_16x16x64_bf16 v[120:123], v[72:75], a[168:175], v0\n" \
  "s_waitcnt lgkmcnt(5)\n" \
  "buffer_load_dwordx4 v[108:111], v1, s[40:43], s85 offen offset:2048 sc1\n" \
  "v_mov_b64 v[32:33], 0\n" \
  "v_smfmac_f32_16x16x64_bf16 v[116:119], v[76:79], a[176:183], v0\n" \
  "v_mov_b64 v[34:35], 0\n" \
  "v_smfmac_f32_16x16x64_bf16 v[120:123], v[76:79], a[184:191], v0\n" \
  "s_waitcnt lgkmcnt(2)\n" \
  "buffer_load_dwordx4 v[112:115], v1, s[40:43], s85 offen offset:3072 sc1\n" \
  "v_smfmac_f32_16x16x64_bf16 v[116:119], v[80:83], v[52:59], v0\n" \
  "s_waitcnt lgkmcnt(0)\n" \
  "ds_read_b128 v[52:55], v6 offset:16384\n" \
  "ds_read_b128 v[56:59], v6 offset:17408\n" \
  "v_smfmac_f32_16x16x64_bf16 v[120:123], v[80:83], v[60:67], v0\n" \
  "ds_read_b128 v[60:63], v6 offset:18432\n" \
  "ds_read_b128 v[64:67], v6 offset:19456\n" \
  "s_nop 5\n" \
  "v_permlane32_swap_b32 v116, v120\n" \
  "v_permlane32_swap_b32 v117, v121\n" \
  "v_permlane32_swap_b32 v118, v122\n" \
  "v_permlane32_swap_b32 v119, v123\n" \
  "v_add_f32 v116, v116, v120\n" \
  "v_add_f32 v117, v117, v121\n" \
  "v_add_f32 v118, v118, v122\n" \
  "v_add_f32 v119, v119, v123\n" \
  "v_cvt_pk_bf16_f32 v124, v116, v117\n" \
  "v_cvt_pk_bf16_f32 v125, v118, v119\n" \
  "ds_write_b64 v13, v[124:125]\n" \
  "s_waitcnt lgkmcnt(0)\n" \
  "s_barrier\n" \
  "ds_read_b64_tr_b16 v[68:69], v14 offset:0\n" \
  "ds_read_b64_tr_b16 v[70:71], v14 offset:256\n" \
  "ds_read_b64_tr_b16 v[72:73], v14 offset:8192\n" \
  "ds_read_b64_tr_b16 v[74:75], v14 offset:8448\n" \
  "ds_read_b64_tr_b16 v[76:77], v14 offset:16384\n" \
  "ds_read_b64_tr_b16 v[78:79], v14 offset:16640\n" \
  "ds_read_b64_tr_b16 v[80:81], v14 offset:24576\n" \
  "ds_read_b64_tr_b16 v[82:83], v14 offset:24832\n" \
  "s_waitcnt lgkmcnt(6)\n" \
  "v_smfmac_f32_16x16x64_bf16 v[36:39], v[68:71], a[192:199], v0\n" \
  "s_waitcnt lgkmcnt(4)\n" \
  "v_smfmac_f32_16x16x64_bf16 v[36:39], v[72:75], a[200:207], v0\n" \
  "v_smfmac_f32_16x16x64_bf16 v[40:43], v[68:71], a[208:215], v0\n" \
  "v_smfmac_f32_16x16x64_bf16 v[40:43], v[72:75], a[216:223], v0\n" \
  "s_waitcnt lgkmcnt(2)\n" \
  "v_smfmac_f32_16x16x64_bf16 v[36:39], v[76:79], a[224:231], v0\n" \
  "s_waitcnt lgkmcnt(0)\n" \
  "v_smfmac_f32_16x16x64_bf16 v[36:39], v[80:83], a[232:239], v0\n" \
  "v_smfmac_f32_16x16x64_bf16 v[40:43], v[76:79], v[52:59], v0\n" \
  "ds_read_b128 v[52:55], v6 offset:20480\n" \
  "ds_read_b128 v[56:59], v6 offset:21504\n" \
  "v_smfmac_f32_16x16x64_bf16 v[40:43], v[80:83], v[60:67], v0\n" \
  "ds_read_b128 v[60:63], v6 offset:22528\n" \
  "ds_read_b128 v[64:67], v6 offset:23552\n" \
  "v_smfmac_f32_16x16x64_bf16 v[44:47], v[68:71], a[240:247], v0\n" \
  "s_nop 0\n" \
  "v_cvt_pk_bf16_f32 v116, v36, v37\n" \
  "v_smfmac_f32_16x16x64_bf16 v[44:47], v[72:75], a[248:255], v0\n" \
  "v_cvt_pk_bf16_f32 v118, v38, v39\n" \
  "v_smfmac_f32_16x16x64_bf16 v[48:51], v[68:71], v[128:135], v0\n" \
  "v_cvt_pk_bf16_f32 v117, v40, v41\n" \
  "v_smfmac_f32_16x16x64_bf16 v[48:51], v[72:75], v[136:143], v0\n" \
  "v_cvt_pk_bf16_f32 v119, v42, v43\n" \
  "v_smfmac_f32_16x16x64_bf16 v[44:47], v[76:79], v[144:151], v0\n" \
  "v_permlane16_swap_b32 v116, v117\n" \
  "v_smfmac_f32_16x16x64_bf16 v[44:47], v[80:83], v[152:159], v0\n" \
  "s_waitcnt lgkmcnt(2)\n" \
  "v_permlane16_swap_b32 v118, v119\n" \
  "v_smfmac_f32_16x16x64_bf16 v[48:51], v[76:79], v[52:59], v0\n" \
  "s_waitcnt lgkmcnt(0)\n" \
  "buffer_store_dwordx4 v[116:119], v15, s[36:39], s88 offen sc1\n" \
  "ds_read_b128 v[52:55], v6 offset:24576\n" \
  "ds_read_b128 v[56:59], v6 offset:25600\n" \
  "v_smfmac_f32_16x16x64_bf16 v[48:51], v[80:83], v[60:67], v0\n" \
  "ds_read_b128 v[60:63], v6 offset:26624\n" \
  "ds_read_b128 v[64:67], v6 offset:27648\n" \
  "v_smfmac_f32_16x16x64_bf16 v[20:23], v[68:71], v[160:167], v0\n" \
  "v_cvt_pk_bf16_f32 v120, v44, v45\n" \
  "v_smfmac_f32_16x16x64_bf16 v[20:23], v[72:75], v[168:175], v0\n" \
  "v_cvt_pk_bf16_f32 v122, v46, v47\n" \
  "v_smfmac_f32_16x16x64_bf16 v[24:27], v[68:71], v[176:183], v0\n" \
  "s_nop 0\n" \
  "v_cvt_pk_bf16_f32 v121, v48, v49\n" \
  "v_smfmac_f32_16x16x64_bf16 v[24:27], v[72:75], v[184:191], v0\n" \
  "v_cvt_pk_bf16_f32 v123, v50, v51\n" \
  "v_smfmac_f32_16x16x64_bf16 v[20:23], v[76:79], v[192:199], v0\n" \
  "v_permlane16_swap_b32 v120, v121\n" \
  "v_smfmac_f32_16x16x64_bf16 v[20:23], v[80:83], v[200:207], v0\n" \
  "s_waitcnt lgkmcnt(2)\n" \
  "v_permlane16_swap_b32 v122, v123\n" \
  "v_smfmac_f32_16x16x64_bf16 v[24:27], v[76:79], v[52:59], v0\n" \
  "s_waitcnt lgkmcnt(0)\n" \
  "buffer_store_dwordx4 v[120:123], v15, s[36:39], s89 offen sc1\n" \
  "ds_read_b128 v[52:55], v6 offset:28672\n" \
  "ds_read_b128 v[56:59], v6 offset:29696\n" \
  "v_smfmac_f32_16x16x64_bf16 v[24:27], v[80:83], v[60:67], v0\n" \
  "ds_read_b128 v[60:63], v6 offset:30720\n" \
  "ds_read_b128 v[64:67], v6 offset:31744\n" \
  "v_smfmac_f32_16x16x64_bf16 v[28:31], v[68:71], v[208:215], v0\n" \
  "v_cvt_pk_bf16_f32 v116, v20, v21\n" \
  "v_smfmac_f32_16x16x64_bf16 v[28:31], v[72:75], v[216:223], v0\n" \
  "v_cvt_pk_bf16_f32 v118, v22, v23\n" \
  "v_smfmac_f32_16x16x64_bf16 v[32:35], v[68:71], v[224:231], v0\n" \
  "s_nop 0\n" \
  "v_cvt_pk_bf16_f32 v117, v24, v25\n" \
  "v_smfmac_f32_16x16x64_bf16 v[32:35], v[72:75], v[232:239], v0\n" \
  "v_cvt_pk_bf16_f32 v119, v26, v27\n" \
  "v_smfmac_f32_16x16x64_bf16 v[28:31], v[76:79], v[240:247], v0\n" \
  "v_permlane16_swap_b32 v116, v117\n" \
  "v_smfmac_f32_16x16x64_bf16 v[28:31], v[80:83], v[248:255], v0\n" \
  "s_waitcnt lgkmcnt(2)\n" \
  "v_permlane16_swap_b32 v118, v119\n" \
  "v_smfmac_f32_16x16x64_bf16 v[32:35], v[76:79], v[52:59], v0\n" \
  "s_waitcnt lgkmcnt(0)\n" \
  "buffer_store_dwordx4 v[116:119], v15, s[36:39], s90 offen sc1\n" \
  "v_smfmac_f32_16x16x64_bf16 v[32:35], v[80:83], v[60:67], v0\n" \
  "s_nop 1\n" \
  "v_cvt_pk_bf16_f32 v120, v28, v29\n" \
  "v_cvt_pk_bf16_f32 v122, v30, v31\n" \
  "s_nop 3\n" \
  "v_cvt_pk_bf16_f32 v121, v32, v33\n" \
  "v_cvt_pk_bf16_f32 v123, v34, v35\n" \
  "s_nop 0\n" \
  "v_permlane16_swap_b32 v120, v121\n" \
  "v_permlane16_swap_b32 v122, v123\n" \
  "buffer_store_dwordx4 v[120:123], v15, s[36:39], s91 offen sc1\n" \
  "s_cmp_ge_u32 s64, 62\n" \
  "s_cbranch_scc0 RB_nd_%=\n" \
  "s_waitcnt vmcnt(0)\n" \
  "s_barrier\n" \
  "s_cmp_eq_u32 s60, 1\n" \
  "s_cbranch_scc0 RB_nf_%=\n" \
  "s_lshl_b32 s96, s62, 7\n" \
  "s_add_u32 s96, s96, s45\n" \
  "s_add_u32 s97, s64, 200\n" \
  "s_mov_b64 exec, 1\n" \
  "v_mov_b32 v18, s97\n" \
  "buffer_store_dword v18, off, s[40:43], s96 offset:192 sc1\n" \
  "s_mov_b64 exec, -1\n" \
  "RB_nf_%=:\n" \
  "s_cmp_eq_u32 s64, 63\n" \
  "s_cbranch_scc1 RB_end_%=\n" \
  "RB_nd_%=:\n" \
  "v_mov_b64 v[116:117], 0\n" \
  "v_mov_b64 v[118:119], 0\n" \
  "v_mov_b64 v[36:37], 0\n" \
  "v_mov_b64 v[38:39], 0\n" \
  "v_mov_b64 v[40:41], 0\n" \
  "v_mov_b64 v[42:43], 0\n" \
  "v_mov_b64 v[44:45], 0\n" \
  "v_mov_b64 v[46:47], 0\n" \
  "v_mov_b64 v[48:49], 0\n" \
  "v_mov_b64 v[50:51], 0\n" \
  "ds_read_b128 v[52:55], v6 offset:0\n" \
  "ds_read_b128 v[56:59], v6 offset:1024\n" \
  "ds_read_b128 v[60:63], v6 offset:2048\n" \
  "ds_read_b128 v[64:67], v6 offset:3072\n" \
  "s_waitcnt lgkmcnt(0)\n" \
  "s_mov_b32 s96, s69\n" \
  ".p2align 3\n" \
  "RB_d2_%=:\n" \
  "s_sub_u32 s96, s96, 1\n" \
  "s_cbranch_scc0 RB_d2_%=\n" \
  "buffer_load_dwordx4 v[68:71], v1, s[36:39], s83 offen sc1\n" \
  "buffer_load_dwordx4 v[72:75], v1, s[36:39], s83 offen offset:1024 sc1\n" \
  "s_mov_b32 s96, s101\n" \
  ".p2align 3\n" \
  "RB_d3_%=:\n" \
  "s_sub_u32 s96, s96, 1\n" \
  "s_cbranch_scc0 RB_d3_%=\n" \
  "RB_p2_%=:\n" \
  "buffer_load_dwordx4 v[20:23], v8, s[36:39], s81 offen sc1\n" \
  "buffer_load_dwordx4 v[24:27], v8, s[36:39], s81 offen offset:2048 sc1\n" \
  "buffer_load_dwordx4 v[28:31], v8, s[36:39], s82 offen sc1\n" \
  "buffer_load_dwordx4 v[32:35], v8, s[36:39], s82 offen offset:2048 sc1\n" \
  "buffer_load_dwordx2 v[76:77], v9, s[36:39], s81 offen sc1\n" \
  "buffer_load_dwordx2 v[78:79], v9, s[36:39], s81 offen offset:1024 sc1\n" \
  "s_waitcnt vmcnt(6)\n" \
  "ds_write2_b32 v11, v17, v17 offset0:128 offset1:160\n" \
  "v_max3_u32 v18, v68, v69, v70\n" \
  "v_max_u32 v18, v18, v71\n" \
  "v_max3_u32 v18, v18, v72, v73\n" \
  "v_max3_u32 v18, v18, v74, v75\n" \
  "s_waitcnt vmcnt(5)\n" \
  "v_smfmac_f32_16x16x64_bf16 v[116:119], v[20:23], v[84:91], v0\n" \
  "v_max3_u32 v18, v18, v20, v21\n" \
  "v_max3_u32 v18, v18, v22, v23\n" \
  "s_waitcnt vmcnt(4)\n" \
  "v_smfmac_f32_16x16x64_bf16 v[116:119], v[24:27], v[92:99], v0\n" \
  "v_max3_u32 v18, v18, v24, v25\n" \
  "v_max3_u32 v18, v18, v26, v27\n" \
  "s_waitcnt vmcnt(3)\n" \
  "v_smfmac_f32_16x16x64_bf16 v[116:119], v[28:31], v[100:107], v0\n" \
  "v_max3_u32 v18, v18, v28, v29\n" \
  "v_max3_u32 v18, v18, v30, v31\n" \
  "s_waitcnt vmcnt(2)\n" \
  "v_smfmac_f32_16x16x64_bf16 v[116:119], v[32:35], v[108:115], v0\n" \
  "v_max3_u32 v18, v18, v32, v33\n" \
  "v_max3_u32 v18, v18, v34, v35\n" \
  "s_waitcnt vmcnt(0)\n" \
  "v_smfmac_f32_16x16x64_bf16 v[116:119], v[76:79], v[68:75], v0\n" \
  "v_max3_u32 v18, v18, v76, v77\n" \
  "v_max3_u32 v18, v18, v78, v79\n" \
  "v_cmp_eq_u32 vcc, -1, v18\n" \
  "s_cbranch_vccnz RB_r2_%=\n" \
  "RB_k2_%=:\n" \
  "s_cmp_eq_u32 s60, 0\n" \
  "s_nop 2\n" \
  "v_permlane32_swap_b32 v116, v117\n" \
  "v_permlane32_swap_b32 v118, v119\n" \
  "v_add_f32 v116, v116, v117\n" \
  "v_add_f32 v118, v118, v119\n" \
  "ds_write2_b32 v11, v116, v118 offset1:32\n" \
  "s_waitcnt lgkmcnt(0)\n" \
  "s_barrier\n" \
  "s_cbranch_scc0 RB_c1_%=\n" \
  "ds_read_b64 v[76:77], v12 offset:0\n" \
  "ds_read_b64 v[78:79], v12 offset:8192\n" \
  "ds_read_b64 v[80:81], v12 offset:16384\n" \
  "ds_read_b64 v[82:83], v12 offset:24576\n" \
  "s_waitcnt lgkmcnt(2)\n" \
  "v_add_f32 v76, v76, v78\n" \
  "v_add_f32 v77, v77, v79\n" \
  "s_waitcnt lgkmcnt(1)\n" \
  "v_add_f32 v76, v76, v80\n" \
  "v_add_f32 v77, v77, v81\n" \
  "s_waitcnt lgkmcnt(0)\n" \
  "v_add_f32 v76, v76, v82\n" \
  "v_add_f32 v77, v77, v83\n" \
  "v_cvt_pk_bf16_f32 v116, v76, v77\n" \
  "buffer_store_dword v116, v10, s[36:39], s77 offen sc1\n" \
  "RB_c1_%=:\n" \
  "ds_read_b32 v17, v12 offset:25088\n" \
  "s_mov_b32 s96, s66\n" \
  "s_mov_b32 s66, s65\n" \
  "s_mov_b32 s65, s67\n" \
  "s_mov_b32 s67, s96\n" \
  "s_add_u32 s64, s64, 1\n" \
  "s_xor_b32 s96, s47, 0x100000\n" \
  "s_mov_b32 s47, s50\n" \
  "s_mov_b32 s50, s51\n" \
  "s_mov_b32 s51, s96\n" \
  "s_branch RB_top_%=\n" \
  "RB_r0_%=:\n" \
  "s_mov_b32 s96, s68\n" \
  ".p2align 3\n" \
  "RB_d0_%=:\n" \
  "s_sub_u32 s96, s96, 1\n" \
  "s_cbranch_scc0 RB_d0_%=\n" \
  "buffer_load_dword v18, v17, s[40:43], s46 offen sc1\n" \
  "buffer_load_dwordx4 v[20:23], v1, s[36:39], s76 offen sc1\n" \
  "buffer_load_dwordx4 v[24:27], v1, s[36:39], s76 offen offset:1024 sc1\n" \
  "buffer_load_dwordx4 v[28:31], v1, s[36:39], s76 offen offset:2048 sc1\n" \
  "buffer_load_dwordx4 v[32:35], v1, s[36:39], s76 offen offset:3072 sc1\n" \
  "s_cmp_eq_u32 s60, 3\n" \
  "s_cbranch_scc0 RB_r0c_%=\n" \
  "s_waitcnt vmcnt(4)\n" \
  "RB_r0b_%=:\n" \
  "v_cmp_gt_u32 vcc, 208, v18\n" \
  "s_cbranch_vccz RB_r0c_%=\n" \
  "s_sleep 2\n" \
  "buffer_load_dword v18, v17, s[40:43], s46 offen sc1\n" \
  "s_waitcnt vmcnt(0)\n" \
  "s_sub_u32 s63, s63, 1\n" \
  "s_cbranch_scc0 RB_r0b_%=\n" \
  "s_mov_b32 s63, 0\n" \
  "RB_r0c_%=:\n" \
  "s_barrier\n" \
  "s_branch RB_p1b_%=\n" \
  "RB_r1_%=:\n" \
  "s_nop 7\n" \
  "s_sub_u32 s63, s63, 1\n" \
  "s_cbranch_scc1 RB_r1d_%=\n" \
  "v_mov_b64 v[116:117], 0\n" \
  "v_mov_b64 v[118:119], 0\n" \
  "v_mov_b64 v[120:121], 0\n" \
  "v_mov_b64 v[122:123], 0\n" \
  "s_branch RB_p1_%=\n" \
  "RB_r1d_%=:\n" \
  "s_mov_b32 s63, 0\n" \
  "s_branch RB_k1_%=\n" \
  "RB_r2_%=:\n" \
  "s_nop 7\n" \
  "s_sub_u32 s63, s63, 1\n" \
  "s_cbranch_scc1 RB_r2d_%=\n" \
  "v_mov_b64 v[116:117], 0\n" \
  "v_mov_b64 v[118:119], 0\n" \
  "buffer_load_dwordx4 v[68:71], v1, s[36:39], s83 offen sc1\n" \
  "buffer_load_dwordx4 v[72:75], v1, s[36:39], s83 offen offset:1024 sc1\n" \
  "s_branch RB_p2_%=\n" \
  "RB_r2d_%=:\n" \
  "s_mov_b32 s63, 0\n" \
  "s_branch RB_k2_%=\n" \
  "RB_ys_%=:\n" \
  "s_sub_u32 s63, s63, 1\n" \
  "s_cbranch_scc1 RB_ysx_%=\n" \
  "s_sleep 2\n" \
  "s_lshr_b32 s96, s35, 17\n" \
  "s_and_b32 s96, s96, 0xf80\n" \
  "s_add_u32 s96, s96, 252182528\n" \
  "buffer_load_dword v244, off, s[40:43], s96 sc1\n" \
  "s_waitcnt vmcnt(0)\n" \
  "s_branch RB_yc_%=\n" \
  "RB_ysx_%=:\n" \
  "s_mov_b32 s63, 0\n" \
  "s_branch RB_ye_%=\n" \
  "RB_yt_%=:\n" \
  "s_sub_u32 s63, s63, 1\n" \
  "s_cbranch_scc1 RB_ytx_%=\n" \
  "s_sleep 2\n" \
  "s_lshr_b32 s96, s35, 16\n" \
  "s_and_b32 s96, s96, 0x1f00\n" \
  "s_add_u32 s96, s96, 252190720\n" \
  "buffer_load_dword v244, v17, s[40:43], s96 offen sc1\n" \
  "s_waitcnt vmcnt(0)\n" \
  "s_branch RB_yf_%=\n" \
  "RB_ytx_%=:\n" \
  "s_mov_b32 s63, 0\n" \
  "s_branch RB_yg_%=\n" \
  "RB_yb_%=:\n" \
  "s_and_b32 s97, s97, 28\n" \
  "s_lshl_b32 s97, s97, 6\n" \
  "s_or_b32 s35, s35, s97\n" \
  "s_or_b32 s35, s35, 1\n" \
  "s_waitcnt vmcnt(0) lgkmcnt(0)\n" \
  "s_branch RB_end_%=\n" \
  "RB_end_%=:\n" \
  "s_waitcnt vmcnt(0) lgkmcnt(0)\n" \
  "RB_fin_%=:\n"
#define REC_CLOBBERS "v17", "v18", "v20", "v21", "v22", "v23", "v24", "v25", "v26", "v27", "v28", "v29", "v30", "v31", "v32", "v33", "v34", "v35", "v36", "v37", "v38", "v39", "v40", "v41", "v42", "v43", "v44", "v45", "v46", "v47", "v48", "v49", "v50", "v51", "v52", "v53", "v54", "v55", "v56", "v57", "v58", "v59", "v60", "v61", "v62", "v63", "v64", "v65", "v66", "v67", "v68", "v69", "v70", "v71", "v72", "v73", "v74", "v75", "v76", "v77", "v78", "v79", "v80", "v81", "v82", "v83", "v84", "v85", "v86", "v87", "v88", "v89", "v90", "v91", "v92", "v93", "v94", "v95", "v96", "v97", "v98", "v99", "v100", "v101", "v102", "v103", "v104", "v105", "v106", "v107", "v108", "v109", "v110", "v111", "v112", "v113", "v114", "v115", "v119", "v121", "v122", "v123", "v124", "v125", "v126", "v127", "v128", "v129", "v130", "v131", "v132", "v133", "v134", "v135", "v136", "v137", "v138", "v139", "v140", "v141", "v142", "v143", "v144", "v145", "v146", "v147", "v148", "v149", "v150", "v151", "v152", "v153", "v154", "v155", "v156", "v157", "v158", "v159", "v160", "v161", "v162", "v163", "v164", "v165", "v166", "v167", "v168", "v169", "v170", "v171", "v172", "v173", "v174", "v175", "v176", "v177", "v178", "v179", "v180", "v181", "v182", "v183", "v184", "v185", "v186", "v187", "v188", "v189", "v190", "v191", "v192", "v193", "v194", "v195", "v196", "v197", "v198", "v199", "v200", "v201", "v202", "v203", "v204", "v205", "v206", "v207", "v208", "v209", "v210", "v211", "v212", "v213", "v214", "v215", "v216", "v217", "v218", "v219", "v220", "v221", "v222", "v223", "v224", "v225", "v226", "v227", "v228", "v229", "v230", "v231", "v232", "v233", "v234", "v235", "v236", "v237", "v238", "v239", "v240", "v241", "v242", "v243", "v244", "v245", "v246", "v247", "v248", "v249", "v250", "v251", "v252", "v253", "v254", "v255", "a0", "a1", "a2", "a3", "a4", "a5", "a6", "a7", "a8", "a9", "a10", "a11", "a12", "a13", "a14", "a15", "a16", "a17", "a18", "a19", "a20", "a21", "a22", "a23", "a24", "a25", "a26", "a27", "a28", "a29", "a30", "a31", "a32", "a33", "a34", "a35", "a36", "a37", "a38", "a39", "a40", "a41", "a42", "a43", "a44", "a45", "a46", "a47", "a48", "a49", "a50", "a51", "a52", "a53", "a54", "a55", "a56", "a57", "a58", "a59", "a60", "a61", "a62", "a63", "a64", "a65", "a66", "a67", "a68", "a69", "a70", "a71", "a72", "a73", "a74", "a75", "a76", "a77", "a78", "a79", "a80", "a81", "a82", "a83", "a84", "a85", "a86", "a87", "a88", "a89", "a90", "a91", "a92", "a93", "a94", "a95", "a96", "a97", "a98", "a99", "a100", "a101", "a102", "a103", "a104", "a105", "a106", "a107", "a108", "a109", "a110", "a111", "a112", "a113", "a114", "a115", "a116", "a117", "a118", "a119", "a120", "a121", "a122", "a123", "a124", "a125", "a126", "a127", "a128", "a129", "a130", "a131", "a132", "a133", "a134", "a135", "a136", "a137", "a138", "a139", "a140", "a141", "a142", "a143", "a144", "a145", "a146", "a147", "a148", "a149", "a150", "a151", "a152", "a153", "a154", "a155", "a156", "a157", "a158", "a159", "a160", "a161", "a162", "a163", "a164", "a165", "a166", "a167", "a168", "a169", "a170", "a171", "a172", "a173", "a174", "a175", "a176", "a177", "a178", "a179", "a180", "a181", "a182", "a183", "a184", "a185", "a186", "a187", "a188", "a189", "a190", "a191", "a192", "a193", "a194", "a195", "a196", "a197", "a198", "a199", "a200", "a201", "a202", "a203", "a204", "a205", "a206", "a207", "a208", "a209", "a210", "a211", "a212", "a213", "a214", "a215", "a216", "a217", "a218", "a219", "a220", "a221", "a222", "a223", "a224", "a225", "a226", "a227", "a228", "a229", "a230", "a231", "a232", "a233", "a234", "a235", "a236", "a237", "a238", "a239", "a240", "a241", "a242", "a243", "a244", "a245", "a246", "a247", "a248", "a249", "a250", "a251", "a252", "a253", "a254", "a255", "s76", "s77", "s78", "s79", "s80", "s81", "s82", "s83", "s84", "s85", "s92", "s93", "s96", "s97", "s64", "s46", "s47", "s50", "s51", "s65", "s66", "s67", "vcc", "scc", "memory"
constexpr unsigned L_TOTAL = 163840, LB = 131072;
constexpr unsigned R_MEM = 0, R_P = 2 * BS, R_PAGE = 4 * MB, R_BYTES = 4 * R_PAGE;
constexpr unsigned OOB = 0x40000000u;
DEV unsigned sopq(unsigned a) { asm volatile("" : "+s"(a)); return a; }
DEV unsigned vopq(unsigned a) { asm volatile("" : "+v"(a)); return a; }
DEV u32x4 fill4(unsigned a) { a = vopq(a); return u32x4{a, a, a, a}; }
DEV u32x4 mksrd(const void* p, unsigned bytes) { uint64_t a = (uint64_t)p; return u32x4{(unsigned)a, (unsigned)(a >> 32) & 0xffffu, sopq(bytes), sopq(0x00020000u)}; }
constexpr unsigned UX_SEGS = 24;
#define GB_ASM \
  "v_add_u32 v6, 0x10000, v5\n" \
  "v_add_u32 v9, 0x10000, v8\n" \
  "v_add_u32 v12, 0x10000, v11\n" \
  "s_add_u32 s50, s45, s51\n" \
  "s_add_u32 m0, s48, 0\n" \
  "s_nop 0\n" \
  "buffer_load_dwordx4 v0, s[36:39], s44 offen sc1 lds\n" \
  "buffer_load_dwordx4 v0, s[36:39], s44 offen offset:1024 sc1 lds\n" \
  "buffer_load_dwordx4 v0, s[36:39], s44 offen offset:2048 sc1 lds\n" \
  "buffer_load_dwordx4 v0, s[36:39], s44 offen offset:3072 sc1 lds\n" \
  "s_add_u32 m0, s49, 16384\n" \
  "s_nop 0\n" \
  "buffer_load_dwordx4 v4, s[40:43], s45 offen lds\n" \
  "s_add_u32 m0, s49, 17280\n" \
  "s_nop 0\n" \
  "buffer_load_dwordx4 v4, s[40:43], s45 offen offset:128 lds\n" \
  "s_add_u32 m0, s49, 24576\n" \
  "s_nop 0\n" \
  "buffer_load_dwordx4 v4, s[40:43], s50 offen lds\n" \
  "s_add_u32 m0, s49, 25472\n" \
  "s_nop 0\n" \
  "buffer_load_dwordx4 v4, s[40:43], s50 offen offset:128 lds\n" \
  "s_add_u32 s44, s44, 4096\n" \
  "s_add_u32 s45, s45, s46\n" \
  "s_add_u32 s50, s50, s46\n" \
  "s_add_u32 m0, s48, 32768\n" \
  "s_nop 0\n" \
  "buffer_load_dwordx4 v0, s[36:39], s44 offen sc1 lds\n" \
  "buffer_load_dwordx4 v0, s[36:39], s44 offen offset:1024 sc1 lds\n" \
  "buffer_load_dwordx4 v0, s[36:39], s44 offen offset:2048 sc1 lds\n" \
  "buffer_load_dwordx4 v0, s[36:39], s44 offen offset:3072 sc1 lds\n" \
  "s_add_u32 m0, s49, 49152\n" \
  "s_nop 0\n" \
  "buffer_load_dwordx4 v4, s[40:43], s45 offen lds\n" \
  "s_add_u32 m0, s49, 50048\n" \
  "s_nop 0\n" \
  "buffer_load_dwordx4 v4, s[40:43], s45 offen offset:128 lds\n" \
  "s_add_u32 m0, s49, 57344\n" \
  "s_nop 0\n" \
  "buffer_load_dwordx4 v4, s[40:43], s50 offen lds\n" \
  "s_add_u32 m0, s49, 58240\n" \
  "s_nop 0\n" \
  "buffer_load_dwordx4 v4, s[40:43], s50 offen offset:128 lds\n" \
  "s_add_u32 s44, s44, s57\n" \
  "s_add_u32 s45, s45, s46\n" \
  "s_add_u32 s50, s50, s46\n" \
  "s_add_u32 m0, s48, 65536\n" \
  "s_nop 0\n" \
  "buffer_load_dwordx4 v0, s[36:39], s44 offen sc1 lds\n" \
  "buffer_load_dwordx4 v0, s[36:39], s44 offen offset:1024 sc1 lds\n" \
  "buffer_load_dwordx4 v0, s[36:39], s44 offen offset:2048 sc1 lds\n" \
  "buffer_load_dwordx4 v0, s[36:39], s44 offen offset:3072 sc1 lds\n" \
  "s_add_u32 m0, s49, 81920\n" \
  "s_nop 0\n" \
  "buffer_load_dwordx4 v4, s[40:43], s45 offen lds\n" \
  "s_add_u32 m0, s49, 82816\n" \
  "s_nop 0\n" \
  "buffer_load_dwordx4 v4, s[40:43], s45 offen offset:128 lds\n" \
  "s_add_u32 m0, s49, 90112\n" \
  "s_nop 0\n" \
  "buffer_load_dwordx4 v4, s[40:43], s50 offen lds\n" \
  "s_add_u32 m0, s49, 91008\n" \
  "s_nop 0\n" \
  "buffer_load_dwordx4 v4, s[40:43], s50 offen offset:128 lds\n" \
  "s_add_u32 s44, s44, 4096\n" \
  "s_add_u32 s45, s45, s46\n" \
  "s_add_u32 s50, s50, s46\n" \
  "s_add_u32 m0, s48, 98304\n" \
  "s_nop 0\n" \
  "buffer_load_dwordx4 v0, s[36:39], s44 offen sc1 lds\n" \
  "buffer_load_dwordx4 v0, s[36:39], s44 offen offset:1024 sc1 lds\n" \
  "v_mov_b64 v[64:65], 0\n" \
  "v_mov_b64 v[66:67], 0\n" \
  "v_mov_b64 v[68:69], 0\n" \
  "v_mov_b64 v[70:71], 0\n" \
  "v_mov_b64 v[72:73], 0\n" \
  "v_mov_b64 v[74:75], 0\n" \
  "v_mov_b64 v[76:77], 0\n" \
  "v_mov_b64 v[78:79], 0\n" \
  "v_mov_b64 v[80:81], 0\n" \
  "v_mov_b64 v[82:83], 0\n" \
  "v_mov_b64 v[84:85], 0\n" \
  "v_mov_b64 v[86:87], 0\n" \
  "v_mov_b64 v[88:89], 0\n" \
  "v_mov_b64 v[90:91], 0\n" \
  "v_mov_b64 v[92:93], 0\n" \
  "v_mov_b64 v[94:95], 0\n" \
  "v_mov_b64 v[96:97], 0\n" \
  "v_mov_b64 v[98:99], 0\n" \
  "v_mov_b64 v[100:101], 0\n" \
  "v_mov_b64 v[102:103], 0\n" \
  "v_mov_b64 v[104:105], 0\n" \
  "v_mov_b64 v[106:107], 0\n" \
  "v_mov_b64 v[108:109], 0\n" \
  "v_mov_b64 v[110:111], 0\n" \
  "v_mov_b64 v[112:113], 0\n" \
  "v_mov_b64 v[114:115], 0\n" \
  "v_mov_b64 v[116:117], 0\n" \
  "v_mov_b64 v[118:119], 0\n" \
  "v_mov_b64 v[120:121], 0\n" \
  "v_mov_b64 v[122:123], 0\n" \
  "v_mov_b64 v[124:125], 0\n" \
  "v_mov_b64 v[126:127], 0\n" \
  "s_waitcnt vmcnt(18)\n" \
  "s_barrier\n" \
  "ds_read_b64_tr_b16 v[16:17], v8 offset:16384\n" \
  "ds_read_b64_tr_b16 v[18:19], v8 offset:18432\n" \
  "ds_read_b128 v[24:27], v5 offset:0\n" \
  "ds_read_b128 v[28:31], v5 offset:4096\n" \
  "ds_read_b64_tr_b16 v[20:21], v11 offset:16384\n" \
  "ds_read_b64_tr_b16 v[22:23], v11 offset:18432\n" \
  ".p2align 4\n" \
  "GB_top_%=:\n" \
  "s_waitcnt lgkmcnt(0)\n" \
  "v_mfma_f32_32x32x16_bf16 v[64:79], v[16:19], v[24:27], v[64:79]\n" \
  "ds_read_b64_tr_b16 v[32:33], v8 offset:20480\n" \
  "ds_read_b64_tr_b16 v[34:35], v8 offset:22528\n" \
  "ds_read_b128 v[40:43], v5 offset:1024\n" \
  "buffer_load_dwordx4 v0, s[36:39], s44 offen offset:2048 sc1 lds\n" \
  "v_mfma_f32_32x32x16_bf16 v[80:95], v[16:19], v[28:31], v[80:95]\n" \
  "ds_read_b128 v[44:47], v5 offset:5120\n" \
  "ds_read_b64_tr_b16 v[36:37], v11 offset:20480\n" \
  "ds_read_b64_tr_b16 v[38:39], v11 offset:22528\n" \
  "v_mfma_f32_32x32x16_bf16 v[96:111], v[20:23], v[24:27], v[96:111]\n" \
  "buffer_load_dwordx4 v0, s[36:39], s44 offen offset:3072 sc1 lds\n" \
  "v_mfma_f32_32x32x16_bf16 v[112:127], v[20:23], v[28:31], v[112:127]\n" \
  "s_waitcnt lgkmcnt(0)\n" \
  "v_mfma_f32_32x32x16_bf16 v[64:79], v[32:35], v[40:43], v[64:79]\n" \
  "s_add_u32 m0, s49, 114688\n" \
  "ds_read_b64_tr_b16 v[16:17], v8 offset:24576\n" \
  "ds_read_b64_tr_b16 v[18:19], v8 offset:26624\n" \
  "ds_read_b128 v[24:27], v5 offset:2048\n" \
  "buffer_load_dwordx4 v4, s[40:43], s45 offen lds\n" \
  "v_mfma_f32_32x32x16_bf16 v[80:95], v[32:35], v[44:47], v[80:95]\n" \
  "ds_read_b128 v[28:31], v5 offset:6144\n" \
  "ds_read_b64_tr_b16 v[20:21], v11 offset:24576\n" \
  "ds_read_b64_tr_b16 v[22:23], v11 offset:26624\n" \
  "v_mfma_f32_32x32x16_bf16 v[96:111], v[36:39], v[40:43], v[96:111]\n" \
  "s_add_u32 m0, s49, 115584\n" \
  "s_nop 0\n" \
  "buffer_load_dwordx4 v4, s[40:43], s45 offen offset:128 lds\n" \
  "v_mfma_f32_32x32x16_bf16 v[112:127], v[36:39], v[44:47], v[112:127]\n" \
  "s_waitcnt lgkmcnt(0)\n" \
  "v_mfma_f32_32x32x16_bf16 v[64:79], v[16:19], v[24:27], v[64:79]\n" \
  "s_add_u32 m0, s49, 122880\n" \
  "ds_read_b64_tr_b16 v[32:33], v8 offset:28672\n" \
  "ds_read_b64_tr_b16 v[34:35], v8 offset:30720\n" \
  "ds_read_b128 v[40:43], v5 offset:3072\n" \
  "buffer_load_dwordx4 v4, s[40:43], s50 offen lds\n" \
  "v_mfma_f32_32x32x16_bf16 v[80:95], v[16:19], v[28:31], v[80:95]\n" \
  "ds_read_b128 v[44:47], v5 offset:7168\n" \
  "ds_read_b64_tr_b16 v[36:37], v11 offset:28672\n" \
  "ds_read_b64_tr_b16 v[38:39], v11 offset:30720\n" \
  "v_mfma_f32_32x32x16_bf16 v[96:111], v[20:23], v[24:27], v[96:111]\n" \
  "s_add_u32 m0, s49, 123776\n" \
  "s_nop 0\n" \
  "buffer_load_dwordx4 v4, s[40:43], s50 offen offset:128 lds\n" \
  "s_add_u32 s44, s44, s57\n" \
  "s_add_u32 s45, s45, s46\n" \
  "s_add_u32 s50, s50, s46\n" \
  "v_mfma_f32_32x32x16_bf16 v[112:127], v[20:23], v[28:31], v[112:127]\n" \
  "s_waitcnt lgkmcnt(0)\n" \
  "v_mfma_f32_32x32x16_bf16 v[64:79], v[32:35], v[40:43], v[64:79]\n" \
  "s_cmp_eq_u32 s47, 1\n" \
  "s_waitcnt vmcnt(16)\n" \
  "s_barrier\n" \
  "s_cselect_b32 s38, 0, s38\n" \
  "s_cselect_b32 s42, 0, s42\n" \
  "ds_read_b64_tr_b16 v[16:17], v8 offset:49152\n" \
  "ds_read_b64_tr_b16 v[18:19], v8 offset:51200\n" \
  "ds_read_b128 v[24:27], v5 offset:32768\n" \
  "v_mfma_f32_32x32x16_bf16 v[80:95], v[32:35], v[44:47], v[80:95]\n" \
  "s_add_u32 m0, s48, 0\n" \
  "ds_read_b128 v[28:31], v5 offset:36864\n" \
  "ds_read_b64_tr_b16 v[20:21], v11 offset:49152\n" \
  "ds_read_b64_tr_b16 v[22:23], v11 offset:51200\n" \
  "buffer_load_dwordx4 v0, s[36:39], s44 offen sc1 lds\n" \
  "v_mfma_f32_32x32x16_bf16 v[96:111], v[36:39], v[40:43], v[96:111]\n" \
  "buffer_load_dwordx4 v0, s[36:39], s44 offen offset:1024 sc1 lds\n" \
  "v_mfma_f32_32x32x16_bf16 v[112:127], v[36:39], v[44:47], v[112:127]\n" \
  "s_waitcnt lgkmcnt(0)\n" \
  "v_mfma_f32_32x32x16_bf16 v[64:79], v[16:19], v[24:27], v[64:79]\n" \
  "ds_read_b64_tr_b16 v[32:33], v8 offset:53248\n" \
  "ds_read_b64_tr_b16 v[34:35], v8 offset:55296\n" \
  "ds_read_b128 v[40:43], v5 offset:33792\n" \
  "buffer_load_dwordx4 v0, s[36:39], s44 offen offset:2048 sc1 lds\n" \
  "v_mfma_f32_32x32x16_bf16 v[80:95], v[16:19], v[28:31], v[80:95]\n" \
  "ds_read_b128 v[44:47], v5 offset:37888\n" \
  "ds_read_b64_tr_b16 v[36:37], v11 offset:53248\n" \
  "ds_read_b64_tr_b16 v[38:39], v11 offset:55296\n" \
  "v_mfma_f32_32x32x16_bf16 v[96:111], v[20:23], v[24:27], v[96:111]\n" \
  "buffer_load_dwordx4 v0, s[36:39], s44 offen offset:3072 sc1 lds\n" \
  "v_mfma_f32_32x32x16_bf16 v[112:127], v[20:23], v[28:31], v[112:127]\n" \
  "s_waitcnt lgkmcnt(0)\n" \
  "v_mfma_f32_32x32x16_bf16 v[64:79], v[32:35], v[40:43], v[64:79]\n" \
  "s_add_u32 m0, s49, 16384\n" \
  "ds_read_b64_tr_b16 v[16:17], v8 offset:57344\n" \
  "ds_read_b64_tr_b16 v[18:19], v8 offset:59392\n" \
  "ds_read_b128 v[24:27], v5 offset:34816\n" \
  "buffer_load_dwordx4 v4, s[40:43], s45 offen lds\n" \
  "v_mfma_f32_32x32x16_bf16 v[80:95], v[32:35], v[44:47], v[80:95]\n" \
  "ds_read_b128 v[28:31], v5 offset:38912\n" \
  "ds_read_b64_tr_b16 v[20:21], v11 offset:57344\n" \
  "ds_read_b64_tr_b16 v[22:23], v11 offset:59392\n" \
  "v_mfma_f32_32x32x16_bf16 v[96:111], v[36:39], v[40:43], v[96:111]\n" \
  "s_add_u32 m0, s49, 17280\n" \
  "s_nop 0\n" \
  "buffer_load_dwordx4 v4, s[40:43], s45 offen offset:128 lds\n" \
  "v_mfma_f32_32x32x16_bf16 v[112:127], v[36:39], v[44:47], v[112:127]\n" \
  "s_waitcnt lgkmcnt(0)\n" \
  "v_mfma_f32_32x32x16_bf16 v[64:79], v[16:19], v[24:27], v[64:79]\n" \
  "s_add_u32 m0, s49, 24576\n" \
  "ds_read_b64_tr_b16 v[32:33], v8 offset:61440\n" \
  "ds_read_b64_tr_b16 v[34:35], v8 offset:63488\n" \
  "ds_read_b128 v[40:43], v5 offset:35840\n" \
  "buffer_load_dwordx4 v4, s[40:43], s50 offen lds\n" \
  "v_mfma_f32_32x32x16_bf16 v[80:95], v[16:19], v[28:31], v[80:95]\n" \
  "ds_read_b128 v[44:47], v5 offset:39936\n" \
  "ds_read_b64_tr_b16 v[36:37], v11 offset:61440\n" \
  "ds_read_b64_tr_b16 v[38:39], v11 offset:63488\n" \
  "v_mfma_f32_32x32x16_bf16 v[96:111], v[20:23], v[24:27], v[96:111]\n" \
  "s_add_u32 m0, s49, 25472\n" \
  "s_nop 0\n" \
  "buffer_load_dwordx4 v4, s[40:43], s50 offen offset:128 lds\n" \
  "s_add_u32 s44, s44, 4096\n" \
  "s_add_u32 s45, s45, s46\n" \
  "s_add_u32 s50, s50, s46\n" \
  "v_mfma_f32_32x32x16_bf16 v[112:127], v[20:23], v[28:31], v[112:127]\n" \
  "s_waitcnt lgkmcnt(0)\n" \
  "v_mfma_f32_32x32x16_bf16 v[64:79], v[32:35], v[40:43], v[64:79]\n" \
  "s_waitcnt vmcnt(16)\n" \
  "s_barrier\n" \
  "ds_read_b64_tr_b16 v[16:17], v9 offset:16384\n" \
  "ds_read_b64_tr_b16 v[18:19], v9 offset:18432\n" \
  "ds_read_b128 v[24:27], v6 offset:0\n" \
  "v_mfma_f32_32x32x16_bf16 v[80:95], v[32:35], v[44:47], v[80:95]\n" \
  "s_add_u32 m0, s48, 32768\n" \
  "ds_read_b128 v[28:31], v6 offset:4096\n" \
  "ds_read_b64_tr_b16 v[20:21], v12 offset:16384\n" \
  "ds_read_b64_tr_b16 v[22:23], v12 offset:18432\n" \
  "buffer_load_dwordx4 v0, s[36:39], s44 offen sc1 lds\n" \
  "v_mfma_f32_32x32x16_bf16 v[96:111], v[36:39], v[40:43], v[96:111]\n" \
  "buffer_load_dwordx4 v0, s[36:39], s44 offen offset:1024 sc1 lds\n" \
  "v_mfma_f32_32x32x16_bf16 v[112:127], v[36:39], v[44:47], v[112:127]\n" \
  "s_waitcnt lgkmcnt(0)\n" \
  "v_mfma_f32_32x32x16_bf16 v[64:79], v[16:19], v[24:27], v[64:79]\n" \
  "ds_read_b64_tr_b16 v[32:33], v9 offset:20480\n" \
  "ds_read_b64_tr_b16 v[34:35], v9 offset:22528\n" \
  "ds_read_b128 v[40:43], v6 offset:1024\n" \
  "buffer_load_dwordx4 v0, s[36:39], s44 offen offset:2048 sc1 lds\n" \
  "v_mfma_f32_32x32x16_bf16 v[80:95], v[16:19], v[28:31], v[80:95]\n" \
  "ds_read_b128 v[44:47], v6 offset:5120\n" \
  "ds_read_b64_tr_b16 v[36:37], v12 offset:20480\n" \
  "ds_read_b64_tr_b16 v[38:39], v12 offset:22528\n" \
  "v_mfma_f32_32x32x16_bf16 v[96:111], v[20:23], v[24:27], v[96:111]\n" \
  "buffer_load_dwordx4 v0, s[36:39], s44 offen offset:3072 sc1 lds\n" \
  "v_mfma_f32_32x32x16_bf16 v[112:127], v[20:23], v[28:31], v[112:127]\n" \
  "s_waitcnt lgkmcnt(0)\n" \
  "v_mfma_f32_32x32x16_bf16 v[64:79], v[32:35], v[40:43], v[64:79]\n" \
  "s_add_u32 m0, s49, 49152\n" \
  "ds_read_b64_tr_b16 v[16:17], v9 offset:24576\n" \
  "ds_read_b64_tr_b16 v[18:19], v9 offset:26624\n" \
  "ds_read_b128 v[24:27], v6 offset:2048\n" \
  "buffer_load_dwordx4 v4, s[40:43], s45 offen lds\n" \
  "v_mfma_f32_32x32x16_bf16 v[80:95], v[32:35], v[44:47], v[80:95]\n" \
  "ds_read_b128 v[28:31], v6 offset:6144\n" \
  "ds_read_b64_tr_b16 v[20:21], v12 offset:24576\n" \
  "ds_read_b64_tr_b16 v[22:23], v12 offset:26624\n" \
  "v_mfma_f32_32x32x16_bf16 v[96:111], v[36:39], v[40:43], v[96:111]\n" \
  "s_add_u32 m0, s49, 50048\n" \
  "s_nop 0\n" \
  "buffer_load_dwordx4 v4, s[40:43], s45 offen offset:128 lds\n" \
  "v_mfma_f32_32x32x16_bf16 v[112:127], v[36:39], v[44:47], v[112:127]\n" \
  "s_waitcnt lgkmcnt(0)\n" \
  "v_mfma_f32_32x32x16_bf16 v[64:79], v[16:19], v[24:27], v[64:79]\n" \
  "s_add_u32 m0, s49, 57344\n" \
  "ds_read_b64_tr_b16 v[32:33], v9 offset:28672\n" \
  "ds_read_b64_tr_b16 v[34:35], v9 offset:30720\n" \
  "ds_read_b128 v[40:43], v6 offset:3072\n" \
  "buffer_load_dwordx4 v4, s[40:43], s50 offen lds\n" \
  "v_mfma_f32_32x32x16_bf16 v[80:95], v[16:19], v[28:31], v[80:95]\n" \
  "ds_read_b128 v[44:47], v6 offset:7168\n" \
  "ds_read_b64_tr_b16 v[36:37], v12 offset:28672\n" \
  "ds_read_b64_tr_b16 v[38:39], v12 offset:30720\n" \
  "v_mfma_f32_32x32x16_bf16 v[96:111], v[20:23], v[24:27], v[96:111]\n" \
  "s_add_u32 m0, s49, 58240\n" \
  "s_nop 0\n" \
  "buffer_load_dwordx4 v4, s[40:43], s50 offen offset:128 lds\n" \
  "s_add_u32 s44, s44, s57\n" \
  "s_add_u32 s45, s45, s46\n" \
  "s_add_u32 s50, s50, s46\n" \
  "v_mfma_f32_32x32x16_bf16 v[112:127], v[20:23], v[28:31], v[112:127]\n" \
  "s_waitcnt lgkmcnt(0)\n" \
  "v_mfma_f32_32x32x16_bf16 v[64:79], v[32:35], v[40:43], v[64:79]\n" \
  "s_waitcnt vmcnt(16)\n" \
  "s_barrier\n" \
  "ds_read_b64_tr_b16 v[16:17], v9 offset:49152\n" \
  "ds_read_b64_tr_b16 v[18:19], v9 offset:51200\n" \
  "ds_read_b128 v[24:27], v6 offset:32768\n" \
  "v_mfma_f32_32x32x16_bf16 v[80:95], v[32:35], v[44:47], v[80:95]\n" \
  "s_add_u32 m0, s48, 65536\n" \
  "ds_read_b128 v[28:31], v6 offset:36864\n" \
  "ds_read_b64_tr_b16 v[20:21], v12 offset:49152\n" \
  "ds_read_b64_tr_b16 v[22:23], v12 offset:51200\n" \
  "buffer_load_dwordx4 v0, s[36:39], s44 offen sc1 lds\n" \
  "v_mfma_f32_32x32x16_bf16 v[96:111], v[36:39], v[40:43], v[96:111]\n" \
  "buffer_load_dwordx4 v0, s[36:39], s44 offen offset:1024 sc1 lds\n" \
  "v_mfma_f32_32x32x16_bf16 v[112:127], v[36:39], v[44:47], v[112:127]\n" \
  "s_waitcnt lgkmcnt(0)\n" \
  "v_mfma_f32_32x32x16_bf16 v[64:79], v[16:19], v[24:27], v[64:79]\n" \
  "ds_read_b64_tr_b16 v[32:33], v9 offset:53248\n" \
  "ds_read_b64_tr_b16 v[34:35], v9 offset:55296\n" \
  "ds_read_b128 v[40:43], v6 offset:33792\n" \
  "buffer_load_dwordx4 v0, s[36:39], s44 offen offset:2048 sc1 lds\n" \
  "v_mfma_f32_32x32x16_bf16 v[80:95], v[16:19], v[28:31], v[80:95]\n" \
  "ds_read_b128 v[44:47], v6 offset:37888\n" \
  "ds_read_b64_tr_b16 v[36:37], v12 offset:53248\n" \
  "ds_read_b64_tr_b16 v[38:39], v12 offset:55296\n" \
  "v_mfma_f32_32x32x16_bf16 v[96:111], v[20:23], v[24:27], v[96:111]\n" \
  "buffer_load_dwordx4 v0, s[36:39], s44 offen offset:3072 sc1 lds\n" \
  "v_mfma_f32_32x32x16_bf16 v[112:127], v[20:23], v[28:31], v[112:127]\n" \
  "s_waitcnt lgkmcnt(0)\n" \
  "v_mfma_f32_32x32x16_bf16 v[64:79], v[32:35], v[40:43], v[64:79]\n" \
  "s_add_u32 m0, s49, 81920\n" \
  "ds_read_b64_tr_b16 v[16:17], v9 offset:57344\n" \
  "ds_read_b64_tr_b16 v[18:19], v9 offset:59392\n" \
  "ds_read_b128 v[24:27], v6 offset:34816\n" \
  "buffer_load_dwordx4 v4, s[40:43], s45 offen lds\n" \
  "v_mfma_f32_32x32x16_bf16 v[80:95], v[32:35], v[44:47], v[80:95]\n" \
  "ds_read_b128 v[28:31], v6 offset:38912\n" \
  "ds_read_b64_tr_b16 v[20:21], v12 offset:57344\n" \
  "ds_read_b64_tr_b16 v[22:23], v12 offset:59392\n" \
  "v_mfma_f32_32x32x16_bf16 v[96:111], v[36:39], v[40:43], v[96:111]\n" \
  "s_add_u32 m0, s49, 82816\n" \
  "s_nop 0\n" \
  "buffer_load_dwordx4 v4, s[40:43], s45 offen offset:128 lds\n" \
  "v_mfma_f32_32x32x16_bf16 v[112:127], v[36:39], v[44:47], v[112:127]\n" \
  "s_waitcnt lgkmcnt(0)\n" \
  "v_mfma_f32_32x32x16_bf16 v[64:79], v[16:19], v[24:27], v[64:79]\n" \
  "s_add_u32 m0, s49, 90112\n" \
  "ds_read_b64_tr_b16 v[32:33], v9 offset:61440\n" \
  "ds_read_b64_tr_b16 v[34:35], v9 offset:63488\n" \
  "ds_read_b128 v[40:43], v6 offset:35840\n" \
  "buffer_load_dwordx4 v4, s[40:43], s50 offen lds\n" \
  "v_mfma_f32_32x32x16_bf16 v[80:95], v[16:19], v[28:31], v[80:95]\n" \
  "ds_read_b128 v[44:47], v6 offset:39936\n" \
  "ds_read_b64_tr_b16 v[36:37], v12 offset:61440\n" \
  "ds_read_b64_tr_b16 v[38:39], v12 offset:63488\n" \
  "v_mfma_f32_32x32x16_bf16 v[96:111], v[20:23], v[24:27], v[96:111]\n" \
  "s_add_u32 m0, s49, 91008\n" \
  "s_nop 0\n" \
  "buffer_load_dwordx4 v4, s[40:43], s50 offen offset:128 lds\n" \
  "s_add_u32 s44, s44, 4096\n" \
  "s_add_u32 s45, s45, s46\n" \
  "s_add_u32 s50, s50, s46\n" \
  "v_mfma_f32_32x32x16_bf16 v[112:127], v[20:23], v[28:31], v[112:127]\n" \
  "s_waitcnt lgkmcnt(0)\n" \
  "v_mfma_f32_32x32x16_bf16 v[64:79], v[32:35], v[40:43], v[64:79]\n" \
  "s_waitcnt vmcnt(16)\n" \
  "s_barrier\n" \
  "ds_read_b64_tr_b16 v[16:17], v8 offset:16384\n" \
  "ds_read_b64_tr_b16 v[18:19], v8 offset:18432\n" \
  "ds_read_b128 v[24:27], v5 offset:0\n" \
  "v_mfma_f32_32x32x16_bf16 v[80:95], v[32:35], v[44:47], v[80:95]\n" \
  "s_add_u32 m0, s48, 98304\n" \
  "ds_read_b128 v[28:31], v5 offset:4096\n" \
  "ds_read_b64_tr_b16 v[20:21], v11 offset:16384\n" \
  "ds_read_b64_tr_b16 v[22:23], v11 offset:18432\n" \
  "buffer_load_dwordx4 v0, s[36:39], s44 offen sc1 lds\n" \
  "v_mfma_f32_32x32x16_bf16 v[96:111], v[36:39], v[40:43], v[96:111]\n" \
  "buffer_load_dwordx4 v0, s[36:39], s44 offen offset:1024 sc1 lds\n" \
  "v_mfma_f32_32x32x16_bf16 v[112:127], v[36:39], v[44:47], v[112:127]\n" \
  "s_sub_u32 s47, s47, 1\n" \
  "s_cmp_lg_u32 s47, 0\n" \
  "s_cbranch_scc1 GB_top_%=\n" \
  "s_waitcnt vmcnt(0) lgkmcnt(0)\n" \
  "s_nop 15\n"
#define GB_CLOB "v1", "v2", "v3", "v6", "v7", "v9", "v10", "v12", "v13", "v14", "v15", "v16", "v17", "v18", "v19", "v20", "v21", "v22", "v23", "v24", "v25", "v26", "v27", "v28", "v29", "v30", "v31", "v32", "v33", "v34", "v35", "v36", "v37", "v38", "v39", "v40", "v41", "v42", "v43", "v44", "v45", "v46", "v47", "s50", "scc", "memory"
#define GB_OUT(c) "={v[64:79]}"(c[0]), "={v[80:95]}"(c[1]), "={v[96:111]}"(c[2]), "={v[112:127]}"(c[3])
#define GC_ASM \
  "v_add_u32 v6, 0x10000, v5\n" \
  "v_add_u32 v9, 0x10000, v8\n" \
  "v_add_u32 v12, 0x10000, v11\n" \
  "s_add_u32 m0, s48, 0\n" \
  "s_nop 0\n" \
  "buffer_load_dwordx4 v0, s[36:39], s44 offen sc1 lds\n" \
  "buffer_load_dwordx4 v0, s[36:39], s44 offen offset:1024 sc1 lds\n" \
  "s_add_u32 m0, s48, 2048\n" \
  "s_nop 0\n" \
  "buffer_load_dwordx4 v1, s[36:39], s44 offen sc1 lds\n" \
  "buffer_load_dwordx4 v1, s[36:39], s44 offen offset:1024 sc1 lds\n" \
  "s_add_u32 m0, s49, 16384\n" \
  "s_nop 0\n" \
  "buffer_load_dwordx4 v4, s[40:43], s45 offen lds\n" \
  "s_add_u32 m0, s49, 17280\n" \
  "s_nop 0\n" \
  "buffer_load_dwordx4 v4, s[40:43], s45 offen offset:128 lds\n" \
  "s_add_u32 s44, s44, 2048\n" \
  "s_add_u32 s45, s45, s46\n" \
  "s_add_u32 m0, s48, 24576\n" \
  "s_nop 0\n" \
  "buffer_load_dwordx4 v0, s[36:39], s44 offen sc1 lds\n" \
  "buffer_load_dwordx4 v0, s[36:39], s44 offen offset:1024 sc1 lds\n" \
  "s_add_u32 m0, s48, 26624\n" \
  "s_nop 0\n" \
  "buffer_load_dwordx4 v1, s[36:39], s44 offen sc1 lds\n" \
  "buffer_load_dwordx4 v1, s[36:39], s44 offen offset:1024 sc1 lds\n" \
  "s_add_u32 m0, s49, 40960\n" \
  "s_nop 0\n" \
  "buffer_load_dwordx4 v4, s[40:43], s45 offen lds\n" \
  "s_add_u32 m0, s49, 41856\n" \
  "s_nop 0\n" \
  "buffer_load_dwordx4 v4, s[40:43], s45 offen offset:128 lds\n" \
  "s_add_u32 s44, s44, 2048\n" \
  "s_add_u32 s45, s45, s46\n" \
  "s_add_u32 m0, s48, 49152\n" \
  "s_nop 0\n" \
  "buffer_load_dwordx4 v0, s[36:39], s44 offen sc1 lds\n" \
  "buffer_load_dwordx4 v0, s[36:39], s44 offen offset:1024 sc1 lds\n" \
  "s_add_u32 m0, s48, 51200\n" \
  "s_nop 0\n" \
  "buffer_load_dwordx4 v1, s[36:39], s44 offen sc1 lds\n" \
  "buffer_load_dwordx4 v1, s[36:39], s44 offen offset:1024 sc1 lds\n" \
  "s_add_u32 m0, s49, 65536\n" \
  "s_nop 0\n" \
  "buffer_load_dwordx4 v4, s[40:43], s45 offen lds\n" \
  "s_add_u32 m0, s49, 66432\n" \
  "s_nop 0\n" \
  "buffer_load_dwordx4 v4, s[40:43], s45 offen offset:128 lds\n" \
  "s_add_u32 s44, s44, 2048\n" \
  "s_add_u32 s45, s45, s46\n" \
  "s_add_u32 m0, s48, 73728\n" \
  "s_nop 0\n" \
  "buffer_load_dwordx4 v0, s[36:39], s44 offen sc1 lds\n" \
  "buffer_load_dwordx4 v0, s[36:39], s44 offen offset:1024 sc1 lds\n" \
  "s_add_u32 m0, s48, 75776\n" \
  "s_nop 0\n" \
  "buffer_load_dwordx4 v1, s[36:39], s44 offen sc1 lds\n" \
  "v_accvgpr_write_b32 a0, 0\n" \
  "v_accvgpr_write_b32 a1, 0\n" \
  "v_accvgpr_write_b32 a2, 0\n" \
  "v_accvgpr_write_b32 a3, 0\n" \
  "v_accvgpr_write_b32 a4, 0\n" \
  "v_accvgpr_write_b32 a5, 0\n" \
  "v_accvgpr_write_b32 a6, 0\n" \
  "v_accvgpr_write_b32 a7, 0\n" \
  "v_accvgpr_write_b32 a8, 0\n" \
  "v_accvgpr_write_b32 a9, 0\n" \
  "v_accvgpr_write_b32 a10, 0\n" \
  "v_accvgpr_write_b32 a11, 0\n" \
  "v_accvgpr_write_b32 a12, 0\n" \
  "v_accvgpr_write_b32 a13, 0\n" \
  "v_accvgpr_write_b32 a14, 0\n" \
  "v_accvgpr_write_b32 a15, 0\n" \
  "v_accvgpr_write_b32 a16, 0\n" \
  "v_accvgpr_write_b32 a17, 0\n" \
  "v_accvgpr_write_b32 a18, 0\n" \
  "v_accvgpr_write_b32 a19, 0\n" \
  "v_accvgpr_write_b32 a20, 0\n" \
  "v_accvgpr_write_b32 a21, 0\n" \
  "v_accvgpr_write_b32 a22, 0\n" \
  "v_accvgpr_write_b32 a23, 0\n" \
  "v_accvgpr_write_b32 a24, 0\n" \
  "v_accvgpr_write_b32 a25, 0\n" \
  "v_accvgpr_write_b32 a26, 0\n" \
  "v_accvgpr_write_b32 a27, 0\n" \
  "v_accvgpr_write_b32 a28, 0\n" \
  "v_accvgpr_write_b32 a29, 0\n" \
  "v_accvgpr_write_b32 a30, 0\n" \
  "v_accvgpr_write_b32 a31, 0\n" \
  "v_accvgpr_write_b32 a32, 0\n" \
  "v_accvgpr_write_b32 a33, 0\n" \
  "v_accvgpr_write_b32 a34, 0\n" \
  "v_accvgpr_write_b32 a35, 0\n" \
  "v_accvgpr_write_b32 a36, 0\n" \
  "v_accvgpr_write_b32 a37, 0\n" \
  "v_accvgpr_write_b32 a38, 0\n" \
  "v_accvgpr_write_b32 a39, 0\n" \
  "v_accvgpr_write_b32 a40, 0\n" \
  "v_accvgpr_write_b32 a41, 0\n" \
  "v_accvgpr_write_b32 a42, 0\n" \
  "v_accvgpr_write_b32 a43, 0\n" \
  "v_accvgpr_write_b32 a44, 0\n" \
  "v_accvgpr_write_b32 a45, 0\n" \
  "v_accvgpr_write_b32 a46, 0\n" \
  "v_accvgpr_write_b32 a47, 0\n" \
  "v_accvgpr_write_b32 a48, 0\n" \
  "v_accvgpr_write_b32 a49, 0\n" \
  "v_accvgpr_write_b32 a50, 0\n" \
  "v_accvgpr_write_b32 a51, 0\n" \
  "v_accvgpr_write_b32 a52, 0\n" \
  "v_accvgpr_write_b32 a53, 0\n" \
  "v_accvgpr_write_b32 a54, 0\n" \
  "v_accvgpr_write_b32 a55, 0\n" \
  "v_accvgpr_write_b32 a56, 0\n" \
  "v_accvgpr_write_b32 a57, 0\n" \
  "v_accvgpr_write_b32 a58, 0\n" \
  "v_accvgpr_write_b32 a59, 0\n" \
  "v_accvgpr_write_b32 a60, 0\n" \
  "v_accvgpr_write_b32 a61, 0\n" \
  "v_accvgpr_write_b32 a62, 0\n" \
  "v_accvgpr_write_b32 a63, 0\n" \
  "v_accvgpr_write_b32 a64, 0\n" \
  "v_accvgpr_write_b32 a65, 0\n" \
  "v_accvgpr_write_b32 a66, 0\n" \
  "v_accvgpr_write_b32 a67, 0\n" \
  "v_accvgpr_write_b32 a68, 0\n" \
  "v_accvgpr_write_b32 a69, 0\n" \
  "v_accvgpr_write_b32 a70, 0\n" \
  "v_accvgpr_write_b32 a71, 0\n" \
  "v_accvgpr_write_b32 a72, 0\n" \
  "v_accvgpr_write_b32 a73, 0\n" \
  "v_accvgpr_write_b32 a74, 0\n" \
  "v_accvgpr_write_b32 a75, 0\n" \
  "v_accvgpr_write_b32 a76, 0\n" \
  "v_accvgpr_write_b32 a77, 0\n" \
  "v_accvgpr_write_b32 a78, 0\n" \
  "v_accvgpr_write_b32 a79, 0\n" \
  "v_accvgpr_write_b32 a80, 0\n" \
  "v_accvgpr_write_b32 a81, 0\n" \
  "v_accvgpr_write_b32 a82, 0\n" \
  "v_accvgpr_write_b32 a83, 0\n" \
  "v_accvgpr_write_b32 a84, 0\n" \
  "v_accvgpr_write_b32 a85, 0\n" \
  "v_accvgpr_write_b32 a86, 0\n" \
  "v_accvgpr_write_b32 a87, 0\n" \
  "v_accvgpr_write_b32 a88, 0\n" \
  "v_accvgpr_write_b32 a89, 0\n" \
  "v_accvgpr_write_b32 a90, 0\n" \
  "v_accvgpr_write_b32 a91, 0\n" \
  "v_accvgpr_write_b32 a92, 0\n" \
  "v_accvgpr_write_b32 a93, 0\n" \
  "v_accvgpr_write_b32 a94, 0\n" \
  "v_accvgpr_write_b32 a95, 0\n" \
  "v_accvgpr_write_b32 a96, 0\n" \
  "v_accvgpr_write_b32 a97, 0\n" \
  "v_accvgpr_write_b32 a98, 0\n" \
  "v_accvgpr_write_b32 a99, 0\n" \
  "v_accvgpr_write_b32 a100, 0\n" \
  "v_accvgpr_write_b32 a101, 0\n" \
  "v_accvgpr_write_b32 a102, 0\n" \
  "v_accvgpr_write_b32 a103, 0\n" \
  "v_accvgpr_write_b32 a104, 0\n" \
  "v_accvgpr_write_b32 a105, 0\n" \
  "v_accvgpr_write_b32 a106, 0\n" \
  "v_accvgpr_write_b32 a107, 0\n" \
  "v_accvgpr_write_b32 a108, 0\n" \
  "v_accvgpr_write_b32 a109, 0\n" \
  "v_accvgpr_write_b32 a110, 0\n" \
  "v_accvgpr_write_b32 a111, 0\n" \
  "v_accvgpr_write_b32 a112, 0\n" \
  "v_accvgpr_write_b32 a113, 0\n" \
  "v_accvgpr_write_b32 a114, 0\n" \
  "v_accvgpr_write_b32 a115, 0\n" \
  "v_accvgpr_write_b32 a116, 0\n" \
  "v_accvgpr_write_b32 a117, 0\n" \
  "v_accvgpr_write_b32 a118, 0\n" \
  "v_accvgpr_write_b32 a119, 0\n" \
  "v_accvgpr_write_b32 a120, 0\n" \
  "v_accvgpr_write_b32 a121, 0\n" \
  "v_accvgpr_write_b32 a122, 0\n" \
  "v_accvgpr_write_b32 a123, 0\n" \
  "v_accvgpr_write_b32 a124, 0\n" \
  "v_accvgpr_write_b32 a125, 0\n" \
  "v_accvgpr_write_b32 a126, 0\n" \
  "v_accvgpr_write_b32 a127, 0\n" \
  "s_waitcnt vmcnt(15)\n" \
  "s_barrier\n" \
  "ds_read_b64_tr_b16 v[16:17], v8 offset:16384\n" \
  "ds_read_b64_tr_b16 v[18:19], v8 offset:18432\n" \
  "ds_read_b128 v[32:35], v5 offset:0\n" \
  "ds_read_b128 v[36:39], v5 offset:2048\n" \
  "ds_read_b64_tr_b16 v[20:21], v11 offset:16384\n" \
  "ds_read_b64_tr_b16 v[22:23], v11 offset:18432\n" \
  "ds_read_b64_tr_b16 v[24:25], v8 offset:17408\n" \
  "ds_read_b64_tr_b16 v[26:27], v8 offset:19456\n" \
  "ds_read_b64_tr_b16 v[28:29], v11 offset:17408\n" \
  "ds_read_b64_tr_b16 v[30:31], v11 offset:19456\n" \
  ".p2align 4\n" \
  "GC_top_%=:\n" \
  "s_waitcnt lgkmcnt(0)\n" \
  "v_mfma_f32_32x32x16_bf16 a[0:15], v[16:19], v[32:35], a[0:15]\n" \
  "ds_read_b64_tr_b16 v[40:41], v8 offset:20480\n" \
  "ds_read_b64_tr_b16 v[42:43], v8 offset:22528\n" \
  "buffer_load_dwordx4 v1, s[36:39], s44 offen offset:1024 sc1 lds\n" \
  "v_mfma_f32_32x32x16_bf16 a[16:31], v[16:19], v[36:39], a[16:31]\n" \
  "ds_read_b128 v[56:59], v5 offset:1024\n" \
  "ds_read_b128 v[60:63], v5 offset:3072\n" \
  "v_mfma_f32_32x32x16_bf16 a[32:47], v[20:23], v[32:35], a[32:47]\n" \
  "ds_read_b64_tr_b16 v[44:45], v11 offset:20480\n" \
  "ds_read_b64_tr_b16 v[46:47], v11 offset:22528\n" \
  "v_mfma_f32_32x32x16_bf16 a[48:63], v[20:23], v[36:39], a[48:63]\n" \
  "s_add_u32 m0, s49, 90112\n" \
  "ds_read_b64_tr_b16 v[48:49], v8 offset:21504\n" \
  "ds_read_b64_tr_b16 v[50:51], v8 offset:23552\n" \
  "buffer_load_dwordx4 v4, s[40:43], s45 offen lds\n" \
  "v_mfma_f32_32x32x16_bf16 a[64:79], v[24:27], v[32:35], a[64:79]\n" \
  "ds_read_b64_tr_b16 v[52:53], v11 offset:21504\n" \
  "ds_read_b64_tr_b16 v[54:55], v11 offset:23552\n" \
  "v_mfma_f32_32x32x16_bf16 a[80:95], v[24:27], v[36:39], a[80:95]\n" \
  "s_add_u32 m0, s49, 91008\n" \
  "s_nop 0\n" \
  "buffer_load_dwordx4 v4, s[40:43], s45 offen offset:128 lds\n" \
  "s_add_u32 s44, s44, s57\n" \
  "s_add_u32 s45, s45, s46\n" \
  "v_mfma_f32_32x32x16_bf16 a[96:111], v[28:31], v[32:35], a[96:111]\n" \
  "v_mfma_f32_32x32x16_bf16 a[112:127], v[28:31], v[36:39], a[112:127]\n" \
  "s_waitcnt lgkmcnt(0)\n" \
  "v_mfma_f32_32x32x16_bf16 a[0:15], v[40:43], v[56:59], a[0:15]\n" \
  "s_cmp_eq_u32 s47, 1\n" \
  "s_waitcnt vmcnt(12)\n" \
  "s_barrier\n" \
  "s_cselect_b32 s38, 0, s38\n" \
  "s_cselect_b32 s42, 0, s42\n" \
  "ds_read_b64_tr_b16 v[16:17], v8 offset:40960\n" \
  "ds_read_b64_tr_b16 v[18:19], v8 offset:43008\n" \
  "v_mfma_f32_32x32x16_bf16 a[16:31], v[40:43], v[60:63], a[16:31]\n" \
  "s_add_u32 m0, s48, 0\n" \
  "ds_read_b128 v[32:35], v5 offset:24576\n" \
  "ds_read_b128 v[36:39], v5 offset:26624\n" \
  "buffer_load_dwordx4 v0, s[36:39], s44 offen sc1 lds\n" \
  "v_mfma_f32_32x32x16_bf16 a[32:47], v[44:47], v[56:59], a[32:47]\n" \
  "ds_read_b64_tr_b16 v[20:21], v11 offset:40960\n" \
  "ds_read_b64_tr_b16 v[22:23], v11 offset:43008\n" \
  "v_mfma_f32_32x32x16_bf16 a[48:63], v[44:47], v[60:63], a[48:63]\n" \
  "ds_read_b64_tr_b16 v[24:25], v8 offset:41984\n" \
  "ds_read_b64_tr_b16 v[26:27], v8 offset:44032\n" \
  "buffer_load_dwordx4 v0, s[36:39], s44 offen offset:1024 sc1 lds\n" \
  "v_mfma_f32_32x32x16_bf16 a[64:79], v[48:51], v[56:59], a[64:79]\n" \
  "ds_read_b64_tr_b16 v[28:29], v11 offset:41984\n" \
  "ds_read_b64_tr_b16 v[30:31], v11 offset:44032\n" \
  "v_mfma_f32_32x32x16_bf16 a[80:95], v[48:51], v[60:63], a[80:95]\n" \
  "v_mfma_f32_32x32x16_bf16 a[96:111], v[52:55], v[56:59], a[96:111]\n" \
  "s_add_u32 m0, s48, 2048\n" \
  "s_nop 0\n" \
  "buffer_load_dwordx4 v1, s[36:39], s44 offen sc1 lds\n" \
  "v_mfma_f32_32x32x16_bf16 a[112:127], v[52:55], v[60:63], a[112:127]\n" \
  "s_waitcnt lgkmcnt(0)\n" \
  "v_mfma_f32_32x32x16_bf16 a[0:15], v[16:19], v[32:35], a[0:15]\n" \
  "ds_read_b64_tr_b16 v[40:41], v8 offset:45056\n" \
  "ds_read_b64_tr_b16 v[42:43], v8 offset:47104\n" \
  "buffer_load_dwordx4 v1, s[36:39], s44 offen offset:1024 sc1 lds\n" \
  "v_mfma_f32_32x32x16_bf16 a[16:31], v[16:19], v[36:39], a[16:31]\n" \
  "ds_read_b128 v[56:59], v5 offset:25600\n" \
  "ds_read_b128 v[60:63], v5 offset:27648\n" \
  "v_mfma_f32_32x32x16_bf16 a[32:47], v[20:23], v[32:35], a[32:47]\n" \
  "ds_read_b64_tr_b16 v[44:45], v11 offset:45056\n" \
  "ds_read_b64_tr_b16 v[46:47], v11 offset:47104\n" \
  "v_mfma_f32_32x32x16_bf16 a[48:63], v[20:23], v[36:39], a[48:63]\n" \
  "s_add_u32 m0, s49, 16384\n" \
  "ds_read_b64_tr_b16 v[48:49], v8 offset:46080\n" \
  "ds_read_b64_tr_b16 v[50:51], v8 offset:48128\n" \
  "buffer_load_dwordx4 v4, s[40:43], s45 offen lds\n" \
  "v_mfma_f32_32x32x16_bf16 a[64:79], v[24:27], v[32:35], a[64:79]\n" \
  "ds_read_b64_tr_b16 v[52:53], v11 offset:46080\n" \
  "ds_read_b64_tr_b16 v[54:55], v11 offset:48128\n" \
  "v_mfma_f32_32x32x16_bf16 a[80:95], v[24:27], v[36:39], a[80:95]\n" \
  "s_add_u32 m0, s49, 17280\n" \
  "s_nop 0\n" \
  "buffer_load_dwordx4 v4, s[40:43], s45 offen offset:128 lds\n" \
  "s_add_u32 s44, s44, 2048\n" \
  "s_add_u32 s45, s45, s46\n" \
  "v_mfma_f32_32x32x16_bf16 a[96:111], v[28:31], v[32:35], a[96:111]\n" \
  "v_mfma_f32_32x32x16_bf16 a[112:127], v[28:31], v[36:39], a[112:127]\n" \
  "s_waitcnt lgkmcnt(0)\n" \
  "v_mfma_f32_32x32x16_bf16 a[0:15], v[40:43], v[56:59], a[0:15]\n" \
  "s_waitcnt vmcnt(12)\n" \
  "s_barrier\n" \
  "ds_read_b64_tr_b16 v[16:17], v9 offset:0\n" \
  "ds_read_b64_tr_b16 v[18:19], v9 offset:2048\n" \
  "v_mfma_f32_32x32x16_bf16 a[16:31], v[40:43], v[60:63], a[16:31]\n" \
  "s_add_u32 m0, s48, 24576\n" \
  "ds_read_b128 v[32:35], v5 offset:49152\n" \
  "ds_read_b128 v[36:39], v5 offset:51200\n" \
  "buffer_load_dwordx4 v0, s[36:39], s44 offen sc1 lds\n" \
  "v_mfma_f32_32x32x16_bf16 a[32:47], v[44:47], v[56:59], a[32:47]\n" \
  "ds_read_b64_tr_b16 v[20:21], v12 offset:0\n" \
  "ds_read_b64_tr_b16 v[22:23], v12 offset:2048\n" \
  "v_mfma_f32_32x32x16_bf16 a[48:63], v[44:47], v[60:63], a[48:63]\n" \
  "ds_read_b64_tr_b16 v[24:25], v9 offset:1024\n" \
  "ds_read_b64_tr_b16 v[26:27], v9 offset:3072\n" \
  "buffer_load_dwordx4 v0, s[36:39], s44 offen offset:1024 sc1 lds\n" \
  "v_mfma_f32_32x32x16_bf16 a[64:79], v[48:51], v[56:59], a[64:79]\n" \
  "ds_read_b64_tr_b16 v[28:29], v12 offset:1024\n" \
  "ds_read_b64_tr_b16 v[30:31], v12 offset:3072\n" \
  "v_mfma_f32_32x32x16_bf16 a[80:95], v[48:51], v[60:63], a[80:95]\n" \
  "v_mfma_f32_32x32x16_bf16 a[96:111], v[52:55], v[56:59], a[96:111]\n" \
  "s_add_u32 m0, s48, 26624\n" \
  "s_nop 0\n" \
  "buffer_load_dwordx4 v1, s[36:39], s44 offen sc1 lds\n" \
  "v_mfma_f32_32x32x16_bf16 a[112:127], v[52:55], v[60:63], a[112:127]\n" \
  "s_waitcnt lgkmcnt(0)\n" \
  "v_mfma_f32_32x32x16_bf16 a[0:15], v[16:19], v[32:35], a[0:15]\n" \
  "ds_read_b64_tr_b16 v[40:41], v9 offset:4096\n" \
  "ds_read_b64_tr_b16 v[42:43], v9 offset:6144\n" \
  "buffer_load_dwordx4 v1, s[36:39], s44 offen offset:1024 sc1 lds\n" \
  "v_mfma_f32_32x32x16_bf16 a[16:31], v[16:19], v[36:39], a[16:31]\n" \
  "ds_read_b128 v[56:59], v5 offset:50176\n" \
  "ds_read_b128 v[60:63], v5 offset:52224\n" \
  "v_mfma_f32_32x32x16_bf16 a[32:47], v[20:23], v[32:35], a[32:47]\n" \
  "ds_read_b64_tr_b16 v[44:45], v12 offset:4096\n" \
  "ds_read_b64_tr_b16 v[46:47], v12 offset:6144\n" \
  "v_mfma_f32_32x32x16_bf16 a[48:63], v[20:23], v[36:39], a[48:63]\n" \
  "s_add_u32 m0, s49, 40960\n" \
  "ds_read_b64_tr_b16 v[48:49], v9 offset:5120\n" \
  "ds_read_b64_tr_b16 v[50:51], v9 offset:7168\n" \
  "buffer_load_dwordx4 v4, s[40:43], s45 offen lds\n" \
  "v_mfma_f32_32x32x16_bf16 a[64:79], v[24:27], v[32:35], a[64:79]\n" \
  "ds_read_b64_tr_b16 v[52:53], v12 offset:5120\n" \
  "ds_read_b64_tr_b16 v[54:55], v12 offset:7168\n" \
  "v_mfma_f32_32x32x16_bf16 a[80:95], v[24:27], v[36:39], a[80:95]\n" \
  "s_add_u32 m0, s49, 41856\n" \
  "s_nop 0\n" \
  "buffer_load_dwordx4 v4, s[40:43], s45 offen offset:128 lds\n" \
  "s_add_u32 s44, s44, 2048\n" \
  "s_add_u32 s45, s45, s46\n" \
  "v_mfma_f32_32x32x16_bf16 a[96:111], v[28:31], v[32:35], a[96:111]\n" \
  "v_mfma_f32_32x32x16_bf16 a[112:127], v[28:31], v[36:39], a[112:127]\n" \
  "s_waitcnt lgkmcnt(0)\n" \
  "v_mfma_f32_32x32x16_bf16 a[0:15], v[40:43], v[56:59], a[0:15]\n" \
  "s_waitcnt vmcnt(12)\n" \
  "s_barrier\n" \
  "ds_read_b64_tr_b16 v[16:17], v9 offset:24576\n" \
  "ds_read_b64_tr_b16 v[18:19], v9 offset:26624\n" \
  "v_mfma_f32_32x32x16_bf16 a[16:31], v[40:43], v[60:63], a[16:31]\n" \
  "s_add_u32 m0, s48, 49152\n" \
  "ds_read_b128 v[32:35], v6 offset:8192\n" \
  "ds_read_b128 v[36:39], v6 offset:10240\n" \
  "buffer_load_dwordx4 v0, s[36:39], s44 offen sc1 lds\n" \
  "v_mfma_f32_32x32x16_bf16 a[32:47], v[44:47], v[56:59], a[32:47]\n" \
  "ds_read_b64_tr_b16 v[20:21], v12 offset:24576\n" \
  "ds_read_b64_tr_b16 v[22:23], v12 offset:26624\n" \
  "v_mfma_f32_32x32x16_bf16 a[48:63], v[44:47], v[60:63], a[48:63]\n" \
  "ds_read_b64_tr_b16 v[24:25], v9 offset:25600\n" \
  "ds_read_b64_tr_b16 v[26:27], v9 offset:27648\n" \
  "buffer_load_dwordx4 v0, s[36:39], s44 offen offset:1024 sc1 lds\n" \
  "v_mfma_f32_32x32x16_bf16 a[64:79], v[48:51], v[56:59], a[64:79]\n" \
  "ds_read_b64_tr_b16 v[28:29], v12 offset:25600\n" \
  "ds_read_b64_tr_b16 v[30:31], v12 offset:27648\n" \
  "v_mfma_f32_32x32x16_bf16 a[80:95], v[48:51], v[60:63], a[80:95]\n" \
  "v_mfma_f32_32x32x16_bf16 a[96:111], v[52:55], v[56:59], a[96:111]\n" \
  "s_add_u32 m0, s48, 51200\n" \
  "s_nop 0\n" \
  "buffer_load_dwordx4 v1, s[36:39], s44 offen sc1 lds\n" \
  "v_mfma_f32_32x32x16_bf16 a[112:127], v[52:55], v[60:63], a[112:127]\n" \
  "s_waitcnt lgkmcnt(0)\n" \
  "v_mfma_f32_32x32x16_bf16 a[0:15], v[16:19], v[32:35], a[0:15]\n" \
  "ds_read_b64_tr_b16 v[40:41], v9 offset:28672\n" \
  "ds_read_b64_tr_b16 v[42:43], v9 offset:30720\n" \
  "buffer_load_dwordx4 v1, s[36:39], s44 offen offset:1024 sc1 lds\n" \
  "v_mfma_f32_32x32x16_bf16 a[16:31], v[16:19], v[36:39], a[16:31]\n" \
  "ds_read_b128 v[56:59], v6 offset:9216\n" \
  "ds_read_b128 v[60:63], v6 offset:11264\n" \
  "v_mfma_f32_32x32x16_bf16 a[32:47], v[20:23], v[32:35], a[32:47]\n" \
  "ds_read_b64_tr_b16 v[44:45], v12 offset:28672\n" \
  "ds_read_b64_tr_b16 v[46:47], v12 offset:30720\n" \
  "v_mfma_f32_32x32x16_bf16 a[48:63], v[20:23], v[36:39], a[48:63]\n" \
  "s_add_u32 m0, s49, 65536\n" \
  "ds_read_b64_tr_b16 v[48:49], v9 offset:29696\n" \
  "ds_read_b64_tr_b16 v[50:51], v9 offset:31744\n" \
  "buffer_load_dwordx4 v4, s[40:43], s45 offen lds\n" \
  "v_mfma_f32_32x32x16_bf16 a[64:79], v[24:27], v[32:35], a[64:79]\n" \
  "ds_read_b64_tr_b16 v[52:53], v12 offset:29696\n" \
  "ds_read_b64_tr_b16 v[54:55], v12 offset:31744\n" \
  "v_mfma_f32_32x32x16_bf16 a[80:95], v[24:27], v[36:39], a[80:95]\n" \
  "s_add_u32 m0, s49, 66432\n" \
  "s_nop 0\n" \
  "buffer_load_dwordx4 v4, s[40:43], s45 offen offset:128 lds\n" \
  "s_add_u32 s44, s44, 2048\n" \
  "s_add_u32 s45, s45, s46\n" \
  "v_mfma_f32_32x32x16_bf16 a[96:111], v[28:31], v[32:35], a[96:111]\n" \
  "v_mfma_f32_32x32x16_bf16 a[112:127], v[28:31], v[36:39], a[112:127]\n" \
  "s_waitcnt lgkmcnt(0)\n" \
  "v_mfma_f32_32x32x16_bf16 a[0:15], v[40:43], v[56:59], a[0:15]\n" \
  "s_waitcnt vmcnt(12)\n" \
  "s_barrier\n" \
  "ds_read_b64_tr_b16 v[16:17], v8 offset:16384\n" \
  "ds_read_b64_tr_b16 v[18:19], v8 offset:18432\n" \
  "v_mfma_f32_32x32x16_bf16 a[16:31], v[40:43], v[60:63], a[16:31]\n" \
  "s_add_u32 m0, s48, 73728\n" \
  "ds_read_b128 v[32:35], v5 offset:0\n" \
  "ds_read_b128 v[36:39], v5 offset:2048\n" \
  "buffer_load_dwordx4 v0, s[36:39], s44 offen sc1 lds\n" \
  "v_mfma_f32_32x32x16_bf16 a[32:47], v[44:47], v[56:59], a[32:47]\n" \
  "ds_read_b64_tr_b16 v[20:21], v11 offset:16384\n" \
  "ds_read_b64_tr_b16 v[22:23], v11 offset:18432\n" \
  "v_mfma_f32_32x32x16_bf16 a[48:63], v[44:47], v[60:63], a[48:63]\n" \
  "ds_read_b64_tr_b16 v[24:25], v8 offset:17408\n" \
  "ds_read_b64_tr_b16 v[26:27], v8 offset:19456\n" \
  "buffer_load_dwordx4 v0, s[36:39], s44 offen offset:1024 sc1 lds\n" \
  "v_mfma_f32_32x32x16_bf16 a[64:79], v[48:51], v[56:59], a[64:79]\n" \
  "ds_read_b64_tr_b16 v[28:29], v11 offset:17408\n" \
  "ds_read_b64_tr_b16 v[30:31], v11 offset:19456\n" \
  "v_mfma_f32_32x32x16_bf16 a[80:95], v[48:51], v[60:63], a[80:95]\n" \
  "v_mfma_f32_32x32x16_bf16 a[96:111], v[52:55], v[56:59], a[96:111]\n" \
  "s_add_u32 m0, s48, 75776\n" \
  "s_nop 0\n" \
  "buffer_load_dwordx4 v1, s[36:39], s44 offen sc1 lds\n" \
  "v_mfma_f32_32x32x16_bf16 a[112:127], v[52:55], v[60:63], a[112:127]\n" \
  "s_sub_u32 s47, s47, 1\n" \
  "s_cmp_lg_u32 s47, 0\n" \
  "s_cbranch_scc1 GC_top_%=\n" \
  "s_waitcnt vmcnt(0) lgkmcnt(0)\n" \
  "s_nop 15\n"
#define GC_CLOB "v2", "v3", "v6", "v7", "v9", "v10", "v12", "v13", "v14", "v15", "v16", "v17", "v18", "v19", "v20", "v21", "v22", "v23", "v24", "v25", "v26", "v27", "v28", "v29", "v30", "v31", "v32", "v33", "v34", "v35", "v36", "v37", "v38", "v39", "v40", "v41", "v42", "v43", "v44", "v45", "v46", "v47", "v48", "v49", "v50", "v51", "v52", "v53", "v54", "v55", "v56", "v57", "v58", "v59", "v60", "v61", "v62", "v63", "s50", "scc", "memory"
#define GC_OUT(c) "={a[0:15]}"(c[0]), "={a[16:31]}"(c[1]), "={a[32:47]}"(c[2]), "={a[48:63]}"(c[3]), "={a[64:79]}"(c[4]), "={a[80:95]}"(c[5]), "={a[96:111]}"(c[6]), "={a[112:127]}"(c[7])
#define GD_ASM \
  "v_add_u32 v6, 0x10000, v5\n" \
  "v_add_u32 v9, 0x10000, v8\n" \
  "v_add_u32 v12, 0x10000, v11\n" \
  "s_add_u32 s50, s45, s51\n" \
  "s_add_u32 m0, s48, 0\n" \
  "s_nop 0\n" \
  "buffer_load_dwordx4 v0, s[36:39], s44 offen sc1 lds\n" \
  "buffer_load_dwordx4 v0, s[36:39], s44 offen offset:1024 sc1 lds\n" \
  "buffer_load_dwordx4 v0, s[36:39], s44 offen offset:2048 sc1 lds\n" \
  "buffer_load_dwordx4 v0, s[36:39], s44 offen offset:3072 sc1 lds\n" \
  "s_add_u32 m0, s49, 16384\n" \
  "s_nop 0\n" \
  "buffer_load_dwordx4 v4, s[40:43], s45 offen lds\n" \
  "s_add_u32 m0, s49, 20480\n" \
  "s_nop 0\n" \
  "buffer_load_dwordx4 v4, s[40:43], s50 offen lds\n" \
  "s_add_u32 s44, s44, 4096\n" \
  "s_add_u32 s45, s45, s46\n" \
  "s_add_u32 s50, s50, s46\n" \
  "s_add_u32 m0, s48, 24576\n" \
  "s_nop 0\n" \
  "buffer_load_dwordx4 v0, s[36:39], s44 offen sc1 lds\n" \
  "buffer_load_dwordx4 v0, s[36:39], s44 offen offset:1024 sc1 lds\n" \
  "buffer_load_dwordx4 v0, s[36:39], s44 offen offset:2048 sc1 lds\n" \
  "buffer_load_dwordx4 v0, s[36:39], s44 offen offset:3072 sc1 lds\n" \
  "s_add_u32 m0, s49, 40960\n" \
  "s_nop 0\n" \
  "buffer_load_dwordx4 v4, s[40:43], s45 offen lds\n" \
  "s_add_u32 m0, s49, 45056\n" \
  "s_nop 0\n" \
  "buffer_load_dwordx4 v4, s[40:43], s50 offen lds\n" \
  "s_add_u32 s44, s44, s57\n" \
  "s_add_u32 s45, s45, s46\n" \
  "s_add_u32 s50, s50, s46\n" \
  "s_add_u32 m0, s48, 49152\n" \
  "s_nop 0\n" \
  "buffer_load_dwordx4 v0, s[36:39], s44 offen sc1 lds\n" \
  "buffer_load_dwordx4 v0, s[36:39], s44 offen offset:1024 sc1 lds\n" \
  "buffer_load_dwordx4 v0, s[36:39], s44 offen offset:2048 sc1 lds\n" \
  "buffer_load_dwordx4 v0, s[36:39], s44 offen offset:3072 sc1 lds\n" \
  "s_add_u32 m0, s49, 65536\n" \
  "s_nop 0\n" \
  "buffer_load_dwordx4 v4, s[40:43], s45 offen lds\n" \
  "s_add_u32 m0, s49, 69632\n" \
  "s_nop 0\n" \
  "buffer_load_dwordx4 v4, s[40:43], s50 offen lds\n" \
  "s_add_u32 s44, s44, 4096\n" \
  "s_add_u32 s45, s45, s46\n" \
  "s_add_u32 s50, s50, s46\n" \
  "s_add_u32 m0, s48, 73728\n" \
  "s_nop 0\n" \
  "buffer_load_dwordx4 v0, s[36:39], s44 offen sc1 lds\n" \
  "buffer_load_dwordx4 v0, s[36:39], s44 offen offset:1024 sc1 lds\n" \
  "buffer_load_dwordx4 v0, s[36:39], s44 offen offset:2048 sc1 lds\n" \
  "v_mov_b64 v[64:65], 0\n" \
  "v_mov_b64 v[66:67], 0\n" \
  "v_mov_b64 v[68:69], 0\n" \
  "v_mov_b64 v[70:71], 0\n" \
  "v_mov_b64 v[72:73], 0\n" \
  "v_mov_b64 v[74:75], 0\n" \
  "v_mov_b64 v[76:77], 0\n" \
  "v_mov_b64 v[78:79], 0\n" \
  "v_mov_b64 v[80:81], 0\n" \
  "v_mov_b64 v[82:83], 0\n" \
  "v_mov_b64 v[84:85], 0\n" \
  "v_mov_b64 v[86:87], 0\n" \
  "v_mov_b64 v[88:89], 0\n" \
  "v_mov_b64 v[90:91], 0\n" \
  "v_mov_b64 v[92:93], 0\n" \
  "v_mov_b64 v[94:95], 0\n" \
  "s_waitcnt vmcnt(15)\n" \
  "s_barrier\n" \
  "ds_read_b64_tr_b16 v[16:17], v8 offset:16384\n" \
  "ds_read_b64_tr_b16 v[18:19], v8 offset:17408\n" \
  "ds_read_b128 v[20:23], v5 offset:0\n" \
  "ds_read_b128 v[24:27], v5 offset:4096\n" \
  "ds_read_b64_tr_b16 v[28:29], v8 offset:18432\n" \
  "ds_read_b64_tr_b16 v[30:31], v8 offset:19456\n" \
  "ds_read_b128 v[32:35], v5 offset:1024\n" \
  "ds_read_b128 v[36:39], v5 offset:5120\n" \
  ".p2align 4\n" \
  "GD_top_%=:\n" \
  "s_waitcnt lgkmcnt(0)\n" \
  "v_mfma_f32_32x32x16_bf16 v[64:79], v[16:19], v[20:23], v[64:79]\n" \
  "ds_read_b64_tr_b16 v[40:41], v8 offset:20480\n" \
  "ds_read_b64_tr_b16 v[42:43], v8 offset:21504\n" \
  "ds_read_b128 v[44:47], v5 offset:2048\n" \
  "ds_read_b128 v[48:51], v5 offset:6144\n" \
  "buffer_load_dwordx4 v0, s[36:39], s44 offen offset:3072 sc1 lds\n" \
  "v_mfma_f32_32x32x16_bf16 v[80:95], v[16:19], v[24:27], v[80:95]\n" \
  "s_add_u32 m0, s49, 90112\n" \
  "ds_read_b64_tr_b16 v[52:53], v8 offset:22528\n" \
  "ds_read_b64_tr_b16 v[54:55], v8 offset:23552\n" \
  "ds_read_b128 v[56:59], v5 offset:3072\n" \
  "ds_read_b128 v[60:63], v5 offset:7168\n" \
  "buffer_load_dwordx4 v4, s[40:43], s45 offen lds\n" \
  "v_mfma_f32_32x32x16_bf16 v[64:79], v[28:31], v[32:35], v[64:79]\n" \
  "s_add_u32 m0, s49, 94208\n" \
  "s_nop 0\n" \
  "buffer_load_dwordx4 v4, s[40:43], s50 offen lds\n" \
  "s_add_u32 s44, s44, s57\n" \
  "s_add_u32 s45, s45, s46\n" \
  "s_add_u32 s50, s50, s46\n" \
  "v_mfma_f32_32x32x16_bf16 v[80:95], v[28:31], v[36:39], v[80:95]\n" \
  "s_waitcnt lgkmcnt(0)\n" \
  "v_mfma_f32_32x32x16_bf16 v[64:79], v[40:43], v[44:47], v[64:79]\n" \
  "s_cmp_eq_u32 s47, 1\n" \
  "s_waitcnt vmcnt(12)\n" \
  "s_barrier\n" \
  "s_cselect_b32 s38, 0, s38\n" \
  "s_cselect_b32 s42, 0, s42\n" \
  "ds_read_b64_tr_b16 v[16:17], v8 offset:40960\n" \
  "ds_read_b64_tr_b16 v[18:19], v8 offset:41984\n" \
  "ds_read_b128 v[20:23], v5 offset:24576\n" \
  "ds_read_b128 v[24:27], v5 offset:28672\n" \
  "v_mfma_f32_32x32x16_bf16 v[80:95], v[40:43], v[48:51], v[80:95]\n" \
  "s_add_u32 m0, s48, 0\n" \
  "ds_read_b64_tr_b16 v[28:29], v8 offset:43008\n" \
  "ds_read_b64_tr_b16 v[30:31], v8 offset:44032\n" \
  "ds_read_b128 v[32:35], v5 offset:25600\n" \
  "ds_read_b128 v[36:39], v5 offset:29696\n" \
  "buffer_load_dwordx4 v0, s[36:39], s44 offen sc1 lds\n" \
  "v_mfma_f32_32x32x16_bf16 v[64:79], v[52:55], v[56:59], v[64:79]\n" \
  "buffer_load_dwordx4 v0, s[36:39], s44 offen offset:1024 sc1 lds\n" \
  "v_mfma_f32_32x32x16_bf16 v[80:95], v[52:55], v[60:63], v[80:95]\n" \
  "buffer_load_dwordx4 v0, s[36:39], s44 offen offset:2048 sc1 lds\n" \
  "s_waitcnt lgkmcnt(0)\n" \
  "v_mfma_f32_32x32x16_bf16 v[64:79], v[16:19], v[20:23], v[64:79]\n" \
  "ds_read_b64_tr_b16 v[40:41], v8 offset:45056\n" \
  "ds_read_b64_tr_b16 v[42:43], v8 offset:46080\n" \
  "ds_read_b128 v[44:47], v5 offset:26624\n" \
  "ds_read_b128 v[48:51], v5 offset:30720\n" \
  "buffer_load_dwordx4 v0, s[36:39], s44 offen offset:3072 sc1 lds\n" \
  "v_mfma_f32_32x32x16_bf16 v[80:95], v[16:19], v[24:27], v[80:95]\n" \
  "s_add_u32 m0, s49, 16384\n" \
  "ds_read_b64_tr_b16 v[52:53], v8 offset:47104\n" \
  "ds_read_b64_tr_b16 v[54:55], v8 offset:48128\n" \
  "ds_read_b128 v[56:59], v5 offset:27648\n" \
  "ds_read_b128 v[60:63], v5 offset:31744\n" \
  "buffer_load_dwordx4 v4, s[40:43], s45 offen lds\n" \
  "v_mfma_f32_32x32x16_bf16 v[64:79], v[28:31], v[32:35], v[64:79]\n" \
  "s_add_u32 m0, s49, 20480\n" \
  "s_nop 0\n" \
  "buffer_load_dwordx4 v4, s[40:43], s50 offen lds\n" \
  "s_add_u32 s44, s44, 4096\n" \
  "s_add_u32 s45, s45, s46\n" \
  "s_add_u32 s50, s50, s46\n" \
  "v_mfma_f32_32x32x16_bf16 v[80:95], v[28:31], v[36:39], v[80:95]\n" \
  "s_waitcnt lgkmcnt(0)\n" \
  "v_mfma_f32_32x32x16_bf16 v[64:79], v[40:43], v[44:47], v[64:79]\n" \
  "s_waitcnt vmcnt(12)\n" \
  "s_barrier\n" \
  "ds_read_b64_tr_b16 v[16:17], v9 offset:0\n" \
  "ds_read_b64_tr_b16 v[18:19], v9 offset:1024\n" \
  "ds_read_b128 v[20:23], v5 offset:49152\n" \
  "ds_read_b128 v[24:27], v5 offset:53248\n" \
  "v_mfma_f32_32x32x16_bf16 v[80:95], v[40:43], v[48:51], v[80:95]\n" \
  "s_add_u32 m0, s48, 24576\n" \
  "ds_read_b64_tr_b16 v[28:29], v9 offset:2048\n" \
  "ds_read_b64_tr_b16 v[30:31], v9 offset:3072\n" \
  "ds_read_b128 v[32:35], v5 offset:50176\n" \
  "ds_read_b128 v[36:39], v5 offset:54272\n" \
  "buffer_load_dwordx4 v0, s[36:39], s44 offen sc1 lds\n" \
  "v_mfma_f32_32x32x16_bf16 v[64:79], v[52:55], v[56:59], v[64:79]\n" \
  "buffer_load_dwordx4 v0, s[36:39], s44 offen offset:1024 sc1 lds\n" \
  "v_mfma_f32_32x32x16_bf16 v[80:95], v[52:55], v[60:63], v[80:95]\n" \
  "buffer_load_dwordx4 v0, s[36:39], s44 offen offset:2048 sc1 lds\n" \
  "s_waitcnt lgkmcnt(0)\n" \
  "v_mfma_f32_32x32x16_bf16 v[64:79], v[16:19], v[20:23], v[64:79]\n" \
  "ds_read_b64_tr_b16 v[40:41], v9 offset:4096\n" \
  "ds_read_b64_tr_b16 v[42:43], v9 offset:5120\n" \
  "ds_read_b128 v[44:47], v5 offset:51200\n" \
  "ds_read_b128 v[48:51], v5 offset:55296\n" \
  "buffer_load_dwordx4 v0, s[36:39], s44 offen offset:3072 sc1 lds\n" \
  "v_mfma_f32_32x32x16_bf16 v[80:95], v[16:19], v[24:27], v[80:95]\n" \
  "s_add_u32 m0, s49, 40960\n" \
  "ds_read_b64_tr_b16 v[52:53], v9 offset:6144\n" \
  "ds_read_b64_tr_b16 v[54:55], v9 offset:7168\n" \
  "ds_read_b128 v[56:59], v5 offset:52224\n" \
  "ds_read_b128 v[60:63], v5 offset:56320\n" \
  "buffer_load_dwordx4 v4, s[40:43], s45 offen lds\n" \
  "v_mfma_f32_32x32x16_bf16 v[64:79], v[28:31], v[32:35], v[64:79]\n" \
  "s_add_u32 m0, s49, 45056\n" \
  "s_nop 0\n" \
  "buffer_load_dwordx4 v4, s[40:43], s50 offen lds\n" \
  "s_add_u32 s44, s44, s57\n" \
  "s_add_u32 s45, s45, s46\n" \
  "s_add_u32 s50, s50, s46\n" \
  "v_mfma_f32_32x32x16_bf16 v[80:95], v[28:31], v[36:39], v[80:95]\n" \
  "s_waitcnt lgkmcnt(0)\n" \
  "v_mfma_f32_32x32x16_bf16 v[64:79], v[40:43], v[44:47], v[64:79]\n" \
  "s_waitcnt vmcnt(12)\n" \
  "s_barrier\n" \
  "ds_read_b64_tr_b16 v[16:17], v9 offset:24576\n" \
  "ds_read_b64_tr_b16 v[18:19], v9 offset:25600\n" \
  "ds_read_b128 v[20:23], v6 offset:8192\n" \
  "ds_read_b128 v[24:27], v6 offset:12288\n" \
  "v_mfma_f32_32x32x16_bf16 v[80:95], v[40:43], v[48:51], v[80:95]\n" \
  "s_add_u32 m0, s48, 49152\n" \
  "ds_read_b64_tr_b16 v[28:29], v9 offset:26624\n" \
  "ds_read_b64_tr_b16 v[30:31], v9 offset:27648\n" \
  "ds_read_b128 v[32:35], v6 offset:9216\n" \
  "ds_read_b128 v[36:39], v6 offset:13312\n" \
  "buffer_load_dwordx4 v0, s[36:39], s44 offen sc1 lds\n" \
  "v_mfma_f32_32x32x16_bf16 v[64:79], v[52:55], v[56:59], v[64:79]\n" \
  "buffer_load_dwordx4 v0, s[36:39], s44 offen offset:1024 sc1 lds\n" \
  "v_mfma_f32_32x32x16_bf16 v[80:95], v[52:55], v[60:63], v[80:95]\n" \
  "buffer_load_dwordx4 v0, s[36:39], s44 offen offset:2048 sc1 lds\n" \
  "s_waitcnt lgkmcnt(0)\n" \
  "v_mfma_f32_32x32x16_bf16 v[64:79], v[16:19], v[20:23], v[64:79]\n" \
  "ds_read_b64_tr_b16 v[40:41], v9 offset:28672\n" \
  "ds_read_b64_tr_b16 v[42:43], v9 offset:29696\n" \
  "ds_read_b128 v[44:47], v6 offset:10240\n" \
  "ds_read_b128 v[48:51], v6 offset:14336\n" \
  "buffer_load_dwordx4 v0, s[36:39], s44 offen offset:3072 sc1 lds\n" \
  "v_mfma_f32_32x32x16_bf16 v[80:95], v[16:19], v[24:27], v[80:95]\n" \
  "s_add_u32 m0, s49, 65536\n" \
  "ds_read_b64_tr_b16 v[52:53], v9 offset:30720\n" \
  "ds_read_b64_tr_b16 v[54:55], v9 offset:31744\n" \
  "ds_read_b128 v[56:59], v6 offset:11264\n" \
  "ds_read_b128 v[60:63], v6 offset:15360\n" \
  "buffer_load_dwordx4 v4, s[40:43], s45 offen lds\n" \
  "v_mfma_f32_32x32x16_bf16 v[64:79], v[28:31], v[32:35], v[64:79]\n" \
  "s_add_u32 m0, s49, 69632\n" \
  "s_nop 0\n" \
  "buffer_load_dwordx4 v4, s[40:43], s50 offen lds\n" \
  "s_add_u32 s44, s44, 4096\n" \
  "s_add_u32 s45, s45, s46\n" \
  "s_add_u32 s50, s50, s46\n" \
  "v_mfma_f32_32x32x16_bf16 v[80:95], v[28:31], v[36:39], v[80:95]\n" \
  "s_waitcnt lgkmcnt(0)\n" \
  "v_mfma_f32_32x32x16_bf16 v[64:79], v[40:43], v[44:47], v[64:79]\n" \
  "s_waitcnt vmcnt(12)\n" \
  "s_barrier\n" \
  "ds_read_b64_tr_b16 v[16:17], v8 offset:16384\n" \
  "ds_read_b64_tr_b16 v[18:19], v8 offset:17408\n" \
  "ds_read_b128 v[20:23], v5 offset:0\n" \
  "ds_read_b128 v[24:27], v5 offset:4096\n" \
  "v_mfma_f32_32x32x16_bf16 v[80:95], v[40:43], v[48:51], v[80:95]\n" \
  "s_add_u32 m0, s48, 73728\n" \
  "ds_read_b64_tr_b16 v[28:29], v8 offset:18432\n" \
  "ds_read_b64_tr_b16 v[30:31], v8 offset:19456\n" \
  "ds_read_b128 v[32:35], v5 offset:1024\n" \
  "ds_read_b128 v[36:39], v5 offset:5120\n" \
  "buffer_load_dwordx4 v0, s[36:39], s44 offen sc1 lds\n" \
  "v_mfma_f32_32x32x16_bf16 v[64:79], v[52:55], v[56:59], v[64:79]\n" \
  "buffer_load_dwordx4 v0, s[36:39], s44 offen offset:1024 sc1 lds\n" \
  "v_mfma_f32_32x32x16_bf16 v[80:95], v[52:55], v[60:63], v[80:95]\n" \
  "buffer_load_dwordx4 v0, s[36:39], s44 offen offset:2048 sc1 lds\n" \
  "s_sub_u32 s47, s47, 1\n" \
  "s_cmp_lg_u32 s47, 0\n" \
  "s_cbranch_scc1 GD_top_%=\n" \
  "s_waitcnt vmcnt(0) lgkmcnt(0)\n" \
  "s_nop 15\n"
#define GD_CLOB "v1", "v2", "v3", "v6", "v7", "v9", "v10", "v12", "v13", "v14", "v15", "v16", "v17", "v18", "v19", "v20", "v21", "v22", "v23", "v24", "v25", "v26", "v27", "v28", "v29", "v30", "v31", "v32", "v33", "v34", "v35", "v36", "v37", "v38", "v39", "v40", "v41", "v42", "v43", "v44", "v45", "v46", "v47", "v48", "v49", "v50", "v51", "v52", "v53", "v54", "v55", "v56", "v57", "v58", "v59", "v60", "v61", "v62", "v63", "s50", "scc", "memory"
#define GD_OUT(c) "={v[64:79]}"(c[0]), "={v[80:95]}"(c[1])
#define GE_ASM \
  "v_add_u32 v6, 0x10000, v5\n" \
  "v_add_u32 v9, 0x10000, v8\n" \
  "v_add_u32 v12, 0x10000, v11\n" \
  "s_add_u32 s50, s45, s51\n" \
  "s_add_u32 m0, s48, 0\n" \
  "s_nop 0\n" \
  "buffer_load_dwordx4 v0, s[36:39], s44 offen sc1 lds\n" \
  "buffer_load_dwordx4 v0, s[36:39], s44 offen offset:1024 sc1 lds\n" \
  "s_add_u32 m0, s49, 8192\n" \
  "s_nop 0\n" \
  "buffer_load_dwordx4 v4, s[40:43], s45 offen lds\n" \
  "s_add_u32 m0, s49, 12288\n" \
  "s_nop 0\n" \
  "buffer_load_dwordx4 v4, s[40:43], s50 offen lds\n" \
  "s_add_u32 s44, s44, 4096\n" \
  "s_add_u32 s45, s45, s46\n" \
  "s_add_u32 s50, s50, s46\n" \
  "s_add_u32 m0, s48, 16384\n" \
  "s_nop 0\n" \
  "buffer_load_dwordx4 v0, s[36:39], s44 offen sc1 lds\n" \
  "buffer_load_dwordx4 v0, s[36:39], s44 offen offset:1024 sc1 lds\n" \
  "s_add_u32 m0, s49, 24576\n" \
  "s_nop 0\n" \
  "buffer_load_dwordx4 v4, s[40:43], s45 offen lds\n" \
  "s_add_u32 m0, s49, 28672\n" \
  "s_nop 0\n" \
  "buffer_load_dwordx4 v4, s[40:43], s50 offen lds\n" \
  "s_add_u32 s44, s44, s57\n" \
  "s_add_u32 s45, s45, s46\n" \
  "s_add_u32 s50, s50, s46\n" \
  "s_add_u32 m0, s48, 32768\n" \
  "s_nop 0\n" \
  "buffer_load_dwordx4 v0, s[36:39], s44 offen sc1 lds\n" \
  "buffer_load_dwordx4 v0, s[36:39], s44 offen offset:1024 sc1 lds\n" \
  "s_add_u32 m0, s49, 40960\n" \
  "s_nop 0\n" \
  "buffer_load_dwordx4 v4, s[40:43], s45 offen lds\n" \
  "s_add_u32 m0, s49, 45056\n" \
  "s_nop 0\n" \
  "buffer_load_dwordx4 v4, s[40:43], s50 offen lds\n" \
  "s_add_u32 s44, s44, 4096\n" \
  "s_add_u32 s45, s45, s46\n" \
  "s_add_u32 s50, s50, s46\n" \
  "s_add_u32 m0, s48, 49152\n" \
  "s_nop 0\n" \
  "buffer_load_dwordx4 v0, s[36:39], s44 offen sc1 lds\n" \
  "buffer_load_dwordx4 v0, s[36:39], s44 offen offset:1024 sc1 lds\n" \
  "s_add_u32 m0, s49, 57344\n" \
  "s_nop 0\n" \
  "buffer_load_dwordx4 v4, s[40:43], s45 offen lds\n" \
  "s_add_u32 m0, s49, 61440\n" \
  "s_nop 0\n" \
  "buffer_load_dwordx4 v4, s[40:43], s50 offen lds\n" \
  "s_add_u32 s44, s44, s57\n" \
  "s_add_u32 s45, s45, s46\n" \
  "s_add_u32 s50, s50, s46\n" \
  "s_add_u32 m0, s48, 65536\n" \
  "s_nop 0\n" \
  "buffer_load_dwordx4 v0, s[36:39], s44 offen sc1 lds\n" \
  "buffer_load_dwordx4 v0, s[36:39], s44 offen offset:1024 sc1 lds\n" \
  "s_add_u32 m0, s49, 73728\n" \
  "s_nop 0\n" \
  "buffer_load_dwordx4 v4, s[40:43], s45 offen lds\n" \
  "s_add_u32 m0, s49, 77824\n" \
  "s_nop 0\n" \
  "buffer_load_dwordx4 v4, s[40:43], s50 offen lds\n" \
  "s_add_u32 s44, s44, 4096\n" \
  "s_add_u32 s45, s45, s46\n" \
  "s_add_u32 s50, s50, s46\n" \
  "s_add_u32 m0, s48, 81920\n" \
  "s_nop 0\n" \
  "buffer_load_dwordx4 v0, s[36:39], s44 offen sc1 lds\n" \
  "buffer_load_dwordx4 v0, s[36:39], s44 offen offset:1024 sc1 lds\n" \
  "s_add_u32 m0, s49, 90112\n" \
  "s_nop 0\n" \
  "buffer_load_dwordx4 v4, s[40:43], s45 offen lds\n" \
  "s_add_u32 m0, s49, 94208\n" \
  "s_nop 0\n" \
  "buffer_load_dwordx4 v4, s[40:43], s50 offen lds\n" \
  "s_add_u32 s44, s44, s57\n" \
  "s_add_u32 s45, s45, s46\n" \
  "s_add_u32 s50, s50, s46\n" \
  "s_add_u32 m0, s48, 98304\n" \
  "s_nop 0\n" \
  "buffer_load_dwordx4 v0, s[36:39], s44 offen sc1 lds\n" \
  "buffer_load_dwordx4 v0, s[36:39], s44 offen offset:1024 sc1 lds\n" \
  "s_add_u32 m0, s49, 106496\n" \
  "s_nop 0\n" \
  "buffer_load_dwordx4 v4, s[40:43], s45 offen lds\n" \
  "s_add_u32 m0, s49, 110592\n" \
  "s_nop 0\n" \
  "buffer_load_dwordx4 v4, s[40:43], s50 offen lds\n" \
  "s_add_u32 s44, s44, 4096\n" \
  "s_add_u32 s45, s45, s46\n" \
  "s_add_u32 s50, s50, s46\n" \
  "s_add_u32 m0, s48, 114688\n" \
  "s_nop 0\n" \
  "buffer_load_dwordx4 v0, s[36:39], s44 offen sc1 lds\n" \
  "buffer_load_dwordx4 v0, s[36:39], s44 offen offset:1024 sc1 lds\n" \
  "s_add_u32 m0, s49, 122880\n" \
  "s_nop 0\n" \
  "buffer_load_dwordx4 v4, s[40:43], s45 offen lds\n" \
  "s_add_u32 m0, s49, 126976\n" \
  "s_nop 0\n" \
  "buffer_load_dwordx4 v4, s[40:43], s50 offen lds\n" \
  "s_add_u32 s44, s44, s57\n" \
  "s_add_u32 s45, s45, s46\n" \
  "s_add_u32 s50, s50, s46\n" \
  "v_mov_b64 v[80:81], 0\n" \
  "v_mov_b64 v[82:83], 0\n" \
  "v_mov_b64 v[84:85], 0\n" \
  "v_mov_b64 v[86:87], 0\n" \
  "v_mov_b64 v[88:89], 0\n" \
  "v_mov_b64 v[90:91], 0\n" \
  "v_mov_b64 v[92:93], 0\n" \
  "v_mov_b64 v[94:95], 0\n" \
  "s_waitcnt vmcnt(28)\n" \
  "s_barrier\n" \
  "ds_read_b64_tr_b16 v[16:17], v8 offset:8192\n" \
  "ds_read_b64_tr_b16 v[18:19], v8 offset:9216\n" \
  "ds_read_b128 v[20:23], v5 offset:0\n" \
  "ds_read_b64_tr_b16 v[24:25], v8 offset:10240\n" \
  "ds_read_b64_tr_b16 v[26:27], v8 offset:11264\n" \
  "ds_read_b128 v[28:31], v5 offset:1024\n" \
  "ds_read_b64_tr_b16 v[32:33], v8 offset:12288\n" \
  "ds_read_b64_tr_b16 v[34:35], v8 offset:13312\n" \
  "ds_read_b128 v[36:39], v5 offset:2048\n" \
  "ds_read_b64_tr_b16 v[40:41], v8 offset:14336\n" \
  "ds_read_b64_tr_b16 v[42:43], v8 offset:15360\n" \
  "ds_read_b128 v[44:47], v5 offset:3072\n" \
  ".p2align 4\n" \
  "GE_top_%=:\n" \
  "s_waitcnt lgkmcnt(0)\n" \
  "v_mfma_f32_32x32x16_bf16 v[80:95], v[16:19], v[20:23], v[80:95]\n" \
  "s_cmp_eq_u32 s47, 1\n" \
  "s_waitcnt vmcnt(24)\n" \
  "s_barrier\n" \
  "s_cselect_b32 s38, 0, s38\n" \
  "s_cselect_b32 s42, 0, s42\n" \
  "ds_read_b64_tr_b16 v[48:49], v8 offset:24576\n" \
  "ds_read_b64_tr_b16 v[50:51], v8 offset:25600\n" \
  "ds_read_b128 v[52:55], v5 offset:16384\n" \
  "ds_read_b64_tr_b16 v[56:57], v8 offset:26624\n" \
  "ds_read_b64_tr_b16 v[58:59], v8 offset:27648\n" \
  "ds_read_b128 v[60:63], v5 offset:17408\n" \
  "v_mfma_f32_32x32x16_bf16 v[80:95], v[24:27], v[28:31], v[80:95]\n" \
  "s_add_u32 m0, s48, 0\n" \
  "ds_read_b64_tr_b16 v[64:65], v8 offset:28672\n" \
  "ds_read_b64_tr_b16 v[66:67], v8 offset:29696\n" \
  "ds_read_b128 v[68:71], v5 offset:18432\n" \
  "ds_read_b64_tr_b16 v[72:73], v8 offset:30720\n" \
  "ds_read_b64_tr_b16 v[74:75], v8 offset:31744\n" \
  "ds_read_b128 v[76:79], v5 offset:19456\n" \
  "buffer_load_dwordx4 v0, s[36:39], s44 offen sc1 lds\n" \
  "buffer_load_dwordx4 v0, s[36:39], s44 offen offset:1024 sc1 lds\n" \
  "v_mfma_f32_32x32x16_bf16 v[80:95], v[32:35], v[36:39], v[80:95]\n" \
  "s_add_u32 m0, s49, 8192\n" \
  "s_nop 0\n" \
  "buffer_load_dwordx4 v4, s[40:43], s45 offen lds\n" \
  "v_mfma_f32_32x32x16_bf16 v[80:95], v[40:43], v[44:47], v[80:95]\n" \
  "s_add_u32 m0, s49, 12288\n" \
  "s_nop 0\n" \
  "buffer_load_dwordx4 v4, s[40:43], s50 offen lds\n" \
  "s_add_u32 s44, s44, 4096\n" \
  "s_add_u32 s45, s45, s46\n" \
  "s_add_u32 s50, s50, s46\n" \
  "s_waitcnt lgkmcnt(0)\n" \
  "v_mfma_f32_32x32x16_bf16 v[80:95], v[48:51], v[52:55], v[80:95]\n" \
  "s_waitcnt vmcnt(24)\n" \
  "s_barrier\n" \
  "ds_read_b64_tr_b16 v[16:17], v8 offset:40960\n" \
  "ds_read_b64_tr_b16 v[18:19], v8 offset:41984\n" \
  "ds_read_b128 v[20:23], v5 offset:32768\n" \
  "ds_read_b64_tr_b16 v[24:25], v8 offset:43008\n" \
  "ds_read_b64_tr_b16 v[26:27], v8 offset:44032\n" \
  "ds_read_b128 v[28:31], v5 offset:33792\n" \
  "v_mfma_f32_32x32x16_bf16 v[80:95], v[56:59], v[60:63], v[80:95]\n" \
  "s_add_u32 m0, s48, 16384\n" \
  "ds_read_b64_tr_b16 v[32:33], v8 offset:45056\n" \
  "ds_read_b64_tr_b16 v[34:35], v8 offset:46080\n" \
  "ds_read_b128 v[36:39], v5 offset:34816\n" \
  "ds_read_b64_tr_b16 v[40:41], v8 offset:47104\n" \
  "ds_read_b64_tr_b16 v[42:43], v8 offset:48128\n" \
  "ds_read_b128 v[44:47], v5 offset:35840\n" \
  "buffer_load_dwordx4 v0, s[36:39], s44 offen sc1 lds\n" \
  "buffer_load_dwordx4 v0, s[36:39], s44 offen offset:1024 sc1 lds\n" \
  "v_mfma_f32_32x32x16_bf16 v[80:95], v[64:67], v[68:71], v[80:95]\n" \
  "s_add_u32 m0, s49, 24576\n" \
  "s_nop 0\n" \
  "buffer_load_dwordx4 v4, s[40:43], s45 offen lds\n" \
  "v_mfma_f32_32x32x16_bf16 v[80:95], v[72:75], v[76:79], v[80:95]\n" \
  "s_add_u32 m0, s49, 28672\n" \
  "s_nop 0\n" \
  "buffer_load_dwordx4 v4, s[40:43], s50 offen lds\n" \
  "s_add_u32 s44, s44, s57\n" \
  "s_add_u32 s45, s45, s46\n" \
  "s_add_u32 s50, s50, s46\n" \
  "s_waitcnt lgkmcnt(0)\n" \
  "v_mfma_f32_32x32x16_bf16 v[80:95], v[16:19], v[20:23], v[80:95]\n" \
  "s_waitcnt vmcnt(24)\n" \
  "s_barrier\n" \
  "ds_read_b64_tr_b16 v[48:49], v8 offset:57344\n" \
  "ds_read_b64_tr_b16 v[50:51], v8 offset:58368\n" \
  "ds_read_b128 v[52:55], v5 offset:49152\n" \
  "ds_read_b64_tr_b16 v[56:57], v8 offset:59392\n" \
  "ds_read_b64_tr_b16 v[58:59], v8 offset:60416\n" \
  "ds_read_b128 v[60:63], v5 offset:50176\n" \
  "v_mfma_f32_32x32x16_bf16 v[80:95], v[24:27], v[28:31], v[80:95]\n" \
  "s_add_u32 m0, s48, 32768\n" \
  "ds_read_b64_tr_b16 v[64:65], v8 offset:61440\n" \
  "ds_read_b64_tr_b16 v[66:67], v8 offset:62464\n" \
  "ds_read_b128 v[68:71], v5 offset:51200\n" \
  "ds_read_b64_tr_b16 v[72:73], v8 offset:63488\n" \
  "ds_read_b64_tr_b16 v[74:75], v8 offset:64512\n" \
  "ds_read_b128 v[76:79], v5 offset:52224\n" \
  "buffer_load_dwordx4 v0, s[36:39], s44 offen sc1 lds\n" \
  "buffer_load_dwordx4 v0, s[36:39], s44 offen offset:1024 sc1 lds\n" \
  "v_mfma_f32_32x32x16_bf16 v[80:95], v[32:35], v[36:39], v[80:95]\n" \
  "s_add_u32 m0, s49, 40960\n" \
  "s_nop 0\n" \
  "buffer_load_dwordx4 v4, s[40:43], s45 offen lds\n" \
  "v_mfma_f32_32x32x16_bf16 v[80:95], v[40:43], v[44:47], v[80:95]\n" \
  "s_add_u32 m0, s49, 45056\n" \
  "s_nop 0\n" \
  "buffer_load_dwordx4 v4, s[40:43], s50 offen lds\n" \
  "s_add_u32 s44, s44, 4096\n" \
  "s_add_u32 s45, s45, s46\n" \
  "s_add_u32 s50, s50, s46\n" \
  "s_waitcnt lgkmcnt(0)\n" \
  "v_mfma_f32_32x32x16_bf16 v[80:95], v[48:51], v[52:55], v[80:95]\n" \
  "s_waitcnt vmcnt(24)\n" \
  "s_barrier\n" \
  "ds_read_b64_tr_b16 v[16:17], v9 offset:8192\n" \
  "ds_read_b64_tr_b16 v[18:19], v9 offset:9216\n" \
  "ds_read_b128 v[20:23], v6 offset:0\n" \
  "ds_read_b64_tr_b16 v[24:25], v9 offset:10240\n" \
  "ds_read_b64_tr_b16 v[26:27], v9 offset:11264\n" \
  "ds_read_b128 v[28:31], v6 offset:1024\n" \
  "v_mfma_f32_32x32x16_bf16 v[80:95], v[56:59], v[60:63], v[80:95]\n" \
  "s_add_u32 m0, s48, 49152\n" \
  "ds_read_b64_tr_b16 v[32:33], v9 offset:12288\n" \
  "ds_read_b64_tr_b16 v[34:35], v9 offset:13312\n" \
  "ds_read_b128 v[36:39], v6 offset:2048\n" \
  "ds_read_b64_tr_b16 v[40:41], v9 offset:14336\n" \
  "ds_read_b64_tr_b16 v[42:43], v9 offset:15360\n" \
  "ds_read_b128 v[44:47], v6 offset:3072\n" \
  "buffer_load_dwordx4 v0, s[36:39], s44 offen sc1 lds\n" \
  "buffer_load_dwordx4 v0, s[36:39], s44 offen offset:1024 sc1 lds\n" \
  "v_mfma_f32_32x32x16_bf16 v[80:95], v[64:67], v[68:71], v[80:95]\n" \
  "s_add_u32 m0, s49, 57344\n" \
  "s_nop 0\n" \
  "buffer_load_dwordx4 v4, s[40:43], s45 offen lds\n" \
  "v_mfma_f32_32x32x16_bf16 v[80:95], v[72:75], v[76:79], v[80:95]\n" \
  "s_add_u32 m0, s49, 61440\n" \
  "s_nop 0\n" \
  "buffer_load_dwordx4 v4, s[40:43], s50 offen lds\n" \
  "s_add_u32 s44, s44, s57\n" \
  "s_add_u32 s45, s45, s46\n" \
  "s_add_u32 s50, s50, s46\n" \
  "s_waitcnt lgkmcnt(0)\n" \
  "v_mfma_f32_32x32x16_bf16 v[80:95], v[16:19], v[20:23], v[80:95]\n" \
  "s_waitcnt vmcnt(24)\n" \
  "s_barrier\n" \
  "ds_read_b64_tr_b16 v[48:49], v9 offset:24576\n" \
  "ds_read_b64_tr_b16 v[50:51], v9 offset:25600\n" \
  "ds_read_b128 v[52:55], v6 offset:16384\n" \
  "ds_read_b64_tr_b16 v[56:57], v9 offset:26624\n" \
  "ds_read_b64_tr_b16 v[58:59], v9 offset:27648\n" \
  "ds_read_b128 v[60:63], v6 offset:17408\n" \
  "v_mfma_f32_32x32x16_bf16 v[80:95], v[24:27], v[28:31], v[80:95]\n" \
  "s_add_u32 m0, s48, 65536\n" \
  "ds_read_b64_tr_b16 v[64:65], v9 offset:28672\n" \
  "ds_read_b64_tr_b16 v[66:67], v9 offset:29696\n" \
  "ds_read_b128 v[68:71], v6 offset:18432\n" \
  "ds_read_b64_tr_b16 v[72:73], v9 offset:30720\n" \
  "ds_read_b64_tr_b16 v[74:75], v9 offset:31744\n" \
  "ds_read_b128 v[76:79], v6 offset:19456\n" \
  "buffer_load_dwordx4 v0, s[36:39], s44 offen sc1 lds\n" \
  "buffer_load_dwordx4 v0, s[36:39], s44 offen offset:1024 sc1 lds\n" \
  "v_mfma_f32_32x32x16_bf16 v[80:95], v[32:35], v[36:39], v[80:95]\n" \
  "s_add_u32 m0, s49, 73728\n" \
  "s_nop 0\n" \
  "buffer_load_dwordx4 v4, s[40:43], s45 offen lds\n" \
  "v_mfma_f32_32x32x16_bf16 v[80:95], v[40:43], v[44:47], v[80:95]\n" \
  "s_add_u32 m0, s49, 77824\n" \
  "s_nop 0\n" \
  "buffer_load_dwordx4 v4, s[40:43], s50 offen lds\n" \
  "s_add_u32 s44, s44, 4096\n" \
  "s_add_u32 s45, s45, s46\n" \
  "s_add_u32 s50, s50, s46\n" \
  "s_waitcnt lgkmcnt(0)\n" \
  "v_mfma_f32_32x32x16_bf16 v[80:95], v[48:51], v[52:55], v[80:95]\n" \
  "s_waitcnt vmcnt(24)\n" \
  "s_barrier\n" \
  "ds_read_b64_tr_b16 v[16:17], v9 offset:40960\n" \
  "ds_read_b64_tr_b16 v[18:19], v9 offset:41984\n" \
  "ds_read_b128 v[20:23], v6 offset:32768\n" \
  "ds_read_b64_tr_b16 v[24:25], v9 offset:43008\n" \
  "ds_read_b64_tr_b16 v[26:27], v9 offset:44032\n" \
  "ds_read_b128 v[28:31], v6 offset:33792\n" \
  "v_mfma_f32_32x32x16_bf16 v[80:95], v[56:59], v[60:63], v[80:95]\n" \
  "s_add_u32 m0, s48, 81920\n" \
  "ds_read_b64_tr_b16 v[32:33], v9 offset:45056\n" \
  "ds_read_b64_tr_b16 v[34:35], v9 offset:46080\n" \
  "ds_read_b128 v[36:39], v6 offset:34816\n" \
  "ds_read_b64_tr_b16 v[40:41], v9 offset:47104\n" \
  "ds_read_b64_tr_b16 v[42:43], v9 offset:48128\n" \
  "ds_read_b128 v[44:47], v6 offset:35840\n" \
  "buffer_load_dwordx4 v0, s[36:39], s44 offen sc1 lds\n" \
  "buffer_load_dwordx4 v0, s[36:39], s44 offen offset:1024 sc1 lds\n" \
  "v_mfma_f32_32x32x16_bf16 v[80:95], v[64:67], v[68:71], v[80:95]\n" \
  "s_add_u32 m0, s49, 90112\n" \
  "s_nop 0\n" \
  "buffer_load_dwordx4 v4, s[40:43], s45 offen lds\n" \
  "v_mfma_f32_32x32x16_bf16 v[80:95], v[72:75], v[76:79], v[80:95]\n" \
  "s_add_u32 m0, s49, 94208\n" \
  "s_nop 0\n" \
  "buffer_load_dwordx4 v4, s[40:43], s50 offen lds\n" \
  "s_add_u32 s44, s44, s57\n" \
  "s_add_u32 s45, s45, s46\n" \
  "s_add_u32 s50, s50, s46\n" \
  "s_waitcnt lgkmcnt(0)\n" \
  "v_mfma_f32_32x32x16_bf16 v[80:95], v[16:19], v[20:23], v[80:95]\n" \
  "s_waitcnt vmcnt(24)\n" \
  "s_barrier\n" \
  "ds_read_b64_tr_b16 v[48:49], v9 offset:57344\n" \
  "ds_read_b64_tr_b16 v[50:51], v9 offset:58368\n" \
  "ds_read_b128 v[52:55], v6 offset:49152\n" \
  "ds_read_b64_tr_b16 v[56:57], v9 offset:59392\n" \
  "ds_read_b64_tr_b16 v[58:59], v9 offset:60416\n" \
  "ds_read_b128 v[60:63], v6 offset:50176\n" \
  "v_mfma_f32_32x32x16_bf16 v[80:95], v[24:27], v[28:31], v[80:95]\n" \
  "s_add_u32 m0, s48, 98304\n" \
  "ds_read_b64_tr_b16 v[64:65], v9 offset:61440\n" \
  "ds_read_b64_tr_b16 v[66:67], v9 offset:62464\n" \
  "ds_read_b128 v[68:71], v6 offset:51200\n" \
  "ds_read_b64_tr_b16 v[72:73], v9 offset:63488\n" \
  "ds_read_b64_tr_b16 v[74:75], v9 offset:64512\n" \
  "ds_read_b128 v[76:79], v6 offset:52224\n" \
  "buffer_load_dwordx4 v0, s[36:39], s44 offen sc1 lds\n" \
  "buffer_load_dwordx4 v0, s[36:39], s44 offen offset:1024 sc1 lds\n" \
  "v_mfma_f32_32x32x16_bf16 v[80:95], v[32:35], v[36:39], v[80:95]\n" \
  "s_add_u32 m0, s49, 106496\n" \
  "s_nop 0\n" \
  "buffer_load_dwordx4 v4, s[40:43], s45 offen lds\n" \
  "v_mfma_f32_32x32x16_bf16 v[80:95], v[40:43], v[44:47], v[80:95]\n" \
  "s_add_u32 m0, s49, 110592\n" \
  "s_nop 0\n" \
  "buffer_load_dwordx4 v4, s[40:43], s50 offen lds\n" \
  "s_add_u32 s44, s44, 4096\n" \
  "s_add_u32 s45, s45, s46\n" \
  "s_add_u32 s50, s50, s46\n" \
  "s_waitcnt lgkmcnt(0)\n" \
  "v_mfma_f32_32x32x16_bf16 v[80:95], v[48:51], v[52:55], v[80:95]\n" \
  "s_waitcnt vmcnt(24)\n" \
  "s_barrier\n" \
  "ds_read_b64_tr_b16 v[16:17], v8 offset:8192\n" \
  "ds_read_b64_tr_b16 v[18:19], v8 offset:9216\n" \
  "ds_read_b128 v[20:23], v5 offset:0\n" \
  "ds_read_b64_tr_b16 v[24:25], v8 offset:10240\n" \
  "ds_read_b64_tr_b16 v[26:27], v8 offset:11264\n" \
  "ds_read_b128 v[28:31], v5 offset:1024\n" \
  "v_mfma_f32_32x32x16_bf16 v[80:95], v[56:59], v[60:63], v[80:95]\n" \
  "s_add_u32 m0, s48, 114688\n" \
  "ds_read_b64_tr_b16 v[32:33], v8 offset:12288\n" \
  "ds_read_b64_tr_b16 v[34:35], v8 offset:13312\n" \
  "ds_read_b128 v[36:39], v5 offset:2048\n" \
  "ds_read_b64_tr_b16 v[40:41], v8 offset:14336\n" \
  "ds_read_b64_tr_b16 v[42:43], v8 offset:15360\n" \
  "ds_read_b128 v[44:47], v5 offset:3072\n" \
  "buffer_load_dwordx4 v0, s[36:39], s44 offen sc1 lds\n" \
  "buffer_load_dwordx4 v0, s[36:39], s44 offen offset:1024 sc1 lds\n" \
  "v_mfma_f32_32x32x16_bf16 v[80:95], v[64:67], v[68:71], v[80:95]\n" \
  "s_add_u32 m0, s49, 122880\n" \
  "s_nop 0\n" \
  "buffer_load_dwordx4 v4, s[40:43], s45 offen lds\n" \
  "v_mfma_f32_32x32x16_bf16 v[80:95], v[72:75], v[76:79], v[80:95]\n" \
  "s_add_u32 m0, s49, 126976\n" \
  "s_nop 0\n" \
  "buffer_load_dwordx4 v4, s[40:43], s50 offen lds\n" \
  "s_add_u32 s44, s44, s57\n" \
  "s_add_u32 s45, s45, s46\n" \
  "s_add_u32 s50, s50, s46\n" \
  "s_sub_u32 s47, s47, 1\n" \
  "s_cmp_lg_u32 s47, 0\n" \
  "s_cbranch_scc1 GE_top_%=\n" \
  "s_waitcnt vmcnt(0) lgkmcnt(0)\n" \
  "s_nop 15\n"
#define GE_CLOB "v1", "v2", "v3", "v6", "v7", "v9", "v10", "v12", "v13", "v14", "v15", "v16", "v17", "v18", "v19", "v20", "v21", "v22", "v23", "v24", "v25", "v26", "v27", "v28", "v29", "v30", "v31", "v32", "v33", "v34", "v35", "v36", "v37", "v38", "v39", "v40", "v41", "v42", "v43", "v44", "v45", "v46", "v47", "v48", "v49", "v50", "v51", "v52", "v53", "v54", "v55", "v56", "v57", "v58", "v59", "v60", "v61", "v62", "v63", "v64", "v65", "v66", "v67", "v68", "v69", "v70", "v71", "v72", "v73", "v74", "v75", "v76", "v77", "v78", "v79", "s50", "scc", "memory"
#define GE_OUT(c) "={v[80:95]}"(c[0])
typedef float f32x16 __attribute__((ext_vector_type(16)));
typedef unsigned u32x16 __attribute__((ext_vector_type(16)));
typedef int i32x4 __attribute__((ext_vector_type(4)));
DEV unsigned lane_id() {
  unsigned l; asm volatile("v_mbcnt_lo_u32_b32 %0, -1, 0\n\tv_mbcnt_hi_u32_b32 %0, -1, %0" : "=v"(l)); __builtin_assume(l < 64); return l; }
DEV unsigned xcc_id() { return __builtin_amdgcn_s_getreg(20 | (3 << 11)); }
DEV unsigned wave_id() { return __builtin_amdgcn_readfirstlane(threadIdx.x >> 6); }
DEV __amdgpu_buffer_rsrc_t mkrs(const void* p, unsigned bytes) { return __builtin_amdgcn_make_buffer_rsrc((void*)p, 0, sopq(bytes), sopq(0x00020000)); }
constexpr int SC1 = 16;
DEV u32x4 ld4(__amdgpu_buffer_rsrc_t r, unsigned v, unsigned s) { return __builtin_amdgcn_raw_buffer_load_b128(r, v, s, SC1); }
DEV u32x2 ld2(__amdgpu_buffer_rsrc_t r, unsigned v, unsigned s) { return __builtin_amdgcn_raw_buffer_load_b64(r, v, s, SC1); }
template <int AUX = SC1> DEV void st4(__amdgpu_buffer_rsrc_t r, unsigned v, unsigned s, u32x4 d) {
  __builtin_amdgcn_raw_buffer_store_b128(d, r, v, s, AUX);
  asm volatile("s_nop 1" ::"v"(d) : "memory");
}
DEV void st2(__amdgpu_buffer_rsrc_t r, unsigned v, unsigned s, u32x2 d) { __builtin_amdgcn_raw_buffer_store_b64(d, r, v, s, SC1); }
DEV float xhalf_add(float a) { auto r = __builtin_amdgcn_permlane32_swap(f2u(a), f2u(a), false, false); return u2f(r[0]) + u2f(r[1]); }
DEV float xhalf_max(float a) { auto r = __builtin_amdgcn_permlane32_swap(f2u(a), f2u(a), false, false); return fmaxf(u2f(r[0]), u2f(r[1])); }
DEV float sum8(float a) {
  a += u2f(__builtin_amdgcn_update_dpp(0, f2u(a), 0x128, 15, 15, true));
  auto r = __builtin_amdgcn_permlane16_swap(f2u(a), f2u(a), false, false);
  return xhalf_add(u2f(r[0]) + u2f(r[1]));
}
template <int C> struct Cfg;
template <> struct Cfg<1> { static constexpr int MI = 2, NI = 2, WM = 2, WN = 2, BK = 64, NS = 4; };
template <> struct Cfg<2> { static constexpr int MI = 2, NI = 4, WM = 4, WN = 1, BK = 32, NS = 4; };
template <> struct Cfg<3> { static constexpr int MI = 2, NI = 1, WM = 2, WN = 2, BK = 64, NS = 4; };
template <> struct Cfg<4> { static constexpr int MI = 1, NI = 1, WM = 2, WN = 2, BK = 64, NS = 8; };
template <int C, unsigned XO>
DEV void gemm_main(unsigned w, f32x16* acc, const char* X, unsigned tb0, const bf16* W, unsigned ldw, unsigned n0, unsigned K) {
  using G = Cfg<C>;
  constexpr int TB = G::MI * G::WM, NG = G::NI * G::WN / 2, KS = G::BK / 16;
  const unsigned l = lane_id(), wm = w / G::WN, wn = w % G::WN, hi = l >> 5, q = (l & 15) >> 2;
  u32x4 xr = mksrd(X + XO, 32 * MB), wr = mksrd((const char*)W + n0 * 2, K * ldw - n0 * 2);
  const unsigned vx0 = l * 16 + rowoff<XO>(tb0 + w * TB / 4) + (TB >= 4 ? 0 : ((w * TB * KS / 4) % KS) * 1024);
  unsigned vx1 = l * 16 + rowoff<XO>(tb0 + w * TB / 4 + 1), stw = G::BK * ldw;
  const unsigned vw = (w * 8 + (l >> 3)) * ldw + ((l & 7) ^ ((l >> 2) & 4)) * 16;
  const unsigned rx = l * 16 + wm * G::MI * KS * 1024, nb0 = wn * G::NI;
  const unsigned rw0 = (((4 * hi + q) * 128 + (q >> 1) * 64 + ((l >> 4) & 1) * 32 + (l & 3) * 8) ^ ((nb0 & 1) * 64)) + (nb0 >> 1) * 1024, rw1 = rw0 ^ 64;
  unsigned kx = 0, kw = 0, cnt = K / (G::BK * G::NS);
  const unsigned hw = 32 * ldw, lx = w * (TB * KS / 4) * 1024, lw = w * NG * 1024;
#define GEMM_IO "+{s[36:39]}"(xr), "+{s[40:43]}"(wr), "+{s44}"(kx), "+{s45}"(kw), "+{s46}"(stw), "+{s47}"(cnt)
#define GEMM_IN "{v0}"(vx0), "{v4}"(vw), "{v5}"(rx), "{v8}"(rw0), "{v11}"(rw1), "{s48}"(lx), "{s49}"(lw), "{s51}"(hw), "{s57}"(KS * 1024 + 8192)
  if constexpr (C == 1) asm volatile(GB_ASM : GB_OUT(acc), GEMM_IO : GEMM_IN : GB_CLOB);
  if constexpr (C == 2) asm volatile(GC_ASM : GC_OUT(acc), GEMM_IO : GEMM_IN, "{v1}"(vx1) : GC_CLOB);
  if constexpr (C == 3) asm volatile(GD_ASM : GD_OUT(acc), GEMM_IO : GEMM_IN : GD_CLOB);
  if constexpr (C == 4) asm volatile(GE_ASM : GE_OUT(acc), GEMM_IO : GEMM_IN : GE_CLOB);
}
DEV u32x4 pk8(const f32x16& a, int i, int j) { return u32x4{pk2(a[i], a[i + 1]), pk2(a[i + 2], a[i + 3]), pk2(a[j], a[j + 1]), pk2(a[j + 2], a[j + 3])}; }
DEV unsigned kf_lane(unsigned l) {
  const unsigned tok = l & 31, T = (tok >> 2) & 1, col = ((tok >> 3) & 1) * 8 + ((tok >> 1) & 1) * 4 + (tok >> 4) * 2 + (tok & 1);
  return T * 4096 + ((l >> 5) * 16 + col) * 16;
}
DEV void stampz(unsigned) {}
DEV void stampl(unsigned, unsigned, unsigned) {}
DEV void stampf(char*, unsigned, unsigned) {}
DEV void stamp(char*, unsigned, unsigned, unsigned) {}
DEV void stampc(char*, unsigned, unsigned, unsigned) {}
struct Ptrs { const bf16 *x, *mem0, *Wq, *Wk, *Wv, *Wo, *w1, *w2, *w3, *w4; bf16* out; char* ws; };
typedef const __attribute__((address_space(4))) Ptrs KPtrs;
DEV f32x16 mfma32(u32x4 a, u32x4 b, f32x16 c) { return __builtin_amdgcn_mfma_f32_32x32x16_bf16(__builtin_bit_cast(bf16x8, a), __builtin_bit_cast(bf16x8, b), c, 0, 0, 0); }
DEV void unpk8(u32x4 d, float* f) {
#pragma unroll
  for (int i = 0; i < 4; ++i) { f[2 * i] = bf_lo(d[i]); f[2 * i + 1] = bf_hi(d[i]); }
}
DEV void store_stats(__amdgpu_buffer_rsrc_t rs, unsigned l, const f32x16& t, unsigned so, unsigned d2, unsigned sst) {
  float f[16], sum = 0, q = 0;
#pragma unroll
  for (int u = 0; u < 2; ++u) { const u32x4 d = pk8(t, 8 * u, 8 * u + 4); st4(rs, l * 16, so + u * d2, d); unpk8(d, f + 8 * u); }
#pragma unroll
  for (int r = 0; r < 16; ++r) { sum += f[r]; q = __builtin_fmaf(f[r], f[r], q); }
  st2(rs, l >= 32 ? OOB : l * 8, sst, u32x2{f2u(xhalf_add(sum)), f2u(xhalf_add(q))});
}
template <int C>
DEV void task_w4(KPtrs& p, char* ws, unsigned w, unsigned tb0, unsigned n0) {
  using G = Cfg<C>; constexpr int MI = G::MI, NI = G::NI;
  const unsigned l = lane_id(), hi = l >> 5, tb = tb0 + (w / G::WN) * MI, nb = (n0 >> 5) + (w % G::WN) * NI;
  const auto rs = mkrs(ws, WSB), ro = mkrs(p.out, 4u << 20);
  const unsigned co = coloff(nb);
  u32x4 r0[MI * NI * 2], r1[MI * NI * 2];
#pragma unroll
  for (int mi = 0; mi < MI; ++mi)
#pragma unroll
    for (int i = 0; i < NI * 2; ++i) {
      r0[mi * NI * 2 + i] = ld4(rs, l * 16 + (i & 3) * 1024, OFF_H + rowoff<OFF_H>(tb + mi) + co + (i >> 2) * 4096);
      r1[mi * NI * 2 + i] = ld4(rs, l * 16 + (i & 3) * 1024, OFF_X1 + rowoff<OFF_X1>(tb + mi) + co + (i >> 2) * 4096);
    }
  f32x16 acc[MI * NI];
  gemm_main<C, OFF_H3>(w, acc, ws, tb0, p.w4, 2048, n0, 3584);
#pragma unroll
  for (int mi = 0; mi < MI; ++mi)
#pragma unroll
    for (int i = 0; i < NI * 2; ++i) {
      float f[8], g[8]; unpk8(r0[mi * NI * 2 + i], f); unpk8(r1[mi * NI * 2 + i], g);
#pragma unroll
      for (int r = 0; r < 8; ++r) f[r] += g[r] + acc[(i >> 1) * MI + mi][8 * (i & 1) + r];
      const auto s0 = __builtin_amdgcn_permlane32_swap(pk2(f[0], f[1]), pk2(f[4], f[5]), false, false), s1 = __builtin_amdgcn_permlane32_swap(pk2(f[2], f[3]), pk2(f[6], f[7]), false, false);
      st4<0>(ro, (l & 31) * 2048 + hi * 16 + i * 32, (tb + mi) * 65536 + nb * 64, u32x4{s0[0], s1[0], s0[1], s1[1]});
    }
}
DEV void task_touch(unsigned w, const bf16* W, unsigned sh, unsigned ld, unsigned so) {
  const u32x4 rw = mksrd(W, so + (1024 >> sh) * ld - ld + (128 << sh));
  const unsigned t = w * 64 + lane_id(), vo = (t >> sh) * ld + (t & ((1u << sh) - 1)) * 128;
#pragma unroll
  for (int i = 0; i < 4; ++i) asm volatile("s_mov_b32 m0, 0x27000\n\ts_nop 0\n\tbuffer_load_dword %0, %1, %2 offen lds" ::"v"(vo), "s"(rw), "s"(so + i * (256 >> sh) * ld) : "memory");
}
DEV void rec_role(KPtrs*& kp, unsigned& st, unsigned& w, bool IS_A, unsigned h, unsigned& c, const bf16* mem0, const bf16* W0, const bf16* W1, char* ws, unsigned d1, unsigned d2, unsigned d3) {
  const unsigned l = lane_id(), g = l >> 4, col = l & 15, q = col >> 2;
  const unsigned cMEMST = R_MEM + (c >> 5) * BS + (c & 31) * 256;
  unsigned m0 = ((l >> 2) & 7) * 2048 + 8 * (l & 3) + 4 * (l >> 5) + c * 32;
  u32x4 m0r = mksrd(mem0, 16384);
  const unsigned idx = (l & 8) ? 0xEEEE : 0x4444, l16 = l * 16, stg = LB + w * 8192 + l16, lwa = w * 32768 + l16;
  unsigned tra = w * 32768 + (8 * g + q) * 256 + ((2 * w) ^ q ^ ((g & 1) << 2)) * 32 + (l & 3) * 8;
  unsigned trb = w * 32768 + (16 * (g & 1) + 4 * (g >> 1) + (q & 1)) * 256 + ((q & 1) | ((g & 1) << 1)) * 64 + (q >> 1) * 32 + (l & 3) * 8;
  const unsigned pxv = (g >> 1) * 1024 + (l & 31) * 16, pmv = 2 * g * 1024 + 512 + col * 8;
  const unsigned l4 = l * 4;
  const unsigned rw = LB + w * 8192 + 7168 + (4 * (g & 1) + (l >> 5)) * 64 + col * 4, rr = LB + 7168 + ((l >> 2) & 7) * 64 + (4 * (l & 3) + 2 * (l >> 5)) * 4;
  unsigned voA = g * 8192 + (col ^ (g << 1)) * 16, voB = g * 2048 + (col ^ ((g & 1) << 2)) * 16;
  const unsigned vw = LB + w * 8192 + 6144 + (16 * (g >> 1) + col) * 16 + (g & 1) * 8, va = LB + 6144 + (4 * g + q) * 16 + (l & 1) * 8;
  const unsigned cPST = R_P + (h >> 3) * BS + (h & 7) * 1024;
  const unsigned pz = l < (w == 3 ? 16u : (w == 2 && IS_A) ? 40u : 0u) ? l16 : OOB, cPZ = w == 3 ? cMEMST : cPST;
  const unsigned x15 = !IS_A ? (((g & 1) << 1) | (g >> 1)) * BS + col * 16 : col >= 8 ? OOB : w == 0 ? (g * 8 + col) * 16 : col * 256 + g * 16;
  const unsigned x16 = !IS_A ? (c < 8 && w == 3 && l == 0 ? OFF_CNT + (C_PROG + (c & 3)) * 128 : OOB) : (col < 8 && g < 2) ? 512 + (g * 8 + col) * 8 : OOB;
  u32x4 ring = mksrd(ws + OFF_RING, R_BYTES), wsr = mksrd(ws, WSB); unsigned role = IS_A;
  const u32x4 w0r = mksrd(W0, 8u << 20), w1r = mksrd(W1, 8u << 20);
  unsigned spin = 1u << 21, ssv = OFF_KM + (h & 3) * 2048 + (h >> 2) * 16384;
  unsigned skx = koff(0, h) + (w & 1) * 4096;
  const unsigned cKXL = LB + (w & 1) * 16384 + 4096;
  const unsigned cMEM = R_MEM + (w >> 1) * BS + (w & 1) * 4096, cPLD = R_P + w * BS, cUMLD = c * BS + w * 2048, cUMST = w * 16 * BS + h * 256;
  const unsigned uxb = OFF_UX + c * 65536 + (c & 32) * 256 + w * 16384;
  unsigned long long t0;
#define PROF_OUT
#define TG_OUT
  asm volatile(REC_ASM : "+{v116}"(voA), "+{v117}"(voB), "+{v118}"(trb), "+{v120}"(tra), "+{s63}"(spin), "+{v19}"(m0), "+{s86}"(skx), "+{s87}"(ssv), "+{s[88:91]}"(m0r),
     "={s[98:99]}"(t0), "+{s[36:39]}"(ring), "+{s[40:43]}"(wsr), "+{s60}"(w), "+{s62}"(c), "+s"(kp), "+{s35}"(st), "+{s100}"(role) PROF_OUT TG_OUT
  : "{v0}"(idx), "{v1}"(l16), "{v2}"(stg), "{v3}"(stg ^ 8192), "{v4}"(stg ^ 16384), "{v5}"(stg ^ 24576), "{v6}"(lwa), "{v7}"(pz), "{v8}"(pxv), "{v9}"(pmv), "{v10}"(l4), "{v11}"(rw), "{v12}"(rr),
    "{v13}"(vw), "{v14}"(va), "{v15}"(x15), "{v16}"(x16),
    "{s44}"(cKXL), "{s45}"(OFF_CNT + (C_RDY - 1) * 128), "{s48}"(uxb), "{s49}"(uxb + UX_SEGS * 4 * MB), "{s[52:55]}"(w0r), "{s[56:59]}"(w1r), "{s61}"(h), "{s68}"(d1), "{s69}"(d2), "{s101}"(d3),
    "{s70}"(cMEM), "{s71}"(cMEMST), "{s72}"(cPST), "{s73}"(cPLD), "{s74}"(cUMLD), "{s75}"(cUMST), "{s94}"(cPZ), "{s95}"(w == 3 ? 0 : OOB) : REC_CLOBBERS);
}
#define S4W_ASM \
  "s_add_u32 m0, s46, 0\n" \
  "s_add_u32 s45, s44, 0\n" \
  "buffer_load_dwordx4 v0, s[40:43], s45 offen lds\n" \
  "s_add_u32 m0, s46, 1024\n" \
  "s_add_u32 s45, s44, 32768\n" \
  "buffer_load_dwordx4 v0, s[40:43], s45 offen lds\n" \
  "s_add_u32 m0, s46, 2048\n" \
  "s_add_u32 s45, s44, 65536\n" \
  "buffer_load_dwordx4 v0, s[40:43], s45 offen lds\n" \
  "s_add_u32 m0, s46, 3072\n" \
  "s_add_u32 s45, s44, 98304\n" \
  "buffer_load_dwordx4 v0, s[40:43], s45 offen lds\n" \
  "s_add_u32 m0, s46, 4096\n" \
  "s_add_u32 s45, s44, 131072\n" \
  "buffer_load_dwordx4 v0, s[40:43], s45 offen lds\n" \
  "s_add_u32 m0, s46, 5120\n" \
  "s_add_u32 s45, s44, 163840\n" \
  "buffer_load_dwordx4 v0, s[40:43], s45 offen lds\n" \
  "s_add_u32 m0, s46, 6144\n" \
  "s_add_u32 s45, s44, 196608\n" \
  "buffer_load_dwordx4 v0, s[40:43], s45 offen lds\n" \
  "s_add_u32 m0, s46, 7168\n" \
  "s_add_u32 s45, s44, 229376\n" \
  "buffer_load_dwordx4 v0, s[40:43], s45 offen lds\n" \
  "s_add_u32 m0, s46, 8192\n" \
  "s_add_u32 s45, s44, 262144\n" \
  "buffer_load_dwordx4 v0, s[40:43], s45 offen lds\n" \
  "s_add_u32 m0, s46, 9216\n" \
  "s_add_u32 s45, s44, 294912\n" \
  "buffer_load_dwordx4 v0, s[40:43], s45 offen lds\n" \
  "s_add_u32 m0, s46, 10240\n" \
  "s_add_u32 s45, s44, 327680\n" \
  "buffer_load_dwordx4 v0, s[40:43], s45 offen lds\n" \
  "s_add_u32 m0, s46, 11264\n" \
  "s_add_u32 s45, s44, 360448\n" \
  "buffer_load_dwordx4 v0, s[40:43], s45 offen lds\n" \
  "s_add_u32 m0, s46, 12288\n" \
  "s_add_u32 s45, s44, 393216\n" \
  "buffer_load_dwordx4 v0, s[40:43], s45 offen lds\n" \
  "s_add_u32 m0, s46, 13312\n" \
  "s_add_u32 s45, s44, 425984\n" \
  "buffer_load_dwordx4 v0, s[40:43], s45 offen lds\n" \
  "s_add_u32 m0, s46, 14336\n" \
  "s_add_u32 s45, s44, 458752\n" \
  "buffer_load_dwordx4 v0, s[40:43], s45 offen lds\n" \
  "s_add_u32 m0, s46, 15360\n" \
  "s_add_u32 s45, s44, 491520\n" \
  "buffer_load_dwordx4 v0, s[40:43], s45 offen lds\n" \
  "s_add_u32 m0, s46, 16384\n" \
  "s_add_u32 s45, s44, 524288\n" \
  "buffer_load_dwordx4 v0, s[40:43], s45 offen lds\n" \
  "s_add_u32 m0, s46, 17408\n" \
  "s_add_u32 s45, s44, 557056\n" \
  "buffer_load_dwordx4 v0, s[40:43], s45 offen lds\n" \
  "s_add_u32 m0, s46, 18432\n" \
  "s_add_u32 s45, s44, 589824\n" \
  "buffer_load_dwordx4 v0, s[40:43], s45 offen lds\n" \
  "s_add_u32 m0, s46, 19456\n" \
  "s_add_u32 s45, s44, 622592\n" \
  "buffer_load_dwordx4 v0, s[40:43], s45 offen lds\n" \
  "s_add_u32 m0, s46, 20480\n" \
  "s_add_u32 s45, s44, 655360\n" \
  "buffer_load_dwordx4 v0, s[40:43], s45 offen lds\n" \
  "s_add_u32 m0, s46, 21504\n" \
  "s_add_u32 s45, s44, 688128\n" \
  "buffer_load_dwordx4 v0, s[40:43], s45 offen lds\n" \
  "s_add_u32 m0, s46, 22528\n" \
  "s_add_u32 s45, s44, 720896\n" \
  "buffer_load_dwordx4 v0, s[40:43], s45 offen lds\n" \
  "s_add_u32 m0, s46, 23552\n" \
  "s_add_u32 s45, s44, 753664\n" \
  "buffer_load_dwordx4 v0, s[40:43], s45 offen lds\n" \
  "s_add_u32 m0, s46, 24576\n" \
  "s_add_u32 s45, s44, 786432\n" \
  "buffer_load_dwordx4 v0, s[40:43], s45 offen lds\n" \
  "s_add_u32 m0, s46, 25600\n" \
  "s_add_u32 s45, s44, 819200\n" \
  "buffer_load_dwordx4 v0, s[40:43], s45 offen lds\n" \
  "s_add_u32 m0, s46, 26624\n" \
  "s_add_u32 s45, s44, 851968\n" \
  "buffer_load_dwordx4 v0, s[40:43], s45 offen lds\n" \
  "s_add_u32 m0, s46, 27648\n" \
  "s_add_u32 s45, s44, 884736\n" \
  "buffer_load_dwordx4 v0, s[40:43], s45 offen lds\n" \
  "s_add_u32 m0, s46, 28672\n" \
  "s_add_u32 s45, s44, 917504\n" \
  "buffer_load_dwordx4 v0, s[40:43], s45 offen lds\n" \
  "s_add_u32 m0, s46, 29696\n" \
  "s_add_u32 s45, s44, 950272\n" \
  "buffer_load_dwordx4 v0, s[40:43], s45 offen lds\n" \
  "s_add_u32 m0, s46, 30720\n" \
  "s_add_u32 s45, s44, 983040\n" \
  "buffer_load_dwordx4 v0, s[40:43], s45 offen lds\n" \
  "s_add_u32 m0, s46, 31744\n" \
  "s_add_u32 s45, s44, 1015808\n" \
  "buffer_load_dwordx4 v0, s[40:43], s45 offen lds\n" \
  "s_waitcnt vmcnt(24)\n" \
  "ds_read_b64_tr_b16 a[0:1], v1 offset:0\n" \
  "ds_read_b64_tr_b16 a[2:3], v1 offset:512\n" \
  "ds_read_b64_tr_b16 a[4:5], v1 offset:1024\n" \
  "ds_read_b64_tr_b16 a[6:7], v1 offset:1536\n" \
  "ds_read_b64_tr_b16 a[8:9], v1 offset:2048\n" \
  "ds_read_b64_tr_b16 a[10:11], v1 offset:2560\n" \
  "ds_read_b64_tr_b16 a[12:13], v1 offset:3072\n" \
  "ds_read_b64_tr_b16 a[14:15], v1 offset:3584\n" \
  "ds_read_b64_tr_b16 a[16:17], v1 offset:4096\n" \
  "ds_read_b64_tr_b16 a[18:19], v1 offset:4608\n" \
  "ds_read_b64_tr_b16 a[20:21], v1 offset:5120\n" \
  "ds_read_b64_tr_b16 a[22:23], v1 offset:5632\n" \
  "ds_read_b64_tr_b16 a[24:25], v1 offset:6144\n" \
  "ds_read_b64_tr_b16 a[26:27], v1 offset:6656\n" \
  "ds_read_b64_tr_b16 a[28:29], v1 offset:7168\n" \
  "ds_read_b64_tr_b16 a[30:31], v1 offset:7680\n" \
  "s_waitcnt lgkmcnt(0)\n" \
  "s_add_u32 m0, s46, 0\n" \
  "s_add_u32 s45, s44, 1048576\n" \
  "buffer_load_dwordx4 v0, s[40:43], s45 offen lds\n" \
  "s_add_u32 m0, s46, 1024\n" \
  "s_add_u32 s45, s44, 1081344\n" \
  "buffer_load_dwordx4 v0, s[40:43], s45 offen lds\n" \
  "s_add_u32 m0, s46, 2048\n" \
  "s_add_u32 s45, s44, 1114112\n" \
  "buffer_load_dwordx4 v0, s[40:43], s45 offen lds\n" \
  "s_add_u32 m0, s46, 3072\n" \
  "s_add_u32 s45, s44, 1146880\n" \
  "buffer_load_dwordx4 v0, s[40:43], s45 offen lds\n" \
  "s_add_u32 m0, s46, 4096\n" \
  "s_add_u32 s45, s44, 1179648\n" \
  "buffer_load_dwordx4 v0, s[40:43], s45 offen lds\n" \
  "s_add_u32 m0, s46, 5120\n" \
  "s_add_u32 s45, s44, 1212416\n" \
  "buffer_load_dwordx4 v0, s[40:43], s45 offen lds\n" \
  "s_add_u32 m0, s46, 6144\n" \
  "s_add_u32 s45, s44, 1245184\n" \
  "buffer_load_dwordx4 v0, s[40:43], s45 offen lds\n" \
  "s_add_u32 m0, s46, 7168\n" \
  "s_add_u32 s45, s44, 1277952\n" \
  "buffer_load_dwordx4 v0, s[40:43], s45 offen lds\n" \
  "s_waitcnt vmcnt(24)\n" \
  "ds_read_b64_tr_b16 a[32:33], v1 offset:8192\n" \
  "ds_read_b64_tr_b16 a[34:35], v1 offset:8704\n" \
  "ds_read_b64_tr_b16 a[36:37], v1 offset:9216\n" \
  "ds_read_b64_tr_b16 a[38:39], v1 offset:9728\n" \
  "ds_read_b64_tr_b16 a[40:41], v1 offset:10240\n" \
  "ds_read_b64_tr_b16 a[42:43], v1 offset:10752\n" \
  "ds_read_b64_tr_b16 a[44:45], v1 offset:11264\n" \
  "ds_read_b64_tr_b16 a[46:47], v1 offset:11776\n" \
  "ds_read_b64_tr_b16 a[48:49], v1 offset:12288\n" \
  "ds_read_b64_tr_b16 a[50:51], v1 offset:12800\n" \
  "ds_read_b64_tr_b16 a[52:53], v1 offset:13312\n" \
  "ds_read_b64_tr_b16 a[54:55], v1 offset:13824\n" \
  "ds_read_b64_tr_b16 a[56:57], v1 offset:14336\n" \
  "ds_read_b64_tr_b16 a[58:59], v1 offset:14848\n" \
  "ds_read_b64_tr_b16 a[60:61], v1 offset:15360\n" \
  "ds_read_b64_tr_b16 a[62:63], v1 offset:15872\n" \
  "s_waitcnt lgkmcnt(0)\n" \
  "s_add_u32 m0, s46, 8192\n" \
  "s_add_u32 s45, s44, 1310720\n" \
  "buffer_load_dwordx4 v0, s[40:43], s45 offen lds\n" \
  "s_add_u32 m0, s46, 9216\n" \
  "s_add_u32 s45, s44, 1343488\n" \
  "buffer_load_dwordx4 v0, s[40:43], s45 offen lds\n" \
  "s_add_u32 m0, s46, 10240\n" \
  "s_add_u32 s45, s44, 1376256\n" \
  "buffer_load_dwordx4 v0, s[40:43], s45 offen lds\n" \
  "s_add_u32 m0, s46, 11264\n" \
  "s_add_u32 s45, s44, 1409024\n" \
  "buffer_load_dwordx4 v0, s[40:43], s45 offen lds\n" \
  "s_add_u32 m0, s46, 12288\n" \
  "s_add_u32 s45, s44, 1441792\n" \
  "buffer_load_dwordx4 v0, s[40:43], s45 offen lds\n" \
  "s_add_u32 m0, s46, 13312\n" \
  "s_add_u32 s45, s44, 1474560\n" \
  "buffer_load_dwordx4 v0, s[40:43], s45 offen lds\n" \
  "s_add_u32 m0, s46, 14336\n" \
  "s_add_u32 s45, s44, 1507328\n" \
  "buffer_load_dwordx4 v0, s[40:43], s45 offen lds\n" \
  "s_add_u32 m0, s46, 15360\n" \
  "s_add_u32 s45, s44, 1540096\n" \
  "buffer_load_dwordx4 v0, s[40:43], s45 offen lds\n" \
  "s_waitcnt vmcnt(24)\n" \
  "ds_read_b64_tr_b16 a[64:65], v1 offset:16384\n" \
  "ds_read_b64_tr_b16 a[66:67], v1 offset:16896\n" \
  "ds_read_b64_tr_b16 a[68:69], v1 offset:17408\n" \
  "ds_read_b64_tr_b16 a[70:71], v1 offset:17920\n" \
  "ds_read_b64_tr_b16 a[72:73], v1 offset:18432\n" \
  "ds_read_b64_tr_b16 a[74:75], v1 offset:18944\n" \
  "ds_read_b64_tr_b16 a[76:77], v1 offset:19456\n" \
  "ds_read_b64_tr_b16 a[78:79], v1 offset:19968\n" \
  "ds_read_b64_tr_b16 a[80:81], v1 offset:20480\n" \
  "ds_read_b64_tr_b16 a[82:83], v1 offset:20992\n" \
  "ds_read_b64_tr_b16 a[84:85], v1 offset:21504\n" \
  "ds_read_b64_tr_b16 a[86:87], v1 offset:22016\n" \
  "ds_read_b64_tr_b16 a[88:89], v1 offset:22528\n" \
  "ds_read_b64_tr_b16 a[90:91], v1 offset:23040\n" \
  "ds_read_b64_tr_b16 a[92:93], v1 offset:23552\n" \
  "ds_read_b64_tr_b16 a[94:95], v1 offset:24064\n" \
  "s_waitcnt lgkmcnt(0)\n" \
  "s_add_u32 m0, s46, 16384\n" \
  "s_add_u32 s45, s44, 1572864\n" \
  "buffer_load_dwordx4 v0, s[40:43], s45 offen lds\n" \
  "s_add_u32 m0, s46, 17408\n" \
  "s_add_u32 s45, s44, 1605632\n" \
  "buffer_load_dwordx4 v0, s[40:43], s45 offen lds\n" \
  "s_add_u32 m0, s46, 18432\n" \
  "s_add_u32 s45, s44, 1638400\n" \
  "buffer_load_dwordx4 v0, s[40:43], s45 offen lds\n" \
  "s_add_u32 m0, s46, 19456\n" \
  "s_add_u32 s45, s44, 1671168\n" \
  "buffer_load_dwordx4 v0, s[40:43], s45 offen lds\n" \
  "s_add_u32 m0, s46, 20480\n" \
  "s_add_u32 s45, s44, 1703936\n" \
  "buffer_load_dwordx4 v0, s[40:43], s45 offen lds\n" \
  "s_add_u32 m0, s46, 21504\n" \
  "s_add_u32 s45, s44, 1736704\n" \
  "buffer_load_dwordx4 v0, s[40:43], s45 offen lds\n" \
  "s_add_u32 m0, s46, 22528\n" \
  "s_add_u32 s45, s44, 1769472\n" \
  "buffer_load_dwordx4 v0, s[40:43], s45 offen lds\n" \
  "s_add_u32 m0, s46, 23552\n" \
  "s_add_u32 s45, s44, 1802240\n" \
  "buffer_load_dwordx4 v0, s[40:43], s45 offen lds\n" \
  "s_waitcnt vmcnt(24)\n" \
  "ds_read_b64_tr_b16 a[96:97], v1 offset:24576\n" \
  "ds_read_b64_tr_b16 a[98:99], v1 offset:25088\n" \
  "ds_read_b64_tr_b16 a[100:101], v1 offset:25600\n" \
  "ds_read_b64_tr_b16 a[102:103], v1 offset:26112\n" \
  "ds_read_b64_tr_b16 a[104:105], v1 offset:26624\n" \
  "ds_read_b64_tr_b16 a[106:107], v1 offset:27136\n" \
  "ds_read_b64_tr_b16 a[108:109], v1 offset:27648\n" \
  "ds_read_b64_tr_b16 a[110:111], v1 offset:28160\n" \
  "ds_read_b64_tr_b16 a[112:113], v1 offset:28672\n" \
  "ds_read_b64_tr_b16 a[114:115], v1 offset:29184\n" \
  "ds_read_b64_tr_b16 a[116:117], v1 offset:29696\n" \
  "ds_read_b64_tr_b16 a[118:119], v1 offset:30208\n" \
  "ds_read_b64_tr_b16 a[120:121], v1 offset:30720\n" \
  "ds_read_b64_tr_b16 a[122:123], v1 offset:31232\n" \
  "ds_read_b64_tr_b16 a[124:125], v1 offset:31744\n" \
  "ds_read_b64_tr_b16 a[126:127], v1 offset:32256\n" \
  "s_waitcnt lgkmcnt(0)\n" \
  "s_waitcnt vmcnt(16)\n" \
  "ds_read_b64_tr_b16 a[128:129], v1 offset:0\n" \
  "ds_read_b64_tr_b16 a[130:131], v1 offset:512\n" \
  "ds_read_b64_tr_b16 a[132:133], v1 offset:1024\n" \
  "ds_read_b64_tr_b16 a[134:135], v1 offset:1536\n" \
  "ds_read_b64_tr_b16 a[136:137], v1 offset:2048\n" \
  "ds_read_b64_tr_b16 a[138:139], v1 offset:2560\n" \
  "ds_read_b64_tr_b16 a[140:141], v1 offset:3072\n" \
  "ds_read_b64_tr_b16 a[142:143], v1 offset:3584\n" \
  "ds_read_b64_tr_b16 a[144:145], v1 offset:4096\n" \
  "ds_read_b64_tr_b16 a[146:147], v1 offset:4608\n" \
  "ds_read_b64_tr_b16 a[148:149], v1 offset:5120\n" \
  "ds_read_b64_tr_b16 a[150:151], v1 offset:5632\n" \
  "ds_read_b64_tr_b16 a[152:153], v1 offset:6144\n" \
  "ds_read_b64_tr_b16 a[154:155], v1 offset:6656\n" \
  "ds_read_b64_tr_b16 a[156:157], v1 offset:7168\n" \
  "ds_read_b64_tr_b16 a[158:159], v1 offset:7680\n" \
  "s_waitcnt lgkmcnt(0)\n" \
  "s_waitcnt vmcnt(8)\n" \
  "ds_read_b64_tr_b16 a[160:161], v1 offset:8192\n" \
  "ds_read_b64_tr_b16 a[162:163], v1 offset:8704\n" \
  "ds_read_b64_tr_b16 a[164:165], v1 offset:9216\n" \
  "ds_read_b64_tr_b16 a[166:167], v1 offset:9728\n" \
  "ds_read_b64_tr_b16 a[168:169], v1 offset:10240\n" \
  "ds_read_b64_tr_b16 a[170:171], v1 offset:10752\n" \
  "ds_read_b64_tr_b16 a[172:173], v1 offset:11264\n" \
  "ds_read_b64_tr_b16 a[174:175], v1 offset:11776\n" \
  "ds_read_b64_tr_b16 a[176:177], v1 offset:12288\n" \
  "ds_read_b64_tr_b16 a[178:179], v1 offset:12800\n" \
  "ds_read_b64_tr_b16 a[180:181], v1 offset:13312\n" \
  "ds_read_b64_tr_b16 a[182:183], v1 offset:13824\n" \
  "ds_read_b64_tr_b16 a[184:185], v1 offset:14336\n" \
  "ds_read_b64_tr_b16 a[186:187], v1 offset:14848\n" \
  "ds_read_b64_tr_b16 a[188:189], v1 offset:15360\n" \
  "ds_read_b64_tr_b16 a[190:191], v1 offset:15872\n" \
  "s_waitcnt lgkmcnt(0)\n" \
  "s_waitcnt vmcnt(0)\n" \
  "ds_read_b64_tr_b16 a[192:193], v1 offset:16384\n" \
  "ds_read_b64_tr_b16 a[194:195], v1 offset:16896\n" \
  "ds_read_b64_tr_b16 a[196:197], v1 offset:17408\n" \
  "ds_read_b64_tr_b16 a[198:199], v1 offset:17920\n" \
  "ds_read_b64_tr_b16 a[200:201], v1 offset:18432\n" \
  "ds_read_b64_tr_b16 a[202:203], v1 offset:18944\n" \
  "ds_read_b64_tr_b16 a[204:205], v1 offset:19456\n" \
  "ds_read_b64_tr_b16 a[206:207], v1 offset:19968\n" \
  "ds_read_b64_tr_b16 a[208:209], v1 offset:20480\n" \
  "ds_read_b64_tr_b16 a[210:211], v1 offset:20992\n" \
  "ds_read_b64_tr_b16 a[212:213], v1 offset:21504\n" \
  "ds_read_b64_tr_b16 a[214:215], v1 offset:22016\n" \
  "ds_read_b64_tr_b16 a[216:217], v1 offset:22528\n" \
  "ds_read_b64_tr_b16 a[218:219], v1 offset:23040\n" \
  "ds_read_b64_tr_b16 a[220:221], v1 offset:23552\n" \
  "ds_read_b64_tr_b16 a[222:223], v1 offset:24064\n" \
  "s_waitcnt lgkmcnt(0)\n"
#define S4M_ASM \
  "buffer_load_dwordx4 v[64:67], v0, s[36:39], s44 offen sc1\n" \
  "buffer_load_dwordx4 v[68:71], v0, s[36:39], s44 offen offset:1024 sc1\n" \
  "buffer_load_dwordx4 v[72:75], v0, s[36:39], s44 offen offset:2048 sc1\n" \
  "buffer_load_dwordx4 v[76:79], v0, s[36:39], s44 offen offset:3072 sc1\n" \
  "s_add_u32 s45, s44, 4096\n" \
  "buffer_load_dwordx4 v[80:83], v0, s[36:39], s45 offen sc1\n" \
  "buffer_load_dwordx4 v[84:87], v0, s[36:39], s45 offen offset:1024 sc1\n" \
  "buffer_load_dwordx4 v[88:91], v0, s[36:39], s45 offen offset:2048 sc1\n" \
  "buffer_load_dwordx4 v[92:95], v0, s[36:39], s45 offen offset:3072 sc1\n" \
  "s_add_u32 s45, s44, 16384\n" \
  "buffer_load_dwordx4 v[96:99], v0, s[36:39], s45 offen sc1\n" \
  "buffer_load_dwordx4 v[100:103], v0, s[36:39], s45 offen offset:1024 sc1\n" \
  "buffer_load_dwordx4 v[104:107], v0, s[36:39], s45 offen offset:2048 sc1\n" \
  "buffer_load_dwordx4 v[108:111], v0, s[36:39], s45 offen offset:3072 sc1\n" \
  "s_add_u32 s45, s44, 20480\n" \
  "buffer_load_dwordx4 v[112:115], v0, s[36:39], s45 offen sc1\n" \
  "buffer_load_dwordx4 v[116:119], v0, s[36:39], s45 offen offset:1024 sc1\n" \
  "buffer_load_dwordx4 v[120:123], v0, s[36:39], s45 offen offset:2048 sc1\n" \
  "buffer_load_dwordx4 v[124:127], v0, s[36:39], s45 offen offset:3072 sc1\n" \
  "s_add_u32 s45, s44, 32768\n" \
  "buffer_load_dwordx4 v[128:131], v0, s[36:39], s45 offen sc1\n" \
  "buffer_load_dwordx4 v[132:135], v0, s[36:39], s45 offen offset:1024 sc1\n" \
  "buffer_load_dwordx4 v[136:139], v0, s[36:39], s45 offen offset:2048 sc1\n" \
  "buffer_load_dwordx4 v[140:143], v0, s[36:39], s45 offen offset:3072 sc1\n" \
  "s_add_u32 s45, s44, 36864\n" \
  "buffer_load_dwordx4 v[144:147], v0, s[36:39], s45 offen sc1\n" \
  "buffer_load_dwordx4 v[148:151], v0, s[36:39], s45 offen offset:1024 sc1\n" \
  "buffer_load_dwordx4 v[152:155], v0, s[36:39], s45 offen offset:2048 sc1\n" \
  "buffer_load_dwordx4 v[156:159], v0, s[36:39], s45 offen offset:3072 sc1\n" \
  "s_waitcnt vmcnt(23)\n" \
  "v_mfma_f32_32x32x16_bf16 v[32:47], a[0:3], v[64:67], 0\n" \
  "s_add_u32 s45, s44, 49152\n" \
  "buffer_load_dwordx4 v[64:67], v0, s[36:39], s45 offen sc1\n" \
  "s_waitcnt vmcnt(23)\n" \
  "v_mfma_f32_32x32x16_bf16 v[32:47], a[4:7], v[68:71], v[32:47]\n" \
  "buffer_load_dwordx4 v[68:71], v0, s[36:39], s45 offen offset:1024 sc1\n" \
  "s_waitcnt vmcnt(23)\n" \
  "v_mfma_f32_32x32x16_bf16 v[32:47], a[8:11], v[72:75], v[32:47]\n" \
  "buffer_load_dwordx4 v[72:75], v0, s[36:39], s45 offen offset:2048 sc1\n" \
  "s_waitcnt vmcnt(23)\n" \
  "v_mfma_f32_32x32x16_bf16 v[32:47], a[12:15], v[76:79], v[32:47]\n" \
  "buffer_load_dwordx4 v[76:79], v0, s[36:39], s45 offen offset:3072 sc1\n" \
  "s_waitcnt vmcnt(23)\n" \
  "v_mfma_f32_32x32x16_bf16 v[32:47], a[16:19], v[80:83], v[32:47]\n" \
  "s_add_u32 s45, s44, 53248\n" \
  "buffer_load_dwordx4 v[80:83], v0, s[36:39], s45 offen sc1\n" \
  "s_waitcnt vmcnt(23)\n" \
  "v_mfma_f32_32x32x16_bf16 v[32:47], a[20:23], v[84:87], v[32:47]\n" \
  "buffer_load_dwordx4 v[84:87], v0, s[36:39], s45 offen offset:1024 sc1\n" \
  "s_waitcnt vmcnt(23)\n" \
  "v_mfma_f32_32x32x16_bf16 v[32:47], a[24:27], v[88:91], v[32:47]\n" \
  "buffer_load_dwordx4 v[88:91], v0, s[36:39], s45 offen offset:2048 sc1\n" \
  "s_waitcnt vmcnt(23)\n" \
  "v_mfma_f32_32x32x16_bf16 v[32:47], a[28:31], v[92:95], v[32:47]\n" \
  "buffer_load_dwordx4 v[92:95], v0, s[36:39], s45 offen offset:3072 sc1\n" \
  "s_waitcnt vmcnt(23)\n" \
  "v_mfma_f32_32x32x16_bf16 v[32:47], a[32:35], v[96:99], v[32:47]\n" \
  "s_add_u32 s45, s44, 65536\n" \
  "buffer_load_dwordx4 v[96:99], v0, s[36:39], s45 offen sc1\n" \
  "s_waitcnt vmcnt(23)\n" \
  "v_mfma_f32_32x32x16_bf16 v[32:47], a[36:39], v[100:103], v[32:47]\n" \
  "buffer_load_dwordx4 v[100:103], v0, s[36:39], s45 offen offset:1024 sc1\n" \
  "s_waitcnt vmcnt(23)\n" \
  "v_mfma_f32_32x32x16_bf16 v[32:47], a[40:43], v[104:107], v[32:47]\n" \
  "buffer_load_dwordx4 v[104:107], v0, s[36:39], s45 offen offset:2048 sc1\n" \
  "s_waitcnt vmcnt(23)\n" \
  "v_mfma_f32_32x32x16_bf16 v[32:47], a[44:47], v[108:111], v[32:47]\n" \
  "buffer_load_dwordx4 v[108:111], v0, s[36:39], s45 offen offset:3072 sc1\n" \
  "s_waitcnt vmcnt(23)\n" \
  "v_mfma_f32_32x32x16_bf16 v[32:47], a[48:51], v[112:115], v[32:47]\n" \
  "s_add_u32 s45, s44, 69632\n" \
  "buffer_load_dwordx4 v[112:115], v0, s[36:39], s45 offen sc1\n" \
  "s_waitcnt vmcnt(23)\n" \
  "v_mfma_f32_32x32x16_bf16 v[32:47], a[52:55], v[116:119], v[32:47]\n" \
  "buffer_load_dwordx4 v[116:119], v0, s[36:39], s45 offen offset:1024 sc1\n" \
  "s_waitcnt vmcnt(23)\n" \
  "v_mfma_f32_32x32x16_bf16 v[32:47], a[56:59], v[120:123], v[32:47]\n" \
  "buffer_load_dwordx4 v[120:123], v0, s[36:39], s45 offen offset:2048 sc1\n" \
  "s_waitcnt vmcnt(23)\n" \
  "v_mfma_f32_32x32x16_bf16 v[32:47], a[60:63], v[124:127], v[32:47]\n" \
  "buffer_load_dwordx4 v[124:127], v0, s[36:39], s45 offen offset:3072 sc1\n" \
  "s_waitcnt vmcnt(23)\n" \
  "v_mfma_f32_32x32x16_bf16 v[32:47], a[64:67], v[128:131], v[32:47]\n" \
  "s_add_u32 s45, s44, 81920\n" \
  "buffer_load_dwordx4 v[128:131], v0, s[36:39], s45 offen sc1\n" \
  "s_waitcnt vmcnt(23)\n" \
  "v_mfma_f32_32x32x16_bf16 v[32:47], a[68:71], v[132:135], v[32:47]\n" \
  "buffer_load_dwordx4 v[132:135], v0, s[36:39], s45 offen offset:1024 sc1\n" \
  "s_waitcnt vmcnt(23)\n" \
  "v_mfma_f32_32x32x16_bf16 v[32:47], a[72:75], v[136:139], v[32:47]\n" \
  "buffer_load_dwordx4 v[136:139], v0, s[36:39], s45 offen offset:2048 sc1\n" \
  "s_waitcnt vmcnt(23)\n" \
  "v_mfma_f32_32x32x16_bf16 v[32:47], a[76:79], v[140:143], v[32:47]\n" \
  "buffer_load_dwordx4 v[140:143], v0, s[36:39], s45 offen offset:3072 sc1\n" \
  "s_waitcnt vmcnt(23)\n" \
  "v_mfma_f32_32x32x16_bf16 v[32:47], a[80:83], v[144:147], v[32:47]\n" \
  "s_add_u32 s45, s44, 86016\n" \
  "buffer_load_dwordx4 v[144:147], v0, s[36:39], s45 offen sc1\n" \
  "s_waitcnt vmcnt(23)\n" \
  "v_mfma_f32_32x32x16_bf16 v[32:47], a[84:87], v[148:151], v[32:47]\n" \
  "buffer_load_dwordx4 v[148:151], v0, s[36:39], s45 offen offset:1024 sc1\n" \
  "s_waitcnt vmcnt(23)\n" \
  "v_mfma_f32_32x32x16_bf16 v[32:47], a[88:91], v[152:155], v[32:47]\n" \
  "buffer_load_dwordx4 v[152:155], v0, s[36:39], s45 offen offset:2048 sc1\n" \
  "s_waitcnt vmcnt(23)\n" \
  "v_mfma_f32_32x32x16_bf16 v[32:47], a[92:95], v[156:159], v[32:47]\n" \
  "buffer_load_dwordx4 v[156:159], v0, s[36:39], s45 offen offset:3072 sc1\n" \
  "s_waitcnt vmcnt(23)\n" \
  "v_mfma_f32_32x32x16_bf16 v[32:47], a[96:99], v[64:67], v[32:47]\n" \
  "s_add_u32 s45, s44, 98304\n" \
  "buffer_load_dwordx4 v[64:67], v0, s[36:39], s45 offen sc1\n" \
  "s_waitcnt vmcnt(23)\n" \
  "v_mfma_f32_32x32x16_bf16 v[32:47], a[100:103], v[68:71], v[32:47]\n" \
  "buffer_load_dwordx4 v[68:71], v0, s[36:39], s45 offen offset:1024 sc1\n" \
  "s_waitcnt vmcnt(23)\n" \
  "v_mfma_f32_32x32x16_bf16 v[32:47], a[104:107], v[72:75], v[32:47]\n" \
  "buffer_load_dwordx4 v[72:75], v0, s[36:39], s45 offen offset:2048 sc1\n" \
  "s_waitcnt vmcnt(23)\n" \
  "v_mfma_f32_32x32x16_bf16 v[32:47], a[108:111], v[76:79], v[32:47]\n" \
  "buffer_load_dwordx4 v[76:79], v0, s[36:39], s45 offen offset:3072 sc1\n" \
  "s_waitcnt vmcnt(23)\n" \
  "v_mfma_f32_32x32x16_bf16 v[32:47], a[112:115], v[80:83], v[32:47]\n" \
  "s_add_u32 s45, s44, 102400\n" \
  "buffer_load_dwordx4 v[80:83], v0, s[36:39], s45 offen sc1\n" \
  "s_waitcnt vmcnt(23)\n" \
  "v_mfma_f32_32x32x16_bf16 v[32:47], a[116:119], v[84:87], v[32:47]\n" \
  "buffer_load_dwordx4 v[84:87], v0, s[36:39], s45 offen offset:1024 sc1\n" \
  "s_waitcnt vmcnt(23)\n" \
  "v_mfma_f32_32x32x16_bf16 v[32:47], a[120:123], v[88:91], v[32:47]\n" \
  "buffer_load_dwordx4 v[88:91], v0, s[36:39], s45 offen offset:2048 sc1\n" \
  "s_waitcnt vmcnt(23)\n" \
  "v_mfma_f32_32x32x16_bf16 v[32:47], a[124:127], v[92:95], v[32:47]\n" \
  "buffer_load_dwordx4 v[92:95], v0, s[36:39], s45 offen offset:3072 sc1\n" \
  "s_waitcnt vmcnt(23)\n" \
  "v_mfma_f32_32x32x16_bf16 v[32:47], a[128:131], v[96:99], v[32:47]\n" \
  "s_waitcnt vmcnt(22)\n" \
  "v_mfma_f32_32x32x16_bf16 v[32:47], a[132:135], v[100:103], v[32:47]\n" \
  "s_waitcnt vmcnt(21)\n" \
  "v_mfma_f32_32x32x16_bf16 v[32:47], a[136:139], v[104:107], v[32:47]\n" \
  "s_waitcnt vmcnt(20)\n" \
  "v_mfma_f32_32x32x16_bf16 v[32:47], a[140:143], v[108:111], v[32:47]\n" \
  "s_waitcnt vmcnt(19)\n" \
  "v_mfma_f32_32x32x16_bf16 v[32:47], a[144:147], v[112:115], v[32:47]\n" \
  "s_waitcnt vmcnt(18)\n" \
  "v_mfma_f32_32x32x16_bf16 v[32:47], a[148:151], v[116:119], v[32:47]\n" \
  "s_waitcnt vmcnt(17)\n" \
  "v_mfma_f32_32x32x16_bf16 v[32:47], a[152:155], v[120:123], v[32:47]\n" \
  "s_waitcnt vmcnt(16)\n" \
  "v_mfma_f32_32x32x16_bf16 v[32:47], a[156:159], v[124:127], v[32:47]\n" \
  "s_waitcnt vmcnt(15)\n" \
  "v_mfma_f32_32x32x16_bf16 v[32:47], a[160:163], v[128:131], v[32:47]\n" \
  "s_waitcnt vmcnt(14)\n" \
  "v_mfma_f32_32x32x16_bf16 v[32:47], a[164:167], v[132:135], v[32:47]\n" \
  "s_waitcnt vmcnt(13)\n" \
  "v_mfma_f32_32x32x16_bf16 v[32:47], a[168:171], v[136:139], v[32:47]\n" \
  "s_waitcnt vmcnt(12)\n" \
  "v_mfma_f32_32x32x16_bf16 v[32:47], a[172:175], v[140:143], v[32:47]\n" \
  "s_waitcnt vmcnt(11)\n" \
  "v_mfma_f32_32x32x16_bf16 v[32:47], a[176:179], v[144:147], v[32:47]\n" \
  "s_waitcnt vmcnt(10)\n" \
  "v_mfma_f32_32x32x16_bf16 v[32:47], a[180:183], v[148:151], v[32:47]\n" \
  "s_waitcnt vmcnt(9)\n" \
  "v_mfma_f32_32x32x16_bf16 v[32:47], a[184:187], v[152:155], v[32:47]\n" \
  "s_waitcnt vmcnt(8)\n" \
  "v_mfma_f32_32x32x16_bf16 v[32:47], a[188:191], v[156:159], v[32:47]\n" \
  "s_waitcnt vmcnt(7)\n" \
  "v_mfma_f32_32x32x16_bf16 v[32:47], a[192:195], v[64:67], v[32:47]\n" \
  "s_waitcnt vmcnt(6)\n" \
  "v_mfma_f32_32x32x16_bf16 v[32:47], a[196:199], v[68:71], v[32:47]\n" \
  "s_waitcnt vmcnt(5)\n" \
  "v_mfma_f32_32x32x16_bf16 v[32:47], a[200:203], v[72:75], v[32:47]\n" \
  "s_waitcnt vmcnt(4)\n" \
  "v_mfma_f32_32x32x16_bf16 v[32:47], a[204:207], v[76:79], v[32:47]\n" \
  "s_waitcnt vmcnt(3)\n" \
  "v_mfma_f32_32x32x16_bf16 v[32:47], a[208:211], v[80:83], v[32:47]\n" \
  "s_waitcnt vmcnt(2)\n" \
  "v_mfma_f32_32x32x16_bf16 v[32:47], a[212:215], v[84:87], v[32:47]\n" \
  "s_waitcnt vmcnt(1)\n" \
  "v_mfma_f32_32x32x16_bf16 v[32:47], a[216:219], v[88:91], v[32:47]\n" \
  "s_waitcnt vmcnt(0)\n" \
  "v_mfma_f32_32x32x16_bf16 v[32:47], a[220:223], v[92:95], v[32:47]\n" \
  "s_nop 15\n"
#define S4M_CLOB "v64", "v65", "v66", "v67", "v68", "v69", "v70", "v71", "v72", "v73", "v74", "v75", "v76", "v77", "v78", "v79", "v80", "v81", "v82", "v83", "v84", "v85", "v86", "v87", "v88", "v89", "v90", "v91", "v92", "v93", "v94", "v95", "v96", "v97", "v98", "v99", "v100", "v101", "v102", "v103", "v104", "v105", "v106", "v107", "v108", "v109", "v110", "v111", "v112", "v113", "v114", "v115", "v116", "v117", "v118", "v119", "v120", "v121", "v122", "v123", "v124", "v125", "v126", "v127", "v128", "v129", "v130", "v131", "v132", "v133", "v134", "v135", "v136", "v137", "v138", "v139", "v140", "v141", "v142", "v143", "v144", "v145", "v146", "v147", "v148", "v149", "v150", "v151", "v152", "v153", "v154", "v155", "v156", "v157", "v158", "v159", "s45", "scc", "memory"
#define SPW_ASM \
  "s_mov_b32 s47, 0\n" \
  "s_mov_b32 s48, s45\n" \
  "v_add_u32 v2, 0x10000, v1\n" \
  "v_mov_b32 v4, 0x3f803f80\n" \
  "v_mov_b32 v5, 0x3f803f80\n" \
  "v_mov_b32 v6, 0x3f803f80\n" \
  "v_mov_b32 v7, 0x3f803f80\n" \
  "s_add_u32 m0, s46, 0\n" \
  "s_nop 0\n" \
  "buffer_load_dwordx4 v0, s[40:43], s47 offen lds\n" \
  "s_add_u32 m0, s46, 896\n" \
  "s_nop 0\n" \
  "buffer_load_dwordx4 v0, s[40:43], s47 offen offset:128 lds\n" \
  "s_add_u32 m0, s46, 8192\n" \
  "s_nop 0\n" \
  "buffer_load_dwordx4 v0, s[40:43], s48 offen lds\n" \
  "s_add_u32 m0, s46, 9088\n" \
  "s_nop 0\n" \
  "buffer_load_dwordx4 v0, s[40:43], s48 offen offset:128 lds\n" \
  "s_add_u32 s47, s47, s44\n" \
  "s_add_u32 s48, s48, s44\n" \
  "s_add_u32 m0, s46, 16384\n" \
  "s_nop 0\n" \
  "buffer_load_dwordx4 v0, s[40:43], s47 offen lds\n" \
  "s_add_u32 m0, s46, 17280\n" \
  "s_nop 0\n" \
  "buffer_load_dwordx4 v0, s[40:43], s47 offen offset:128 lds\n" \
  "s_add_u32 m0, s46, 24576\n" \
  "s_nop 0\n" \
  "buffer_load_dwordx4 v0, s[40:43], s48 offen lds\n" \
  "s_add_u32 m0, s46, 25472\n" \
  "s_nop 0\n" \
  "buffer_load_dwordx4 v0, s[40:43], s48 offen offset:128 lds\n" \
  "s_add_u32 s47, s47, s44\n" \
  "s_add_u32 s48, s48, s44\n" \
  "s_add_u32 m0, s46, 32768\n" \
  "s_nop 0\n" \
  "buffer_load_dwordx4 v0, s[40:43], s47 offen lds\n" \
  "s_add_u32 m0, s46, 33664\n" \
  "s_nop 0\n" \
  "buffer_load_dwordx4 v0, s[40:43], s47 offen offset:128 lds\n" \
  "s_add_u32 m0, s46, 40960\n" \
  "s_nop 0\n" \
  "buffer_load_dwordx4 v0, s[40:43], s48 offen lds\n" \
  "s_add_u32 m0, s46, 41856\n" \
  "s_nop 0\n" \
  "buffer_load_dwordx4 v0, s[40:43], s48 offen offset:128 lds\n" \
  "s_add_u32 s47, s47, s44\n" \
  "s_add_u32 s48, s48, s44\n" \
  "s_add_u32 m0, s46, 49152\n" \
  "s_nop 0\n" \
  "buffer_load_dwordx4 v0, s[40:43], s47 offen lds\n" \
  "s_add_u32 m0, s46, 50048\n" \
  "s_nop 0\n" \
  "buffer_load_dwordx4 v0, s[40:43], s47 offen offset:128 lds\n" \
  "s_add_u32 m0, s46, 57344\n" \
  "s_nop 0\n" \
  "buffer_load_dwordx4 v0, s[40:43], s48 offen lds\n" \
  "s_add_u32 m0, s46, 58240\n" \
  "s_nop 0\n" \
  "buffer_load_dwordx4 v0, s[40:43], s48 offen offset:128 lds\n" \
  "s_add_u32 s47, s47, s44\n" \
  "s_add_u32 s48, s48, s44\n" \
  "s_add_u32 m0, s46, 65536\n" \
  "s_nop 0\n" \
  "buffer_load_dwordx4 v0, s[40:43], s47 offen lds\n" \
  "s_add_u32 m0, s46, 66432\n" \
  "s_nop 0\n" \
  "buffer_load_dwordx4 v0, s[40:43], s47 offen offset:128 lds\n" \
  "s_add_u32 m0, s46, 73728\n" \
  "s_nop 0\n" \
  "buffer_load_dwordx4 v0, s[40:43], s48 offen lds\n" \
  "s_add_u32 m0, s46, 74624\n" \
  "s_nop 0\n" \
  "buffer_load_dwordx4 v0, s[40:43], s48 offen offset:128 lds\n" \
  "s_add_u32 s47, s47, s44\n" \
  "s_add_u32 s48, s48, s44\n" \
  "s_add_u32 m0, s46, 81920\n" \
  "s_nop 0\n" \
  "buffer_load_dwordx4 v0, s[40:43], s47 offen lds\n" \
  "s_add_u32 m0, s46, 82816\n" \
  "s_nop 0\n" \
  "buffer_load_dwordx4 v0, s[40:43], s47 offen offset:128 lds\n" \
  "s_add_u32 m0, s46, 90112\n" \
  "s_nop 0\n" \
  "buffer_load_dwordx4 v0, s[40:43], s48 offen lds\n" \
  "s_add_u32 m0, s46, 91008\n" \
  "s_nop 0\n" \
  "buffer_load_dwordx4 v0, s[40:43], s48 offen offset:128 lds\n" \
  "s_add_u32 s47, s47, s44\n" \
  "s_add_u32 s48, s48, s44\n" \
  "s_add_u32 m0, s46, 98304\n" \
  "s_nop 0\n" \
  "buffer_load_dwordx4 v0, s[40:43], s47 offen lds\n" \
  "s_add_u32 m0, s46, 99200\n" \
  "s_nop 0\n" \
  "buffer_load_dwordx4 v0, s[40:43], s47 offen offset:128 lds\n" \
  "s_add_u32 m0, s46, 106496\n" \
  "s_nop 0\n" \
  "buffer_load_dwordx4 v0, s[40:43], s48 offen lds\n" \
  "s_add_u32 m0, s46, 107392\n" \
  "s_nop 0\n" \
  "buffer_load_dwordx4 v0, s[40:43], s48 offen offset:128 lds\n" \
  "s_add_u32 s47, s47, s44\n" \
  "s_add_u32 s48, s48, s44\n" \
  "s_add_u32 m0, s46, 114688\n" \
  "s_nop 0\n" \
  "buffer_load_dwordx4 v0, s[40:43], s47 offen lds\n" \
  "s_add_u32 m0, s46, 115584\n" \
  "s_nop 0\n" \
  "buffer_load_dwordx4 v0, s[40:43], s47 offen offset:128 lds\n" \
  "s_add_u32 m0, s46, 122880\n" \
  "s_nop 0\n" \
  "buffer_load_dwordx4 v0, s[40:43], s48 offen lds\n" \
  "s_add_u32 m0, s46, 123776\n" \
  "s_nop 0\n" \
  "buffer_load_dwordx4 v0, s[40:43], s48 offen offset:128 lds\n" \
  "s_add_u32 s47, s47, s44\n" \
  "s_add_u32 s48, s48, s44\n" \
  "s_waitcnt vmcnt(28) lgkmcnt(0)\n" \
  "s_barrier\n" \
  "ds_read_b64_tr_b16 a[0:1], v1 offset:0\n" \
  "ds_read_b64_tr_b16 a[2:3], v1 offset:2048\n" \
  "ds_read_b64_tr_b16 a[4:5], v1 offset:4096\n" \
  "ds_read_b64_tr_b16 a[6:7], v1 offset:6144\n" \
  "ds_read_b64_tr_b16 a[8:9], v1 offset:8192\n" \
  "ds_read_b64_tr_b16 a[10:11], v1 offset:10240\n" \
  "ds_read_b64_tr_b16 a[12:13], v1 offset:12288\n" \
  "ds_read_b64_tr_b16 a[14:15], v1 offset:14336\n" \
  "s_waitcnt vmcnt(24) lgkmcnt(0)\n" \
  "s_barrier\n" \
  "ds_read_b64_tr_b16 a[16:17], v1 offset:16384\n" \
  "ds_read_b64_tr_b16 a[18:19], v1 offset:18432\n" \
  "ds_read_b64_tr_b16 a[20:21], v1 offset:20480\n" \
  "ds_read_b64_tr_b16 a[22:23], v1 offset:22528\n" \
  "ds_read_b64_tr_b16 a[24:25], v1 offset:24576\n" \
  "ds_read_b64_tr_b16 a[26:27], v1 offset:26624\n" \
  "ds_read_b64_tr_b16 a[28:29], v1 offset:28672\n" \
  "ds_read_b64_tr_b16 a[30:31], v1 offset:30720\n" \
  "s_add_u32 m0, s46, 0\n" \
  "s_nop 0\n" \
  "buffer_load_dwordx4 v0, s[40:43], s47 offen lds\n" \
  "s_add_u32 m0, s46, 896\n" \
  "s_nop 0\n" \
  "buffer_load_dwordx4 v0, s[40:43], s47 offen offset:128 lds\n" \
  "s_add_u32 m0, s46, 8192\n" \
  "s_nop 0\n" \
  "buffer_load_dwordx4 v0, s[40:43], s48 offen lds\n" \
  "s_add_u32 m0, s46, 9088\n" \
  "s_nop 0\n" \
  "buffer_load_dwordx4 v0, s[40:43], s48 offen offset:128 lds\n" \
  "s_add_u32 s47, s47, s44\n" \
  "s_add_u32 s48, s48, s44\n" \
  "v_mfma_f32_32x32x16_bf16 v[16:31], a[0:3], v[4:7], 0\n" \
  "v_mfma_f32_32x32x16_bf16 v[16:31], a[4:7], v[4:7], v[16:31]\n" \
  "v_mfma_f32_32x32x16_bf16 v[16:31], a[8:11], v[4:7], v[16:31]\n" \
  "v_mfma_f32_32x32x16_bf16 v[16:31], a[12:15], v[4:7], v[16:31]\n" \
  "s_waitcnt vmcnt(24) lgkmcnt(0)\n" \
  "s_barrier\n" \
  "ds_read_b64_tr_b16 a[32:33], v1 offset:32768\n" \
  "ds_read_b64_tr_b16 a[34:35], v1 offset:34816\n" \
  "ds_read_b64_tr_b16 a[36:37], v1 offset:36864\n" \
  "ds_read_b64_tr_b16 a[38:39], v1 offset:38912\n" \
  "ds_read_b64_tr_b16 a[40:41], v1 offset:40960\n" \
  "ds_read_b64_tr_b16 a[42:43], v1 offset:43008\n" \
  "ds_read_b64_tr_b16 a[44:45], v1 offset:45056\n" \
  "ds_read_b64_tr_b16 a[46:47], v1 offset:47104\n" \
  "s_add_u32 m0, s46, 16384\n" \
  "s_nop 0\n" \
  "buffer_load_dwordx4 v0, s[40:43], s47 offen lds\n" \
  "s_add_u32 m0, s46, 17280\n" \
  "s_nop 0\n" \
  "buffer_load_dwordx4 v0, s[40:43], s47 offen offset:128 lds\n" \
  "s_add_u32 m0, s46, 24576\n" \
  "s_nop 0\n" \
  "buffer_load_dwordx4 v0, s[40:43], s48 offen lds\n" \
  "s_add_u32 m0, s46, 25472\n" \
  "s_nop 0\n" \
  "buffer_load_dwordx4 v0, s[40:43], s48 offen offset:128 lds\n" \
  "s_add_u32 s47, s47, s44\n" \
  "s_add_u32 s48, s48, s44\n" \
  "v_mfma_f32_32x32x16_bf16 v[16:31], a[16:19], v[4:7], v[16:31]\n" \
  "v_mfma_f32_32x32x16_bf16 v[16:31], a[20:23], v[4:7], v[16:31]\n" \
  "v_mfma_f32_32x32x16_bf16 v[16:31], a[24:27], v[4:7], v[16:31]\n" \
  "v_mfma_f32_32x32x16_bf16 v[16:31], a[28:31], v[4:7], v[16:31]\n" \
  "s_waitcnt vmcnt(24) lgkmcnt(0)\n" \
  "s_barrier\n" \
  "ds_read_b64_tr_b16 a[48:49], v1 offset:49152\n" \
  "ds_read_b64_tr_b16 a[50:51], v1 offset:51200\n" \
  "ds_read_b64_tr_b16 a[52:53], v1 offset:53248\n" \
  "ds_read_b64_tr_b16 a[54:55], v1 offset:55296\n" \
  "ds_read_b64_tr_b16 a[56:57], v1 offset:57344\n" \
  "ds_read_b64_tr_b16 a[58:59], v1 offset:59392\n" \
  "ds_read_b64_tr_b16 a[60:61], v1 offset:61440\n" \
  "ds_read_b64_tr_b16 a[62:63], v1 offset:63488\n" \
  "s_add_u32 m0, s46, 32768\n" \
  "s_nop 0\n" \
  "buffer_load_dwordx4 v0, s[40:43], s47 offen lds\n" \
  "s_add_u32 m0, s46, 33664\n" \
  "s_nop 0\n" \
  "buffer_load_dwordx4 v0, s[40:43], s47 offen offset:128 lds\n" \
  "s_add_u32 m0, s46, 40960\n" \
  "s_nop 0\n" \
  "buffer_load_dwordx4 v0, s[40:43], s48 offen lds\n" \
  "s_add_u32 m0, s46, 41856\n" \
  "s_nop 0\n" \
  "buffer_load_dwordx4 v0, s[40:43], s48 offen offset:128 lds\n" \
  "s_add_u32 s47, s47, s44\n" \
  "s_add_u32 s48, s48, s44\n" \
  "v_mfma_f32_32x32x16_bf16 v[16:31], a[32:35], v[4:7], v[16:31]\n" \
  "v_mfma_f32_32x32x16_bf16 v[16:31], a[36:39], v[4:7], v[16:31]\n" \
  "v_mfma_f32_32x32x16_bf16 v[16:31], a[40:43], v[4:7], v[16:31]\n" \
  "v_mfma_f32_32x32x16_bf16 v[16:31], a[44:47], v[4:7], v[16:31]\n" \
  "s_waitcnt vmcnt(24) lgkmcnt(0)\n" \
  "s_barrier\n" \
  "ds_read_b64_tr_b16 a[64:65], v2 offset:0\n" \
  "ds_read_b64_tr_b16 a[66:67], v2 offset:2048\n" \
  "ds_read_b64_tr_b16 a[68:69], v2 offset:4096\n" \
  "ds_read_b64_tr_b16 a[70:71], v2 offset:6144\n" \
  "ds_read_b64_tr_b16 a[72:73], v2 offset:8192\n" \
  "ds_read_b64_tr_b16 a[74:75], v2 offset:10240\n" \
  "ds_read_b64_tr_b16 a[76:77], v2 offset:12288\n" \
  "ds_read_b64_tr_b16 a[78:79], v2 offset:14336\n" \
  "s_add_u32 m0, s46, 49152\n" \
  "s_nop 0\n" \
  "buffer_load_dwordx4 v0, s[40:43], s47 offen lds\n" \
  "s_add_u32 m0, s46, 50048\n" \
  "s_nop 0\n" \
  "buffer_load_dwordx4 v0, s[40:43], s47 offen offset:128 lds\n" \
  "s_add_u32 m0, s46, 57344\n" \
  "s_nop 0\n" \
  "buffer_load_dwordx4 v0, s[40:43], s48 offen lds\n" \
  "s_add_u32 m0, s46, 58240\n" \
  "s_nop 0\n" \
  "buffer_load_dwordx4 v0, s[40:43], s48 offen offset:128 lds\n" \
  "s_add_u32 s47, s47, s44\n" \
  "s_add_u32 s48, s48, s44\n" \
  "v_mfma_f32_32x32x16_bf16 v[16:31], a[48:51], v[4:7], v[16:31]\n" \
  "v_mfma_f32_32x32x16_bf16 v[16:31], a[52:55], v[4:7], v[16:31]\n" \
  "v_mfma_f32_32x32x16_bf16 v[16:31], a[56:59], v[4:7], v[16:31]\n" \
  "v_mfma_f32_32x32x16_bf16 v[16:31], a[60:63], v[4:7], v[16:31]\n" \
  "s_waitcnt vmcnt(24) lgkmcnt(0)\n" \
  "s_barrier\n" \
  "ds_read_b64_tr_b16 a[80:81], v2 offset:16384\n" \
  "ds_read_b64_tr_b16 a[82:83], v2 offset:18432\n" \
  "ds_read_b64_tr_b16 a[84:85], v2 offset:20480\n" \
  "ds_read_b64_tr_b16 a[86:87], v2 offset:22528\n" \
  "ds_read_b64_tr_b16 a[88:89], v2 offset:24576\n" \
  "ds_read_b64_tr_b16 a[90:91], v2 offset:26624\n" \
  "ds_read_b64_tr_b16 a[92:93], v2 offset:28672\n" \
  "ds_read_b64_tr_b16 a[94:95], v2 offset:30720\n" \
  "s_add_u32 m0, s46, 65536\n" \
  "s_nop 0\n" \
  "buffer_load_dwordx4 v0, s[40:43], s47 offen lds\n" \
  "s_add_u32 m0, s46, 66432\n" \
  "s_nop 0\n" \
  "buffer_load_dwordx4 v0, s[40:43], s47 offen offset:128 lds\n" \
  "s_add_u32 m0, s46, 73728\n" \
  "s_nop 0\n" \
  "buffer_load_dwordx4 v0, s[40:43], s48 offen lds\n" \
  "s_add_u32 m0, s46, 74624\n" \
  "s_nop 0\n" \
  "buffer_load_dwordx4 v0, s[40:43], s48 offen offset:128 lds\n" \
  "s_add_u32 s47, s47, s44\n" \
  "s_add_u32 s48, s48, s44\n" \
  "v_mfma_f32_32x32x16_bf16 v[16:31], a[64:67], v[4:7], v[16:31]\n" \
  "v_mfma_f32_32x32x16_bf16 v[16:31], a[68:71], v[4:7], v[16:31]\n" \
  "v_mfma_f32_32x32x16_bf16 v[16:31], a[72:75], v[4:7], v[16:31]\n" \
  "v_mfma_f32_32x32x16_bf16 v[16:31], a[76:79], v[4:7], v[16:31]\n" \
  "s_waitcnt vmcnt(24) lgkmcnt(0)\n" \
  "s_barrier\n" \
  "ds_read_b64_tr_b16 a[96:97], v2 offset:32768\n" \
  "ds_read_b64_tr_b16 a[98:99], v2 offset:34816\n" \
  "ds_read_b64_tr_b16 a[100:101], v2 offset:36864\n" \
  "ds_read_b64_tr_b16 a[102:103], v2 offset:38912\n" \
  "ds_read_b64_tr_b16 a[104:105], v2 offset:40960\n" \
  "ds_read_b64_tr_b16 a[106:107], v2 offset:43008\n" \
  "ds_read_b64_tr_b16 a[108:109], v2 offset:45056\n" \
  "ds_read_b64_tr_b16 a[110:111], v2 offset:47104\n" \
  "s_add_u32 m0, s46, 81920\n" \
  "s_nop 0\n" \
  "buffer_load_dwordx4 v0, s[40:43], s47 offen lds\n" \
  "s_add_u32 m0, s46, 82816\n" \
  "s_nop 0\n" \
  "buffer_load_dwordx4 v0, s[40:43], s47 offen offset:128 lds\n" \
  "s_add_u32 m0, s46, 90112\n" \
  "s_nop 0\n" \
  "buffer_load_dwordx4 v0, s[40:43], s48 offen lds\n" \
  "s_add_u32 m0, s46, 91008\n" \
  "s_nop 0\n" \
  "buffer_load_dwordx4 v0, s[40:43], s48 offen offset:128 lds\n" \
  "s_add_u32 s47, s47, s44\n" \
  "s_add_u32 s48, s48, s44\n" \
  "v_mfma_f32_32x32x16_bf16 v[16:31], a[80:83], v[4:7], v[16:31]\n" \
  "v_mfma_f32_32x32x16_bf16 v[16:31], a[84:87], v[4:7], v[16:31]\n" \
  "v_mfma_f32_32x32x16_bf16 v[16:31], a[88:91], v[4:7], v[16:31]\n" \
  "v_mfma_f32_32x32x16_bf16 v[16:31], a[92:95], v[4:7], v[16:31]\n" \
  "s_waitcnt vmcnt(24) lgkmcnt(0)\n" \
  "s_barrier\n" \
  "ds_read_b64_tr_b16 a[112:113], v2 offset:49152\n" \
  "ds_read_b64_tr_b16 a[114:115], v2 offset:51200\n" \
  "ds_read_b64_tr_b16 a[116:117], v2 offset:53248\n" \
  "ds_read_b64_tr_b16 a[118:119], v2 offset:55296\n" \
  "ds_read_b64_tr_b16 a[120:121], v2 offset:57344\n" \
  "ds_read_b64_tr_b16 a[122:123], v2 offset:59392\n" \
  "ds_read_b64_tr_b16 a[124:125], v2 offset:61440\n" \
  "ds_read_b64_tr_b16 a[126:127], v2 offset:63488\n" \
  "s_add_u32 m0, s46, 98304\n" \
  "s_nop 0\n" \
  "buffer_load_dwordx4 v0, s[40:43], s47 offen lds\n" \
  "s_add_u32 m0, s46, 99200\n" \
  "s_nop 0\n" \
  "buffer_load_dwordx4 v0, s[40:43], s47 offen offset:128 lds\n" \
  "s_add_u32 m0, s46, 106496\n" \
  "s_nop 0\n" \
  "buffer_load_dwordx4 v0, s[40:43], s48 offen lds\n" \
  "s_add_u32 m0, s46, 107392\n" \
  "s_nop 0\n" \
  "buffer_load_dwordx4 v0, s[40:43], s48 offen offset:128 lds\n" \
  "s_add_u32 s47, s47, s44\n" \
  "s_add_u32 s48, s48, s44\n" \
  "v_mfma_f32_32x32x16_bf16 v[16:31], a[96:99], v[4:7], v[16:31]\n" \
  "v_mfma_f32_32x32x16_bf16 v[16:31], a[100:103], v[4:7], v[16:31]\n" \
  "v_mfma_f32_32x32x16_bf16 v[16:31], a[104:107], v[4:7], v[16:31]\n" \
  "v_mfma_f32_32x32x16_bf16 v[16:31], a[108:111], v[4:7], v[16:31]\n" \
  "s_waitcnt vmcnt(24) lgkmcnt(0)\n" \
  "s_barrier\n" \
  "ds_read_b64_tr_b16 a[128:129], v1 offset:0\n" \
  "ds_read_b64_tr_b16 a[130:131], v1 offset:2048\n" \
  "ds_read_b64_tr_b16 a[132:133], v1 offset:4096\n" \
  "ds_read_b64_tr_b16 a[134:135], v1 offset:6144\n" \
  "ds_read_b64_tr_b16 a[136:137], v1 offset:8192\n" \
  "ds_read_b64_tr_b16 a[138:139], v1 offset:10240\n" \
  "ds_read_b64_tr_b16 a[140:141], v1 offset:12288\n" \
  "ds_read_b64_tr_b16 a[142:143], v1 offset:14336\n" \
  "s_add_u32 m0, s46, 114688\n" \
  "s_nop 0\n" \
  "buffer_load_dwordx4 v0, s[40:43], s47 offen lds\n" \
  "s_add_u32 m0, s46, 115584\n" \
  "s_nop 0\n" \
  "buffer_load_dwordx4 v0, s[40:43], s47 offen offset:128 lds\n" \
  "s_add_u32 m0, s46, 122880\n" \
  "s_nop 0\n" \
  "buffer_load_dwordx4 v0, s[40:43], s48 offen lds\n" \
  "s_add_u32 m0, s46, 123776\n" \
  "s_nop 0\n" \
  "buffer_load_dwordx4 v0, s[40:43], s48 offen offset:128 lds\n" \
  "s_add_u32 s47, s47, s44\n" \
  "s_add_u32 s48, s48, s44\n" \
  "v_mfma_f32_32x32x16_bf16 v[16:31], a[112:115], v[4:7], v[16:31]\n" \
  "v_mfma_f32_32x32x16_bf16 v[16:31], a[116:119], v[4:7], v[16:31]\n" \
  "v_mfma_f32_32x32x16_bf16 v[16:31], a[120:123], v[4:7], v[16:31]\n" \
  "v_mfma_f32_32x32x16_bf16 v[16:31], a[124:127], v[4:7], v[16:31]\n" \
  "s_waitcnt vmcnt(24) lgkmcnt(0)\n" \
  "s_barrier\n" \
  "ds_read_b64_tr_b16 a[144:145], v1 offset:16384\n" \
  "ds_read_b64_tr_b16 a[146:147], v1 offset:18432\n" \
  "ds_read_b64_tr_b16 a[148:149], v1 offset:20480\n" \
  "ds_read_b64_tr_b16 a[150:151], v1 offset:22528\n" \
  "ds_read_b64_tr_b16 a[152:153], v1 offset:24576\n" \
  "ds_read_b64_tr_b16 a[154:155], v1 offset:26624\n" \
  "ds_read_b64_tr_b16 a[156:157], v1 offset:28672\n" \
  "ds_read_b64_tr_b16 a[158:159], v1 offset:30720\n" \
  "v_mfma_f32_32x32x16_bf16 v[16:31], a[128:131], v[4:7], v[16:31]\n" \
  "v_mfma_f32_32x32x16_bf16 v[16:31], a[132:135], v[4:7], v[16:31]\n" \
  "v_mfma_f32_32x32x16_bf16 v[16:31], a[136:139], v[4:7], v[16:31]\n" \
  "v_mfma_f32_32x32x16_bf16 v[16:31], a[140:143], v[4:7], v[16:31]\n" \
  "s_waitcnt vmcnt(20) lgkmcnt(0)\n" \
  "s_barrier\n" \
  "ds_read_b64_tr_b16 a[160:161], v1 offset:32768\n" \
  "ds_read_b64_tr_b16 a[162:163], v1 offset:34816\n" \
  "ds_read_b64_tr_b16 a[164:165], v1 offset:36864\n" \
  "ds_read_b64_tr_b16 a[166:167], v1 offset:38912\n" \
  "ds_read_b64_tr_b16 a[168:169], v1 offset:40960\n" \
  "ds_read_b64_tr_b16 a[170:171], v1 offset:43008\n" \
  "ds_read_b64_tr_b16 a[172:173], v1 offset:45056\n" \
  "ds_read_b64_tr_b16 a[174:175], v1 offset:47104\n" \
  "v_mfma_f32_32x32x16_bf16 v[16:31], a[144:147], v[4:7], v[16:31]\n" \
  "v_mfma_f32_32x32x16_bf16 v[16:31], a[148:151], v[4:7], v[16:31]\n" \
  "v_mfma_f32_32x32x16_bf16 v[16:31], a[152:155], v[4:7], v[16:31]\n" \
  "v_mfma_f32_32x32x16_bf16 v[16:31], a[156:159], v[4:7], v[16:31]\n" \
  "s_waitcnt vmcnt(16) lgkmcnt(0)\n" \
  "s_barrier\n" \
  "ds_read_b64_tr_b16 a[176:177], v1 offset:49152\n" \
  "ds_read_b64_tr_b16 a[178:179], v1 offset:51200\n" \
  "ds_read_b64_tr_b16 a[180:181], v1 offset:53248\n" \
  "ds_read_b64_tr_b16 a[182:183], v1 offset:55296\n" \
  "ds_read_b64_tr_b16 a[184:185], v1 offset:57344\n" \
  "ds_read_b64_tr_b16 a[186:187], v1 offset:59392\n" \
  "ds_read_b64_tr_b16 a[188:189], v1 offset:61440\n" \
  "ds_read_b64_tr_b16 a[190:191], v1 offset:63488\n" \
  "v_mfma_f32_32x32x16_bf16 v[16:31], a[160:163], v[4:7], v[16:31]\n" \
  "v_mfma_f32_32x32x16_bf16 v[16:31], a[164:167], v[4:7], v[16:31]\n" \
  "v_mfma_f32_32x32x16_bf16 v[16:31], a[168:171], v[4:7], v[16:31]\n" \
  "v_mfma_f32_32x32x16_bf16 v[16:31], a[172:175], v[4:7], v[16:31]\n" \
  "s_waitcnt vmcnt(12) lgkmcnt(0)\n" \
  "s_barrier\n" \
  "ds_read_b64_tr_b16 a[192:193], v2 offset:0\n" \
  "ds_read_b64_tr_b16 a[194:195], v2 offset:2048\n" \
  "ds_read_b64_tr_b16 a[196:197], v2 offset:4096\n" \
  "ds_read_b64_tr_b16 a[198:199], v2 offset:6144\n" \
  "ds_read_b64_tr_b16 a[200:201], v2 offset:8192\n" \
  "ds_read_b64_tr_b16 a[202:203], v2 offset:10240\n" \
  "ds_read_b64_tr_b16 a[204:205], v2 offset:12288\n" \
  "ds_read_b64_tr_b16 a[206:207], v2 offset:14336\n" \
  "v_mfma_f32_32x32x16_bf16 v[16:31], a[176:179], v[4:7], v[16:31]\n" \
  "v_mfma_f32_32x32x16_bf16 v[16:31], a[180:183], v[4:7], v[16:31]\n" \
  "v_mfma_f32_32x32x16_bf16 v[16:31], a[184:187], v[4:7], v[16:31]\n" \
  "v_mfma_f32_32x32x16_bf16 v[16:31], a[188:191], v[4:7], v[16:31]\n" \
  "s_waitcnt vmcnt(8) lgkmcnt(0)\n" \
  "s_barrier\n" \
  "ds_read_b64_tr_b16 a[208:209], v2 offset:16384\n" \
  "ds_read_b64_tr_b16 a[210:211], v2 offset:18432\n" \
  "ds_read_b64_tr_b16 a[212:213], v2 offset:20480\n" \
  "ds_read_b64_tr_b16 a[214:215], v2 offset:22528\n" \
  "ds_read_b64_tr_b16 a[216:217], v2 offset:24576\n" \
  "ds_read_b64_tr_b16 a[218:219], v2 offset:26624\n" \
  "ds_read_b64_tr_b16 a[220:221], v2 offset:28672\n" \
  "ds_read_b64_tr_b16 a[222:223], v2 offset:30720\n" \
  "v_mfma_f32_32x32x16_bf16 v[16:31], a[192:195], v[4:7], v[16:31]\n" \
  "v_mfma_f32_32x32x16_bf16 v[16:31], a[196:199], v[4:7], v[16:31]\n" \
  "v_mfma_f32_32x32x16_bf16 v[16:31], a[200:203], v[4:7], v[16:31]\n" \
  "v_mfma_f32_32x32x16_bf16 v[16:31], a[204:207], v[4:7], v[16:31]\n" \
  "s_waitcnt vmcnt(4) lgkmcnt(0)\n" \
  "s_barrier\n" \
  "ds_read_b64_tr_b16 a[224:225], v2 offset:32768\n" \
  "ds_read_b64_tr_b16 a[226:227], v2 offset:34816\n" \
  "ds_read_b64_tr_b16 a[228:229], v2 offset:36864\n" \
  "ds_read_b64_tr_b16 a[230:231], v2 offset:38912\n" \
  "ds_read_b64_tr_b16 a[232:233], v2 offset:40960\n" \
  "ds_read_b64_tr_b16 a[234:235], v2 offset:43008\n" \
  "ds_read_b64_tr_b16 a[236:237], v2 offset:45056\n" \
  "ds_read_b64_tr_b16 a[238:239], v2 offset:47104\n" \
  "v_mfma_f32_32x32x16_bf16 v[16:31], a[208:211], v[4:7], v[16:31]\n" \
  "v_mfma_f32_32x32x16_bf16 v[16:31], a[212:215], v[4:7], v[16:31]\n" \
  "v_mfma_f32_32x32x16_bf16 v[16:31], a[216:219], v[4:7], v[16:31]\n" \
  "v_mfma_f32_32x32x16_bf16 v[16:31], a[220:223], v[4:7], v[16:31]\n" \
  "s_waitcnt vmcnt(0) lgkmcnt(0)\n" \
  "s_barrier\n" \
  "ds_read_b64_tr_b16 a[240:241], v2 offset:49152\n" \
  "ds_read_b64_tr_b16 a[242:243], v2 offset:51200\n" \
  "ds_read_b64_tr_b16 a[244:245], v2 offset:53248\n" \
  "ds_read_b64_tr_b16 a[246:247], v2 offset:55296\n" \
  "ds_read_b64_tr_b16 a[248:249], v2 offset:57344\n" \
  "ds_read_b64_tr_b16 a[250:251], v2 offset:59392\n" \
  "ds_read_b64_tr_b16 a[252:253], v2 offset:61440\n" \
  "ds_read_b64_tr_b16 a[254:255], v2 offset:63488\n" \
  "v_mfma_f32_32x32x16_bf16 v[16:31], a[224:227], v[4:7], v[16:31]\n" \
  "v_mfma_f32_32x32x16_bf16 v[16:31], a[228:231], v[4:7], v[16:31]\n" \
  "v_mfma_f32_32x32x16_bf16 v[16:31], a[232:235], v[4:7], v[16:31]\n" \
  "v_mfma_f32_32x32x16_bf16 v[16:31], a[236:239], v[4:7], v[16:31]\n" \
  "s_waitcnt lgkmcnt(0)\n" \
  "s_barrier\n" \
  "v_mfma_f32_32x32x16_bf16 v[16:31], a[240:243], v[4:7], v[16:31]\n" \
  "v_mfma_f32_32x32x16_bf16 v[16:31], a[244:247], v[4:7], v[16:31]\n" \
  "v_mfma_f32_32x32x16_bf16 v[16:31], a[248:251], v[4:7], v[16:31]\n" \
  "v_mfma_f32_32x32x16_bf16 v[16:31], a[252:255], v[4:7], v[16:31]\n" \
  "s_nop 15\n"
#define SPW_CLOB "v2", "v4", "v5", "v6", "v7", "s47", "s48", "scc", "memory"
#define SPM1_ASM \
  "s_mov_b64 s[40:41], s[36:37]\n" \
  "s_mov_b32 s43, s39\n" \
  "s_waitcnt vmcnt(14)\n" \
  "s_barrier\n" \
  "ds_read_b128 v[64:67], v3 offset:0\n" \
  "ds_read_b128 v[68:71], v3 offset:1024\n" \
  "ds_read_b32 v14, v6\n" \
  "ds_read_b128 v[72:75], v3 offset:2048\n" \
  "ds_read_b128 v[76:79], v3 offset:3072\n" \
  "ds_read_b128 v[80:83], v3 offset:4096\n" \
  "ds_read_b128 v[84:87], v3 offset:5120\n" \
  "s_waitcnt lgkmcnt(5)\n" \
  "v_mfma_f32_32x32x16_bf16 v[32:47], a[0:3], v[64:67], 0\n" \
  "ds_read_b128 v[88:91], v3 offset:6144\n" \
  "v_mov_b32 v176, 0xbdd2d3e7\n" \
  "v_mul_f32 v160, v144, v144\n" \
  "v_fmaak_f32 v160, v160, v176, 0xc0135761\n" \
  "v_mul_f32 v161, v145, v145\n" \
  "v_mfma_f32_32x32x16_bf16 v[32:47], a[4:7], v[68:71], v[32:47]\n" \
  "ds_read_b128 v[92:95], v3 offset:7168\n" \
  "v_mul_f32 v160, v160, v144\n" \
  "v_fmaak_f32 v161, v161, v176, 0xc0135761\n" \
  "v_mul_f32 v162, v146, v146\n" \
  "v_exp_f32 v160, v160\n" \
  "s_waitcnt lgkmcnt(4)\n" \
  "v_mfma_f32_32x32x16_bf16 v[32:47], a[8:11], v[72:75], v[32:47]\n" \
  "ds_read_b128 v[64:67], v3 offset:8192\n" \
  "v_cmp_gt_u32 vcc, s50, v14\n" \
  "v_mul_f32 v161, v161, v145\n" \
  "v_fmaak_f32 v162, v162, v176, 0xc0135761\n" \
  "v_mul_f32 v163, v147, v147\n" \
  "v_add_f32 v160, 1.0, v160\n" \
  "v_mfma_f32_32x32x16_bf16 v[32:47], a[12:15], v[76:79], v[32:47]\n" \
  "ds_read_b128 v[68:71], v3 offset:9216\n" \
  "s_cmp_eq_u64 vcc, 0\n" \
  "s_cselect_b32 s42, s38, 0\n" \
  "v_exp_f32 v161, v161\n" \
  "v_mul_f32 v162, v162, v146\n" \
  "v_fmaak_f32 v163, v163, v176, 0xc0135761\n" \
  "s_waitcnt lgkmcnt(4)\n" \
  "v_mfma_f32_32x32x16_bf16 v[32:47], a[16:19], v[80:83], v[32:47]\n" \
  "ds_read_b128 v[72:75], v3 offset:10240\n" \
  "v_mul_f32 v164, v148, v148\n" \
  "v_rcp_f32 v160, v160\n" \
  "s_add_u32 m0, s47, 0\n" \
  "s_nop 0\n" \
  "buffer_load_dwordx4 v0, s[40:43], s44 offen sc1 lds\n" \
  "v_mfma_f32_32x32x16_bf16 v[32:47], a[20:23], v[84:87], v[32:47]\n" \
  "ds_read_b128 v[76:79], v3 offset:11264\n" \
  "v_add_f32 v161, 1.0, v161\n" \
  "v_exp_f32 v162, v162\n" \
  "v_mul_f32 v163, v163, v147\n" \
  "s_waitcnt lgkmcnt(4)\n" \
  "v_mfma_f32_32x32x16_bf16 v[32:47], a[24:27], v[88:91], v[32:47]\n" \
  "ds_read_b128 v[80:83], v3 offset:12288\n" \
  "v_fmaak_f32 v164, v164, v176, 0xc0135761\n" \
  "v_mul_f32 v165, v149, v149\n" \
  "buffer_load_dwordx4 v0, s[40:43], s44 offen offset:1024 sc1 lds\n" \
  "v_mfma_f32_32x32x16_bf16 v[32:47], a[28:31], v[92:95], v[32:47]\n" \
  "ds_read_b128 v[84:87], v3 offset:13312\n" \
  "v_mul_f32 v144, v144, v160\n" \
  "v_rcp_f32 v161, v161\n" \
  "v_add_f32 v162, 1.0, v162\n" \
  "s_waitcnt lgkmcnt(4)\n" \
  "v_mfma_f32_32x32x16_bf16 v[32:47], a[32:35], v[64:67], v[32:47]\n" \
  "ds_read_b128 v[88:91], v3 offset:14336\n" \
  "v_exp_f32 v163, v163\n" \
  "v_mul_f32 v164, v164, v148\n" \
  "buffer_load_dwordx4 v0, s[40:43], s44 offen offset:2048 sc1 lds\n" \
  "v_mfma_f32_32x32x16_bf16 v[32:47], a[36:39], v[68:71], v[32:47]\n" \
  "ds_read_b128 v[92:95], v3 offset:15360\n" \
  "v_fmaak_f32 v165, v165, v176, 0xc0135761\n" \
  "v_mul_f32 v166, v150, v150\n" \
  "v_mul_f32 v145, v145, v161\n" \
  "v_rcp_f32 v162, v162\n" \
  "s_waitcnt lgkmcnt(4)\n" \
  "v_mfma_f32_32x32x16_bf16 v[32:47], a[40:43], v[72:75], v[32:47]\n" \
  "s_waitcnt vmcnt(11)\n" \
  "s_barrier\n" \
  "ds_read_b128 v[64:67], v3 offset:16384\n" \
  "ds_read2st64_b64 v[96:99], v4 offset0:0 offset1:1\n" \
  "v_add_f32 v163, 1.0, v163\n" \
  "v_mfma_f32_32x32x16_bf16 v[32:47], a[44:47], v[76:79], v[32:47]\n" \
  "ds_read_b128 v[68:71], v3 offset:17408\n" \
  "ds_read2st64_b64 v[100:103], v4 offset0:2 offset1:3\n" \
  "v_exp_f32 v164, v164\n" \
  "buffer_load_dwordx4 v0, s[40:43], s44 offen offset:3072 sc1 lds\n" \
  "s_waitcnt lgkmcnt(6)\n" \
  "v_mfma_f32_32x32x16_bf16 v[32:47], a[48:51], v[80:83], v[32:47]\n" \
  "ds_read_b128 v[72:75], v3 offset:18432\n" \
  "ds_read2st64_b64 v[104:107], v4 offset0:4 offset1:5\n" \
  "v_mul_f32 v165, v165, v149\n" \
  "v_fmaak_f32 v166, v166, v176, 0xc0135761\n" \
  "v_mfma_f32_32x32x16_bf16 v[32:47], a[52:55], v[84:87], v[32:47]\n" \
  "ds_read_b128 v[76:79], v3 offset:19456\n" \
  "ds_read2st64_b64 v[108:111], v4 offset0:6 offset1:7\n" \
  "v_mul_f32 v167, v151, v151\n" \
  "s_xor_b32 m0, s49, 0\n" \
  "s_nop 0\n" \
  "buffer_load_dword v7, s[40:43], 0 offen sc1 lds\n" \
  "s_waitcnt lgkmcnt(8)\n" \
  "v_mfma_f32_32x32x16_bf16 v[32:47], a[56:59], v[88:91], v[32:47]\n" \
  "ds_read_b128 v[80:83], v3 offset:20480\n" \
  "ds_read2st64_b64 v[112:115], v4 offset0:8 offset1:9\n" \
  "v_cvt_pk_bf16_f32 v168, v144, v145\n" \
  "v_mul_f32 v146, v146, v162\n" \
  "v_mfma_f32_32x32x16_bf16 v[32:47], a[60:63], v[92:95], v[32:47]\n" \
  "ds_read_b128 v[84:87], v3 offset:21504\n" \
  "ds_read2st64_b64 v[116:119], v4 offset0:10 offset1:11\n" \
  "v_rcp_f32 v163, v163\n" \
  "v_add_f32 v164, 1.0, v164\n" \
  "s_waitcnt lgkmcnt(9)\n" \
  "v_mfma_f32_32x32x16_bf16 v[32:47], a[64:67], v[64:67], v[32:47]\n" \
  "ds_read_b128 v[88:91], v3 offset:22528\n" \
  "ds_read2st64_b64 v[120:123], v4 offset0:12 offset1:13\n" \
  "s_add_u32 m0, s47, 16384\n" \
  "s_add_u32 s51, s44, 32768\n" \
  "buffer_load_dwordx4 v0, s[40:43], s51 offen sc1 lds\n" \
  "v_mfma_f32_32x32x16_bf16 v[32:47], a[68:71], v[68:71], v[32:47]\n" \
  "ds_read_b128 v[92:95], v3 offset:23552\n" \
  "ds_read2st64_b64 v[124:127], v4 offset0:14 offset1:15\n" \
  "v_exp_f32 v165, v165\n" \
  "v_mul_f32 v166, v166, v150\n" \
  "s_waitcnt lgkmcnt(9)\n" \
  "v_mfma_f32_32x32x16_bf16 v[32:47], a[72:75], v[72:75], v[32:47]\n" \
  "ds_read_b128 v[64:67], v3 offset:24576\n" \
  "v_fmaak_f32 v167, v167, v176, 0xc0135761\n" \
  "v_mul_f32 v160, v152, v152\n" \
  "v_mul_f32 v147, v147, v163\n" \
  "buffer_load_dwordx4 v0, s[40:43], s51 offen offset:1024 sc1 lds\n" \
  "v_mfma_f32_32x32x16_bf16 v[32:47], a[76:79], v[76:79], v[32:47]\n" \
  "ds_read_b128 v[68:71], v3 offset:25600\n" \
  "v_rcp_f32 v164, v164\n" \
  "v_add_f32 v165, 1.0, v165\n" \
  "s_waitcnt lgkmcnt(7)\n" \
  "v_mfma_f32_32x32x16_bf16 v[32:47], a[80:83], v[80:83], v[32:47]\n" \
  "ds_read_b128 v[72:75], v3 offset:26624\n" \
  "v_exp_f32 v166, v166\n" \
  "v_mul_f32 v167, v167, v151\n" \
  "v_fmaak_f32 v160, v160, v176, 0xc0135761\n" \
  "v_mul_f32 v161, v153, v153\n" \
  "v_mfma_f32_32x32x16_bf16 v[32:47], a[84:87], v[84:87], v[32:47]\n" \
  "ds_read_b128 v[76:79], v3 offset:27648\n" \
  "v_cvt_pk_bf16_f32 v169, v146, v147\n" \
  "v_mul_f32 v148, v148, v164\n" \
  "buffer_load_dwordx4 v0, s[40:43], s51 offen offset:2048 sc1 lds\n" \
  "s_waitcnt lgkmcnt(5)\n" \
  "v_mfma_f32_32x32x16_bf16 v[32:47], a[88:91], v[88:91], v[32:47]\n" \
  "ds_read_b128 v[80:83], v3 offset:28672\n" \
  "v_rcp_f32 v165, v165\n" \
  "v_add_f32 v166, 1.0, v166\n" \
  "v_exp_f32 v167, v167\n" \
  "v_mfma_f32_32x32x16_bf16 v[32:47], a[92:95], v[92:95], v[32:47]\n" \
  "ds_read_b128 v[84:87], v3 offset:29696\n" \
  "v_mul_f32 v160, v160, v152\n" \
  "v_fmaak_f32 v161, v161, v176, 0xc0135761\n" \
  "buffer_load_dwordx4 v0, s[40:43], s51 offen offset:3072 sc1 lds\n" \
  "s_waitcnt lgkmcnt(4)\n" \
  "v_mfma_f32_32x32x16_bf16 v[32:47], a[96:99], v[64:67], v[32:47]\n" \
  "ds_read_b128 v[88:91], v3 offset:30720\n" \
  "v_mul_f32 v162, v154, v154\n" \
  "v_mul_f32 v149, v149, v165\n" \
  "v_rcp_f32 v166, v166\n" \
  "v_mfma_f32_32x32x16_bf16 v[32:47], a[100:103], v[68:71], v[32:47]\n" \
  "v_mov_b32 v14, 4\n" \
  "ds_read_b128 v[92:95], v3 offset:31744\n" \
  "v_add_f32 v167, 1.0, v167\n" \
  "v_exp_f32 v160, v160\n" \
  "v_mul_f32 v161, v161, v153\n" \
  "v_fmaak_f32 v162, v162, v176, 0xc0135761\n" \
  "s_waitcnt lgkmcnt(4)\n" \
  "v_mfma_f32_32x32x16_bf16 v[32:47], a[104:107], v[72:75], v[32:47]\n" \
  "s_waitcnt vmcnt(9)\n" \
  "s_barrier\n" \
  "buffer_atomic_add v14, v8, s[36:39], 0 offen\n" \
  "ds_read_b128 v[64:67], v3 offset:32768\n" \
  "v_mul_f32 v163, v155, v155\n" \
  "v_cvt_pk_bf16_f32 v170, v148, v149\n" \
  "v_mul_f32 v150, v150, v166\n" \
  "v_rcp_f32 v167, v167\n" \
  "v_mfma_f32_32x32x16_bf16 v[32:47], a[108:111], v[76:79], v[32:47]\n" \
  "ds_read_b128 v[68:71], v3 offset:33792\n" \
  "v_add_f32 v160, 1.0, v160\n" \
  "s_add_u32 m0, s48, 0\n" \
  "s_nop 0\n" \
  "buffer_load_dwordx4 v1, s[40:43], s45 offen sc1 lds\n" \
  "s_waitcnt lgkmcnt(4)\n" \
  "v_mfma_f32_32x32x16_bf16 v[32:47], a[112:115], v[80:83], v[32:47]\n" \
  "ds_read_b128 v[72:75], v3 offset:34816\n" \
  "v_exp_f32 v161, v161\n" \
  "v_mul_f32 v162, v162, v154\n" \
  "v_fmaak_f32 v163, v163, v176, 0xc0135761\n" \
  "v_mul_f32 v164, v156, v156\n" \
  "v_mfma_f32_32x32x16_bf16 v[32:47], a[116:119], v[84:87], v[32:47]\n" \
  "ds_read_b128 v[76:79], v3 offset:35840\n" \
  "v_mul_f32 v151, v151, v167\n" \
  "buffer_load_dwordx4 v1, s[40:43], s45 offen offset:1024 sc1 lds\n" \
  "s_waitcnt lgkmcnt(4)\n" \
  "v_mfma_f32_32x32x16_bf16 v[32:47], a[120:123], v[88:91], v[32:47]\n" \
  "ds_read_b128 v[80:83], v3 offset:36864\n" \
  "v_rcp_f32 v160, v160\n" \
  "v_add_f32 v161, 1.0, v161\n" \
  "v_exp_f32 v162, v162\n" \
  "v_mfma_f32_32x32x16_bf16 v[32:47], a[124:127], v[92:95], v[32:47]\n" \
  "ds_read_b128 v[84:87], v3 offset:37888\n" \
  "v_mul_f32 v163, v163, v155\n" \
  "v_fmaak_f32 v164, v164, v176, 0xc0135761\n" \
  "v_mul_f32 v165, v157, v157\n" \
  "v_cvt_pk_bf16_f32 v171, v150, v151\n" \
  "buffer_store_dwordx4 v[168:171], v142, s[36:39], s54 offen sc1\n" \
  "s_waitcnt lgkmcnt(4)\n" \
  "v_mfma_f32_32x32x16_bf16 v[32:47], a[128:131], v[64:67], v[32:47]\n" \
  "ds_read_b128 v[88:91], v3 offset:38912\n" \
  "v_mul_f32 v152, v152, v160\n" \
  "s_add_u32 m0, s47, 32768\n" \
  "s_add_u32 s51, s44, 65536\n" \
  "buffer_load_dwordx4 v0, s[40:43], s51 offen sc1 lds\n" \
  "v_mfma_f32_32x32x16_bf16 v[32:47], a[132:135], v[68:71], v[32:47]\n" \
  "ds_read_b128 v[92:95], v3 offset:39936\n" \
  "v_rcp_f32 v161, v161\n" \
  "v_add_f32 v162, 1.0, v162\n" \
  "v_exp_f32 v163, v163\n" \
  "s_waitcnt lgkmcnt(4)\n" \
  "v_mfma_f32_32x32x16_bf16 v[32:47], a[136:139], v[72:75], v[32:47]\n" \
  "ds_read_b128 v[64:67], v3 offset:40960\n" \
  "v_mul_f32 v164, v164, v156\n" \
  "v_fmaak_f32 v165, v165, v176, 0xc0135761\n" \
  "v_mul_f32 v166, v158, v158\n" \
  "v_mul_f32 v153, v153, v161\n" \
  "v_mfma_f32_32x32x16_bf16 v[32:47], a[140:143], v[76:79], v[32:47]\n" \
  "ds_read_b128 v[68:71], v3 offset:41984\n" \
  "v_rcp_f32 v162, v162\n" \
  "v_add_f32 v163, 1.0, v163\n" \
  "buffer_load_dwordx4 v0, s[40:43], s51 offen offset:1024 sc1 lds\n" \
  "s_waitcnt lgkmcnt(4)\n" \
  "v_mfma_f32_32x32x16_bf16 v[32:47], a[144:147], v[80:83], v[32:47]\n" \
  "ds_read_b128 v[72:75], v3 offset:43008\n" \
  "v_exp_f32 v164, v164\n" \
  "v_mul_f32 v165, v165, v157\n" \
  "v_fmaak_f32 v166, v166, v176, 0xc0135761\n" \
  "v_mfma_f32_32x32x16_bf16 v[32:47], a[148:151], v[84:87], v[32:47]\n" \
  "ds_read_b128 v[76:79], v3 offset:44032\n" \
  "v_mul_f32 v167, v159, v159\n" \
  "v_cvt_pk_bf16_f32 v172, v152, v153\n" \
  "v_mul_f32 v154, v154, v162\n" \
  "v_rcp_f32 v163, v163\n" \
  "s_waitcnt lgkmcnt(4)\n" \
  "v_mfma_f32_32x32x16_bf16 v[32:47], a[152:155], v[88:91], v[32:47]\n" \
  "ds_read_b128 v[80:83], v3 offset:45056\n" \
  "v_add_f32 v164, 1.0, v164\n" \
  "buffer_load_dwordx4 v0, s[40:43], s51 offen offset:2048 sc1 lds\n" \
  "v_mfma_f32_32x32x16_bf16 v[32:47], a[156:159], v[92:95], v[32:47]\n" \
  "ds_read_b128 v[84:87], v3 offset:46080\n" \
  "v_exp_f32 v165, v165\n" \
  "v_mul_f32 v166, v166, v158\n" \
  "v_fmaak_f32 v167, v167, v176, 0xc0135761\n" \
  "v_mul_f32 v155, v155, v163\n" \
  "s_waitcnt lgkmcnt(4)\n" \
  "v_mfma_f32_32x32x16_bf16 v[32:47], a[160:163], v[64:67], v[32:47]\n" \
  "ds_read_b128 v[88:91], v3 offset:47104\n" \
  "v_rcp_f32 v164, v164\n" \
  "v_add_f32 v165, 1.0, v165\n" \
  "v_exp_f32 v166, v166\n" \
  "v_mfma_f32_32x32x16_bf16 v[32:47], a[164:167], v[68:71], v[32:47]\n" \
  "ds_read_b128 v[92:95], v3 offset:48128\n" \
  "v_mul_f32 v167, v167, v159\n" \
  "v_cvt_pk_bf16_f32 v173, v154, v155\n" \
  "buffer_load_dwordx4 v0, s[40:43], s51 offen offset:3072 sc1 lds\n" \
  "s_waitcnt lgkmcnt(4)\n" \
  "v_mfma_f32_32x32x16_bf16 v[32:47], a[168:171], v[72:75], v[32:47]\n" \
  "ds_read_b128 v[64:67], v3 offset:49152\n" \
  "v_mul_f32 v156, v156, v164\n" \
  "v_rcp_f32 v165, v165\n" \
  "v_add_f32 v166, 1.0, v166\n" \
  "v_mfma_f32_32x32x16_bf16 v[32:47], a[172:175], v[76:79], v[32:47]\n" \
  "ds_read_b128 v[68:71], v3 offset:50176\n" \
  "v_exp_f32 v167, v167\n" \
  "s_add_u32 m0, s47, 49152\n" \
  "s_add_u32 s51, s44, 98304\n" \
  "buffer_load_dwordx4 v0, s[40:43], s51 offen sc1 lds\n" \
  "s_waitcnt lgkmcnt(4)\n" \
  "v_mfma_f32_32x32x16_bf16 v[32:47], a[176:179], v[80:83], v[32:47]\n" \
  "ds_read_b128 v[72:75], v3 offset:51200\n" \
  "v_mul_f32 v157, v157, v165\n" \
  "v_rcp_f32 v166, v166\n" \
  "v_add_f32 v167, 1.0, v167\n" \
  "v_mfma_f32_32x32x16_bf16 v[32:47], a[180:183], v[84:87], v[32:47]\n" \
  "ds_read_b128 v[76:79], v3 offset:52224\n" \
  "v_cvt_pk_bf16_f32 v174, v156, v157\n" \
  "v_mul_f32 v158, v158, v166\n" \
  "v_rcp_f32 v167, v167\n" \
  "s_nop 0\n" \
  "v_mul_f32 v159, v159, v167\n" \
  "s_waitcnt lgkmcnt(4)\n" \
  "v_mfma_f32_32x32x16_bf16 v[32:47], a[184:187], v[88:91], v[32:47]\n" \
  "ds_read_b128 v[80:83], v3 offset:53248\n" \
  "v_cvt_pk_bf16_f32 v175, v158, v159\n" \
  "buffer_store_dwordx4 v[172:175], v142, s[36:39], s54 offen offset:1024 sc1\n" \
  "buffer_load_dwordx4 v0, s[40:43], s51 offen offset:1024 sc1 lds\n" \
  "v_mfma_f32_32x32x16_bf16 v[32:47], a[188:191], v[92:95], v[32:47]\n" \
  "ds_read_b128 v[84:87], v3 offset:54272\n" \
  "v_add_f32 v10, v96, v98\n" \
  "v_add_f32 v11, v97, v99\n" \
  "v_add_f32 v10, v10, v100\n" \
  "s_waitcnt lgkmcnt(4)\n" \
  "v_mfma_f32_32x32x16_bf16 v[32:47], a[192:195], v[64:67], v[32:47]\n" \
  "ds_read_b128 v[88:91], v3 offset:55296\n" \
  "v_add_f32 v11, v11, v101\n" \
  "v_add_f32 v10, v10, v102\n" \
  "v_add_f32 v11, v11, v103\n" \
  "v_mfma_f32_32x32x16_bf16 v[32:47], a[196:199], v[68:71], v[32:47]\n" \
  "ds_read_b128 v[92:95], v3 offset:56320\n" \
  "v_add_f32 v10, v10, v104\n" \
  "v_add_f32 v11, v11, v105\n" \
  "buffer_load_dwordx4 v0, s[40:43], s51 offen offset:2048 sc1 lds\n" \
  "s_waitcnt lgkmcnt(4)\n" \
  "v_mfma_f32_32x32x16_bf16 v[32:47], a[200:203], v[72:75], v[32:47]\n" \
  "ds_read_b128 v[64:67], v3 offset:57344\n" \
  "v_add_f32 v10, v10, v106\n" \
  "v_add_f32 v11, v11, v107\n" \
  "v_add_f32 v10, v10, v108\n" \
  "v_mfma_f32_32x32x16_bf16 v[32:47], a[204:207], v[76:79], v[32:47]\n" \
  "ds_read_b128 v[68:71], v3 offset:58368\n" \
  "v_add_f32 v11, v11, v109\n" \
  "v_add_f32 v10, v10, v110\n" \
  "v_add_f32 v11, v11, v111\n" \
  "s_waitcnt lgkmcnt(4)\n" \
  "v_mfma_f32_32x32x16_bf16 v[32:47], a[208:211], v[80:83], v[32:47]\n" \
  "ds_read_b128 v[72:75], v3 offset:59392\n" \
  "v_add_f32 v10, v10, v112\n" \
  "v_add_f32 v11, v11, v113\n" \
  "buffer_load_dwordx4 v0, s[40:43], s51 offen offset:3072 sc1 lds\n" \
  "v_mfma_f32_32x32x16_bf16 v[32:47], a[212:215], v[84:87], v[32:47]\n" \
  "ds_read_b128 v[76:79], v3 offset:60416\n" \
  "v_add_f32 v10, v10, v114\n" \
  "v_add_f32 v11, v11, v115\n" \
  "v_add_f32 v10, v10, v116\n" \
  "s_waitcnt lgkmcnt(4)\n" \
  "v_mfma_f32_32x32x16_bf16 v[32:47], a[216:219], v[88:91], v[32:47]\n" \
  "ds_read_b128 v[80:83], v3 offset:61440\n" \
  "v_add_f32 v11, v11, v117\n" \
  "v_add_f32 v10, v10, v118\n" \
  "v_add_f32 v11, v11, v119\n" \
  "v_mfma_f32_32x32x16_bf16 v[32:47], a[220:223], v[92:95], v[32:47]\n" \
  "ds_read_b128 v[84:87], v3 offset:62464\n" \
  "v_add_f32 v10, v10, v120\n" \
  "v_add_f32 v11, v11, v121\n" \
  "v_add_f32 v10, v10, v122\n" \
  "s_waitcnt lgkmcnt(4)\n" \
  "v_mfma_f32_32x32x16_bf16 v[32:47], a[224:227], v[64:67], v[32:47]\n" \
  "ds_read_b128 v[88:91], v3 offset:63488\n" \
  "v_add_f32 v11, v11, v123\n" \
  "v_add_f32 v10, v10, v124\n" \
  "v_add_f32 v11, v11, v125\n" \
  "v_mfma_f32_32x32x16_bf16 v[32:47], a[228:231], v[68:71], v[32:47]\n" \
  "ds_read_b128 v[92:95], v3 offset:64512\n" \
  "v_add_f32 v10, v10, v126\n" \
  "v_add_f32 v11, v11, v127\n" \
  "v_mul_f32 v10, 0x3a800000, v10\n" \
  "s_waitcnt lgkmcnt(4)\n" \
  "v_mfma_f32_32x32x16_bf16 v[32:47], a[232:235], v[72:75], v[32:47]\n" \
  "v_mul_f32 v11, 0x3a800000, v11\n" \
  "v_mov_b32 v13, v10\n" \
  "v_mov_b32 v12, v11\n" \
  "s_nop 0\n" \
  "v_permlane32_swap_b32 v13, v10\n" \
  "v_permlane32_swap_b32 v12, v11\n" \
  "v_add_f32 v10, v13, v10\n" \
  "v_mfma_f32_32x32x16_bf16 v[32:47], a[236:239], v[76:79], v[32:47]\n" \
  "v_add_f32 v11, v12, v11\n" \
  "v_fma_f32 v11, -v10, v10, v11\n" \
  "v_max_f32 v11, 0, v11\n" \
  "v_add_f32 v13, 0x3727c5ac, v11\n" \
  "v_rsq_f32 v12, v13\n" \
  "s_nop 0\n" \
  "v_mul_f32 v11, v11, v12\n" \
  "s_waitcnt lgkmcnt(2)\n" \
  "v_mfma_f32_32x32x16_bf16 v[32:47], a[240:243], v[80:83], v[32:47]\n" \
  "v_fmaak_f32 v13, v11, v12, 0x3727c5ac\n" \
  "v_rsq_f32 v13, v13\n" \
  "s_nop 0\n" \
  "v_mul_f32 v12, v12, v13\n" \
  "v_mul_f32 v10, -v10, v12\n" \
  "v_mul_f32 v48, v10, v16\n" \
  "v_mfma_f32_32x32x16_bf16 v[32:47], a[244:247], v[84:87], v[32:47]\n" \
  "v_mul_f32 v49, v10, v17\n" \
  "v_mul_f32 v50, v10, v18\n" \
  "v_mul_f32 v51, v10, v19\n" \
  "v_mul_f32 v52, v10, v20\n" \
  "v_mul_f32 v53, v10, v21\n" \
  "s_waitcnt lgkmcnt(0)\n" \
  "v_mfma_f32_32x32x16_bf16 v[32:47], a[248:251], v[88:91], v[32:47]\n" \
  "v_mul_f32 v54, v10, v22\n" \
  "v_mul_f32 v55, v10, v23\n" \
  "v_mul_f32 v56, v10, v24\n" \
  "v_mul_f32 v57, v10, v25\n" \
  "v_mul_f32 v58, v10, v26\n" \
  "v_mfma_f32_32x32x16_bf16 v[32:47], a[252:255], v[92:95], v[32:47]\n" \
  "v_mul_f32 v59, v10, v27\n" \
  "v_mul_f32 v60, v10, v28\n" \
  "v_mul_f32 v61, v10, v29\n" \
  "v_mul_f32 v62, v10, v30\n" \
  "v_mul_f32 v63, v10, v31\n" \
  "s_cmp_lg_u32 s42, 0\n" \
  "s_nop 5\n" \
  "v_pk_fma_f32 v[144:145], v[12:13], v[32:33], v[48:49] op_sel_hi:[0,1,1]\n" \
  "v_pk_fma_f32 v[146:147], v[12:13], v[34:35], v[50:51] op_sel_hi:[0,1,1]\n" \
  "v_pk_fma_f32 v[148:149], v[12:13], v[36:37], v[52:53] op_sel_hi:[0,1,1]\n" \
  "v_pk_fma_f32 v[150:151], v[12:13], v[38:39], v[54:55] op_sel_hi:[0,1,1]\n" \
  "v_pk_fma_f32 v[152:153], v[12:13], v[40:41], v[56:57] op_sel_hi:[0,1,1]\n" \
  "v_pk_fma_f32 v[154:155], v[12:13], v[42:43], v[58:59] op_sel_hi:[0,1,1]\n" \
  "v_pk_fma_f32 v[156:157], v[12:13], v[44:45], v[60:61] op_sel_hi:[0,1,1]\n" \
  "v_pk_fma_f32 v[158:159], v[12:13], v[46:47], v[62:63] op_sel_hi:[0,1,1]\n" \
  "s_cbranch_scc1 SPM1_e_%=\n" \
  "v_mov_b32 v176, 0xbdd2d3e7\n" \
  "v_mov_b32 v178, 0xc0135761\n" \
  "v_pk_mul_f32 v[160:161], v[144:145], v[144:145]\n" \
  "v_pk_mul_f32 v[162:163], v[146:147], v[146:147]\n" \
  "v_pk_mul_f32 v[164:165], v[148:149], v[148:149]\n" \
  "v_pk_mul_f32 v[166:167], v[150:151], v[150:151]\n" \
  "v_pk_fma_f32 v[160:161], v[160:161], v[176:177], v[178:179] op_sel_hi:[1,0,0]\n" \
  "v_pk_fma_f32 v[162:163], v[162:163], v[176:177], v[178:179] op_sel_hi:[1,0,0]\n" \
  "v_pk_fma_f32 v[164:165], v[164:165], v[176:177], v[178:179] op_sel_hi:[1,0,0]\n" \
  "v_pk_fma_f32 v[166:167], v[166:167], v[176:177], v[178:179] op_sel_hi:[1,0,0]\n" \
  "v_pk_mul_f32 v[160:161], v[160:161], v[144:145]\n" \
  "v_pk_mul_f32 v[162:163], v[162:163], v[146:147]\n" \
  "v_pk_mul_f32 v[164:165], v[164:165], v[148:149]\n" \
  "v_pk_mul_f32 v[166:167], v[166:167], v[150:151]\n" \
  "v_exp_f32 v160, v160\n" \
  "v_exp_f32 v161, v161\n" \
  "v_exp_f32 v162, v162\n" \
  "v_pk_add_f32 v[160:161], v[160:161], 1.0 op_sel_hi:[1,0]\n" \
  "v_exp_f32 v163, v163\n" \
  "v_exp_f32 v164, v164\n" \
  "v_pk_add_f32 v[162:163], v[162:163], 1.0 op_sel_hi:[1,0]\n" \
  "v_exp_f32 v165, v165\n" \
  "v_exp_f32 v166, v166\n" \
  "v_pk_add_f32 v[164:165], v[164:165], 1.0 op_sel_hi:[1,0]\n" \
  "v_exp_f32 v167, v167\n" \
  "v_rcp_f32 v160, v160\n" \
  "v_pk_add_f32 v[166:167], v[166:167], 1.0 op_sel_hi:[1,0]\n" \
  "v_rcp_f32 v161, v161\n" \
  "v_rcp_f32 v162, v162\n" \
  "v_pk_mul_f32 v[144:145], v[144:145], v[160:161]\n" \
  "v_rcp_f32 v163, v163\n" \
  "v_cvt_pk_bf16_f32 v168, v144, v145\n" \
  "v_rcp_f32 v164, v164\n" \
  "v_pk_mul_f32 v[146:147], v[146:147], v[162:163]\n" \
  "v_rcp_f32 v165, v165\n" \
  "v_cvt_pk_bf16_f32 v169, v146, v147\n" \
  "v_rcp_f32 v166, v166\n" \
  "v_pk_mul_f32 v[148:149], v[148:149], v[164:165]\n" \
  "v_rcp_f32 v167, v167\n" \
  "v_cvt_pk_bf16_f32 v170, v148, v149\n" \
  "v_pk_mul_f32 v[150:151], v[150:151], v[166:167]\n" \
  "v_cvt_pk_bf16_f32 v171, v150, v151\n" \
  "buffer_store_dwordx4 v[168:171], v15, s[36:39], s52 offen sc1\n" \
  "v_pk_mul_f32 v[160:161], v[152:153], v[152:153]\n" \
  "v_pk_mul_f32 v[162:163], v[154:155], v[154:155]\n" \
  "v_pk_mul_f32 v[164:165], v[156:157], v[156:157]\n" \
  "v_pk_mul_f32 v[166:167], v[158:159], v[158:159]\n" \
  "v_pk_fma_f32 v[160:161], v[160:161], v[176:177], v[178:179] op_sel_hi:[1,0,0]\n" \
  "v_pk_fma_f32 v[162:163], v[162:163], v[176:177], v[178:179] op_sel_hi:[1,0,0]\n" \
  "v_pk_fma_f32 v[164:165], v[164:165], v[176:177], v[178:179] op_sel_hi:[1,0,0]\n" \
  "v_pk_fma_f32 v[166:167], v[166:167], v[176:177], v[178:179] op_sel_hi:[1,0,0]\n" \
  "v_pk_mul_f32 v[160:161], v[160:161], v[152:153]\n" \
  "v_pk_mul_f32 v[162:163], v[162:163], v[154:155]\n" \
  "v_pk_mul_f32 v[164:165], v[164:165], v[156:157]\n" \
  "v_pk_mul_f32 v[166:167], v[166:167], v[158:159]\n" \
  "v_exp_f32 v160, v160\n" \
  "v_exp_f32 v161, v161\n" \
  "v_exp_f32 v162, v162\n" \
  "v_pk_add_f32 v[160:161], v[160:161], 1.0 op_sel_hi:[1,0]\n" \
  "v_exp_f32 v163, v163\n" \
  "v_exp_f32 v164, v164\n" \
  "v_pk_add_f32 v[162:163], v[162:163], 1.0 op_sel_hi:[1,0]\n" \
  "v_exp_f32 v165, v165\n" \
  "v_exp_f32 v166, v166\n" \
  "v_pk_add_f32 v[164:165], v[164:165], 1.0 op_sel_hi:[1,0]\n" \
  "v_exp_f32 v167, v167\n" \
  "v_rcp_f32 v160, v160\n" \
  "v_pk_add_f32 v[166:167], v[166:167], 1.0 op_sel_hi:[1,0]\n" \
  "v_rcp_f32 v161, v161\n" \
  "v_rcp_f32 v162, v162\n" \
  "v_pk_mul_f32 v[152:153], v[152:153], v[160:161]\n" \
  "v_rcp_f32 v163, v163\n" \
  "v_cvt_pk_bf16_f32 v172, v152, v153\n" \
  "v_rcp_f32 v164, v164\n" \
  "v_pk_mul_f32 v[154:155], v[154:155], v[162:163]\n" \
  "v_rcp_f32 v165, v165\n" \
  "v_cvt_pk_bf16_f32 v173, v154, v155\n" \
  "v_rcp_f32 v166, v166\n" \
  "v_pk_mul_f32 v[156:157], v[156:157], v[164:165]\n" \
  "v_rcp_f32 v167, v167\n" \
  "v_cvt_pk_bf16_f32 v174, v156, v157\n" \
  "v_pk_mul_f32 v[158:159], v[158:159], v[166:167]\n" \
  "v_cvt_pk_bf16_f32 v175, v158, v159\n" \
  "buffer_store_dwordx4 v[172:175], v15, s[36:39], s52 offen offset:1024 sc1\n" \
  "SPM1_e_%=:\n" \
  "s_nop 1\n"
#define SPM_CLOB "v10", "v11", "v12", "v13", "v14", "v32", "v33", "v34", "v35", "v36", "v37", "v38", "v39", "v40", "v41", "v42", "v43", "v44", "v45", "v46", "v47", "v48", "v49", "v50", "v51", "v52", "v53", "v54", "v55", "v56", "v57", "v58", "v59", "v60", "v61", "v62", "v63", "v64", "v65", "v66", "v67", "v68", "v69", "v70", "v71", "v72", "v73", "v74", "v75", "v76", "v77", "v78", "v79", "v80", "v81", "v82", "v83", "v84", "v85", "v86", "v87", "v88", "v89", "v90", "v91", "v92", "v93", "v94", "v95", "v96", "v97", "v98", "v99", "v100", "v101", "v102", "v103", "v104", "v105", "v106", "v107", "v108", "v109", "v110", "v111", "v112", "v113", "v114", "v115", "v116", "v117", "v118", "v119", "v120", "v121", "v122", "v123", "v124", "v125", "v126", "v127", "v128", "v129", "v130", "v131", "v132", "v133", "v134", "v135", "v136", "v137", "v138", "v139", "v160", "v161", "v162", "v163", "v164", "v165", "v166", "v167", "v168", "v169", "v170", "v171", "v172", "v173", "v174", "v175", "v176", "v177", "v178", "v179", "s51", "vcc", "scc", "memory"
#define SPL1_ASM \
  "s_add_u32 m0, s59, 0\n" \
  "s_nop 0\n" \
  "buffer_load_dwordx4 v0, s[36:39], s56 offen sc1 lds\n" \
  "buffer_load_dwordx4 v0, s[36:39], s56 offen offset:1024 sc1 lds\n" \
  "buffer_load_dwordx4 v0, s[36:39], s56 offen offset:2048 sc1 lds\n" \
  "buffer_load_dwordx4 v0, s[36:39], s56 offen offset:3072 sc1 lds\n" \
  "s_add_u32 m0, s59, 16384\n" \
  "s_add_u32 s51, s56, 32768\n" \
  "buffer_load_dwordx4 v0, s[36:39], s51 offen sc1 lds\n" \
  "buffer_load_dwordx4 v0, s[36:39], s51 offen offset:1024 sc1 lds\n" \
  "buffer_load_dwordx4 v0, s[36:39], s51 offen offset:2048 sc1 lds\n" \
  "buffer_load_dwordx4 v0, s[36:39], s51 offen offset:3072 sc1 lds\n" \
  "s_xor_b32 m0, s49, 1024\n" \
  "s_nop 0\n" \
  "buffer_load_dword v9, s[36:39], 0 offen sc1 lds\n" \
  "s_add_u32 m0, s59, 32768\n" \
  "s_add_u32 s51, s56, 65536\n" \
  "buffer_load_dwordx4 v0, s[36:39], s51 offen sc1 lds\n" \
  "buffer_load_dwordx4 v0, s[36:39], s51 offen offset:1024 sc1 lds\n" \
  "buffer_load_dwordx4 v0, s[36:39], s51 offen offset:2048 sc1 lds\n" \
  "buffer_load_dwordx4 v0, s[36:39], s51 offen offset:3072 sc1 lds\n" \
  "s_mov_b64 s[40:41], s[36:37]\n" \
  "s_mov_b32 s43, s39\n" \
  "s_waitcnt vmcnt(9)\n" \
  "s_barrier\n" \
  "ds_read_b128 v[64:67], v3 offset:0\n" \
  "ds_read_b128 v[68:71], v3 offset:1024\n" \
  "ds_read_b128 v[72:75], v3 offset:2048\n" \
  "ds_read_b128 v[76:79], v3 offset:3072\n" \
  "ds_read_b128 v[80:83], v3 offset:4096\n" \
  "ds_read_b128 v[84:87], v3 offset:5120\n" \
  "s_waitcnt lgkmcnt(4)\n" \
  "v_mfma_f32_32x32x16_bf16 v[32:47], a[0:3], v[64:67], 0\n" \
  "ds_read_b128 v[88:91], v3 offset:6144\n" \
  "s_add_u32 m0, s48, 0\n" \
  "s_nop 0\n" \
  "buffer_load_dwordx4 v1, s[36:39], s57 offen sc1 lds\n" \
  "v_mfma_f32_32x32x16_bf16 v[32:47], a[4:7], v[68:71], v[32:47]\n" \
  "ds_read_b128 v[92:95], v3 offset:7168\n" \
  "s_waitcnt lgkmcnt(4)\n" \
  "v_mfma_f32_32x32x16_bf16 v[32:47], a[8:11], v[72:75], v[32:47]\n" \
  "ds_read_b128 v[64:67], v3 offset:8192\n" \
  "buffer_load_dwordx4 v1, s[36:39], s57 offen offset:1024 sc1 lds\n" \
  "v_mfma_f32_32x32x16_bf16 v[32:47], a[12:15], v[76:79], v[32:47]\n" \
  "ds_read_b128 v[68:71], v3 offset:9216\n" \
  "s_waitcnt lgkmcnt(4)\n" \
  "v_mfma_f32_32x32x16_bf16 v[32:47], a[16:19], v[80:83], v[32:47]\n" \
  "ds_read_b128 v[72:75], v3 offset:10240\n" \
  "s_add_u32 m0, s59, 49152\n" \
  "s_add_u32 s51, s56, 98304\n" \
  "buffer_load_dwordx4 v0, s[36:39], s51 offen sc1 lds\n" \
  "v_mfma_f32_32x32x16_bf16 v[32:47], a[20:23], v[84:87], v[32:47]\n" \
  "ds_read_b128 v[76:79], v3 offset:11264\n" \
  "s_waitcnt lgkmcnt(4)\n" \
  "v_mfma_f32_32x32x16_bf16 v[32:47], a[24:27], v[88:91], v[32:47]\n" \
  "ds_read_b128 v[80:83], v3 offset:12288\n" \
  "buffer_load_dwordx4 v0, s[36:39], s51 offen offset:1024 sc1 lds\n" \
  "v_mfma_f32_32x32x16_bf16 v[32:47], a[28:31], v[92:95], v[32:47]\n" \
  "ds_read_b128 v[84:87], v3 offset:13312\n" \
  "s_waitcnt lgkmcnt(4)\n" \
  "v_mfma_f32_32x32x16_bf16 v[32:47], a[32:35], v[64:67], v[32:47]\n" \
  "ds_read_b128 v[88:91], v3 offset:14336\n" \
  "buffer_load_dwordx4 v0, s[36:39], s51 offen offset:2048 sc1 lds\n" \
  "v_mfma_f32_32x32x16_bf16 v[32:47], a[36:39], v[68:71], v[32:47]\n" \
  "ds_read_b128 v[92:95], v3 offset:15360\n" \
  "s_waitcnt lgkmcnt(4)\n" \
  "v_mfma_f32_32x32x16_bf16 v[32:47], a[40:43], v[72:75], v[32:47]\n" \
  "s_waitcnt vmcnt(9)\n" \
  "s_barrier\n" \
  "ds_read_b128 v[64:67], v3 offset:16384\n" \
  "ds_read_b32 v14, v6\n" \
  "buffer_load_dwordx4 v0, s[36:39], s51 offen offset:3072 sc1 lds\n" \
  "v_mfma_f32_32x32x16_bf16 v[32:47], a[44:47], v[76:79], v[32:47]\n" \
  "ds_read_b128 v[68:71], v3 offset:17408\n" \
  "s_waitcnt lgkmcnt(5)\n" \
  "v_mfma_f32_32x32x16_bf16 v[32:47], a[48:51], v[80:83], v[32:47]\n" \
  "ds_read_b128 v[72:75], v3 offset:18432\n" \
  "v_mfma_f32_32x32x16_bf16 v[32:47], a[52:55], v[84:87], v[32:47]\n" \
  "ds_read_b128 v[76:79], v3 offset:19456\n" \
  "s_waitcnt lgkmcnt(5)\n" \
  "v_mfma_f32_32x32x16_bf16 v[32:47], a[56:59], v[88:91], v[32:47]\n" \
  "ds_read_b128 v[80:83], v3 offset:20480\n" \
  "s_waitcnt lgkmcnt(4)\n" \
  "v_cmp_gt_u32 vcc, s50, v14\n" \
  "v_mfma_f32_32x32x16_bf16 v[32:47], a[60:63], v[92:95], v[32:47]\n" \
  "ds_read_b128 v[84:87], v3 offset:21504\n" \
  "s_cmp_eq_u64 vcc, 0\n" \
  "s_cselect_b32 s42, s38, 0\n" \
  "s_waitcnt lgkmcnt(4)\n" \
  "v_mfma_f32_32x32x16_bf16 v[32:47], a[64:67], v[64:67], v[32:47]\n" \
  "ds_read_b128 v[88:91], v3 offset:22528\n" \
  "v_mfma_f32_32x32x16_bf16 v[32:47], a[68:71], v[68:71], v[32:47]\n" \
  "ds_read_b128 v[92:95], v3 offset:23552\n" \
  "s_add_u32 m0, s47, 0\n" \
  "s_nop 0\n" \
  "buffer_load_dwordx4 v0, s[40:43], s44 offen sc1 lds\n" \
  "s_waitcnt lgkmcnt(4)\n" \
  "v_mfma_f32_32x32x16_bf16 v[32:47], a[72:75], v[72:75], v[32:47]\n" \
  "ds_read_b128 v[64:67], v3 offset:24576\n" \
  "v_mfma_f32_32x32x16_bf16 v[32:47], a[76:79], v[76:79], v[32:47]\n" \
  "ds_read_b128 v[68:71], v3 offset:25600\n" \
  "buffer_load_dwordx4 v0, s[40:43], s44 offen offset:1024 sc1 lds\n" \
  "s_waitcnt lgkmcnt(4)\n" \
  "v_mfma_f32_32x32x16_bf16 v[32:47], a[80:83], v[80:83], v[32:47]\n" \
  "ds_read_b128 v[72:75], v3 offset:26624\n" \
  "v_mfma_f32_32x32x16_bf16 v[32:47], a[84:87], v[84:87], v[32:47]\n" \
  "ds_read_b128 v[76:79], v3 offset:27648\n" \
  "s_waitcnt lgkmcnt(4)\n" \
  "v_mfma_f32_32x32x16_bf16 v[32:47], a[88:91], v[88:91], v[32:47]\n" \
  "ds_read_b128 v[80:83], v3 offset:28672\n" \
  "buffer_load_dwordx4 v0, s[40:43], s44 offen offset:2048 sc1 lds\n" \
  "v_mfma_f32_32x32x16_bf16 v[32:47], a[92:95], v[92:95], v[32:47]\n" \
  "ds_read_b128 v[84:87], v3 offset:29696\n" \
  "s_waitcnt lgkmcnt(4)\n" \
  "v_mfma_f32_32x32x16_bf16 v[32:47], a[96:99], v[64:67], v[32:47]\n" \
  "ds_read_b128 v[88:91], v3 offset:30720\n" \
  "v_mfma_f32_32x32x16_bf16 v[32:47], a[100:103], v[68:71], v[32:47]\n" \
  "v_mov_b32 v14, 4\n" \
  "ds_read_b128 v[92:95], v3 offset:31744\n" \
  "buffer_load_dwordx4 v0, s[40:43], s44 offen offset:3072 sc1 lds\n" \
  "s_waitcnt lgkmcnt(4)\n" \
  "v_mfma_f32_32x32x16_bf16 v[32:47], a[104:107], v[72:75], v[32:47]\n" \
  "s_waitcnt vmcnt(8)\n" \
  "s_barrier\n" \
  "ds_read_b128 v[64:67], v3 offset:32768\n" \
  "ds_read2st64_b64 v[96:99], v4 offset0:0 offset1:1\n" \
  "v_mfma_f32_32x32x16_bf16 v[32:47], a[108:111], v[76:79], v[32:47]\n" \
  "ds_read_b128 v[68:71], v3 offset:33792\n" \
  "ds_read2st64_b64 v[100:103], v4 offset0:2 offset1:3\n" \
  "s_waitcnt lgkmcnt(6)\n" \
  "v_mfma_f32_32x32x16_bf16 v[32:47], a[112:115], v[80:83], v[32:47]\n" \
  "ds_read_b128 v[72:75], v3 offset:34816\n" \
  "ds_read2st64_b64 v[104:107], v4 offset0:4 offset1:5\n" \
  "s_xor_b32 m0, s49, 0\n" \
  "s_nop 0\n" \
  "buffer_load_dword v7, s[40:43], 0 offen sc1 lds\n" \
  "v_mfma_f32_32x32x16_bf16 v[32:47], a[116:119], v[84:87], v[32:47]\n" \
  "ds_read_b128 v[76:79], v3 offset:35840\n" \
  "ds_read2st64_b64 v[108:111], v4 offset0:6 offset1:7\n" \
  "s_waitcnt lgkmcnt(8)\n" \
  "v_mfma_f32_32x32x16_bf16 v[32:47], a[120:123], v[88:91], v[32:47]\n" \
  "ds_read_b128 v[80:83], v3 offset:36864\n" \
  "ds_read2st64_b64 v[112:115], v4 offset0:8 offset1:9\n" \
  "s_add_u32 m0, s47, 16384\n" \
  "s_add_u32 s51, s44, 32768\n" \
  "buffer_load_dwordx4 v0, s[40:43], s51 offen sc1 lds\n" \
  "v_mfma_f32_32x32x16_bf16 v[32:47], a[124:127], v[92:95], v[32:47]\n" \
  "ds_read_b128 v[84:87], v3 offset:37888\n" \
  "ds_read2st64_b64 v[116:119], v4 offset0:10 offset1:11\n" \
  "s_waitcnt lgkmcnt(9)\n" \
  "v_mfma_f32_32x32x16_bf16 v[32:47], a[128:131], v[64:67], v[32:47]\n" \
  "ds_read_b128 v[88:91], v3 offset:38912\n" \
  "ds_read2st64_b64 v[120:123], v4 offset0:12 offset1:13\n" \
  "v_mfma_f32_32x32x16_bf16 v[32:47], a[132:135], v[68:71], v[32:47]\n" \
  "ds_read_b128 v[92:95], v3 offset:39936\n" \
  "ds_read2st64_b64 v[124:127], v4 offset0:14 offset1:15\n" \
  "v_add_f32 v10, v96, v98\n" \
  "v_add_f32 v11, v97, v99\n" \
  "buffer_load_dwordx4 v0, s[40:43], s51 offen offset:1024 sc1 lds\n" \
  "s_waitcnt lgkmcnt(9)\n" \
  "v_mfma_f32_32x32x16_bf16 v[32:47], a[136:139], v[72:75], v[32:47]\n" \
  "ds_read_b128 v[64:67], v3 offset:40960\n" \
  "v_add_f32 v10, v10, v100\n" \
  "v_add_f32 v11, v11, v101\n" \
  "v_add_f32 v10, v10, v102\n" \
  "v_mfma_f32_32x32x16_bf16 v[32:47], a[140:143], v[76:79], v[32:47]\n" \
  "ds_read_b128 v[68:71], v3 offset:41984\n" \
  "v_add_f32 v11, v11, v103\n" \
  "v_add_f32 v10, v10, v104\n" \
  "v_add_f32 v11, v11, v105\n" \
  "s_waitcnt lgkmcnt(7)\n" \
  "v_mfma_f32_32x32x16_bf16 v[32:47], a[144:147], v[80:83], v[32:47]\n" \
  "ds_read_b128 v[72:75], v3 offset:43008\n" \
  "v_add_f32 v10, v10, v106\n" \
  "v_add_f32 v11, v11, v107\n" \
  "buffer_load_dwordx4 v0, s[40:43], s51 offen offset:2048 sc1 lds\n" \
  "v_mfma_f32_32x32x16_bf16 v[32:47], a[148:151], v[84:87], v[32:47]\n" \
  "ds_read_b128 v[76:79], v3 offset:44032\n" \
  "v_add_f32 v10, v10, v108\n" \
  "v_add_f32 v11, v11, v109\n" \
  "v_add_f32 v10, v10, v110\n" \
  "s_waitcnt lgkmcnt(5)\n" \
  "v_mfma_f32_32x32x16_bf16 v[32:47], a[152:155], v[88:91], v[32:47]\n" \
  "ds_read_b128 v[80:83], v3 offset:45056\n" \
  "v_add_f32 v11, v11, v111\n" \
  "v_add_f32 v10, v10, v112\n" \
  "v_add_f32 v11, v11, v113\n" \
  "v_mfma_f32_32x32x16_bf16 v[32:47], a[156:159], v[92:95], v[32:47]\n" \
  "ds_read_b128 v[84:87], v3 offset:46080\n" \
  "v_add_f32 v10, v10, v114\n" \
  "buffer_load_dwordx4 v0, s[40:43], s51 offen offset:3072 sc1 lds\n" \
  "s_waitcnt lgkmcnt(4)\n" \
  "v_mfma_f32_32x32x16_bf16 v[32:47], a[160:163], v[64:67], v[32:47]\n" \
  "ds_read_b128 v[88:91], v3 offset:47104\n" \
  "v_add_f32 v11, v11, v115\n" \
  "v_add_f32 v10, v10, v116\n" \
  "v_mfma_f32_32x32x16_bf16 v[32:47], a[164:167], v[68:71], v[32:47]\n" \
  "ds_read_b128 v[92:95], v3 offset:48128\n" \
  "v_add_f32 v11, v11, v117\n" \
  "v_add_f32 v10, v10, v118\n" \
  "s_waitcnt lgkmcnt(4)\n" \
  "v_mfma_f32_32x32x16_bf16 v[32:47], a[168:171], v[72:75], v[32:47]\n" \
  "s_waitcnt vmcnt(9)\n" \
  "s_barrier\n" \
  "ds_read_b128 v[64:67], v3 offset:49152\n" \
  "v_add_f32 v11, v11, v119\n" \
  "v_add_f32 v10, v10, v120\n" \
  "v_mfma_f32_32x32x16_bf16 v[32:47], a[172:175], v[76:79], v[32:47]\n" \
  "ds_read_b128 v[68:71], v3 offset:50176\n" \
  "v_add_f32 v11, v11, v121\n" \
  "s_add_u32 m0, s48, 0\n" \
  "s_nop 0\n" \
  "buffer_load_dwordx4 v1, s[40:43], s45 offen sc1 lds\n" \
  "s_waitcnt lgkmcnt(4)\n" \
  "v_mfma_f32_32x32x16_bf16 v[32:47], a[176:179], v[80:83], v[32:47]\n" \
  "ds_read_b128 v[72:75], v3 offset:51200\n" \
  "v_add_f32 v10, v10, v122\n" \
  "v_add_f32 v11, v11, v123\n" \
  "v_mfma_f32_32x32x16_bf16 v[32:47], a[180:183], v[84:87], v[32:47]\n" \
  "ds_read_b128 v[76:79], v3 offset:52224\n" \
  "v_add_f32 v10, v10, v124\n" \
  "buffer_load_dwordx4 v1, s[40:43], s45 offen offset:1024 sc1 lds\n" \
  "s_waitcnt lgkmcnt(4)\n" \
  "v_mfma_f32_32x32x16_bf16 v[32:47], a[184:187], v[88:91], v[32:47]\n" \
  "ds_read_b128 v[80:83], v3 offset:53248\n" \
  "v_add_f32 v11, v11, v125\n" \
  "v_add_f32 v10, v10, v126\n" \
  "v_mfma_f32_32x32x16_bf16 v[32:47], a[188:191], v[92:95], v[32:47]\n" \
  "ds_read_b128 v[84:87], v3 offset:54272\n" \
  "v_add_f32 v11, v11, v127\n" \
  "s_add_u32 m0, s47, 32768\n" \
  "s_add_u32 s51, s44, 65536\n" \
  "buffer_load_dwordx4 v0, s[40:43], s51 offen sc1 lds\n" \
  "s_waitcnt lgkmcnt(4)\n" \
  "v_mfma_f32_32x32x16_bf16 v[32:47], a[192:195], v[64:67], v[32:47]\n" \
  "ds_read_b128 v[88:91], v3 offset:55296\n" \
  "v_mul_f32 v10, 0x3a800000, v10\n" \
  "v_mul_f32 v11, 0x3a800000, v11\n" \
  "v_mfma_f32_32x32x16_bf16 v[32:47], a[196:199], v[68:71], v[32:47]\n" \
  "ds_read_b128 v[92:95], v3 offset:56320\n" \
  "v_mov_b32 v13, v10\n" \
  "buffer_load_dwordx4 v0, s[40:43], s51 offen offset:1024 sc1 lds\n" \
  "s_waitcnt lgkmcnt(4)\n" \
  "v_mfma_f32_32x32x16_bf16 v[32:47], a[200:203], v[72:75], v[32:47]\n" \
  "ds_read_b128 v[64:67], v3 offset:57344\n" \
  "v_mov_b32 v12, v11\n" \
  "v_permlane32_swap_b32 v13, v10\n" \
  "v_mfma_f32_32x32x16_bf16 v[32:47], a[204:207], v[76:79], v[32:47]\n" \
  "ds_read_b128 v[68:71], v3 offset:58368\n" \
  "v_permlane32_swap_b32 v12, v11\n" \
  "buffer_load_dwordx4 v0, s[40:43], s51 offen offset:2048 sc1 lds\n" \
  "s_waitcnt lgkmcnt(4)\n" \
  "v_mfma_f32_32x32x16_bf16 v[32:47], a[208:211], v[80:83], v[32:47]\n" \
  "ds_read_b128 v[72:75], v3 offset:59392\n" \
  "v_add_f32 v10, v13, v10\n" \
  "v_add_f32 v11, v12, v11\n" \
  "v_mfma_f32_32x32x16_bf16 v[32:47], a[212:215], v[84:87], v[32:47]\n" \
  "ds_read_b128 v[76:79], v3 offset:60416\n" \
  "v_fma_f32 v11, -v10, v10, v11\n" \
  "buffer_load_dwordx4 v0, s[40:43], s51 offen offset:3072 sc1 lds\n" \
  "s_waitcnt lgkmcnt(4)\n" \
  "v_mfma_f32_32x32x16_bf16 v[32:47], a[216:219], v[88:91], v[32:47]\n" \
  "ds_read_b128 v[80:83], v3 offset:61440\n" \
  "v_max_f32 v11, 0, v11\n" \
  "v_add_f32 v13, 0x3727c5ac, v11\n" \
  "v_mfma_f32_32x32x16_bf16 v[32:47], a[220:223], v[92:95], v[32:47]\n" \
  "ds_read_b128 v[84:87], v3 offset:62464\n" \
  "v_rsq_f32 v12, v13\n" \
  "s_add_u32 m0, s47, 49152\n" \
  "s_add_u32 s51, s44, 98304\n" \
  "buffer_load_dwordx4 v0, s[40:43], s51 offen sc1 lds\n" \
  "s_waitcnt lgkmcnt(4)\n" \
  "v_mfma_f32_32x32x16_bf16 v[32:47], a[224:227], v[64:67], v[32:47]\n" \
  "ds_read_b128 v[88:91], v3 offset:63488\n" \
  "v_mul_f32 v11, v11, v12\n" \
  "v_fmaak_f32 v13, v11, v12, 0x3727c5ac\n" \
  "v_mfma_f32_32x32x16_bf16 v[32:47], a[228:231], v[68:71], v[32:47]\n" \
  "ds_read_b128 v[92:95], v3 offset:64512\n" \
  "v_rsq_f32 v13, v13\n" \
  "buffer_load_dwordx4 v0, s[40:43], s51 offen offset:1024 sc1 lds\n" \
  "s_waitcnt lgkmcnt(4)\n" \
  "v_mfma_f32_32x32x16_bf16 v[32:47], a[232:235], v[72:75], v[32:47]\n" \
  "v_mul_f32 v12, v12, v13\n" \
  "v_mul_f32 v10, -v10, v12\n" \
  "v_mul_f32 v48, v10, v16\n" \
  "v_mul_f32 v49, v10, v17\n" \
  "v_mfma_f32_32x32x16_bf16 v[32:47], a[236:239], v[76:79], v[32:47]\n" \
  "v_mul_f32 v50, v10, v18\n" \
  "buffer_load_dwordx4 v0, s[40:43], s51 offen offset:2048 sc1 lds\n" \
  "s_waitcnt lgkmcnt(2)\n" \
  "v_mfma_f32_32x32x16_bf16 v[32:47], a[240:243], v[80:83], v[32:47]\n" \
  "v_mul_f32 v51, v10, v19\n" \
  "v_mul_f32 v52, v10, v20\n" \
  "v_mul_f32 v53, v10, v21\n" \
  "v_mul_f32 v54, v10, v22\n" \
  "v_mfma_f32_32x32x16_bf16 v[32:47], a[244:247], v[84:87], v[32:47]\n" \
  "v_mul_f32 v55, v10, v23\n" \
  "buffer_load_dwordx4 v0, s[40:43], s51 offen offset:3072 sc1 lds\n" \
  "s_waitcnt lgkmcnt(0)\n" \
  "v_mfma_f32_32x32x16_bf16 v[32:47], a[248:251], v[88:91], v[32:47]\n" \
  "v_mul_f32 v56, v10, v24\n" \
  "v_mul_f32 v57, v10, v25\n" \
  "v_mul_f32 v58, v10, v26\n" \
  "v_mul_f32 v59, v10, v27\n" \
  "v_mfma_f32_32x32x16_bf16 v[32:47], a[252:255], v[92:95], v[32:47]\n" \
  "v_mul_f32 v60, v10, v28\n" \
  "v_mul_f32 v61, v10, v29\n" \
  "v_mul_f32 v62, v10, v30\n" \
  "v_mul_f32 v63, v10, v31\n" \
  "s_cmp_lg_u32 s42, 0\n" \
  "s_nop 6\n" \
  "v_pk_fma_f32 v[144:145], v[12:13], v[32:33], v[48:49] op_sel_hi:[0,1,1]\n" \
  "v_pk_fma_f32 v[146:147], v[12:13], v[34:35], v[50:51] op_sel_hi:[0,1,1]\n" \
  "v_pk_fma_f32 v[148:149], v[12:13], v[36:37], v[52:53] op_sel_hi:[0,1,1]\n" \
  "v_pk_fma_f32 v[150:151], v[12:13], v[38:39], v[54:55] op_sel_hi:[0,1,1]\n" \
  "v_pk_fma_f32 v[152:153], v[12:13], v[40:41], v[56:57] op_sel_hi:[0,1,1]\n" \
  "v_pk_fma_f32 v[154:155], v[12:13], v[42:43], v[58:59] op_sel_hi:[0,1,1]\n" \
  "v_pk_fma_f32 v[156:157], v[12:13], v[44:45], v[60:61] op_sel_hi:[0,1,1]\n" \
  "v_pk_fma_f32 v[158:159], v[12:13], v[46:47], v[62:63] op_sel_hi:[0,1,1]\n" \
  "s_cbranch_scc1 SPL1_e_%=\n" \
  "v_mov_b32 v176, 0xbdd2d3e7\n" \
  "v_mov_b32 v178, 0xc0135761\n" \
  "v_pk_mul_f32 v[160:161], v[144:145], v[144:145]\n" \
  "v_pk_mul_f32 v[162:163], v[146:147], v[146:147]\n" \
  "v_pk_mul_f32 v[164:165], v[148:149], v[148:149]\n" \
  "v_pk_mul_f32 v[166:167], v[150:151], v[150:151]\n" \
  "v_pk_fma_f32 v[160:161], v[160:161], v[176:177], v[178:179] op_sel_hi:[1,0,0]\n" \
  "v_pk_fma_f32 v[162:163], v[162:163], v[176:177], v[178:179] op_sel_hi:[1,0,0]\n" \
  "v_pk_fma_f32 v[164:165], v[164:165], v[176:177], v[178:179] op_sel_hi:[1,0,0]\n" \
  "v_pk_fma_f32 v[166:167], v[166:167], v[176:177], v[178:179] op_sel_hi:[1,0,0]\n" \
  "v_pk_mul_f32 v[160:161], v[160:161], v[144:145]\n" \
  "v_pk_mul_f32 v[162:163], v[162:163], v[146:147]\n" \
  "v_pk_mul_f32 v[164:165], v[164:165], v[148:149]\n" \
  "v_pk_mul_f32 v[166:167], v[166:167], v[150:151]\n" \
  "v_exp_f32 v160, v160\n" \
  "v_exp_f32 v161, v161\n" \
  "v_exp_f32 v162, v162\n" \
  "v_pk_add_f32 v[160:161], v[160:161], 1.0 op_sel_hi:[1,0]\n" \
  "v_exp_f32 v163, v163\n" \
  "v_exp_f32 v164, v164\n" \
  "v_pk_add_f32 v[162:163], v[162:163], 1.0 op_sel_hi:[1,0]\n" \
  "v_exp_f32 v165, v165\n" \
  "v_exp_f32 v166, v166\n" \
  "v_pk_add_f32 v[164:165], v[164:165], 1.0 op_sel_hi:[1,0]\n" \
  "v_exp_f32 v167, v167\n" \
  "v_rcp_f32 v160, v160\n" \
  "v_pk_add_f32 v[166:167], v[166:167], 1.0 op_sel_hi:[1,0]\n" \
  "v_rcp_f32 v161, v161\n" \
  "v_rcp_f32 v162, v162\n" \
  "v_pk_mul_f32 v[144:145], v[144:145], v[160:161]\n" \
  "v_rcp_f32 v163, v163\n" \
  "v_cvt_pk_bf16_f32 v168, v144, v145\n" \
  "v_rcp_f32 v164, v164\n" \
  "v_pk_mul_f32 v[146:147], v[146:147], v[162:163]\n" \
  "v_rcp_f32 v165, v165\n" \
  "v_cvt_pk_bf16_f32 v169, v146, v147\n" \
  "v_rcp_f32 v166, v166\n" \
  "v_pk_mul_f32 v[148:149], v[148:149], v[164:165]\n" \
  "v_rcp_f32 v167, v167\n" \
  "v_cvt_pk_bf16_f32 v170, v148, v149\n" \
  "v_pk_mul_f32 v[150:151], v[150:151], v[166:167]\n" \
  "v_cvt_pk_bf16_f32 v171, v150, v151\n" \
  "buffer_store_dwordx4 v[168:171], v15, s[36:39], s52 offen sc1\n" \
  "v_pk_mul_f32 v[160:161], v[152:153], v[152:153]\n" \
  "v_pk_mul_f32 v[162:163], v[154:155], v[154:155]\n" \
  "v_pk_mul_f32 v[164:165], v[156:157], v[156:157]\n" \
  "v_pk_mul_f32 v[166:167], v[158:159], v[158:159]\n" \
  "v_pk_fma_f32 v[160:161], v[160:161], v[176:177], v[178:179] op_sel_hi:[1,0,0]\n" \
  "v_pk_fma_f32 v[162:163], v[162:163], v[176:177], v[178:179] op_sel_hi:[1,0,0]\n" \
  "v_pk_fma_f32 v[164:165], v[164:165], v[176:177], v[178:179] op_sel_hi:[1,0,0]\n" \
  "v_pk_fma_f32 v[166:167], v[166:167], v[176:177], v[178:179] op_sel_hi:[1,0,0]\n" \
  "v_pk_mul_f32 v[160:161], v[160:161], v[152:153]\n" \
  "v_pk_mul_f32 v[162:163], v[162:163], v[154:155]\n" \
  "v_pk_mul_f32 v[164:165], v[164:165], v[156:157]\n" \
  "v_pk_mul_f32 v[166:167], v[166:167], v[158:159]\n" \
  "v_exp_f32 v160, v160\n" \
  "v_exp_f32 v161, v161\n" \
  "v_exp_f32 v162, v162\n" \
  "v_pk_add_f32 v[160:161], v[160:161], 1.0 op_sel_hi:[1,0]\n" \
  "v_exp_f32 v163, v163\n" \
  "v_exp_f32 v164, v164\n" \
  "v_pk_add_f32 v[162:163], v[162:163], 1.0 op_sel_hi:[1,0]\n" \
  "v_exp_f32 v165, v165\n" \
  "v_exp_f32 v166, v166\n" \
  "v_pk_add_f32 v[164:165], v[164:165], 1.0 op_sel_hi:[1,0]\n" \
  "v_exp_f32 v167, v167\n" \
  "v_rcp_f32 v160, v160\n" \
  "v_pk_add_f32 v[166:167], v[166:167], 1.0 op_sel_hi:[1,0]\n" \
  "v_rcp_f32 v161, v161\n" \
  "v_rcp_f32 v162, v162\n" \
  "v_pk_mul_f32 v[152:153], v[152:153], v[160:161]\n" \
  "v_rcp_f32 v163, v163\n" \
  "v_cvt_pk_bf16_f32 v172, v152, v153\n" \
  "v_rcp_f32 v164, v164\n" \
  "v_pk_mul_f32 v[154:155], v[154:155], v[162:163]\n" \
  "v_rcp_f32 v165, v165\n" \
  "v_cvt_pk_bf16_f32 v173, v154, v155\n" \
  "v_rcp_f32 v166, v166\n" \
  "v_pk_mul_f32 v[156:157], v[156:157], v[164:165]\n" \
  "v_rcp_f32 v167, v167\n" \
  "v_cvt_pk_bf16_f32 v174, v156, v157\n" \
  "v_pk_mul_f32 v[158:159], v[158:159], v[166:167]\n" \
  "v_cvt_pk_bf16_f32 v175, v158, v159\n" \
  "buffer_store_dwordx4 v[172:175], v15, s[36:39], s52 offen offset:1024 sc1\n" \
  "SPL1_e_%=:\n" \
  "s_nop 1\n"
#define SPM2_ASM \
  "s_mov_b64 s[40:41], s[36:37]\n" \
  "s_mov_b32 s43, s39\n" \
  "s_waitcnt vmcnt(16)\n" \
  "s_barrier\n" \
  "ds_read_b128 v[64:67], v3 offset:0\n" \
  "ds_read_b128 v[68:71], v3 offset:1024\n" \
  "ds_read_b32 v14, v6\n" \
  "ds_read_b128 v[72:75], v3 offset:2048\n" \
  "ds_read_b128 v[76:79], v3 offset:3072\n" \
  "ds_read_b128 v[80:83], v3 offset:4096\n" \
  "ds_read_b128 v[84:87], v3 offset:5120\n" \
  "s_waitcnt lgkmcnt(5)\n" \
  "v_mfma_f32_32x32x16_bf16 v[32:47], a[0:3], v[64:67], 0\n" \
  "ds_read_b128 v[88:91], v3 offset:6144\n" \
  "v_cvt_pk_bf16_f32 v168, v144, v145\n" \
  "v_cvt_pk_bf16_f32 v169, v146, v147\n" \
  "v_cvt_pk_bf16_f32 v170, v148, v149\n" \
  "v_mfma_f32_32x32x16_bf16 v[32:47], a[4:7], v[68:71], v[32:47]\n" \
  "ds_read_b128 v[92:95], v3 offset:7168\n" \
  "v_cvt_pk_bf16_f32 v171, v150, v151\n" \
  "buffer_store_dwordx4 v[168:171], v142, s[36:39], s54 offen sc1\n" \
  "v_cvt_pk_bf16_f32 v172, v152, v153\n" \
  "s_waitcnt lgkmcnt(4)\n" \
  "v_mfma_f32_32x32x16_bf16 v[32:47], a[8:11], v[72:75], v[32:47]\n" \
  "ds_read_b128 v[64:67], v3 offset:8192\n" \
  "v_cmp_gt_u32 vcc, s50, v14\n" \
  "v_cvt_pk_bf16_f32 v173, v154, v155\n" \
  "v_cvt_pk_bf16_f32 v174, v156, v157\n" \
  "v_cvt_pk_bf16_f32 v175, v158, v159\n" \
  "buffer_store_dwordx4 v[172:175], v142, s[36:39], s54 offen offset:1024 sc1\n" \
  "v_mfma_f32_32x32x16_bf16 v[32:47], a[12:15], v[76:79], v[32:47]\n" \
  "ds_read_b128 v[68:71], v3 offset:9216\n" \
  "s_cmp_eq_u64 vcc, 0\n" \
  "s_cselect_b32 s42, s38, 0\n" \
  "v_lshlrev_b32 v144, 16, v168\n" \
  "v_and_b32 v145, 0xffff0000, v168\n" \
  "v_lshlrev_b32 v146, 16, v169\n" \
  "s_waitcnt lgkmcnt(4)\n" \
  "v_mfma_f32_32x32x16_bf16 v[32:47], a[16:19], v[80:83], v[32:47]\n" \
  "ds_read_b128 v[72:75], v3 offset:10240\n" \
  "v_and_b32 v147, 0xffff0000, v169\n" \
  "s_add_u32 m0, s47, 0\n" \
  "s_nop 0\n" \
  "buffer_load_dwordx4 v0, s[40:43], s44 offen sc1 lds\n" \
  "v_mfma_f32_32x32x16_bf16 v[32:47], a[20:23], v[84:87], v[32:47]\n" \
  "ds_read_b128 v[76:79], v3 offset:11264\n" \
  "v_lshlrev_b32 v148, 16, v170\n" \
  "v_and_b32 v149, 0xffff0000, v170\n" \
  "v_lshlrev_b32 v150, 16, v171\n" \
  "s_waitcnt lgkmcnt(4)\n" \
  "v_mfma_f32_32x32x16_bf16 v[32:47], a[24:27], v[88:91], v[32:47]\n" \
  "ds_read_b128 v[80:83], v3 offset:12288\n" \
  "v_and_b32 v151, 0xffff0000, v171\n" \
  "v_lshlrev_b32 v152, 16, v172\n" \
  "buffer_load_dwordx4 v0, s[40:43], s44 offen offset:1024 sc1 lds\n" \
  "v_mfma_f32_32x32x16_bf16 v[32:47], a[28:31], v[92:95], v[32:47]\n" \
  "ds_read_b128 v[84:87], v3 offset:13312\n" \
  "v_and_b32 v153, 0xffff0000, v172\n" \
  "v_lshlrev_b32 v154, 16, v173\n" \
  "v_and_b32 v155, 0xffff0000, v173\n" \
  "s_waitcnt lgkmcnt(4)\n" \
  "v_mfma_f32_32x32x16_bf16 v[32:47], a[32:35], v[64:67], v[32:47]\n" \
  "ds_read_b128 v[88:91], v3 offset:14336\n" \
  "v_lshlrev_b32 v156, 16, v174\n" \
  "v_and_b32 v157, 0xffff0000, v174\n" \
  "buffer_load_dwordx4 v0, s[40:43], s44 offen offset:2048 sc1 lds\n" \
  "v_mfma_f32_32x32x16_bf16 v[32:47], a[36:39], v[68:71], v[32:47]\n" \
  "ds_read_b128 v[92:95], v3 offset:15360\n" \
  "v_lshlrev_b32 v158, 16, v175\n" \
  "v_and_b32 v159, 0xffff0000, v175\n" \
  "v_add_f32 v160, v144, v145\n" \
  "s_waitcnt lgkmcnt(4)\n" \
  "v_mfma_f32_32x32x16_bf16 v[32:47], a[40:43], v[72:75], v[32:47]\n" \
  "s_waitcnt vmcnt(13)\n" \
  "s_barrier\n" \
  "ds_read_b128 v[64:67], v3 offset:16384\n" \
  "ds_read2st64_b64 v[96:99], v4 offset0:0 offset1:1\n" \
  "v_mul_f32 v161, v144, v144\n" \
  "v_fmac_f32 v161, v145, v145\n" \
  "v_mfma_f32_32x32x16_bf16 v[32:47], a[44:47], v[76:79], v[32:47]\n" \
  "ds_read_b128 v[68:71], v3 offset:17408\n" \
  "ds_read2st64_b64 v[100:103], v4 offset0:2 offset1:3\n" \
  "buffer_load_dwordx4 v0, s[40:43], s44 offen offset:3072 sc1 lds\n" \
  "s_waitcnt lgkmcnt(6)\n" \
  "v_mfma_f32_32x32x16_bf16 v[32:47], a[48:51], v[80:83], v[32:47]\n" \
  "ds_read_b128 v[72:75], v3 offset:18432\n" \
  "ds_read2st64_b64 v[104:107], v4 offset0:4 offset1:5\n" \
  "v_add_f32 v160, v160, v146\n" \
  "v_fmac_f32 v161, v146, v146\n" \
  "v_mfma_f32_32x32x16_bf16 v[32:47], a[52:55], v[84:87], v[32:47]\n" \
  "ds_read_b128 v[76:79], v3 offset:19456\n" \
  "ds_read2st64_b64 v[108:111], v4 offset0:6 offset1:7\n" \
  "v_add_f32 v160, v160, v147\n" \
  "s_xor_b32 m0, s49, 0\n" \
  "s_nop 0\n" \
  "buffer_load_dword v7, s[40:43], 0 offen sc1 lds\n" \
  "s_waitcnt lgkmcnt(8)\n" \
  "v_mfma_f32_32x32x16_bf16 v[32:47], a[56:59], v[88:91], v[32:47]\n" \
  "ds_read_b128 v[80:83], v3 offset:20480\n" \
  "ds_read2st64_b64 v[112:115], v4 offset0:8 offset1:9\n" \
  "v_fmac_f32 v161, v147, v147\n" \
  "v_add_f32 v160, v160, v148\n" \
  "v_mfma_f32_32x32x16_bf16 v[32:47], a[60:63], v[92:95], v[32:47]\n" \
  "ds_read_b128 v[84:87], v3 offset:21504\n" \
  "ds_read2st64_b64 v[116:119], v4 offset0:10 offset1:11\n" \
  "v_fmac_f32 v161, v148, v148\n" \
  "v_add_f32 v160, v160, v149\n" \
  "s_waitcnt lgkmcnt(9)\n" \
  "v_mfma_f32_32x32x16_bf16 v[32:47], a[64:67], v[64:67], v[32:47]\n" \
  "ds_read_b128 v[88:91], v3 offset:22528\n" \
  "ds_read2st64_b64 v[120:123], v4 offset0:12 offset1:13\n" \
  "s_add_u32 m0, s47, 16384\n" \
  "s_add_u32 s51, s44, 32768\n" \
  "buffer_load_dwordx4 v0, s[40:43], s51 offen sc1 lds\n" \
  "v_mfma_f32_32x32x16_bf16 v[32:47], a[68:71], v[68:71], v[32:47]\n" \
  "ds_read_b128 v[92:95], v3 offset:23552\n" \
  "ds_read2st64_b64 v[124:127], v4 offset0:14 offset1:15\n" \
  "v_fmac_f32 v161, v149, v149\n" \
  "v_add_f32 v160, v160, v150\n" \
  "s_waitcnt lgkmcnt(9)\n" \
  "v_mfma_f32_32x32x16_bf16 v[32:47], a[72:75], v[72:75], v[32:47]\n" \
  "ds_read_b128 v[64:67], v3 offset:24576\n" \
  "ds_read_b128 v[128:131], v5\n" \
  "buffer_load_dwordx4 v0, s[40:43], s51 offen offset:1024 sc1 lds\n" \
  "v_mfma_f32_32x32x16_bf16 v[32:47], a[76:79], v[76:79], v[32:47]\n" \
  "ds_read_b128 v[68:71], v3 offset:25600\n" \
  "ds_read_b128 v[132:135], v5 offset:1024\n" \
  "v_fmac_f32 v161, v150, v150\n" \
  "v_add_f32 v160, v160, v151\n" \
  "s_waitcnt lgkmcnt(9)\n" \
  "v_mfma_f32_32x32x16_bf16 v[32:47], a[80:83], v[80:83], v[32:47]\n" \
  "ds_read_b128 v[72:75], v3 offset:26624\n" \
  "v_fmac_f32 v161, v151, v151\n" \
  "v_add_f32 v160, v160, v152\n" \
  "v_fmac_f32 v161, v152, v152\n" \
  "v_add_f32 v160, v160, v153\n" \
  "v_mfma_f32_32x32x16_bf16 v[32:47], a[84:87], v[84:87], v[32:47]\n" \
  "ds_read_b128 v[76:79], v3 offset:27648\n" \
  "v_fmac_f32 v161, v153, v153\n" \
  "buffer_load_dwordx4 v0, s[40:43], s51 offen offset:2048 sc1 lds\n" \
  "s_waitcnt lgkmcnt(7)\n" \
  "v_mfma_f32_32x32x16_bf16 v[32:47], a[88:91], v[88:91], v[32:47]\n" \
  "ds_read_b128 v[80:83], v3 offset:28672\n" \
  "v_add_f32 v160, v160, v154\n" \
  "v_fmac_f32 v161, v154, v154\n" \
  "v_add_f32 v160, v160, v155\n" \
  "v_mfma_f32_32x32x16_bf16 v[32:47], a[92:95], v[92:95], v[32:47]\n" \
  "ds_read_b128 v[84:87], v3 offset:29696\n" \
  "v_fmac_f32 v161, v155, v155\n" \
  "v_add_f32 v160, v160, v156\n" \
  "buffer_load_dwordx4 v0, s[40:43], s51 offen offset:3072 sc1 lds\n" \
  "s_waitcnt lgkmcnt(5)\n" \
  "v_mfma_f32_32x32x16_bf16 v[32:47], a[96:99], v[64:67], v[32:47]\n" \
  "ds_read_b128 v[88:91], v3 offset:30720\n" \
  "v_fmac_f32 v161, v156, v156\n" \
  "v_add_f32 v160, v160, v157\n" \
  "v_fmac_f32 v161, v157, v157\n" \
  "v_mfma_f32_32x32x16_bf16 v[32:47], a[100:103], v[68:71], v[32:47]\n" \
  "v_mov_b32 v14, 4\n" \
  "ds_read_b128 v[92:95], v3 offset:31744\n" \
  "v_add_f32 v160, v160, v158\n" \
  "v_fmac_f32 v161, v158, v158\n" \
  "v_add_f32 v160, v160, v159\n" \
  "s_waitcnt lgkmcnt(4)\n" \
  "v_mfma_f32_32x32x16_bf16 v[32:47], a[104:107], v[72:75], v[32:47]\n" \
  "s_waitcnt vmcnt(11)\n" \
  "s_barrier\n" \
  "buffer_atomic_add v14, v8, s[36:39], 0 offen\n" \
  "ds_read_b128 v[64:67], v3 offset:32768\n" \
  "v_fmac_f32 v161, v159, v159\n" \
  "s_nop 1\n" \
  "v_permlane32_swap_b32 v160, v161\n" \
  "v_add_f32 v160, v160, v161\n" \
  "v_mfma_f32_32x32x16_bf16 v[32:47], a[108:111], v[76:79], v[32:47]\n" \
  "ds_read_b128 v[68:71], v3 offset:33792\n" \
  "v_permlane32_swap_b32 v160, v161\n" \
  "buffer_store_dwordx2 v[160:161], v143, s[36:39], 0 offen sc1\n" \
  "s_add_u32 m0, s48, 0\n" \
  "s_nop 0\n" \
  "buffer_load_dwordx4 v1, s[40:43], s45 offen sc1 lds\n" \
  "s_waitcnt lgkmcnt(4)\n" \
  "v_mfma_f32_32x32x16_bf16 v[32:47], a[112:115], v[80:83], v[32:47]\n" \
  "ds_read_b128 v[72:75], v3 offset:34816\n" \
  "v_add_f32 v10, v96, v98\n" \
  "v_add_f32 v11, v97, v99\n" \
  "v_add_f32 v10, v10, v100\n" \
  "v_mfma_f32_32x32x16_bf16 v[32:47], a[116:119], v[84:87], v[32:47]\n" \
  "ds_read_b128 v[76:79], v3 offset:35840\n" \
  "v_add_f32 v11, v11, v101\n" \
  "v_add_f32 v10, v10, v102\n" \
  "buffer_load_dwordx4 v1, s[40:43], s45 offen offset:1024 sc1 lds\n" \
  "s_waitcnt lgkmcnt(4)\n" \
  "v_mfma_f32_32x32x16_bf16 v[32:47], a[120:123], v[88:91], v[32:47]\n" \
  "ds_read_b128 v[80:83], v3 offset:36864\n" \
  "v_add_f32 v11, v11, v103\n" \
  "v_add_f32 v10, v10, v104\n" \
  "v_mfma_f32_32x32x16_bf16 v[32:47], a[124:127], v[92:95], v[32:47]\n" \
  "ds_read_b128 v[84:87], v3 offset:37888\n" \
  "v_add_f32 v11, v11, v105\n" \
  "s_add_u32 m0, s48, 8192\n" \
  "s_nop 0\n" \
  "buffer_load_dwordx4 v2, s[40:43], s46 offen sc1 lds\n" \
  "s_waitcnt lgkmcnt(4)\n" \
  "v_mfma_f32_32x32x16_bf16 v[32:47], a[128:131], v[64:67], v[32:47]\n" \
  "ds_read_b128 v[88:91], v3 offset:38912\n" \
  "v_add_f32 v10, v10, v106\n" \
  "v_add_f32 v11, v11, v107\n" \
  "v_mfma_f32_32x32x16_bf16 v[32:47], a[132:135], v[68:71], v[32:47]\n" \
  "ds_read_b128 v[92:95], v3 offset:39936\n" \
  "v_add_f32 v10, v10, v108\n" \
  "v_add_f32 v11, v11, v109\n" \
  "s_waitcnt lgkmcnt(4)\n" \
  "v_mfma_f32_32x32x16_bf16 v[32:47], a[136:139], v[72:75], v[32:47]\n" \
  "ds_read_b128 v[64:67], v3 offset:40960\n" \
  "v_add_f32 v10, v10, v110\n" \
  "buffer_load_dwordx4 v2, s[40:43], s46 offen offset:1024 sc1 lds\n" \
  "v_mfma_f32_32x32x16_bf16 v[32:47], a[140:143], v[76:79], v[32:47]\n" \
  "ds_read_b128 v[68:71], v3 offset:41984\n" \
  "v_add_f32 v11, v11, v111\n" \
  "v_add_f32 v10, v10, v112\n" \
  "s_waitcnt lgkmcnt(4)\n" \
  "v_mfma_f32_32x32x16_bf16 v[32:47], a[144:147], v[80:83], v[32:47]\n" \
  "ds_read_b128 v[72:75], v3 offset:43008\n" \
  "v_add_f32 v11, v11, v113\n" \
  "s_add_u32 m0, s47, 32768\n" \
  "s_add_u32 s51, s44, 65536\n" \
  "buffer_load_dwordx4 v0, s[40:43], s51 offen sc1 lds\n" \
  "v_mfma_f32_32x32x16_bf16 v[32:47], a[148:151], v[84:87], v[32:47]\n" \
  "ds_read_b128 v[76:79], v3 offset:44032\n" \
  "v_add_f32 v10, v10, v114\n" \
  "v_add_f32 v11, v11, v115\n" \
  "s_waitcnt lgkmcnt(4)\n" \
  "v_mfma_f32_32x32x16_bf16 v[32:47], a[152:155], v[88:91], v[32:47]\n" \
  "ds_read_b128 v[80:83], v3 offset:45056\n" \
  "v_add_f32 v10, v10, v116\n" \
  "buffer_load_dwordx4 v0, s[40:43], s51 offen offset:1024 sc1 lds\n" \
  "v_mfma_f32_32x32x16_bf16 v[32:47], a[156:159], v[92:95], v[32:47]\n" \
  "ds_read_b128 v[84:87], v3 offset:46080\n" \
  "v_add_f32 v11, v11, v117\n" \
  "v_add_f32 v10, v10, v118\n" \
  "s_waitcnt lgkmcnt(4)\n" \
  "v_mfma_f32_32x32x16_bf16 v[32:47], a[160:163], v[64:67], v[32:47]\n" \
  "ds_read_b128 v[88:91], v3 offset:47104\n" \
  "v_add_f32 v11, v11, v119\n" \
  "v_add_f32 v10, v10, v120\n" \
  "v_mfma_f32_32x32x16_bf16 v[32:47], a[164:167], v[68:71], v[32:47]\n" \
  "ds_read_b128 v[92:95], v3 offset:48128\n" \
  "v_add_f32 v11, v11, v121\n" \
  "buffer_load_dwordx4 v0, s[40:43], s51 offen offset:2048 sc1 lds\n" \
  "s_waitcnt lgkmcnt(4)\n" \
  "v_mfma_f32_32x32x16_bf16 v[32:47], a[168:171], v[72:75], v[32:47]\n" \
  "ds_read_b128 v[64:67], v3 offset:49152\n" \
  "v_add_f32 v10, v10, v122\n" \
  "v_add_f32 v11, v11, v123\n" \
  "v_mfma_f32_32x32x16_bf16 v[32:47], a[172:175], v[76:79], v[32:47]\n" \
  "ds_read_b128 v[68:71], v3 offset:50176\n" \
  "v_add_f32 v10, v10, v124\n" \
  "buffer_load_dwordx4 v0, s[40:43], s51 offen offset:3072 sc1 lds\n" \
  "s_waitcnt lgkmcnt(4)\n" \
  "v_mfma_f32_32x32x16_bf16 v[32:47], a[176:179], v[80:83], v[32:47]\n" \
  "ds_read_b128 v[72:75], v3 offset:51200\n" \
  "v_add_f32 v11, v11, v125\n" \
  "v_add_f32 v10, v10, v126\n" \
  "v_mfma_f32_32x32x16_bf16 v[32:47], a[180:183], v[84:87], v[32:47]\n" \
  "ds_read_b128 v[76:79], v3 offset:52224\n" \
  "v_add_f32 v11, v11, v127\n" \
  "s_add_u32 m0, s47, 49152\n" \
  "s_add_u32 s51, s44, 98304\n" \
  "buffer_load_dwordx4 v0, s[40:43], s51 offen sc1 lds\n" \
  "s_waitcnt lgkmcnt(4)\n" \
  "v_mfma_f32_32x32x16_bf16 v[32:47], a[184:187], v[88:91], v[32:47]\n" \
  "ds_read_b128 v[80:83], v3 offset:53248\n" \
  "v_mul_f32 v10, 0x3a800000, v10\n" \
  "v_mul_f32 v11, 0x3a800000, v11\n" \
  "v_mfma_f32_32x32x16_bf16 v[32:47], a[188:191], v[92:95], v[32:47]\n" \
  "ds_read_b128 v[84:87], v3 offset:54272\n" \
  "v_mov_b32 v13, v10\n" \
  "v_mov_b32 v12, v11\n" \
  "s_waitcnt lgkmcnt(4)\n" \
  "v_mfma_f32_32x32x16_bf16 v[32:47], a[192:195], v[64:67], v[32:47]\n" \
  "ds_read_b128 v[88:91], v3 offset:55296\n" \
  "v_permlane32_swap_b32 v13, v10\n" \
  "buffer_load_dwordx4 v0, s[40:43], s51 offen offset:1024 sc1 lds\n" \
  "v_mfma_f32_32x32x16_bf16 v[32:47], a[196:199], v[68:71], v[32:47]\n" \
  "ds_read_b128 v[92:95], v3 offset:56320\n" \
  "v_permlane32_swap_b32 v12, v11\n" \
  "v_add_f32 v10, v13, v10\n" \
  "s_waitcnt lgkmcnt(4)\n" \
  "v_mfma_f32_32x32x16_bf16 v[32:47], a[200:203], v[72:75], v[32:47]\n" \
  "ds_read_b128 v[64:67], v3 offset:57344\n" \
  "v_add_f32 v11, v12, v11\n" \
  "buffer_load_dwordx4 v0, s[40:43], s51 offen offset:2048 sc1 lds\n" \
  "v_mfma_f32_32x32x16_bf16 v[32:47], a[204:207], v[76:79], v[32:47]\n" \
  "ds_read_b128 v[68:71], v3 offset:58368\n" \
  "v_fma_f32 v11, -v10, v10, v11\n" \
  "v_max_f32 v11, 0, v11\n" \
  "s_waitcnt lgkmcnt(4)\n" \
  "v_mfma_f32_32x32x16_bf16 v[32:47], a[208:211], v[80:83], v[32:47]\n" \
  "ds_read_b128 v[72:75], v3 offset:59392\n" \
  "v_add_f32 v13, 0x3727c5ac, v11\n" \
  "buffer_load_dwordx4 v0, s[40:43], s51 offen offset:3072 sc1 lds\n" \
  "v_mfma_f32_32x32x16_bf16 v[32:47], a[212:215], v[84:87], v[32:47]\n" \
  "ds_read_b128 v[76:79], v3 offset:60416\n" \
  "v_rsq_f32 v12, v13\n" \
  "v_lshlrev_b32 v48, 16, v128\n" \
  "s_waitcnt lgkmcnt(4)\n" \
  "v_mfma_f32_32x32x16_bf16 v[32:47], a[216:219], v[88:91], v[32:47]\n" \
  "ds_read_b128 v[80:83], v3 offset:61440\n" \
  "v_and_b32 v49, 0xffff0000, v128\n" \
  "v_lshlrev_b32 v50, 16, v129\n" \
  "v_mfma_f32_32x32x16_bf16 v[32:47], a[220:223], v[92:95], v[32:47]\n" \
  "ds_read_b128 v[84:87], v3 offset:62464\n" \
  "v_and_b32 v51, 0xffff0000, v129\n" \
  "v_lshlrev_b32 v52, 16, v130\n" \
  "s_waitcnt lgkmcnt(4)\n" \
  "v_mfma_f32_32x32x16_bf16 v[32:47], a[224:227], v[64:67], v[32:47]\n" \
  "ds_read_b128 v[88:91], v3 offset:63488\n" \
  "v_and_b32 v53, 0xffff0000, v130\n" \
  "v_lshlrev_b32 v54, 16, v131\n" \
  "v_mfma_f32_32x32x16_bf16 v[32:47], a[228:231], v[68:71], v[32:47]\n" \
  "ds_read_b128 v[92:95], v3 offset:64512\n" \
  "v_and_b32 v55, 0xffff0000, v131\n" \
  "v_lshlrev_b32 v56, 16, v132\n" \
  "s_waitcnt lgkmcnt(4)\n" \
  "v_mfma_f32_32x32x16_bf16 v[32:47], a[232:235], v[72:75], v[32:47]\n" \
  "v_and_b32 v57, 0xffff0000, v132\n" \
  "v_lshlrev_b32 v58, 16, v133\n" \
  "v_and_b32 v59, 0xffff0000, v133\n" \
  "v_lshlrev_b32 v60, 16, v134\n" \
  "v_mfma_f32_32x32x16_bf16 v[32:47], a[236:239], v[76:79], v[32:47]\n" \
  "v_and_b32 v61, 0xffff0000, v134\n" \
  "v_lshlrev_b32 v62, 16, v135\n" \
  "v_and_b32 v63, 0xffff0000, v135\n" \
  "v_mul_f32 v10, -v10, v12\n" \
  "s_waitcnt lgkmcnt(2)\n" \
  "v_mfma_f32_32x32x16_bf16 v[32:47], a[240:243], v[80:83], v[32:47]\n" \
  "v_fma_f32 v48, v48, v12, v10\n" \
  "v_fma_f32 v49, v49, v12, v10\n" \
  "v_fma_f32 v50, v50, v12, v10\n" \
  "v_fma_f32 v51, v51, v12, v10\n" \
  "v_mfma_f32_32x32x16_bf16 v[32:47], a[244:247], v[84:87], v[32:47]\n" \
  "v_fma_f32 v52, v52, v12, v10\n" \
  "v_fma_f32 v53, v53, v12, v10\n" \
  "v_fma_f32 v54, v54, v12, v10\n" \
  "v_fma_f32 v55, v55, v12, v10\n" \
  "s_waitcnt lgkmcnt(0)\n" \
  "v_mfma_f32_32x32x16_bf16 v[32:47], a[248:251], v[88:91], v[32:47]\n" \
  "v_fma_f32 v56, v56, v12, v10\n" \
  "v_fma_f32 v57, v57, v12, v10\n" \
  "v_fma_f32 v58, v58, v12, v10\n" \
  "v_fma_f32 v59, v59, v12, v10\n" \
  "v_mfma_f32_32x32x16_bf16 v[32:47], a[252:255], v[92:95], v[32:47]\n" \
  "v_fma_f32 v60, v60, v12, v10\n" \
  "v_fma_f32 v61, v61, v12, v10\n" \
  "v_fma_f32 v62, v62, v12, v10\n" \
  "v_fma_f32 v63, v63, v12, v10\n" \
  "s_cmp_lg_u32 s42, 0\n" \
  "s_nop 6\n" \
  "v_pk_add_f32 v[144:145], v[48:49], v[32:33]\n" \
  "v_pk_add_f32 v[146:147], v[50:51], v[34:35]\n" \
  "v_pk_add_f32 v[148:149], v[52:53], v[36:37]\n" \
  "v_pk_add_f32 v[150:151], v[54:55], v[38:39]\n" \
  "v_pk_add_f32 v[152:153], v[56:57], v[40:41]\n" \
  "v_pk_add_f32 v[154:155], v[58:59], v[42:43]\n" \
  "v_pk_add_f32 v[156:157], v[60:61], v[44:45]\n" \
  "v_pk_add_f32 v[158:159], v[62:63], v[46:47]\n" \
  "s_cbranch_scc1 SPM2_e_%=\n" \
  "v_cvt_pk_bf16_f32 v168, v144, v145\n" \
  "v_cvt_pk_bf16_f32 v169, v146, v147\n" \
  "v_cvt_pk_bf16_f32 v170, v148, v149\n" \
  "v_cvt_pk_bf16_f32 v171, v150, v151\n" \
  "buffer_store_dwordx4 v[168:171], v15, s[36:39], s52 offen sc1\n" \
  "v_cvt_pk_bf16_f32 v172, v152, v153\n" \
  "v_cvt_pk_bf16_f32 v173, v154, v155\n" \
  "v_cvt_pk_bf16_f32 v174, v156, v157\n" \
  "v_cvt_pk_bf16_f32 v175, v158, v159\n" \
  "buffer_store_dwordx4 v[172:175], v15, s[36:39], s52 offen offset:1024 sc1\n" \
  "v_lshlrev_b32 v144, 16, v168\n" \
  "v_and_b32 v145, 0xffff0000, v168\n" \
  "v_lshlrev_b32 v146, 16, v169\n" \
  "v_and_b32 v147, 0xffff0000, v169\n" \
  "v_lshlrev_b32 v148, 16, v170\n" \
  "v_and_b32 v149, 0xffff0000, v170\n" \
  "v_lshlrev_b32 v150, 16, v171\n" \
  "v_and_b32 v151, 0xffff0000, v171\n" \
  "v_lshlrev_b32 v152, 16, v172\n" \
  "v_and_b32 v153, 0xffff0000, v172\n" \
  "v_lshlrev_b32 v154, 16, v173\n" \
  "v_and_b32 v155, 0xffff0000, v173\n" \
  "v_lshlrev_b32 v156, 16, v174\n" \
  "v_and_b32 v157, 0xffff0000, v174\n" \
  "v_lshlrev_b32 v158, 16, v175\n" \
  "v_and_b32 v159, 0xffff0000, v175\n" \
  "v_add_f32 v160, v144, v145\n" \
  "v_mul_f32 v161, v144, v144\n" \
  "v_fmac_f32 v161, v145, v145\n" \
  "v_add_f32 v160, v160, v146\n" \
  "v_fmac_f32 v161, v146, v146\n" \
  "v_add_f32 v160, v160, v147\n" \
  "v_fmac_f32 v161, v147, v147\n" \
  "v_add_f32 v160, v160, v148\n" \
  "v_fmac_f32 v161, v148, v148\n" \
  "v_add_f32 v160, v160, v149\n" \
  "v_fmac_f32 v161, v149, v149\n" \
  "v_add_f32 v160, v160, v150\n" \
  "v_fmac_f32 v161, v150, v150\n" \
  "v_add_f32 v160, v160, v151\n" \
  "v_fmac_f32 v161, v151, v151\n" \
  "v_add_f32 v160, v160, v152\n" \
  "v_fmac_f32 v161, v152, v152\n" \
  "v_add_f32 v160, v160, v153\n" \
  "v_fmac_f32 v161, v153, v153\n" \
  "v_add_f32 v160, v160, v154\n" \
  "v_fmac_f32 v161, v154, v154\n" \
  "v_add_f32 v160, v160, v155\n" \
  "v_fmac_f32 v161, v155, v155\n" \
  "v_add_f32 v160, v160, v156\n" \
  "v_fmac_f32 v161, v156, v156\n" \
  "v_add_f32 v160, v160, v157\n" \
  "v_fmac_f32 v161, v157, v157\n" \
  "v_add_f32 v160, v160, v158\n" \
  "v_fmac_f32 v161, v158, v158\n" \
  "v_add_f32 v160, v160, v159\n" \
  "v_fmac_f32 v161, v159, v159\n" \
  "s_nop 1\n" \
  "v_permlane32_swap_b32 v160, v161\n" \
  "v_add_f32 v160, v160, v161\n" \
  "s_nop 1\n" \
  "v_permlane32_swap_b32 v160, v161\n" \
  "buffer_store_dwordx2 v[160:161], v140, s[36:39], s53 offen sc1\n" \
  "SPM2_e_%=:\n" \
  "s_nop 1\n"
#define SPL2_ASM \
  "s_add_u32 m0, s59, 0\n" \
  "s_nop 0\n" \
  "buffer_load_dwordx4 v0, s[36:39], s56 offen sc1 lds\n" \
  "buffer_load_dwordx4 v0, s[36:39], s56 offen offset:1024 sc1 lds\n" \
  "buffer_load_dwordx4 v0, s[36:39], s56 offen offset:2048 sc1 lds\n" \
  "buffer_load_dwordx4 v0, s[36:39], s56 offen offset:3072 sc1 lds\n" \
  "s_add_u32 m0, s59, 16384\n" \
  "s_add_u32 s51, s56, 32768\n" \
  "buffer_load_dwordx4 v0, s[36:39], s51 offen sc1 lds\n" \
  "buffer_load_dwordx4 v0, s[36:39], s51 offen offset:1024 sc1 lds\n" \
  "buffer_load_dwordx4 v0, s[36:39], s51 offen offset:2048 sc1 lds\n" \
  "buffer_load_dwordx4 v0, s[36:39], s51 offen offset:3072 sc1 lds\n" \
  "s_xor_b32 m0, s49, 1024\n" \
  "s_nop 0\n" \
  "buffer_load_dword v9, s[36:39], 0 offen sc1 lds\n" \
  "s_add_u32 m0, s59, 32768\n" \
  "s_add_u32 s51, s56, 65536\n" \
  "buffer_load_dwordx4 v0, s[36:39], s51 offen sc1 lds\n" \
  "buffer_load_dwordx4 v0, s[36:39], s51 offen offset:1024 sc1 lds\n" \
  "buffer_load_dwordx4 v0, s[36:39], s51 offen offset:2048 sc1 lds\n" \
  "buffer_load_dwordx4 v0, s[36:39], s51 offen offset:3072 sc1 lds\n" \
  "s_mov_b64 s[40:41], s[36:37]\n" \
  "s_mov_b32 s43, s39\n" \
  "s_waitcnt vmcnt(9)\n" \
  "s_barrier\n" \
  "ds_read_b128 v[64:67], v3 offset:0\n" \
  "ds_read_b128 v[68:71], v3 offset:1024\n" \
  "ds_read_b128 v[72:75], v3 offset:2048\n" \
  "ds_read_b128 v[76:79], v3 offset:3072\n" \
  "ds_read_b128 v[80:83], v3 offset:4096\n" \
  "ds_read_b128 v[84:87], v3 offset:5120\n" \
  "s_waitcnt lgkmcnt(4)\n" \
  "v_mfma_f32_32x32x16_bf16 v[32:47], a[0:3], v[64:67], 0\n" \
  "ds_read_b128 v[88:91], v3 offset:6144\n" \
  "s_add_u32 m0, s48, 0\n" \
  "s_nop 0\n" \
  "buffer_load_dwordx4 v1, s[36:39], s57 offen sc1 lds\n" \
  "v_mfma_f32_32x32x16_bf16 v[32:47], a[4:7], v[68:71], v[32:47]\n" \
  "ds_read_b128 v[92:95], v3 offset:7168\n" \
  "s_waitcnt lgkmcnt(4)\n" \
  "v_mfma_f32_32x32x16_bf16 v[32:47], a[8:11], v[72:75], v[32:47]\n" \
  "ds_read_b128 v[64:67], v3 offset:8192\n" \
  "buffer_load_dwordx4 v1, s[36:39], s57 offen offset:1024 sc1 lds\n" \
  "v_mfma_f32_32x32x16_bf16 v[32:47], a[12:15], v[76:79], v[32:47]\n" \
  "ds_read_b128 v[68:71], v3 offset:9216\n" \
  "s_waitcnt lgkmcnt(4)\n" \
  "v_mfma_f32_32x32x16_bf16 v[32:47], a[16:19], v[80:83], v[32:47]\n" \
  "ds_read_b128 v[72:75], v3 offset:10240\n" \
  "s_add_u32 m0, s48, 8192\n" \
  "s_nop 0\n" \
  "buffer_load_dwordx4 v2, s[36:39], s58 offen sc1 lds\n" \
  "v_mfma_f32_32x32x16_bf16 v[32:47], a[20:23], v[84:87], v[32:47]\n" \
  "ds_read_b128 v[76:79], v3 offset:11264\n" \
  "s_waitcnt lgkmcnt(4)\n" \
  "v_mfma_f32_32x32x16_bf16 v[32:47], a[24:27], v[88:91], v[32:47]\n" \
  "ds_read_b128 v[80:83], v3 offset:12288\n" \
  "buffer_load_dwordx4 v2, s[36:39], s58 offen offset:1024 sc1 lds\n" \
  "v_mfma_f32_32x32x16_bf16 v[32:47], a[28:31], v[92:95], v[32:47]\n" \
  "ds_read_b128 v[84:87], v3 offset:13312\n" \
  "s_waitcnt lgkmcnt(4)\n" \
  "v_mfma_f32_32x32x16_bf16 v[32:47], a[32:35], v[64:67], v[32:47]\n" \
  "ds_read_b128 v[88:91], v3 offset:14336\n" \
  "s_add_u32 m0, s59, 49152\n" \
  "s_add_u32 s51, s56, 98304\n" \
  "buffer_load_dwordx4 v0, s[36:39], s51 offen sc1 lds\n" \
  "v_mfma_f32_32x32x16_bf16 v[32:47], a[36:39], v[68:71], v[32:47]\n" \
  "ds_read_b128 v[92:95], v3 offset:15360\n" \
  "s_waitcnt lgkmcnt(4)\n" \
  "v_mfma_f32_32x32x16_bf16 v[32:47], a[40:43], v[72:75], v[32:47]\n" \
  "s_waitcnt vmcnt(9)\n" \
  "s_barrier\n" \
  "ds_read_b128 v[64:67], v3 offset:16384\n" \
  "ds_read_b32 v14, v6\n" \
  "buffer_load_dwordx4 v0, s[36:39], s51 offen offset:1024 sc1 lds\n" \
  "v_mfma_f32_32x32x16_bf16 v[32:47], a[44:47], v[76:79], v[32:47]\n" \
  "ds_read_b128 v[68:71], v3 offset:17408\n" \
  "s_waitcnt lgkmcnt(5)\n" \
  "v_mfma_f32_32x32x16_bf16 v[32:47], a[48:51], v[80:83], v[32:47]\n" \
  "ds_read_b128 v[72:75], v3 offset:18432\n" \
  "buffer_load_dwordx4 v0, s[36:39], s51 offen offset:2048 sc1 lds\n" \
  "v_mfma_f32_32x32x16_bf16 v[32:47], a[52:55], v[84:87], v[32:47]\n" \
  "ds_read_b128 v[76:79], v3 offset:19456\n" \
  "s_waitcnt lgkmcnt(5)\n" \
  "v_mfma_f32_32x32x16_bf16 v[32:47], a[56:59], v[88:91], v[32:47]\n" \
  "ds_read_b128 v[80:83], v3 offset:20480\n" \
  "s_waitcnt lgkmcnt(4)\n" \
  "v_cmp_gt_u32 vcc, s50, v14\n" \
  "buffer_load_dwordx4 v0, s[36:39], s51 offen offset:3072 sc1 lds\n" \
  "v_mfma_f32_32x32x16_bf16 v[32:47], a[60:63], v[92:95], v[32:47]\n" \
  "ds_read_b128 v[84:87], v3 offset:21504\n" \
  "s_cmp_eq_u64 vcc, 0\n" \
  "s_cselect_b32 s42, s38, 0\n" \
  "s_waitcnt lgkmcnt(4)\n" \
  "v_mfma_f32_32x32x16_bf16 v[32:47], a[64:67], v[64:67], v[32:47]\n" \
  "ds_read_b128 v[88:91], v3 offset:22528\n" \
  "v_mfma_f32_32x32x16_bf16 v[32:47], a[68:71], v[68:71], v[32:47]\n" \
  "ds_read_b128 v[92:95], v3 offset:23552\n" \
  "s_add_u32 m0, s47, 0\n" \
  "s_nop 0\n" \
  "buffer_load_dwordx4 v0, s[40:43], s44 offen sc1 lds\n" \
  "s_waitcnt lgkmcnt(4)\n" \
  "v_mfma_f32_32x32x16_bf16 v[32:47], a[72:75], v[72:75], v[32:47]\n" \
  "ds_read_b128 v[64:67], v3 offset:24576\n" \
  "v_mfma_f32_32x32x16_bf16 v[32:47], a[76:79], v[76:79], v[32:47]\n" \
  "ds_read_b128 v[68:71], v3 offset:25600\n" \
  "buffer_load_dwordx4 v0, s[40:43], s44 offen offset:1024 sc1 lds\n" \
  "s_waitcnt lgkmcnt(4)\n" \
  "v_mfma_f32_32x32x16_bf16 v[32:47], a[80:83], v[80:83], v[32:47]\n" \
  "ds_read_b128 v[72:75], v3 offset:26624\n" \
  "v_mfma_f32_32x32x16_bf16 v[32:47], a[84:87], v[84:87], v[32:47]\n" \
  "ds_read_b128 v[76:79], v3 offset:27648\n" \
  "s_waitcnt lgkmcnt(4)\n" \
  "v_mfma_f32_32x32x16_bf16 v[32:47], a[88:91], v[88:91], v[32:47]\n" \
  "ds_read_b128 v[80:83], v3 offset:28672\n" \
  "buffer_load_dwordx4 v0, s[40:43], s44 offen offset:2048 sc1 lds\n" \
  "v_mfma_f32_32x32x16_bf16 v[32:47], a[92:95], v[92:95], v[32:47]\n" \
  "ds_read_b128 v[84:87], v3 offset:29696\n" \
  "s_waitcnt lgkmcnt(4)\n" \
  "v_mfma_f32_32x32x16_bf16 v[32:47], a[96:99], v[64:67], v[32:47]\n" \
  "ds_read_b128 v[88:91], v3 offset:30720\n" \
  "v_mfma_f32_32x32x16_bf16 v[32:47], a[100:103], v[68:71], v[32:47]\n" \
  "v_mov_b32 v14, 4\n" \
  "ds_read_b128 v[92:95], v3 offset:31744\n" \
  "buffer_load_dwordx4 v0, s[40:43], s44 offen offset:3072 sc1 lds\n" \
  "s_waitcnt lgkmcnt(4)\n" \
  "v_mfma_f32_32x32x16_bf16 v[32:47], a[104:107], v[72:75], v[32:47]\n" \
  "s_waitcnt vmcnt(8)\n" \
  "s_barrier\n" \
  "ds_read_b128 v[64:67], v3 offset:32768\n" \
  "ds_read2st64_b64 v[96:99], v4 offset0:0 offset1:1\n" \
  "v_mfma_f32_32x32x16_bf16 v[32:47], a[108:111], v[76:79], v[32:47]\n" \
  "ds_read_b128 v[68:71], v3 offset:33792\n" \
  "ds_read2st64_b64 v[100:103], v4 offset0:2 offset1:3\n" \
  "s_waitcnt lgkmcnt(6)\n" \
  "v_mfma_f32_32x32x16_bf16 v[32:47], a[112:115], v[80:83], v[32:47]\n" \
  "ds_read_b128 v[72:75], v3 offset:34816\n" \
  "ds_read2st64_b64 v[104:107], v4 offset0:4 offset1:5\n" \
  "s_xor_b32 m0, s49, 0\n" \
  "s_nop 0\n" \
  "buffer_load_dword v7, s[40:43], 0 offen sc1 lds\n" \
  "v_mfma_f32_32x32x16_bf16 v[32:47], a[116:119], v[84:87], v[32:47]\n" \
  "ds_read_b128 v[76:79], v3 offset:35840\n" \
  "ds_read2st64_b64 v[108:111], v4 offset0:6 offset1:7\n" \
  "s_waitcnt lgkmcnt(8)\n" \
  "v_mfma_f32_32x32x16_bf16 v[32:47], a[120:123], v[88:91], v[32:47]\n" \
  "ds_read_b128 v[80:83], v3 offset:36864\n" \
  "ds_read2st64_b64 v[112:115], v4 offset0:8 offset1:9\n" \
  "s_add_u32 m0, s47, 16384\n" \
  "s_add_u32 s51, s44, 32768\n" \
  "buffer_load_dwordx4 v0, s[40:43], s51 offen sc1 lds\n" \
  "v_mfma_f32_32x32x16_bf16 v[32:47], a[124:127], v[92:95], v[32:47]\n" \
  "ds_read_b128 v[84:87], v3 offset:37888\n" \
  "ds_read2st64_b64 v[116:119], v4 offset0:10 offset1:11\n" \
  "s_waitcnt lgkmcnt(9)\n" \
  "v_mfma_f32_32x32x16_bf16 v[32:47], a[128:131], v[64:67], v[32:47]\n" \
  "ds_read_b128 v[88:91], v3 offset:38912\n" \
  "ds_read2st64_b64 v[120:123], v4 offset0:12 offset1:13\n" \
  "v_mfma_f32_32x32x16_bf16 v[32:47], a[132:135], v[68:71], v[32:47]\n" \
  "ds_read_b128 v[92:95], v3 offset:39936\n" \
  "ds_read2st64_b64 v[124:127], v4 offset0:14 offset1:15\n" \
  "v_add_f32 v10, v96, v98\n" \
  "v_add_f32 v11, v97, v99\n" \
  "buffer_load_dwordx4 v0, s[40:43], s51 offen offset:1024 sc1 lds\n" \
  "s_waitcnt lgkmcnt(9)\n" \
  "v_mfma_f32_32x32x16_bf16 v[32:47], a[136:139], v[72:75], v[32:47]\n" \
  "ds_read_b128 v[64:67], v3 offset:40960\n" \
  "ds_read_b128 v[128:131], v5\n" \
  "v_add_f32 v10, v10, v100\n" \
  "v_add_f32 v11, v11, v101\n" \
  "v_add_f32 v10, v10, v102\n" \
  "v_mfma_f32_32x32x16_bf16 v[32:47], a[140:143], v[76:79], v[32:47]\n" \
  "ds_read_b128 v[68:71], v3 offset:41984\n" \
  "ds_read_b128 v[132:135], v5 offset:1024\n" \
  "v_add_f32 v11, v11, v103\n" \
  "v_add_f32 v10, v10, v104\n" \
  "v_add_f32 v11, v11, v105\n" \
  "s_waitcnt lgkmcnt(9)\n" \
  "v_mfma_f32_32x32x16_bf16 v[32:47], a[144:147], v[80:83], v[32:47]\n" \
  "ds_read_b128 v[72:75], v3 offset:43008\n" \
  "v_add_f32 v10, v10, v106\n" \
  "v_add_f32 v11, v11, v107\n" \
  "buffer_load_dwordx4 v0, s[40:43], s51 offen offset:2048 sc1 lds\n" \
  "v_mfma_f32_32x32x16_bf16 v[32:47], a[148:151], v[84:87], v[32:47]\n" \
  "ds_read_b128 v[76:79], v3 offset:44032\n" \
  "v_add_f32 v10, v10, v108\n" \
  "v_add_f32 v11, v11, v109\n" \
  "v_add_f32 v10, v10, v110\n" \
  "s_waitcnt lgkmcnt(7)\n" \
  "v_mfma_f32_32x32x16_bf16 v[32:47], a[152:155], v[88:91], v[32:47]\n" \
  "ds_read_b128 v[80:83], v3 offset:45056\n" \
  "v_add_f32 v11, v11, v111\n" \
  "v_add_f32 v10, v10, v112\n" \
  "v_add_f32 v11, v11, v113\n" \
  "v_mfma_f32_32x32x16_bf16 v[32:47], a[156:159], v[92:95], v[32:47]\n" \
  "ds_read_b128 v[84:87], v3 offset:46080\n" \
  "v_add_f32 v10, v10, v114\n" \
  "v_add_f32 v11, v11, v115\n" \
  "buffer_load_dwordx4 v0, s[40:43], s51 offen offset:3072 sc1 lds\n" \
  "s_waitcnt lgkmcnt(5)\n" \
  "v_mfma_f32_32x32x16_bf16 v[32:47], a[160:163], v[64:67], v[32:47]\n" \
  "ds_read_b128 v[88:91], v3 offset:47104\n" \
  "v_add_f32 v10, v10, v116\n" \
  "v_add_f32 v11, v11, v117\n" \
  "v_add_f32 v10, v10, v118\n" \
  "v_mfma_f32_32x32x16_bf16 v[32:47], a[164:167], v[68:71], v[32:47]\n" \
  "ds_read_b128 v[92:95], v3 offset:48128\n" \
  "v_add_f32 v11, v11, v119\n" \
  "v_add_f32 v10, v10, v120\n" \
  "v_add_f32 v11, v11, v121\n" \
  "s_waitcnt lgkmcnt(4)\n" \
  "v_mfma_f32_32x32x16_bf16 v[32:47], a[168:171], v[72:75], v[32:47]\n" \
  "s_waitcnt vmcnt(9)\n" \
  "s_barrier\n" \
  "ds_read_b128 v[64:67], v3 offset:49152\n" \
  "v_add_f32 v10, v10, v122\n" \
  "v_add_f32 v11, v11, v123\n" \
  "v_add_f32 v10, v10, v124\n" \
  "v_mfma_f32_32x32x16_bf16 v[32:47], a[172:175], v[76:79], v[32:47]\n" \
  "ds_read_b128 v[68:71], v3 offset:50176\n" \
  "v_add_f32 v11, v11, v125\n" \
  "v_add_f32 v10, v10, v126\n" \
  "s_add_u32 m0, s48, 0\n" \
  "s_nop 0\n" \
  "buffer_load_dwordx4 v1, s[40:43], s45 offen sc1 lds\n" \
  "s_waitcnt lgkmcnt(4)\n" \
  "v_mfma_f32_32x32x16_bf16 v[32:47], a[176:179], v[80:83], v[32:47]\n" \
  "ds_read_b128 v[72:75], v3 offset:51200\n" \
  "v_add_f32 v11, v11, v127\n" \
  "v_mul_f32 v10, 0x3a800000, v10\n" \
  "buffer_load_dwordx4 v1, s[40:43], s45 offen offset:1024 sc1 lds\n" \
  "v_mfma_f32_32x32x16_bf16 v[32:47], a[180:183], v[84:87], v[32:47]\n" \
  "ds_read_b128 v[76:79], v3 offset:52224\n" \
  "v_mul_f32 v11, 0x3a800000, v11\n" \
  "v_mov_b32 v13, v10\n" \
  "v_mov_b32 v12, v11\n" \
  "s_waitcnt lgkmcnt(4)\n" \
  "v_mfma_f32_32x32x16_bf16 v[32:47], a[184:187], v[88:91], v[32:47]\n" \
  "ds_read_b128 v[80:83], v3 offset:53248\n" \
  "v_permlane32_swap_b32 v13, v10\n" \
  "v_permlane32_swap_b32 v12, v11\n" \
  "s_add_u32 m0, s48, 8192\n" \
  "s_nop 0\n" \
  "buffer_load_dwordx4 v2, s[40:43], s46 offen sc1 lds\n" \
  "v_mfma_f32_32x32x16_bf16 v[32:47], a[188:191], v[92:95], v[32:47]\n" \
  "ds_read_b128 v[84:87], v3 offset:54272\n" \
  "v_add_f32 v10, v13, v10\n" \
  "v_add_f32 v11, v12, v11\n" \
  "v_fma_f32 v11, -v10, v10, v11\n" \
  "s_waitcnt lgkmcnt(4)\n" \
  "v_mfma_f32_32x32x16_bf16 v[32:47], a[192:195], v[64:67], v[32:47]\n" \
  "ds_read_b128 v[88:91], v3 offset:55296\n" \
  "v_max_f32 v11, 0, v11\n" \
  "v_add_f32 v13, 0x3727c5ac, v11\n" \
  "buffer_load_dwordx4 v2, s[40:43], s46 offen offset:1024 sc1 lds\n" \
  "v_mfma_f32_32x32x16_bf16 v[32:47], a[196:199], v[68:71], v[32:47]\n" \
  "ds_read_b128 v[92:95], v3 offset:56320\n" \
  "v_rsq_f32 v12, v13\n" \
  "v_lshlrev_b32 v48, 16, v128\n" \
  "v_and_b32 v49, 0xffff0000, v128\n" \
  "s_waitcnt lgkmcnt(4)\n" \
  "v_mfma_f32_32x32x16_bf16 v[32:47], a[200:203], v[72:75], v[32:47]\n" \
  "ds_read_b128 v[64:67], v3 offset:57344\n" \
  "v_lshlrev_b32 v50, 16, v129\n" \
  "v_and_b32 v51, 0xffff0000, v129\n" \
  "s_add_u32 m0, s47, 32768\n" \
  "s_add_u32 s51, s44, 65536\n" \
  "buffer_load_dwordx4 v0, s[40:43], s51 offen sc1 lds\n" \
  "v_mfma_f32_32x32x16_bf16 v[32:47], a[204:207], v[76:79], v[32:47]\n" \
  "ds_read_b128 v[68:71], v3 offset:58368\n" \
  "v_lshlrev_b32 v52, 16, v130\n" \
  "v_and_b32 v53, 0xffff0000, v130\n" \
  "buffer_load_dwordx4 v0, s[40:43], s51 offen offset:1024 sc1 lds\n" \
  "s_waitcnt lgkmcnt(4)\n" \
  "v_mfma_f32_32x32x16_bf16 v[32:47], a[208:211], v[80:83], v[32:47]\n" \
  "ds_read_b128 v[72:75], v3 offset:59392\n" \
  "v_lshlrev_b32 v54, 16, v131\n" \
  "v_and_b32 v55, 0xffff0000, v131\n" \
  "v_lshlrev_b32 v56, 16, v132\n" \
  "v_mfma_f32_32x32x16_bf16 v[32:47], a[212:215], v[84:87], v[32:47]\n" \
  "ds_read_b128 v[76:79], v3 offset:60416\n" \
  "v_and_b32 v57, 0xffff0000, v132\n" \
  "v_lshlrev_b32 v58, 16, v133\n" \
  "buffer_load_dwordx4 v0, s[40:43], s51 offen offset:2048 sc1 lds\n" \
  "s_waitcnt lgkmcnt(4)\n" \
  "v_mfma_f32_32x32x16_bf16 v[32:47], a[216:219], v[88:91], v[32:47]\n" \
  "ds_read_b128 v[80:83], v3 offset:61440\n" \
  "v_and_b32 v59, 0xffff0000, v133\n" \
  "v_lshlrev_b32 v60, 16, v134\n" \
  "v_and_b32 v61, 0xffff0000, v134\n" \
  "v_mfma_f32_32x32x16_bf16 v[32:47], a[220:223], v[92:95], v[32:47]\n" \
  "ds_read_b128 v[84:87], v3 offset:62464\n" \
  "v_lshlrev_b32 v62, 16, v135\n" \
  "buffer_load_dwordx4 v0, s[40:43], s51 offen offset:3072 sc1 lds\n" \
  "s_waitcnt lgkmcnt(4)\n" \
  "v_mfma_f32_32x32x16_bf16 v[32:47], a[224:227], v[64:67], v[32:47]\n" \
  "ds_read_b128 v[88:91], v3 offset:63488\n" \
  "v_and_b32 v63, 0xffff0000, v135\n" \
  "v_mul_f32 v10, -v10, v12\n" \
  "v_mfma_f32_32x32x16_bf16 v[32:47], a[228:231], v[68:71], v[32:47]\n" \
  "ds_read_b128 v[92:95], v3 offset:64512\n" \
  "v_fma_f32 v48, v48, v12, v10\n" \
  "s_add_u32 m0, s47, 49152\n" \
  "s_add_u32 s51, s44, 98304\n" \
  "buffer_load_dwordx4 v0, s[40:43], s51 offen sc1 lds\n" \
  "s_waitcnt lgkmcnt(4)\n" \
  "v_mfma_f32_32x32x16_bf16 v[32:47], a[232:235], v[72:75], v[32:47]\n" \
  "v_fma_f32 v49, v49, v12, v10\n" \
  "buffer_load_dwordx4 v0, s[40:43], s51 offen offset:1024 sc1 lds\n" \
  "v_mfma_f32_32x32x16_bf16 v[32:47], a[236:239], v[76:79], v[32:47]\n" \
  "v_fma_f32 v50, v50, v12, v10\n" \
  "v_fma_f32 v51, v51, v12, v10\n" \
  "v_fma_f32 v52, v52, v12, v10\n" \
  "v_fma_f32 v53, v53, v12, v10\n" \
  "s_waitcnt lgkmcnt(2)\n" \
  "v_mfma_f32_32x32x16_bf16 v[32:47], a[240:243], v[80:83], v[32:47]\n" \
  "v_fma_f32 v54, v54, v12, v10\n" \
  "buffer_load_dwordx4 v0, s[40:43], s51 offen offset:2048 sc1 lds\n" \
  "v_mfma_f32_32x32x16_bf16 v[32:47], a[244:247], v[84:87], v[32:47]\n" \
  "v_fma_f32 v55, v55, v12, v10\n" \
  "v_fma_f32 v56, v56, v12, v10\n" \
  "v_fma_f32 v57, v57, v12, v10\n" \
  "v_fma_f32 v58, v58, v12, v10\n" \
  "s_waitcnt lgkmcnt(0)\n" \
  "v_mfma_f32_32x32x16_bf16 v[32:47], a[248:251], v[88:91], v[32:47]\n" \
  "v_fma_f32 v59, v59, v12, v10\n" \
  "buffer_load_dwordx4 v0, s[40:43], s51 offen offset:3072 sc1 lds\n" \
  "v_mfma_f32_32x32x16_bf16 v[32:47], a[252:255], v[92:95], v[32:47]\n" \
  "v_fma_f32 v60, v60, v12, v10\n" \
  "v_fma_f32 v61, v61, v12, v10\n" \
  "v_fma_f32 v62, v62, v12, v10\n" \
  "v_fma_f32 v63, v63, v12, v10\n" \
  "s_cmp_lg_u32 s42, 0\n" \
  "s_nop 6\n" \
  "v_pk_add_f32 v[144:145], v[48:49], v[32:33]\n" \
  "v_pk_add_f32 v[146:147], v[50:51], v[34:35]\n" \
  "v_pk_add_f32 v[148:149], v[52:53], v[36:37]\n" \
  "v_pk_add_f32 v[150:151], v[54:55], v[38:39]\n" \
  "v_pk_add_f32 v[152:153], v[56:57], v[40:41]\n" \
  "v_pk_add_f32 v[154:155], v[58:59], v[42:43]\n" \
  "v_pk_add_f32 v[156:157], v[60:61], v[44:45]\n" \
  "v_pk_add_f32 v[158:159], v[62:63], v[46:47]\n" \
  "s_cbranch_scc1 SPL2_e_%=\n" \
  "v_cvt_pk_bf16_f32 v168, v144, v145\n" \
  "v_cvt_pk_bf16_f32 v169, v146, v147\n" \
  "v_cvt_pk_bf16_f32 v170, v148, v149\n" \
  "v_cvt_pk_bf16_f32 v171, v150, v151\n" \
  "buffer_store_dwordx4 v[168:171], v15, s[36:39], s52 offen sc1\n" \
  "v_cvt_pk_bf16_f32 v172, v152, v153\n" \
  "v_cvt_pk_bf16_f32 v173, v154, v155\n" \
  "v_cvt_pk_bf16_f32 v174, v156, v157\n" \
  "v_cvt_pk_bf16_f32 v175, v158, v159\n" \
  "buffer_store_dwordx4 v[172:175], v15, s[36:39], s52 offen offset:1024 sc1\n" \
  "v_lshlrev_b32 v144, 16, v168\n" \
  "v_and_b32 v145, 0xffff0000, v168\n" \
  "v_lshlrev_b32 v146, 16, v169\n" \
  "v_and_b32 v147, 0xffff0000, v169\n" \
  "v_lshlrev_b32 v148, 16, v170\n" \
  "v_and_b32 v149, 0xffff0000, v170\n" \
  "v_lshlrev_b32 v150, 16, v171\n" \
  "v_and_b32 v151, 0xffff0000, v171\n" \
  "v_lshlrev_b32 v152, 16, v172\n" \
  "v_and_b32 v153, 0xffff0000, v172\n" \
  "v_lshlrev_b32 v154, 16, v173\n" \
  "v_and_b32 v155, 0xffff0000, v173\n" \
  "v_lshlrev_b32 v156, 16, v174\n" \
  "v_and_b32 v157, 0xffff0000, v174\n" \
  "v_lshlrev_b32 v158, 16, v175\n" \
  "v_and_b32 v159, 0xffff0000, v175\n" \
  "v_add_f32 v160, v144, v145\n" \
  "v_mul_f32 v161, v144, v144\n" \
  "v_fmac_f32 v161, v145, v145\n" \
  "v_add_f32 v160, v160, v146\n" \
  "v_fmac_f32 v161, v146, v146\n" \
  "v_add_f32 v160, v160, v147\n" \
  "v_fmac_f32 v161, v147, v147\n" \
  "v_add_f32 v160, v160, v148\n" \
  "v_fmac_f32 v161, v148, v148\n" \
  "v_add_f32 v160, v160, v149\n" \
  "v_fmac_f32 v161, v149, v149\n" \
  "v_add_f32 v160, v160, v150\n" \
  "v_fmac_f32 v161, v150, v150\n" \
  "v_add_f32 v160, v160, v151\n" \
  "v_fmac_f32 v161, v151, v151\n" \
  "v_add_f32 v160, v160, v152\n" \
  "v_fmac_f32 v161, v152, v152\n" \
  "v_add_f32 v160, v160, v153\n" \
  "v_fmac_f32 v161, v153, v153\n" \
  "v_add_f32 v160, v160, v154\n" \
  "v_fmac_f32 v161, v154, v154\n" \
  "v_add_f32 v160, v160, v155\n" \
  "v_fmac_f32 v161, v155, v155\n" \
  "v_add_f32 v160, v160, v156\n" \
  "v_fmac_f32 v161, v156, v156\n" \
  "v_add_f32 v160, v160, v157\n" \
  "v_fmac_f32 v161, v157, v157\n" \
  "v_add_f32 v160, v160, v158\n" \
  "v_fmac_f32 v161, v158, v158\n" \
  "v_add_f32 v160, v160, v159\n" \
  "v_fmac_f32 v161, v159, v159\n" \
  "s_nop 1\n" \
  "v_permlane32_swap_b32 v160, v161\n" \
  "v_add_f32 v160, v160, v161\n" \
  "s_nop 1\n" \
  "v_permlane32_swap_b32 v160, v161\n" \
  "buffer_store_dwordx2 v[160:161], v140, s[36:39], s53 offen sc1\n" \
  "SPL2_e_%=:\n" \
  "s_nop 1\n"
#define SPM3_ASM \
  "s_mov_b64 s[40:41], s[36:37]\n" \
  "s_mov_b32 s43, s39\n" \
  "s_waitcnt vmcnt(14)\n" \
  "s_barrier\n" \
  "ds_read_b128 v[64:67], v3 offset:0\n" \
  "ds_read_b128 v[68:71], v3 offset:1024\n" \
  "ds_read_b32 v14, v6\n" \
  "ds_read_b128 v[72:75], v3 offset:2048\n" \
  "ds_read_b128 v[76:79], v3 offset:3072\n" \
  "ds_read_b128 v[80:83], v3 offset:4096\n" \
  "ds_read_b128 v[84:87], v3 offset:5120\n" \
  "s_waitcnt lgkmcnt(5)\n" \
  "v_mfma_f32_32x32x16_bf16 v[32:47], a[0:3], v[64:67], 0\n" \
  "ds_read_b128 v[88:91], v3 offset:6144\n" \
  "v_mov_b32 v176, 0xbdd2d3e7\n" \
  "v_mul_f32 v160, v144, v144\n" \
  "v_fmaak_f32 v160, v160, v176, 0xc0135761\n" \
  "v_mul_f32 v161, v145, v145\n" \
  "v_mfma_f32_32x32x16_bf16 v[32:47], a[4:7], v[68:71], v[32:47]\n" \
  "ds_read_b128 v[92:95], v3 offset:7168\n" \
  "v_mul_f32 v160, v160, v144\n" \
  "v_fmaak_f32 v161, v161, v176, 0xc0135761\n" \
  "v_mul_f32 v162, v146, v146\n" \
  "v_exp_f32 v160, v160\n" \
  "s_waitcnt lgkmcnt(4)\n" \
  "v_mfma_f32_32x32x16_bf16 v[32:47], a[8:11], v[72:75], v[32:47]\n" \
  "ds_read_b128 v[64:67], v3 offset:8192\n" \
  "v_cmp_gt_u32 vcc, s50, v14\n" \
  "v_mul_f32 v161, v161, v145\n" \
  "v_fmaak_f32 v162, v162, v176, 0xc0135761\n" \
  "v_mul_f32 v163, v147, v147\n" \
  "v_add_f32 v160, 1.0, v160\n" \
  "v_mfma_f32_32x32x16_bf16 v[32:47], a[12:15], v[76:79], v[32:47]\n" \
  "ds_read_b128 v[68:71], v3 offset:9216\n" \
  "s_cmp_eq_u64 vcc, 0\n" \
  "s_cselect_b32 s42, s38, 0\n" \
  "v_exp_f32 v161, v161\n" \
  "v_mul_f32 v162, v162, v146\n" \
  "v_fmaak_f32 v163, v163, v176, 0xc0135761\n" \
  "s_waitcnt lgkmcnt(4)\n" \
  "v_mfma_f32_32x32x16_bf16 v[32:47], a[16:19], v[80:83], v[32:47]\n" \
  "ds_read_b128 v[72:75], v3 offset:10240\n" \
  "v_mul_f32 v164, v148, v148\n" \
  "v_rcp_f32 v160, v160\n" \
  "s_add_u32 m0, s47, 0\n" \
  "s_nop 0\n" \
  "buffer_load_dwordx4 v0, s[40:43], s44 offen sc1 lds\n" \
  "v_mfma_f32_32x32x16_bf16 v[32:47], a[20:23], v[84:87], v[32:47]\n" \
  "ds_read_b128 v[76:79], v3 offset:11264\n" \
  "v_add_f32 v161, 1.0, v161\n" \
  "v_exp_f32 v162, v162\n" \
  "v_mul_f32 v163, v163, v147\n" \
  "s_waitcnt lgkmcnt(4)\n" \
  "v_mfma_f32_32x32x16_bf16 v[32:47], a[24:27], v[88:91], v[32:47]\n" \
  "ds_read_b128 v[80:83], v3 offset:12288\n" \
  "v_fmaak_f32 v164, v164, v176, 0xc0135761\n" \
  "v_mul_f32 v165, v149, v149\n" \
  "buffer_load_dwordx4 v0, s[40:43], s44 offen offset:1024 sc1 lds\n" \
  "v_mfma_f32_32x32x16_bf16 v[32:47], a[28:31], v[92:95], v[32:47]\n" \
  "ds_read_b128 v[84:87], v3 offset:13312\n" \
  "v_mul_f32 v144, v144, v160\n" \
  "v_rcp_f32 v161, v161\n" \
  "v_add_f32 v162, 1.0, v162\n" \
  "s_waitcnt lgkmcnt(4)\n" \
  "v_mfma_f32_32x32x16_bf16 v[32:47], a[32:35], v[64:67], v[32:47]\n" \
  "ds_read_b128 v[88:91], v3 offset:14336\n" \
  "v_exp_f32 v163, v163\n" \
  "v_mul_f32 v164, v164, v148\n" \
  "buffer_load_dwordx4 v0, s[40:43], s44 offen offset:2048 sc1 lds\n" \
  "v_mfma_f32_32x32x16_bf16 v[32:47], a[36:39], v[68:71], v[32:47]\n" \
  "ds_read_b128 v[92:95], v3 offset:15360\n" \
  "v_fmaak_f32 v165, v165, v176, 0xc0135761\n" \
  "v_mul_f32 v166, v150, v150\n" \
  "v_mul_f32 v145, v145, v161\n" \
  "v_rcp_f32 v162, v162\n" \
  "s_waitcnt lgkmcnt(4)\n" \
  "v_mfma_f32_32x32x16_bf16 v[32:47], a[40:43], v[72:75], v[32:47]\n" \
  "s_waitcnt vmcnt(11)\n" \
  "s_barrier\n" \
  "ds_read_b128 v[64:67], v3 offset:16384\n" \
  "ds_read2st64_b64 v[96:99], v4 offset0:0 offset1:1\n" \
  "v_add_f32 v163, 1.0, v163\n" \
  "v_mfma_f32_32x32x16_bf16 v[32:47], a[44:47], v[76:79], v[32:47]\n" \
  "ds_read_b128 v[68:71], v3 offset:17408\n" \
  "ds_read2st64_b64 v[100:103], v4 offset0:2 offset1:3\n" \
  "v_exp_f32 v164, v164\n" \
  "buffer_load_dwordx4 v0, s[40:43], s44 offen offset:3072 sc1 lds\n" \
  "s_waitcnt lgkmcnt(6)\n" \
  "v_mfma_f32_32x32x16_bf16 v[32:47], a[48:51], v[80:83], v[32:47]\n" \
  "ds_read_b128 v[72:75], v3 offset:18432\n" \
  "ds_read2st64_b64 v[104:107], v4 offset0:4 offset1:5\n" \
  "v_mul_f32 v165, v165, v149\n" \
  "v_fmaak_f32 v166, v166, v176, 0xc0135761\n" \
  "v_mfma_f32_32x32x16_bf16 v[32:47], a[52:55], v[84:87], v[32:47]\n" \
  "ds_read_b128 v[76:79], v3 offset:19456\n" \
  "ds_read2st64_b64 v[108:111], v4 offset0:6 offset1:7\n" \
  "v_mul_f32 v167, v151, v151\n" \
  "s_xor_b32 m0, s49, 0\n" \
  "s_nop 0\n" \
  "buffer_load_dword v7, s[40:43], 0 offen sc1 lds\n" \
  "s_waitcnt lgkmcnt(8)\n" \
  "v_mfma_f32_32x32x16_bf16 v[32:47], a[56:59], v[88:91], v[32:47]\n" \
  "ds_read_b128 v[80:83], v3 offset:20480\n" \
  "ds_read2st64_b64 v[112:115], v4 offset0:8 offset1:9\n" \
  "v_cvt_pk_bf16_f32 v168, v144, v145\n" \
  "v_mul_f32 v146, v146, v162\n" \
  "v_mfma_f32_32x32x16_bf16 v[32:47], a[60:63], v[92:95], v[32:47]\n" \
  "ds_read_b128 v[84:87], v3 offset:21504\n" \
  "ds_read2st64_b64 v[116:119], v4 offset0:10 offset1:11\n" \
  "v_rcp_f32 v163, v163\n" \
  "v_add_f32 v164, 1.0, v164\n" \
  "s_waitcnt lgkmcnt(9)\n" \
  "v_mfma_f32_32x32x16_bf16 v[32:47], a[64:67], v[64:67], v[32:47]\n" \
  "ds_read_b128 v[88:91], v3 offset:22528\n" \
  "ds_read2st64_b64 v[120:123], v4 offset0:12 offset1:13\n" \
  "s_add_u32 m0, s47, 16384\n" \
  "s_add_u32 s51, s44, 32768\n" \
  "buffer_load_dwordx4 v0, s[40:43], s51 offen sc1 lds\n" \
  "v_mfma_f32_32x32x16_bf16 v[32:47], a[68:71], v[68:71], v[32:47]\n" \
  "ds_read_b128 v[92:95], v3 offset:23552\n" \
  "ds_read2st64_b64 v[124:127], v4 offset0:14 offset1:15\n" \
  "v_exp_f32 v165, v165\n" \
  "v_mul_f32 v166, v166, v150\n" \
  "s_waitcnt lgkmcnt(9)\n" \
  "v_mfma_f32_32x32x16_bf16 v[32:47], a[72:75], v[72:75], v[32:47]\n" \
  "ds_read_b128 v[64:67], v3 offset:24576\n" \
  "v_fmaak_f32 v167, v167, v176, 0xc0135761\n" \
  "v_mul_f32 v160, v152, v152\n" \
  "v_mul_f32 v147, v147, v163\n" \
  "buffer_load_dwordx4 v0, s[40:43], s51 offen offset:1024 sc1 lds\n" \
  "v_mfma_f32_32x32x16_bf16 v[32:47], a[76:79], v[76:79], v[32:47]\n" \
  "ds_read_b128 v[68:71], v3 offset:25600\n" \
  "v_rcp_f32 v164, v164\n" \
  "v_add_f32 v165, 1.0, v165\n" \
  "s_waitcnt lgkmcnt(7)\n" \
  "v_mfma_f32_32x32x16_bf16 v[32:47], a[80:83], v[80:83], v[32:47]\n" \
  "ds_read_b128 v[72:75], v3 offset:26624\n" \
  "v_exp_f32 v166, v166\n" \
  "v_mul_f32 v167, v167, v151\n" \
  "v_fmaak_f32 v160, v160, v176, 0xc0135761\n" \
  "v_mul_f32 v161, v153, v153\n" \
  "v_mfma_f32_32x32x16_bf16 v[32:47], a[84:87], v[84:87], v[32:47]\n" \
  "ds_read_b128 v[76:79], v3 offset:27648\n" \
  "v_cvt_pk_bf16_f32 v169, v146, v147\n" \
  "v_mul_f32 v148, v148, v164\n" \
  "buffer_load_dwordx4 v0, s[40:43], s51 offen offset:2048 sc1 lds\n" \
  "s_waitcnt lgkmcnt(5)\n" \
  "v_mfma_f32_32x32x16_bf16 v[32:47], a[88:91], v[88:91], v[32:47]\n" \
  "ds_read_b128 v[80:83], v3 offset:28672\n" \
  "v_rcp_f32 v165, v165\n" \
  "v_add_f32 v166, 1.0, v166\n" \
  "v_exp_f32 v167, v167\n" \
  "v_mfma_f32_32x32x16_bf16 v[32:47], a[92:95], v[92:95], v[32:47]\n" \
  "ds_read_b128 v[84:87], v3 offset:29696\n" \
  "v_mul_f32 v160, v160, v152\n" \
  "v_fmaak_f32 v161, v161, v176, 0xc0135761\n" \
  "buffer_load_dwordx4 v0, s[40:43], s51 offen offset:3072 sc1 lds\n" \
  "s_waitcnt lgkmcnt(4)\n" \
  "v_mfma_f32_32x32x16_bf16 v[32:47], a[96:99], v[64:67], v[32:47]\n" \
  "ds_read_b128 v[88:91], v3 offset:30720\n" \
  "v_mul_f32 v162, v154, v154\n" \
  "v_mul_f32 v149, v149, v165\n" \
  "v_rcp_f32 v166, v166\n" \
  "v_mfma_f32_32x32x16_bf16 v[32:47], a[100:103], v[68:71], v[32:47]\n" \
  "v_mov_b32 v14, 4\n" \
  "ds_read_b128 v[92:95], v3 offset:31744\n" \
  "v_add_f32 v167, 1.0, v167\n" \
  "v_exp_f32 v160, v160\n" \
  "v_mul_f32 v161, v161, v153\n" \
  "v_fmaak_f32 v162, v162, v176, 0xc0135761\n" \
  "s_waitcnt lgkmcnt(4)\n" \
  "v_mfma_f32_32x32x16_bf16 v[32:47], a[104:107], v[72:75], v[32:47]\n" \
  "s_waitcnt vmcnt(9)\n" \
  "s_barrier\n" \
  "buffer_atomic_add v14, v8, s[36:39], 0 offen\n" \
  "ds_read_b128 v[64:67], v3 offset:32768\n" \
  "v_mul_f32 v163, v155, v155\n" \
  "v_cvt_pk_bf16_f32 v170, v148, v149\n" \
  "v_mul_f32 v150, v150, v166\n" \
  "v_rcp_f32 v167, v167\n" \
  "v_mfma_f32_32x32x16_bf16 v[32:47], a[108:111], v[76:79], v[32:47]\n" \
  "ds_read_b128 v[68:71], v3 offset:33792\n" \
  "v_add_f32 v160, 1.0, v160\n" \
  "s_add_u32 m0, s48, 0\n" \
  "s_nop 0\n" \
  "buffer_load_dwordx4 v1, s[40:43], s45 offen sc1 lds\n" \
  "s_waitcnt lgkmcnt(4)\n" \
  "v_mfma_f32_32x32x16_bf16 v[32:47], a[112:115], v[80:83], v[32:47]\n" \
  "ds_read_b128 v[72:75], v3 offset:34816\n" \
  "v_exp_f32 v161, v161\n" \
  "v_mul_f32 v162, v162, v154\n" \
  "v_fmaak_f32 v163, v163, v176, 0xc0135761\n" \
  "v_mul_f32 v164, v156, v156\n" \
  "v_mfma_f32_32x32x16_bf16 v[32:47], a[116:119], v[84:87], v[32:47]\n" \
  "ds_read_b128 v[76:79], v3 offset:35840\n" \
  "v_mul_f32 v151, v151, v167\n" \
  "buffer_load_dwordx4 v1, s[40:43], s45 offen offset:1024 sc1 lds\n" \
  "s_waitcnt lgkmcnt(4)\n" \
  "v_mfma_f32_32x32x16_bf16 v[32:47], a[120:123], v[88:91], v[32:47]\n" \
  "ds_read_b128 v[80:83], v3 offset:36864\n" \
  "v_rcp_f32 v160, v160\n" \
  "v_add_f32 v161, 1.0, v161\n" \
  "v_exp_f32 v162, v162\n" \
  "v_mfma_f32_32x32x16_bf16 v[32:47], a[124:127], v[92:95], v[32:47]\n" \
  "ds_read_b128 v[84:87], v3 offset:37888\n" \
  "v_mul_f32 v163, v163, v155\n" \
  "v_fmaak_f32 v164, v164, v176, 0xc0135761\n" \
  "v_mul_f32 v165, v157, v157\n" \
  "v_cvt_pk_bf16_f32 v171, v150, v151\n" \
  "buffer_store_dwordx4 v[168:171], v142, s[36:39], s54 offen sc1\n" \
  "s_waitcnt lgkmcnt(4)\n" \
  "v_mfma_f32_32x32x16_bf16 v[32:47], a[128:131], v[64:67], v[32:47]\n" \
  "ds_read_b128 v[88:91], v3 offset:38912\n" \
  "v_mul_f32 v152, v152, v160\n" \
  "s_add_u32 m0, s47, 32768\n" \
  "s_add_u32 s51, s44, 65536\n" \
  "buffer_load_dwordx4 v0, s[40:43], s51 offen sc1 lds\n" \
  "v_mfma_f32_32x32x16_bf16 v[32:47], a[132:135], v[68:71], v[32:47]\n" \
  "ds_read_b128 v[92:95], v3 offset:39936\n" \
  "v_rcp_f32 v161, v161\n" \
  "v_add_f32 v162, 1.0, v162\n" \
  "v_exp_f32 v163, v163\n" \
  "s_waitcnt lgkmcnt(4)\n" \
  "v_mfma_f32_32x32x16_bf16 v[32:47], a[136:139], v[72:75], v[32:47]\n" \
  "ds_read_b128 v[64:67], v3 offset:40960\n" \
  "v_mul_f32 v164, v164, v156\n" \
  "v_fmaak_f32 v165, v165, v176, 0xc0135761\n" \
  "v_mul_f32 v166, v158, v158\n" \
  "v_mul_f32 v153, v153, v161\n" \
  "v_mfma_f32_32x32x16_bf16 v[32:47], a[140:143], v[76:79], v[32:47]\n" \
  "ds_read_b128 v[68:71], v3 offset:41984\n" \
  "v_rcp_f32 v162, v162\n" \
  "v_add_f32 v163, 1.0, v163\n" \
  "buffer_load_dwordx4 v0, s[40:43], s51 offen offset:1024 sc1 lds\n" \
  "s_waitcnt lgkmcnt(4)\n" \
  "v_mfma_f32_32x32x16_bf16 v[32:47], a[144:147], v[80:83], v[32:47]\n" \
  "ds_read_b128 v[72:75], v3 offset:43008\n" \
  "v_exp_f32 v164, v164\n" \
  "v_mul_f32 v165, v165, v157\n" \
  "v_fmaak_f32 v166, v166, v176, 0xc0135761\n" \
  "v_mfma_f32_32x32x16_bf16 v[32:47], a[148:151], v[84:87], v[32:47]\n" \
  "ds_read_b128 v[76:79], v3 offset:44032\n" \
  "v_mul_f32 v167, v159, v159\n" \
  "v_cvt_pk_bf16_f32 v172, v152, v153\n" \
  "v_mul_f32 v154, v154, v162\n" \
  "v_rcp_f32 v163, v163\n" \
  "s_waitcnt lgkmcnt(4)\n" \
  "v_mfma_f32_32x32x16_bf16 v[32:47], a[152:155], v[88:91], v[32:47]\n" \
  "ds_read_b128 v[80:83], v3 offset:45056\n" \
  "v_add_f32 v164, 1.0, v164\n" \
  "buffer_load_dwordx4 v0, s[40:43], s51 offen offset:2048 sc1 lds\n" \
  "v_mfma_f32_32x32x16_bf16 v[32:47], a[156:159], v[92:95], v[32:47]\n" \
  "ds_read_b128 v[84:87], v3 offset:46080\n" \
  "v_exp_f32 v165, v165\n" \
  "v_mul_f32 v166, v166, v158\n" \
  "v_fmaak_f32 v167, v167, v176, 0xc0135761\n" \
  "v_mul_f32 v155, v155, v163\n" \
  "s_waitcnt lgkmcnt(4)\n" \
  "v_mfma_f32_32x32x16_bf16 v[32:47], a[160:163], v[64:67], v[32:47]\n" \
  "ds_read_b128 v[88:91], v3 offset:47104\n" \
  "v_rcp_f32 v164, v164\n" \
  "v_add_f32 v165, 1.0, v165\n" \
  "v_exp_f32 v166, v166\n" \
  "v_mfma_f32_32x32x16_bf16 v[32:47], a[164:167], v[68:71], v[32:47]\n" \
  "ds_read_b128 v[92:95], v3 offset:48128\n" \
  "v_mul_f32 v167, v167, v159\n" \
  "v_cvt_pk_bf16_f32 v173, v154, v155\n" \
  "buffer_load_dwordx4 v0, s[40:43], s51 offen offset:3072 sc1 lds\n" \
  "s_waitcnt lgkmcnt(4)\n" \
  "v_mfma_f32_32x32x16_bf16 v[32:47], a[168:171], v[72:75], v[32:47]\n" \
  "ds_read_b128 v[64:67], v3 offset:49152\n" \
  "v_mul_f32 v156, v156, v164\n" \
  "v_rcp_f32 v165, v165\n" \
  "v_add_f32 v166, 1.0, v166\n" \
  "v_mfma_f32_32x32x16_bf16 v[32:47], a[172:175], v[76:79], v[32:47]\n" \
  "ds_read_b128 v[68:71], v3 offset:50176\n" \
  "v_exp_f32 v167, v167\n" \
  "s_add_u32 m0, s47, 49152\n" \
  "s_add_u32 s51, s44, 98304\n" \
  "buffer_load_dwordx4 v0, s[40:43], s51 offen sc1 lds\n" \
  "s_waitcnt lgkmcnt(4)\n" \
  "v_mfma_f32_32x32x16_bf16 v[32:47], a[176:179], v[80:83], v[32:47]\n" \
  "ds_read_b128 v[72:75], v3 offset:51200\n" \
  "v_mul_f32 v157, v157, v165\n" \
  "v_rcp_f32 v166, v166\n" \
  "v_add_f32 v167, 1.0, v167\n" \
  "v_mfma_f32_32x32x16_bf16 v[32:47], a[180:183], v[84:87], v[32:47]\n" \
  "ds_read_b128 v[76:79], v3 offset:52224\n" \
  "v_cvt_pk_bf16_f32 v174, v156, v157\n" \
  "v_mul_f32 v158, v158, v166\n" \
  "v_rcp_f32 v167, v167\n" \
  "s_nop 0\n" \
  "v_mul_f32 v159, v159, v167\n" \
  "s_waitcnt lgkmcnt(4)\n" \
  "v_mfma_f32_32x32x16_bf16 v[32:47], a[184:187], v[88:91], v[32:47]\n" \
  "ds_read_b128 v[80:83], v3 offset:53248\n" \
  "v_cvt_pk_bf16_f32 v175, v158, v159\n" \
  "buffer_store_dwordx4 v[172:175], v142, s[36:39], s54 offen offset:1024 sc1\n" \
  "buffer_load_dwordx4 v0, s[40:43], s51 offen offset:1024 sc1 lds\n" \
  "v_mfma_f32_32x32x16_bf16 v[32:47], a[188:191], v[92:95], v[32:47]\n" \
  "ds_read_b128 v[84:87], v3 offset:54272\n" \
  "v_add_f32 v10, v96, v98\n" \
  "v_add_f32 v11, v97, v99\n" \
  "v_add_f32 v10, v10, v100\n" \
  "s_waitcnt lgkmcnt(4)\n" \
  "v_mfma_f32_32x32x16_bf16 v[32:47], a[192:195], v[64:67], v[32:47]\n" \
  "ds_read_b128 v[88:91], v3 offset:55296\n" \
  "v_add_f32 v11, v11, v101\n" \
  "v_add_f32 v10, v10, v102\n" \
  "v_add_f32 v11, v11, v103\n" \
  "v_mfma_f32_32x32x16_bf16 v[32:47], a[196:199], v[68:71], v[32:47]\n" \
  "ds_read_b128 v[92:95], v3 offset:56320\n" \
  "v_add_f32 v10, v10, v104\n" \
  "v_add_f32 v11, v11, v105\n" \
  "buffer_load_dwordx4 v0, s[40:43], s51 offen offset:2048 sc1 lds\n" \
  "s_waitcnt lgkmcnt(4)\n" \
  "v_mfma_f32_32x32x16_bf16 v[32:47], a[200:203], v[72:75], v[32:47]\n" \
  "ds_read_b128 v[64:67], v3 offset:57344\n" \
  "v_add_f32 v10, v10, v106\n" \
  "v_add_f32 v11, v11, v107\n" \
  "v_add_f32 v10, v10, v108\n" \
  "v_mfma_f32_32x32x16_bf16 v[32:47], a[204:207], v[76:79], v[32:47]\n" \
  "ds_read_b128 v[68:71], v3 offset:58368\n" \
  "v_add_f32 v11, v11, v109\n" \
  "v_add_f32 v10, v10, v110\n" \
  "v_add_f32 v11, v11, v111\n" \
  "s_waitcnt lgkmcnt(4)\n" \
  "v_mfma_f32_32x32x16_bf16 v[32:47], a[208:211], v[80:83], v[32:47]\n" \
  "ds_read_b128 v[72:75], v3 offset:59392\n" \
  "v_add_f32 v10, v10, v112\n" \
  "v_add_f32 v11, v11, v113\n" \
  "buffer_load_dwordx4 v0, s[40:43], s51 offen offset:3072 sc1 lds\n" \
  "v_mfma_f32_32x32x16_bf16 v[32:47], a[212:215], v[84:87], v[32:47]\n" \
  "ds_read_b128 v[76:79], v3 offset:60416\n" \
  "v_add_f32 v10, v10, v114\n" \
  "v_add_f32 v11, v11, v115\n" \
  "v_add_f32 v10, v10, v116\n" \
  "s_waitcnt lgkmcnt(4)\n" \
  "v_mfma_f32_32x32x16_bf16 v[32:47], a[216:219], v[88:91], v[32:47]\n" \
  "ds_read_b128 v[80:83], v3 offset:61440\n" \
  "v_add_f32 v11, v11, v117\n" \
  "v_add_f32 v10, v10, v118\n" \
  "v_add_f32 v11, v11, v119\n" \
  "v_mfma_f32_32x32x16_bf16 v[32:47], a[220:223], v[92:95], v[32:47]\n" \
  "ds_read_b128 v[84:87], v3 offset:62464\n" \
  "v_add_f32 v10, v10, v120\n" \
  "v_add_f32 v11, v11, v121\n" \
  "v_add_f32 v10, v10, v122\n" \
  "s_waitcnt lgkmcnt(4)\n" \
  "v_mfma_f32_32x32x16_bf16 v[32:47], a[224:227], v[64:67], v[32:47]\n" \
  "ds_read_b128 v[88:91], v3 offset:63488\n" \
  "v_add_f32 v11, v11, v123\n" \
  "v_add_f32 v10, v10, v124\n" \
  "v_add_f32 v11, v11, v125\n" \
  "v_mfma_f32_32x32x16_bf16 v[32:47], a[228:231], v[68:71], v[32:47]\n" \
  "ds_read_b128 v[92:95], v3 offset:64512\n" \
  "v_add_f32 v10, v10, v126\n" \
  "v_add_f32 v11, v11, v127\n" \
  "v_mul_f32 v10, 0x3a800000, v10\n" \
  "s_waitcnt lgkmcnt(4)\n" \
  "v_mfma_f32_32x32x16_bf16 v[32:47], a[232:235], v[72:75], v[32:47]\n" \
  "v_mul_f32 v11, 0x3a800000, v11\n" \
  "v_mov_b32 v13, v10\n" \
  "v_mov_b32 v12, v11\n" \
  "s_nop 0\n" \
  "v_permlane32_swap_b32 v13, v10\n" \
  "v_permlane32_swap_b32 v12, v11\n" \
  "v_mfma_f32_32x32x16_bf16 v[32:47], a[236:239], v[76:79], v[32:47]\n" \
  "v_add_f32 v10, v13, v10\n" \
  "v_add_f32 v11, v12, v11\n" \
  "v_fma_f32 v11, -v10, v10, v11\n" \
  "v_max_f32 v11, 0, v11\n" \
  "v_add_f32 v13, 0x3727c5ac, v11\n" \
  "s_waitcnt lgkmcnt(2)\n" \
  "v_mfma_f32_32x32x16_bf16 v[32:47], a[240:243], v[80:83], v[32:47]\n" \
  "v_rsq_f32 v12, v13\n" \
  "s_nop 0\n" \
  "v_mul_f32 v10, -v10, v12\n" \
  "v_mul_f32 v48, v10, v16\n" \
  "v_mul_f32 v49, v10, v17\n" \
  "v_mul_f32 v50, v10, v18\n" \
  "v_mfma_f32_32x32x16_bf16 v[32:47], a[244:247], v[84:87], v[32:47]\n" \
  "v_mul_f32 v51, v10, v19\n" \
  "v_mul_f32 v52, v10, v20\n" \
  "v_mul_f32 v53, v10, v21\n" \
  "v_mul_f32 v54, v10, v22\n" \
  "v_mul_f32 v55, v10, v23\n" \
  "s_waitcnt lgkmcnt(0)\n" \
  "v_mfma_f32_32x32x16_bf16 v[32:47], a[248:251], v[88:91], v[32:47]\n" \
  "v_mul_f32 v56, v10, v24\n" \
  "v_mul_f32 v57, v10, v25\n" \
  "v_mul_f32 v58, v10, v26\n" \
  "v_mul_f32 v59, v10, v27\n" \
  "v_mfma_f32_32x32x16_bf16 v[32:47], a[252:255], v[92:95], v[32:47]\n" \
  "v_mul_f32 v60, v10, v28\n" \
  "v_mul_f32 v61, v10, v29\n" \
  "v_mul_f32 v62, v10, v30\n" \
  "v_mul_f32 v63, v10, v31\n" \
  "s_cmp_lg_u32 s42, 0\n" \
  "s_nop 6\n" \
  "v_pk_fma_f32 v[144:145], v[12:13], v[32:33], v[48:49] op_sel_hi:[0,1,1]\n" \
  "v_pk_fma_f32 v[146:147], v[12:13], v[34:35], v[50:51] op_sel_hi:[0,1,1]\n" \
  "v_pk_fma_f32 v[148:149], v[12:13], v[36:37], v[52:53] op_sel_hi:[0,1,1]\n" \
  "v_pk_fma_f32 v[150:151], v[12:13], v[38:39], v[54:55] op_sel_hi:[0,1,1]\n" \
  "v_pk_fma_f32 v[152:153], v[12:13], v[40:41], v[56:57] op_sel_hi:[0,1,1]\n" \
  "v_pk_fma_f32 v[154:155], v[12:13], v[42:43], v[58:59] op_sel_hi:[0,1,1]\n" \
  "v_pk_fma_f32 v[156:157], v[12:13], v[44:45], v[60:61] op_sel_hi:[0,1,1]\n" \
  "v_pk_fma_f32 v[158:159], v[12:13], v[46:47], v[62:63] op_sel_hi:[0,1,1]\n" \
  "s_cbranch_scc1 SPM3_e_%=\n" \
  "v_mov_b32 v176, 0xbdd2d3e7\n" \
  "v_mov_b32 v178, 0xc0135761\n" \
  "v_pk_mul_f32 v[160:161], v[144:145], v[144:145]\n" \
  "v_pk_mul_f32 v[162:163], v[146:147], v[146:147]\n" \
  "v_pk_mul_f32 v[164:165], v[148:149], v[148:149]\n" \
  "v_pk_mul_f32 v[166:167], v[150:151], v[150:151]\n" \
  "v_pk_fma_f32 v[160:161], v[160:161], v[176:177], v[178:179] op_sel_hi:[1,0,0]\n" \
  "v_pk_fma_f32 v[162:163], v[162:163], v[176:177], v[178:179] op_sel_hi:[1,0,0]\n" \
  "v_pk_fma_f32 v[164:165], v[164:165], v[176:177], v[178:179] op_sel_hi:[1,0,0]\n" \
  "v_pk_fma_f32 v[166:167], v[166:167], v[176:177], v[178:179] op_sel_hi:[1,0,0]\n" \
  "v_pk_mul_f32 v[160:161], v[160:161], v[144:145]\n" \
  "v_pk_mul_f32 v[162:163], v[162:163], v[146:147]\n" \
  "v_pk_mul_f32 v[164:165], v[164:165], v[148:149]\n" \
  "v_pk_mul_f32 v[166:167], v[166:167], v[150:151]\n" \
  "v_exp_f32 v160, v160\n" \
  "v_exp_f32 v161, v161\n" \
  "v_exp_f32 v162, v162\n" \
  "v_pk_add_f32 v[160:161], v[160:161], 1.0 op_sel_hi:[1,0]\n" \
  "v_exp_f32 v163, v163\n" \
  "v_exp_f32 v164, v164\n" \
  "v_pk_add_f32 v[162:163], v[162:163], 1.0 op_sel_hi:[1,0]\n" \
  "v_exp_f32 v165, v165\n" \
  "v_exp_f32 v166, v166\n" \
  "v_pk_add_f32 v[164:165], v[164:165], 1.0 op_sel_hi:[1,0]\n" \
  "v_exp_f32 v167, v167\n" \
  "v_rcp_f32 v160, v160\n" \
  "v_pk_add_f32 v[166:167], v[166:167], 1.0 op_sel_hi:[1,0]\n" \
  "v_rcp_f32 v161, v161\n" \
  "v_rcp_f32 v162, v162\n" \
  "v_pk_mul_f32 v[144:145], v[144:145], v[160:161]\n" \
  "v_rcp_f32 v163, v163\n" \
  "v_cvt_pk_bf16_f32 v168, v144, v145\n" \
  "v_rcp_f32 v164, v164\n" \
  "v_pk_mul_f32 v[146:147], v[146:147], v[162:163]\n" \
  "v_rcp_f32 v165, v165\n" \
  "v_cvt_pk_bf16_f32 v169, v146, v147\n" \
  "v_rcp_f32 v166, v166\n" \
  "v_pk_mul_f32 v[148:149], v[148:149], v[164:165]\n" \
  "v_rcp_f32 v167, v167\n" \
  "v_cvt_pk_bf16_f32 v170, v148, v149\n" \
  "v_pk_mul_f32 v[150:151], v[150:151], v[166:167]\n" \
  "v_cvt_pk_bf16_f32 v171, v150, v151\n" \
  "buffer_store_dwordx4 v[168:171], v15, s[36:39], s52 offen sc1\n" \
  "v_pk_mul_f32 v[160:161], v[152:153], v[152:153]\n" \
  "v_pk_mul_f32 v[162:163], v[154:155], v[154:155]\n" \
  "v_pk_mul_f32 v[164:165], v[156:157], v[156:157]\n" \
  "v_pk_mul_f32 v[166:167], v[158:159], v[158:159]\n" \
  "v_pk_fma_f32 v[160:161], v[160:161], v[176:177], v[178:179] op_sel_hi:[1,0,0]\n" \
  "v_pk_fma_f32 v[162:163], v[162:163], v[176:177], v[178:179] op_sel_hi:[1,0,0]\n" \
  "v_pk_fma_f32 v[164:165], v[164:165], v[176:177], v[178:179] op_sel_hi:[1,0,0]\n" \
  "v_pk_fma_f32 v[166:167], v[166:167], v[176:177], v[178:179] op_sel_hi:[1,0,0]\n" \
  "v_pk_mul_f32 v[160:161], v[160:161], v[152:153]\n" \
  "v_pk_mul_f32 v[162:163], v[162:163], v[154:155]\n" \
  "v_pk_mul_f32 v[164:165], v[164:165], v[156:157]\n" \
  "v_pk_mul_f32 v[166:167], v[166:167], v[158:159]\n" \
  "v_exp_f32 v160, v160\n" \
  "v_exp_f32 v161, v161\n" \
  "v_exp_f32 v162, v162\n" \
  "v_pk_add_f32 v[160:161], v[160:161], 1.0 op_sel_hi:[1,0]\n" \
  "v_exp_f32 v163, v163\n" \
  "v_exp_f32 v164, v164\n" \
  "v_pk_add_f32 v[162:163], v[162:163], 1.0 op_sel_hi:[1,0]\n" \
  "v_exp_f32 v165, v165\n" \
  "v_exp_f32 v166, v166\n" \
  "v_pk_add_f32 v[164:165], v[164:165], 1.0 op_sel_hi:[1,0]\n" \
  "v_exp_f32 v167, v167\n" \
  "v_rcp_f32 v160, v160\n" \
  "v_pk_add_f32 v[166:167], v[166:167], 1.0 op_sel_hi:[1,0]\n" \
  "v_rcp_f32 v161, v161\n" \
  "v_rcp_f32 v162, v162\n" \
  "v_pk_mul_f32 v[152:153], v[152:153], v[160:161]\n" \
  "v_rcp_f32 v163, v163\n" \
  "v_cvt_pk_bf16_f32 v172, v152, v153\n" \
  "v_rcp_f32 v164, v164\n" \
  "v_pk_mul_f32 v[154:155], v[154:155], v[162:163]\n" \
  "v_rcp_f32 v165, v165\n" \
  "v_cvt_pk_bf16_f32 v173, v154, v155\n" \
  "v_rcp_f32 v166, v166\n" \
  "v_pk_mul_f32 v[156:157], v[156:157], v[164:165]\n" \
  "v_rcp_f32 v167, v167\n" \
  "v_cvt_pk_bf16_f32 v174, v156, v157\n" \
  "v_pk_mul_f32 v[158:159], v[158:159], v[166:167]\n" \
  "v_cvt_pk_bf16_f32 v175, v158, v159\n" \
  "buffer_store_dwordx4 v[172:175], v15, s[36:39], s52 offen offset:1024 sc1\n" \
  "SPM3_e_%=:\n" \
  "s_nop 1\n"
#define SPL3_ASM \
  "s_add_u32 m0, s59, 0\n" \
  "s_nop 0\n" \
  "buffer_load_dwordx4 v0, s[36:39], s56 offen sc1 lds\n" \
  "buffer_load_dwordx4 v0, s[36:39], s56 offen offset:1024 sc1 lds\n" \
  "buffer_load_dwordx4 v0, s[36:39], s56 offen offset:2048 sc1 lds\n" \
  "buffer_load_dwordx4 v0, s[36:39], s56 offen offset:3072 sc1 lds\n" \
  "s_add_u32 m0, s59, 16384\n" \
  "s_add_u32 s51, s56, 32768\n" \
  "buffer_load_dwordx4 v0, s[36:39], s51 offen sc1 lds\n" \
  "buffer_load_dwordx4 v0, s[36:39], s51 offen offset:1024 sc1 lds\n" \
  "buffer_load_dwordx4 v0, s[36:39], s51 offen offset:2048 sc1 lds\n" \
  "buffer_load_dwordx4 v0, s[36:39], s51 offen offset:3072 sc1 lds\n" \
  "s_xor_b32 m0, s49, 1024\n" \
  "s_nop 0\n" \
  "buffer_load_dword v9, s[36:39], 0 offen sc1 lds\n" \
  "s_add_u32 m0, s59, 32768\n" \
  "s_add_u32 s51, s56, 65536\n" \
  "buffer_load_dwordx4 v0, s[36:39], s51 offen sc1 lds\n" \
  "buffer_load_dwordx4 v0, s[36:39], s51 offen offset:1024 sc1 lds\n" \
  "buffer_load_dwordx4 v0, s[36:39], s51 offen offset:2048 sc1 lds\n" \
  "buffer_load_dwordx4 v0, s[36:39], s51 offen offset:3072 sc1 lds\n" \
  "s_mov_b64 s[40:41], s[36:37]\n" \
  "s_mov_b32 s43, s39\n" \
  "s_waitcnt vmcnt(9)\n" \
  "s_barrier\n" \
  "ds_read_b128 v[64:67], v3 offset:0\n" \
  "ds_read_b128 v[68:71], v3 offset:1024\n" \
  "ds_read_b128 v[72:75], v3 offset:2048\n" \
  "ds_read_b128 v[76:79], v3 offset:3072\n" \
  "ds_read_b128 v[80:83], v3 offset:4096\n" \
  "ds_read_b128 v[84:87], v3 offset:5120\n" \
  "s_waitcnt lgkmcnt(4)\n" \
  "v_mfma_f32_32x32x16_bf16 v[32:47], a[0:3], v[64:67], 0\n" \
  "ds_read_b128 v[88:91], v3 offset:6144\n" \
  "s_add_u32 m0, s48, 0\n" \
  "s_nop 0\n" \
  "buffer_load_dwordx4 v1, s[36:39], s57 offen sc1 lds\n" \
  "v_mfma_f32_32x32x16_bf16 v[32:47], a[4:7], v[68:71], v[32:47]\n" \
  "ds_read_b128 v[92:95], v3 offset:7168\n" \
  "s_waitcnt lgkmcnt(4)\n" \
  "v_mfma_f32_32x32x16_bf16 v[32:47], a[8:11], v[72:75], v[32:47]\n" \
  "ds_read_b128 v[64:67], v3 offset:8192\n" \
  "buffer_load_dwordx4 v1, s[36:39], s57 offen offset:1024 sc1 lds\n" \
  "v_mfma_f32_32x32x16_bf16 v[32:47], a[12:15], v[76:79], v[32:47]\n" \
  "ds_read_b128 v[68:71], v3 offset:9216\n" \
  "s_waitcnt lgkmcnt(4)\n" \
  "v_mfma_f32_32x32x16_bf16 v[32:47], a[16:19], v[80:83], v[32:47]\n" \
  "ds_read_b128 v[72:75], v3 offset:10240\n" \
  "s_add_u32 m0, s59, 49152\n" \
  "s_add_u32 s51, s56, 98304\n" \
  "buffer_load_dwordx4 v0, s[36:39], s51 offen sc1 lds\n" \
  "v_mfma_f32_32x32x16_bf16 v[32:47], a[20:23], v[84:87], v[32:47]\n" \
  "ds_read_b128 v[76:79], v3 offset:11264\n" \
  "s_waitcnt lgkmcnt(4)\n" \
  "v_mfma_f32_32x32x16_bf16 v[32:47], a[24:27], v[88:91], v[32:47]\n" \
  "ds_read_b128 v[80:83], v3 offset:12288\n" \
  "buffer_load_dwordx4 v0, s[36:39], s51 offen offset:1024 sc1 lds\n" \
  "v_mfma_f32_32x32x16_bf16 v[32:47], a[28:31], v[92:95], v[32:47]\n" \
  "ds_read_b128 v[84:87], v3 offset:13312\n" \
  "s_waitcnt lgkmcnt(4)\n" \
  "v_mfma_f32_32x32x16_bf16 v[32:47], a[32:35], v[64:67], v[32:47]\n" \
  "ds_read_b128 v[88:91], v3 offset:14336\n" \
  "buffer_load_dwordx4 v0, s[36:39], s51 offen offset:2048 sc1 lds\n" \
  "v_mfma_f32_32x32x16_bf16 v[32:47], a[36:39], v[68:71], v[32:47]\n" \
  "ds_read_b128 v[92:95], v3 offset:15360\n" \
  "s_waitcnt lgkmcnt(4)\n" \
  "v_mfma_f32_32x32x16_bf16 v[32:47], a[40:43], v[72:75], v[32:47]\n" \
  "s_waitcnt vmcnt(9)\n" \
  "s_barrier\n" \
  "ds_read_b128 v[64:67], v3 offset:16384\n" \
  "ds_read_b32 v14, v6\n" \
  "buffer_load_dwordx4 v0, s[36:39], s51 offen offset:3072 sc1 lds\n" \
  "v_mfma_f32_32x32x16_bf16 v[32:47], a[44:47], v[76:79], v[32:47]\n" \
  "ds_read_b128 v[68:71], v3 offset:17408\n" \
  "s_waitcnt lgkmcnt(5)\n" \
  "v_mfma_f32_32x32x16_bf16 v[32:47], a[48:51], v[80:83], v[32:47]\n" \
  "ds_read_b128 v[72:75], v3 offset:18432\n" \
  "v_mfma_f32_32x32x16_bf16 v[32:47], a[52:55], v[84:87], v[32:47]\n" \
  "ds_read_b128 v[76:79], v3 offset:19456\n" \
  "s_waitcnt lgkmcnt(5)\n" \
  "v_mfma_f32_32x32x16_bf16 v[32:47], a[56:59], v[88:91], v[32:47]\n" \
  "ds_read_b128 v[80:83], v3 offset:20480\n" \
  "s_waitcnt lgkmcnt(4)\n" \
  "v_cmp_gt_u32 vcc, s50, v14\n" \
  "v_mfma_f32_32x32x16_bf16 v[32:47], a[60:63], v[92:95], v[32:47]\n" \
  "ds_read_b128 v[84:87], v3 offset:21504\n" \
  "s_cmp_eq_u64 vcc, 0\n" \
  "s_cselect_b32 s42, s38, 0\n" \
  "s_waitcnt lgkmcnt(4)\n" \
  "v_mfma_f32_32x32x16_bf16 v[32:47], a[64:67], v[64:67], v[32:47]\n" \
  "ds_read_b128 v[88:91], v3 offset:22528\n" \
  "v_mfma_f32_32x32x16_bf16 v[32:47], a[68:71], v[68:71], v[32:47]\n" \
  "ds_read_b128 v[92:95], v3 offset:23552\n" \
  "s_add_u32 m0, s47, 0\n" \
  "s_nop 0\n" \
  "buffer_load_dwordx4 v0, s[40:43], s44 offen sc1 lds\n" \
  "s_waitcnt lgkmcnt(4)\n" \
  "v_mfma_f32_32x32x16_bf16 v[32:47], a[72:75], v[72:75], v[32:47]\n" \
  "ds_read_b128 v[64:67], v3 offset:24576\n" \
  "v_mfma_f32_32x32x16_bf16 v[32:47], a[76:79], v[76:79], v[32:47]\n" \
  "ds_read_b128 v[68:71], v3 offset:25600\n" \
  "buffer_load_dwordx4 v0, s[40:43], s44 offen offset:1024 sc1 lds\n" \
  "s_waitcnt lgkmcnt(4)\n" \
  "v_mfma_f32_32x32x16_bf16 v[32:47], a[80:83], v[80:83], v[32:47]\n" \
  "ds_read_b128 v[72:75], v3 offset:26624\n" \
  "v_mfma_f32_32x32x16_bf16 v[32:47], a[84:87], v[84:87], v[32:47]\n" \
  "ds_read_b128 v[76:79], v3 offset:27648\n" \
  "s_waitcnt lgkmcnt(4)\n" \
  "v_mfma_f32_32x32x16_bf16 v[32:47], a[88:91], v[88:91], v[32:47]\n" \
  "ds_read_b128 v[80:83], v3 offset:28672\n" \
  "buffer_load_dwordx4 v0, s[40:43], s44 offen offset:2048 sc1 lds\n" \
  "v_mfma_f32_32x32x16_bf16 v[32:47], a[92:95], v[92:95], v[32:47]\n" \
  "ds_read_b128 v[84:87], v3 offset:29696\n" \
  "s_waitcnt lgkmcnt(4)\n" \
  "v_mfma_f32_32x32x16_bf16 v[32:47], a[96:99], v[64:67], v[32:47]\n" \
  "ds_read_b128 v[88:91], v3 offset:30720\n" \
  "v_mfma_f32_32x32x16_bf16 v[32:47], a[100:103], v[68:71], v[32:47]\n" \
  "v_mov_b32 v14, 4\n" \
  "ds_read_b128 v[92:95], v3 offset:31744\n" \
  "buffer_load_dwordx4 v0, s[40:43], s44 offen offset:3072 sc1 lds\n" \
  "s_waitcnt lgkmcnt(4)\n" \
  "v_mfma_f32_32x32x16_bf16 v[32:47], a[104:107], v[72:75], v[32:47]\n" \
  "s_waitcnt vmcnt(8)\n" \
  "s_barrier\n" \
  "ds_read_b128 v[64:67], v3 offset:32768\n" \
  "ds_read2st64_b64 v[96:99], v4 offset0:0 offset1:1\n" \
  "v_mfma_f32_32x32x16_bf16 v[32:47], a[108:111], v[76:79], v[32:47]\n" \
  "ds_read_b128 v[68:71], v3 offset:33792\n" \
  "ds_read2st64_b64 v[100:103], v4 offset0:2 offset1:3\n" \
  "s_waitcnt lgkmcnt(6)\n" \
  "v_mfma_f32_32x32x16_bf16 v[32:47], a[112:115], v[80:83], v[32:47]\n" \
  "ds_read_b128 v[72:75], v3 offset:34816\n" \
  "ds_read2st64_b64 v[104:107], v4 offset0:4 offset1:5\n" \
  "s_xor_b32 m0, s49, 0\n" \
  "s_nop 0\n" \
  "buffer_load_dword v7, s[40:43], 0 offen sc1 lds\n" \
  "v_mfma_f32_32x32x16_bf16 v[32:47], a[116:119], v[84:87], v[32:47]\n" \
  "ds_read_b128 v[76:79], v3 offset:35840\n" \
  "ds_read2st64_b64 v[108:111], v4 offset0:6 offset1:7\n" \
  "s_waitcnt lgkmcnt(8)\n" \
  "v_mfma_f32_32x32x16_bf16 v[32:47], a[120:123], v[88:91], v[32:47]\n" \
  "ds_read_b128 v[80:83], v3 offset:36864\n" \
  "ds_read2st64_b64 v[112:115], v4 offset0:8 offset1:9\n" \
  "s_add_u32 m0, s47, 16384\n" \
  "s_add_u32 s51, s44, 32768\n" \
  "buffer_load_dwordx4 v0, s[40:43], s51 offen sc1 lds\n" \
  "v_mfma_f32_32x32x16_bf16 v[32:47], a[124:127], v[92:95], v[32:47]\n" \
  "ds_read_b128 v[84:87], v3 offset:37888\n" \
  "ds_read2st64_b64 v[116:119], v4 offset0:10 offset1:11\n" \
  "s_waitcnt lgkmcnt(9)\n" \
  "v_mfma_f32_32x32x16_bf16 v[32:47], a[128:131], v[64:67], v[32:47]\n" \
  "ds_read_b128 v[88:91], v3 offset:38912\n" \
  "ds_read2st64_b64 v[120:123], v4 offset0:12 offset1:13\n" \
  "v_mfma_f32_32x32x16_bf16 v[32:47], a[132:135], v[68:71], v[32:47]\n" \
  "ds_read_b128 v[92:95], v3 offset:39936\n" \
  "ds_read2st64_b64 v[124:127], v4 offset0:14 offset1:15\n" \
  "v_add_f32 v10, v96, v98\n" \
  "v_add_f32 v11, v97, v99\n" \
  "buffer_load_dwordx4 v0, s[40:43], s51 offen offset:1024 sc1 lds\n" \
  "s_waitcnt lgkmcnt(9)\n" \
  "v_mfma_f32_32x32x16_bf16 v[32:47], a[136:139], v[72:75], v[32:47]\n" \
  "ds_read_b128 v[64:67], v3 offset:40960\n" \
  "v_add_f32 v10, v10, v100\n" \
  "v_add_f32 v11, v11, v101\n" \
  "v_add_f32 v10, v10, v102\n" \
  "v_mfma_f32_32x32x16_bf16 v[32:47], a[140:143], v[76:79], v[32:47]\n" \
  "ds_read_b128 v[68:71], v3 offset:41984\n" \
  "v_add_f32 v11, v11, v103\n" \
  "v_add_f32 v10, v10, v104\n" \
  "s_waitcnt lgkmcnt(7)\n" \
  "v_mfma_f32_32x32x16_bf16 v[32:47], a[144:147], v[80:83], v[32:47]\n" \
  "ds_read_b128 v[72:75], v3 offset:43008\n" \
  "v_add_f32 v11, v11, v105\n" \
  "buffer_load_dwordx4 v0, s[40:43], s51 offen offset:2048 sc1 lds\n" \
  "v_mfma_f32_32x32x16_bf16 v[32:47], a[148:151], v[84:87], v[32:47]\n" \
  "ds_read_b128 v[76:79], v3 offset:44032\n" \
  "v_add_f32 v10, v10, v106\n" \
  "v_add_f32 v11, v11, v107\n" \
  "s_waitcnt lgkmcnt(5)\n" \
  "v_mfma_f32_32x32x16_bf16 v[32:47], a[152:155], v[88:91], v[32:47]\n" \
  "ds_read_b128 v[80:83], v3 offset:45056\n" \
  "v_add_f32 v10, v10, v108\n" \
  "v_add_f32 v11, v11, v109\n" \
  "v_mfma_f32_32x32x16_bf16 v[32:47], a[156:159], v[92:95], v[32:47]\n" \
  "ds_read_b128 v[84:87], v3 offset:46080\n" \
  "v_add_f32 v10, v10, v110\n" \
  "buffer_load_dwordx4 v0, s[40:43], s51 offen offset:3072 sc1 lds\n" \
  "s_waitcnt lgkmcnt(4)\n" \
  "v_mfma_f32_32x32x16_bf16 v[32:47], a[160:163], v[64:67], v[32:47]\n" \
  "ds_read_b128 v[88:91], v3 offset:47104\n" \
  "v_add_f32 v11, v11, v111\n" \
  "v_add_f32 v10, v10, v112\n" \
  "v_mfma_f32_32x32x16_bf16 v[32:47], a[164:167], v[68:71], v[32:47]\n" \
  "ds_read_b128 v[92:95], v3 offset:48128\n" \
  "v_add_f32 v11, v11, v113\n" \
  "v_add_f32 v10, v10, v114\n" \
  "s_waitcnt lgkmcnt(4)\n" \
  "v_mfma_f32_32x32x16_bf16 v[32:47], a[168:171], v[72:75], v[32:47]\n" \
  "s_waitcnt vmcnt(9)\n" \
  "s_barrier\n" \
  "ds_read_b128 v[64:67], v3 offset:49152\n" \
  "v_add_f32 v11, v11, v115\n" \
  "v_add_f32 v10, v10, v116\n" \
  "v_mfma_f32_32x32x16_bf16 v[32:47], a[172:175], v[76:79], v[32:47]\n" \
  "ds_read_b128 v[68:71], v3 offset:50176\n" \
  "v_add_f32 v11, v11, v117\n" \
  "s_add_u32 m0, s48, 0\n" \
  "s_nop 0\n" \
  "buffer_load_dwordx4 v1, s[40:43], s45 offen sc1 lds\n" \
  "s_waitcnt lgkmcnt(4)\n" \
  "v_mfma_f32_32x32x16_bf16 v[32:47], a[176:179], v[80:83], v[32:47]\n" \
  "ds_read_b128 v[72:75], v3 offset:51200\n" \
  "v_add_f32 v10, v10, v118\n" \
  "v_add_f32 v11, v11, v119\n" \
  "v_mfma_f32_32x32x16_bf16 v[32:47], a[180:183], v[84:87], v[32:47]\n" \
  "ds_read_b128 v[76:79], v3 offset:52224\n" \
  "v_add_f32 v10, v10, v120\n" \
  "buffer_load_dwordx4 v1, s[40:43], s45 offen offset:1024 sc1 lds\n" \
  "s_waitcnt lgkmcnt(4)\n" \
  "v_mfma_f32_32x32x16_bf16 v[32:47], a[184:187], v[88:91], v[32:47]\n" \
  "ds_read_b128 v[80:83], v3 offset:53248\n" \
  "v_add_f32 v11, v11, v121\n" \
  "v_add_f32 v10, v10, v122\n" \
  "v_mfma_f32_32x32x16_bf16 v[32:47], a[188:191], v[92:95], v[32:47]\n" \
  "ds_read_b128 v[84:87], v3 offset:54272\n" \
  "v_add_f32 v11, v11, v123\n" \
  "s_add_u32 m0, s47, 32768\n" \
  "s_add_u32 s51, s44, 65536\n" \
  "buffer_load_dwordx4 v0, s[40:43], s51 offen sc1 lds\n" \
  "s_waitcnt lgkmcnt(4)\n" \
  "v_mfma_f32_32x32x16_bf16 v[32:47], a[192:195], v[64:67], v[32:47]\n" \
  "ds_read_b128 v[88:91], v3 offset:55296\n" \
  "v_add_f32 v10, v10, v124\n" \
  "v_add_f32 v11, v11, v125\n" \
  "v_mfma_f32_32x32x16_bf16 v[32:47], a[196:199], v[68:71], v[32:47]\n" \
  "ds_read_b128 v[92:95], v3 offset:56320\n" \
  "v_add_f32 v10, v10, v126\n" \
  "buffer_load_dwordx4 v0, s[40:43], s51 offen offset:1024 sc1 lds\n" \
  "s_waitcnt lgkmcnt(4)\n" \
  "v_mfma_f32_32x32x16_bf16 v[32:47], a[200:203], v[72:75], v[32:47]\n" \
  "ds_read_b128 v[64:67], v3 offset:57344\n" \
  "v_add_f32 v11, v11, v127\n" \
  "v_mul_f32 v10, 0x3a800000, v10\n" \
  "v_mfma_f32_32x32x16_bf16 v[32:47], a[204:207], v[76:79], v[32:47]\n" \
  "ds_read_b128 v[68:71], v3 offset:58368\n" \
  "v_mul_f32 v11, 0x3a800000, v11\n" \
  "buffer_load_dwordx4 v0, s[40:43], s51 offen offset:2048 sc1 lds\n" \
  "s_waitcnt lgkmcnt(4)\n" \
  "v_mfma_f32_32x32x16_bf16 v[32:47], a[208:211], v[80:83], v[32:47]\n" \
  "ds_read_b128 v[72:75], v3 offset:59392\n" \
  "v_mov_b32 v13, v10\n" \
  "v_mov_b32 v12, v11\n" \
  "v_mfma_f32_32x32x16_bf16 v[32:47], a[212:215], v[84:87], v[32:47]\n" \
  "ds_read_b128 v[76:79], v3 offset:60416\n" \
  "v_permlane32_swap_b32 v13, v10\n" \
  "buffer_load_dwordx4 v0, s[40:43], s51 offen offset:3072 sc1 lds\n" \
  "s_waitcnt lgkmcnt(4)\n" \
  "v_mfma_f32_32x32x16_bf16 v[32:47], a[216:219], v[88:91], v[32:47]\n" \
  "ds_read_b128 v[80:83], v3 offset:61440\n" \
  "v_permlane32_swap_b32 v12, v11\n" \
  "v_add_f32 v10, v13, v10\n" \
  "v_mfma_f32_32x32x16_bf16 v[32:47], a[220:223], v[92:95], v[32:47]\n" \
  "ds_read_b128 v[84:87], v3 offset:62464\n" \
  "v_add_f32 v11, v12, v11\n" \
  "s_add_u32 m0, s47, 49152\n" \
  "s_add_u32 s51, s44, 98304\n" \
  "buffer_load_dwordx4 v0, s[40:43], s51 offen sc1 lds\n" \
  "s_waitcnt lgkmcnt(4)\n" \
  "v_mfma_f32_32x32x16_bf16 v[32:47], a[224:227], v[64:67], v[32:47]\n" \
  "ds_read_b128 v[88:91], v3 offset:63488\n" \
  "v_fma_f32 v11, -v10, v10, v11\n" \
  "v_max_f32 v11, 0, v11\n" \
  "v_mfma_f32_32x32x16_bf16 v[32:47], a[228:231], v[68:71], v[32:47]\n" \
  "ds_read_b128 v[92:95], v3 offset:64512\n" \
  "v_add_f32 v13, 0x3727c5ac, v11\n" \
  "buffer_load_dwordx4 v0, s[40:43], s51 offen offset:1024 sc1 lds\n" \
  "s_waitcnt lgkmcnt(4)\n" \
  "v_mfma_f32_32x32x16_bf16 v[32:47], a[232:235], v[72:75], v[32:47]\n" \
  "v_rsq_f32 v12, v13\n" \
  "s_nop 0\n" \
  "v_mul_f32 v10, -v10, v12\n" \
  "v_mul_f32 v48, v10, v16\n" \
  "v_mul_f32 v49, v10, v17\n" \
  "v_mfma_f32_32x32x16_bf16 v[32:47], a[236:239], v[76:79], v[32:47]\n" \
  "v_mul_f32 v50, v10, v18\n" \
  "buffer_load_dwordx4 v0, s[40:43], s51 offen offset:2048 sc1 lds\n" \
  "s_waitcnt lgkmcnt(2)\n" \
  "v_mfma_f32_32x32x16_bf16 v[32:47], a[240:243], v[80:83], v[32:47]\n" \
  "v_mul_f32 v51, v10, v19\n" \
  "v_mul_f32 v52, v10, v20\n" \
  "v_mul_f32 v53, v10, v21\n" \
  "v_mul_f32 v54, v10, v22\n" \
  "v_mfma_f32_32x32x16_bf16 v[32:47], a[244:247], v[84:87], v[32:47]\n" \
  "v_mul_f32 v55, v10, v23\n" \
  "buffer_load_dwordx4 v0, s[40:43], s51 offen offset:3072 sc1 lds\n" \
  "s_waitcnt lgkmcnt(0)\n" \
  "v_mfma_f32_32x32x16_bf16 v[32:47], a[248:251], v[88:91], v[32:47]\n" \
  "v_mul_f32 v56, v10, v24\n" \
  "v_mul_f32 v57, v10, v25\n" \
  "v_mul_f32 v58, v10, v26\n" \
  "v_mul_f32 v59, v10, v27\n" \
  "v_mfma_f32_32x32x16_bf16 v[32:47], a[252:255], v[92:95], v[32:47]\n" \
  "v_mul_f32 v60, v10, v28\n" \
  "v_mul_f32 v61, v10, v29\n" \
  "v_mul_f32 v62, v10, v30\n" \
  "v_mul_f32 v63, v10, v31\n" \
  "s_cmp_lg_u32 s42, 0\n" \
  "s_nop 6\n" \
  "v_pk_fma_f32 v[144:145], v[12:13], v[32:33], v[48:49] op_sel_hi:[0,1,1]\n" \
  "v_pk_fma_f32 v[146:147], v[12:13], v[34:35], v[50:51] op_sel_hi:[0,1,1]\n" \
  "v_pk_fma_f32 v[148:149], v[12:13], v[36:37], v[52:53] op_sel_hi:[0,1,1]\n" \
  "v_pk_fma_f32 v[150:151], v[12:13], v[38:39], v[54:55] op_sel_hi:[0,1,1]\n" \
  "v_pk_fma_f32 v[152:153], v[12:13], v[40:41], v[56:57] op_sel_hi:[0,1,1]\n" \
  "v_pk_fma_f32 v[154:155], v[12:13], v[42:43], v[58:59] op_sel_hi:[0,1,1]\n" \
  "v_pk_fma_f32 v[156:157], v[12:13], v[44:45], v[60:61] op_sel_hi:[0,1,1]\n" \
  "v_pk_fma_f32 v[158:159], v[12:13], v[46:47], v[62:63] op_sel_hi:[0,1,1]\n" \
  "s_cbranch_scc1 SPL3_e_%=\n" \
  "v_mov_b32 v176, 0xbdd2d3e7\n" \
  "v_mov_b32 v178, 0xc0135761\n" \
  "v_pk_mul_f32 v[160:161], v[144:145], v[144:145]\n" \
  "v_pk_mul_f32 v[162:163], v[146:147], v[146:147]\n" \
  "v_pk_mul_f32 v[164:165], v[148:149], v[148:149]\n" \
  "v_pk_mul_f32 v[166:167], v[150:151], v[150:151]\n" \
  "v_pk_fma_f32 v[160:161], v[160:161], v[176:177], v[178:179] op_sel_hi:[1,0,0]\n" \
  "v_pk_fma_f32 v[162:163], v[162:163], v[176:177], v[178:179] op_sel_hi:[1,0,0]\n" \
  "v_pk_fma_f32 v[164:165], v[164:165], v[176:177], v[178:179] op_sel_hi:[1,0,0]\n" \
  "v_pk_fma_f32 v[166:167], v[166:167], v[176:177], v[178:179] op_sel_hi:[1,0,0]\n" \
  "v_pk_mul_f32 v[160:161], v[160:161], v[144:145]\n" \
  "v_pk_mul_f32 v[162:163], v[162:163], v[146:147]\n" \
  "v_pk_mul_f32 v[164:165], v[164:165], v[148:149]\n" \
  "v_pk_mul_f32 v[166:167], v[166:167], v[150:151]\n" \
  "v_exp_f32 v160, v160\n" \
  "v_exp_f32 v161, v161\n" \
  "v_exp_f32 v162, v162\n" \
  "v_pk_add_f32 v[160:161], v[160:161], 1.0 op_sel_hi:[1,0]\n" \
  "v_exp_f32 v163, v163\n" \
  "v_exp_f32 v164, v164\n" \
  "v_pk_add_f32 v[162:163], v[162:163], 1.0 op_sel_hi:[1,0]\n" \
  "v_exp_f32 v165, v165\n" \
  "v_exp_f32 v166, v166\n" \
  "v_pk_add_f32 v[164:165], v[164:165], 1.0 op_sel_hi:[1,0]\n" \
  "v_exp_f32 v167, v167\n" \
  "v_rcp_f32 v160, v160\n" \
  "v_pk_add_f32 v[166:167], v[166:167], 1.0 op_sel_hi:[1,0]\n" \
  "v_rcp_f32 v161, v161\n" \
  "v_rcp_f32 v162, v162\n" \
  "v_pk_mul_f32 v[144:145], v[144:145], v[160:161]\n" \
  "v_rcp_f32 v163, v163\n" \
  "v_cvt_pk_bf16_f32 v168, v144, v145\n" \
  "v_rcp_f32 v164, v164\n" \
  "v_pk_mul_f32 v[146:147], v[146:147], v[162:163]\n" \
  "v_rcp_f32 v165, v165\n" \
  "v_cvt_pk_bf16_f32 v169, v146, v147\n" \
  "v_rcp_f32 v166, v166\n" \
  "v_pk_mul_f32 v[148:149], v[148:149], v[164:165]\n" \
  "v_rcp_f32 v167, v167\n" \
  "v_cvt_pk_bf16_f32 v170, v148, v149\n" \
  "v_pk_mul_f32 v[150:151], v[150:151], v[166:167]\n" \
  "v_cvt_pk_bf16_f32 v171, v150, v151\n" \
  "buffer_store_dwordx4 v[168:171], v15, s[36:39], s52 offen sc1\n" \
  "v_pk_mul_f32 v[160:161], v[152:153], v[152:153]\n" \
  "v_pk_mul_f32 v[162:163], v[154:155], v[154:155]\n" \
  "v_pk_mul_f32 v[164:165], v[156:157], v[156:157]\n" \
  "v_pk_mul_f32 v[166:167], v[158:159], v[158:159]\n" \
  "v_pk_fma_f32 v[160:161], v[160:161], v[176:177], v[178:179] op_sel_hi:[1,0,0]\n" \
  "v_pk_fma_f32 v[162:163], v[162:163], v[176:177], v[178:179] op_sel_hi:[1,0,0]\n" \
  "v_pk_fma_f32 v[164:165], v[164:165], v[176:177], v[178:179] op_sel_hi:[1,0,0]\n" \
  "v_pk_fma_f32 v[166:167], v[166:167], v[176:177], v[178:179] op_sel_hi:[1,0,0]\n" \
  "v_pk_mul_f32 v[160:161], v[160:161], v[152:153]\n" \
  "v_pk_mul_f32 v[162:163], v[162:163], v[154:155]\n" \
  "v_pk_mul_f32 v[164:165], v[164:165], v[156:157]\n" \
  "v_pk_mul_f32 v[166:167], v[166:167], v[158:159]\n" \
  "v_exp_f32 v160, v160\n" \
  "v_exp_f32 v161, v161\n" \
  "v_exp_f32 v162, v162\n" \
  "v_pk_add_f32 v[160:161], v[160:161], 1.0 op_sel_hi:[1,0]\n" \
  "v_exp_f32 v163, v163\n" \
  "v_exp_f32 v164, v164\n" \
  "v_pk_add_f32 v[162:163], v[162:163], 1.0 op_sel_hi:[1,0]\n" \
  "v_exp_f32 v165, v165\n" \
  "v_exp_f32 v166, v166\n" \
  "v_pk_add_f32 v[164:165], v[164:165], 1.0 op_sel_hi:[1,0]\n" \
  "v_exp_f32 v167, v167\n" \
  "v_rcp_f32 v160, v160\n" \
  "v_pk_add_f32 v[166:167], v[166:167], 1.0 op_sel_hi:[1,0]\n" \
  "v_rcp_f32 v161, v161\n" \
  "v_rcp_f32 v162, v162\n" \
  "v_pk_mul_f32 v[152:153], v[152:153], v[160:161]\n" \
  "v_rcp_f32 v163, v163\n" \
  "v_cvt_pk_bf16_f32 v172, v152, v153\n" \
  "v_rcp_f32 v164, v164\n" \
  "v_pk_mul_f32 v[154:155], v[154:155], v[162:163]\n" \
  "v_rcp_f32 v165, v165\n" \
  "v_cvt_pk_bf16_f32 v173, v154, v155\n" \
  "v_rcp_f32 v166, v166\n" \
  "v_pk_mul_f32 v[156:157], v[156:157], v[164:165]\n" \
  "v_rcp_f32 v167, v167\n" \
  "v_cvt_pk_bf16_f32 v174, v156, v157\n" \
  "v_pk_mul_f32 v[158:159], v[158:159], v[166:167]\n" \
  "v_cvt_pk_bf16_f32 v175, v158, v159\n" \
  "buffer_store_dwordx4 v[172:175], v15, s[36:39], s52 offen offset:1024 sc1\n" \
  "SPL3_e_%=:\n" \
  "s_nop 1\n"
#define SQ_ASM \
  "v_add_u32 v2, 0x10000, v1\n" \
  "v_xor_b32 v3, 32, v1\n" \
  "v_xor_b32 v4, 32, v2\n" \
  "v_add_u32 v7, 0x10000, v6\n" \
  "v_mov_b32 v11, 0x40000000\n" \
  "v_mov_b32 v12, 0x40000000\n" \
  "s_lshr_b32 s56, s59, 17\n" \
  "s_and_b32 s56, s56, 0xf80\n" \
  "s_add_u32 s56, s56, 252182528\n" \
  "buffer_load_dword v13, off, s[36:39], s56 sc1\n" \
  "s_mov_b32 s56, 0\n" \
  "s_add_u32 m0, s48, 0\n" \
  "s_nop 0\n" \
  "buffer_load_dwordx4 v0, s[40:43], s56 offen lds\n" \
  "s_add_u32 m0, s48, 896\n" \
  "s_nop 0\n" \
  "buffer_load_dwordx4 v0, s[40:43], s56 offen offset:128 lds\n" \
  "s_mov_b32 s56, 262144\n" \
  "s_add_u32 m0, s48, 8192\n" \
  "s_nop 0\n" \
  "buffer_load_dwordx4 v0, s[40:43], s56 offen lds\n" \
  "s_add_u32 m0, s48, 9088\n" \
  "s_nop 0\n" \
  "buffer_load_dwordx4 v0, s[40:43], s56 offen offset:128 lds\n" \
  "s_mov_b32 s56, 524288\n" \
  "s_add_u32 m0, s48, 16384\n" \
  "s_nop 0\n" \
  "buffer_load_dwordx4 v0, s[40:43], s56 offen lds\n" \
  "s_add_u32 m0, s48, 17280\n" \
  "s_nop 0\n" \
  "buffer_load_dwordx4 v0, s[40:43], s56 offen offset:128 lds\n" \
  "s_mov_b32 s56, 786432\n" \
  "s_add_u32 m0, s48, 24576\n" \
  "s_nop 0\n" \
  "buffer_load_dwordx4 v0, s[40:43], s56 offen lds\n" \
  "s_add_u32 m0, s48, 25472\n" \
  "s_nop 0\n" \
  "buffer_load_dwordx4 v0, s[40:43], s56 offen offset:128 lds\n" \
  "s_mov_b32 s56, 1048576\n" \
  "s_add_u32 m0, s48, 32768\n" \
  "s_nop 0\n" \
  "buffer_load_dwordx4 v0, s[40:43], s56 offen lds\n" \
  "s_add_u32 m0, s48, 33664\n" \
  "s_nop 0\n" \
  "buffer_load_dwordx4 v0, s[40:43], s56 offen offset:128 lds\n" \
  "s_mov_b32 s56, 1310720\n" \
  "s_add_u32 m0, s48, 40960\n" \
  "s_nop 0\n" \
  "buffer_load_dwordx4 v0, s[40:43], s56 offen lds\n" \
  "s_add_u32 m0, s48, 41856\n" \
  "s_nop 0\n" \
  "buffer_load_dwordx4 v0, s[40:43], s56 offen offset:128 lds\n" \
  "s_mov_b32 s56, 1572864\n" \
  "s_add_u32 m0, s48, 49152\n" \
  "s_nop 0\n" \
  "buffer_load_dwordx4 v0, s[40:43], s56 offen lds\n" \
  "s_add_u32 m0, s48, 50048\n" \
  "s_nop 0\n" \
  "buffer_load_dwordx4 v0, s[40:43], s56 offen offset:128 lds\n" \
  "s_mov_b32 s56, 1835008\n" \
  "s_add_u32 m0, s48, 57344\n" \
  "s_nop 0\n" \
  "buffer_load_dwordx4 v0, s[40:43], s56 offen lds\n" \
  "s_add_u32 m0, s48, 58240\n" \
  "s_nop 0\n" \
  "buffer_load_dwordx4 v0, s[40:43], s56 offen offset:128 lds\n" \
  "s_mov_b32 s56, 2097152\n" \
  "s_add_u32 m0, s48, 65536\n" \
  "s_nop 0\n" \
  "buffer_load_dwordx4 v0, s[40:43], s56 offen lds\n" \
  "s_add_u32 m0, s48, 66432\n" \
  "s_nop 0\n" \
  "buffer_load_dwordx4 v0, s[40:43], s56 offen offset:128 lds\n" \
  "s_mov_b32 s56, 2359296\n" \
  "s_add_u32 m0, s48, 73728\n" \
  "s_nop 0\n" \
  "buffer_load_dwordx4 v0, s[40:43], s56 offen lds\n" \
  "s_add_u32 m0, s48, 74624\n" \
  "s_nop 0\n" \
  "buffer_load_dwordx4 v0, s[40:43], s56 offen offset:128 lds\n" \
  "s_mov_b32 s56, 2621440\n" \
  "s_add_u32 m0, s48, 81920\n" \
  "s_nop 0\n" \
  "buffer_load_dwordx4 v0, s[40:43], s56 offen lds\n" \
  "s_add_u32 m0, s48, 82816\n" \
  "s_nop 0\n" \
  "buffer_load_dwordx4 v0, s[40:43], s56 offen offset:128 lds\n" \
  "s_mov_b32 s56, 2883584\n" \
  "s_add_u32 m0, s48, 90112\n" \
  "s_nop 0\n" \
  "buffer_load_dwordx4 v0, s[40:43], s56 offen lds\n" \
  "s_add_u32 m0, s48, 91008\n" \
  "s_nop 0\n" \
  "buffer_load_dwordx4 v0, s[40:43], s56 offen offset:128 lds\n" \
  "s_mov_b32 s56, 3145728\n" \
  "s_add_u32 m0, s48, 98304\n" \
  "s_nop 0\n" \
  "buffer_load_dwordx4 v0, s[40:43], s56 offen lds\n" \
  "s_add_u32 m0, s48, 99200\n" \
  "s_nop 0\n" \
  "buffer_load_dwordx4 v0, s[40:43], s56 offen offset:128 lds\n" \
  "s_mov_b32 s56, 3407872\n" \
  "s_add_u32 m0, s48, 106496\n" \
  "s_nop 0\n" \
  "buffer_load_dwordx4 v0, s[40:43], s56 offen lds\n" \
  "s_add_u32 m0, s48, 107392\n" \
  "s_nop 0\n" \
  "buffer_load_dwordx4 v0, s[40:43], s56 offen offset:128 lds\n" \
  "s_mov_b32 s56, 3670016\n" \
  "s_add_u32 m0, s48, 114688\n" \
  "s_nop 0\n" \
  "buffer_load_dwordx4 v0, s[40:43], s56 offen lds\n" \
  "s_add_u32 m0, s48, 115584\n" \
  "s_nop 0\n" \
  "buffer_load_dwordx4 v0, s[40:43], s56 offen offset:128 lds\n" \
  "s_mov_b32 s56, 3932160\n" \
  "s_add_u32 m0, s48, 122880\n" \
  "s_nop 0\n" \
  "buffer_load_dwordx4 v0, s[40:43], s56 offen lds\n" \
  "s_add_u32 m0, s48, 123776\n" \
  "s_nop 0\n" \
  "buffer_load_dwordx4 v0, s[40:43], s56 offen offset:128 lds\n" \
  "s_waitcnt vmcnt(28)\n" \
  "s_waitcnt lgkmcnt(0)\n" \
  "s_barrier\n" \
  "ds_read_b64_tr_b16 a[0:1], v1 offset:0\n" \
  "ds_read_b64_tr_b16 a[2:3], v1 offset:2048\n" \
  "ds_read_b64_tr_b16 a[4:5], v3 offset:0\n" \
  "ds_read_b64_tr_b16 a[6:7], v3 offset:2048\n" \
  "ds_read_b64_tr_b16 a[8:9], v1 offset:8192\n" \
  "ds_read_b64_tr_b16 a[10:11], v1 offset:10240\n" \
  "ds_read_b64_tr_b16 a[12:13], v3 offset:8192\n" \
  "ds_read_b64_tr_b16 a[14:15], v3 offset:10240\n" \
  "s_waitcnt vmcnt(24)\n" \
  "s_waitcnt lgkmcnt(0)\n" \
  "s_barrier\n" \
  "ds_read_b64_tr_b16 a[16:17], v1 offset:16384\n" \
  "ds_read_b64_tr_b16 a[18:19], v1 offset:18432\n" \
  "ds_read_b64_tr_b16 a[20:21], v3 offset:16384\n" \
  "ds_read_b64_tr_b16 a[22:23], v3 offset:18432\n" \
  "ds_read_b64_tr_b16 a[24:25], v1 offset:24576\n" \
  "ds_read_b64_tr_b16 a[26:27], v1 offset:26624\n" \
  "ds_read_b64_tr_b16 a[28:29], v3 offset:24576\n" \
  "ds_read_b64_tr_b16 a[30:31], v3 offset:26624\n" \
  "s_mov_b32 s56, 4194304\n" \
  "s_add_u32 m0, s48, 0\n" \
  "s_nop 0\n" \
  "buffer_load_dwordx4 v0, s[40:43], s56 offen lds\n" \
  "s_add_u32 m0, s48, 896\n" \
  "s_nop 0\n" \
  "buffer_load_dwordx4 v0, s[40:43], s56 offen offset:128 lds\n" \
  "s_mov_b32 s56, 4456448\n" \
  "s_add_u32 m0, s48, 8192\n" \
  "s_nop 0\n" \
  "buffer_load_dwordx4 v0, s[40:43], s56 offen lds\n" \
  "s_add_u32 m0, s48, 9088\n" \
  "s_nop 0\n" \
  "buffer_load_dwordx4 v0, s[40:43], s56 offen offset:128 lds\n" \
  "s_waitcnt vmcnt(24)\n" \
  "s_waitcnt lgkmcnt(0)\n" \
  "s_barrier\n" \
  "ds_read_b64_tr_b16 a[32:33], v1 offset:32768\n" \
  "ds_read_b64_tr_b16 a[34:35], v1 offset:34816\n" \
  "ds_read_b64_tr_b16 a[36:37], v3 offset:32768\n" \
  "ds_read_b64_tr_b16 a[38:39], v3 offset:34816\n" \
  "ds_read_b64_tr_b16 a[40:41], v1 offset:40960\n" \
  "ds_read_b64_tr_b16 a[42:43], v1 offset:43008\n" \
  "ds_read_b64_tr_b16 a[44:45], v3 offset:40960\n" \
  "ds_read_b64_tr_b16 a[46:47], v3 offset:43008\n" \
  "s_mov_b32 s56, 4718592\n" \
  "s_add_u32 m0, s48, 16384\n" \
  "s_nop 0\n" \
  "buffer_load_dwordx4 v0, s[40:43], s56 offen lds\n" \
  "s_add_u32 m0, s48, 17280\n" \
  "s_nop 0\n" \
  "buffer_load_dwordx4 v0, s[40:43], s56 offen offset:128 lds\n" \
  "s_mov_b32 s56, 4980736\n" \
  "s_add_u32 m0, s48, 24576\n" \
  "s_nop 0\n" \
  "buffer_load_dwordx4 v0, s[40:43], s56 offen lds\n" \
  "s_add_u32 m0, s48, 25472\n" \
  "s_nop 0\n" \
  "buffer_load_dwordx4 v0, s[40:43], s56 offen offset:128 lds\n" \
  "s_waitcnt vmcnt(24)\n" \
  "s_waitcnt lgkmcnt(0)\n" \
  "s_barrier\n" \
  "ds_read_b64_tr_b16 a[48:49], v1 offset:49152\n" \
  "ds_read_b64_tr_b16 a[50:51], v1 offset:51200\n" \
  "ds_read_b64_tr_b16 a[52:53], v3 offset:49152\n" \
  "ds_read_b64_tr_b16 a[54:55], v3 offset:51200\n" \
  "ds_read_b64_tr_b16 a[56:57], v1 offset:57344\n" \
  "ds_read_b64_tr_b16 a[58:59], v1 offset:59392\n" \
  "ds_read_b64_tr_b16 a[60:61], v3 offset:57344\n" \
  "ds_read_b64_tr_b16 a[62:63], v3 offset:59392\n" \
  "s_mov_b32 s56, 5242880\n" \
  "s_add_u32 m0, s48, 32768\n" \
  "s_nop 0\n" \
  "buffer_load_dwordx4 v0, s[40:43], s56 offen lds\n" \
  "s_add_u32 m0, s48, 33664\n" \
  "s_nop 0\n" \
  "buffer_load_dwordx4 v0, s[40:43], s56 offen offset:128 lds\n" \
  "s_mov_b32 s56, 5505024\n" \
  "s_add_u32 m0, s48, 40960\n" \
  "s_nop 0\n" \
  "buffer_load_dwordx4 v0, s[40:43], s56 offen lds\n" \
  "s_add_u32 m0, s48, 41856\n" \
  "s_nop 0\n" \
  "buffer_load_dwordx4 v0, s[40:43], s56 offen offset:128 lds\n" \
  "s_waitcnt vmcnt(24)\n" \
  "s_waitcnt lgkmcnt(0)\n" \
  "s_barrier\n" \
  "ds_read_b64_tr_b16 a[64:65], v2 offset:0\n" \
  "ds_read_b64_tr_b16 a[66:67], v2 offset:2048\n" \
  "ds_read_b64_tr_b16 a[68:69], v4 offset:0\n" \
  "ds_read_b64_tr_b16 a[70:71], v4 offset:2048\n" \
  "ds_read_b64_tr_b16 a[72:73], v2 offset:8192\n" \
  "ds_read_b64_tr_b16 a[74:75], v2 offset:10240\n" \
  "ds_read_b64_tr_b16 a[76:77], v4 offset:8192\n" \
  "ds_read_b64_tr_b16 a[78:79], v4 offset:10240\n" \
  "s_mov_b32 s56, 5767168\n" \
  "s_add_u32 m0, s48, 49152\n" \
  "s_nop 0\n" \
  "buffer_load_dwordx4 v0, s[40:43], s56 offen lds\n" \
  "s_add_u32 m0, s48, 50048\n" \
  "s_nop 0\n" \
  "buffer_load_dwordx4 v0, s[40:43], s56 offen offset:128 lds\n" \
  "s_mov_b32 s56, 6029312\n" \
  "s_add_u32 m0, s48, 57344\n" \
  "s_nop 0\n" \
  "buffer_load_dwordx4 v0, s[40:43], s56 offen lds\n" \
  "s_add_u32 m0, s48, 58240\n" \
  "s_nop 0\n" \
  "buffer_load_dwordx4 v0, s[40:43], s56 offen offset:128 lds\n" \
  "s_waitcnt vmcnt(24)\n" \
  "s_waitcnt lgkmcnt(0)\n" \
  "s_barrier\n" \
  "ds_read_b64_tr_b16 a[80:81], v2 offset:16384\n" \
  "ds_read_b64_tr_b16 a[82:83], v2 offset:18432\n" \
  "ds_read_b64_tr_b16 a[84:85], v4 offset:16384\n" \
  "ds_read_b64_tr_b16 a[86:87], v4 offset:18432\n" \
  "ds_read_b64_tr_b16 a[88:89], v2 offset:24576\n" \
  "ds_read_b64_tr_b16 a[90:91], v2 offset:26624\n" \
  "ds_read_b64_tr_b16 a[92:93], v4 offset:24576\n" \
  "ds_read_b64_tr_b16 a[94:95], v4 offset:26624\n" \
  "s_mov_b32 s56, 6291456\n" \
  "s_add_u32 m0, s48, 65536\n" \
  "s_nop 0\n" \
  "buffer_load_dwordx4 v0, s[40:43], s56 offen lds\n" \
  "s_add_u32 m0, s48, 66432\n" \
  "s_nop 0\n" \
  "buffer_load_dwordx4 v0, s[40:43], s56 offen offset:128 lds\n" \
  "s_mov_b32 s56, 6553600\n" \
  "s_add_u32 m0, s48, 73728\n" \
  "s_nop 0\n" \
  "buffer_load_dwordx4 v0, s[40:43], s56 offen lds\n" \
  "s_add_u32 m0, s48, 74624\n" \
  "s_nop 0\n" \
  "buffer_load_dwordx4 v0, s[40:43], s56 offen offset:128 lds\n" \
  "s_lshr_b32 s56, s59, 17\n" \
  "s_and_b32 s56, s56, 0xf80\n" \
  "s_add_u32 s56, s56, 252182528\n" \
  "buffer_load_dword v14, off, s[36:39], s56 sc1\n" \
  "s_waitcnt vmcnt(25)\n" \
  "s_waitcnt lgkmcnt(0)\n" \
  "s_barrier\n" \
  "ds_read_b64_tr_b16 a[96:97], v2 offset:32768\n" \
  "ds_read_b64_tr_b16 a[98:99], v2 offset:34816\n" \
  "ds_read_b64_tr_b16 a[100:101], v4 offset:32768\n" \
  "ds_read_b64_tr_b16 a[102:103], v4 offset:34816\n" \
  "ds_read_b64_tr_b16 a[104:105], v2 offset:40960\n" \
  "ds_read_b64_tr_b16 a[106:107], v2 offset:43008\n" \
  "ds_read_b64_tr_b16 a[108:109], v4 offset:40960\n" \
  "ds_read_b64_tr_b16 a[110:111], v4 offset:43008\n" \
  "s_mov_b32 s56, 6815744\n" \
  "s_add_u32 m0, s48, 81920\n" \
  "s_nop 0\n" \
  "buffer_load_dwordx4 v0, s[40:43], s56 offen lds\n" \
  "s_add_u32 m0, s48, 82816\n" \
  "s_nop 0\n" \
  "buffer_load_dwordx4 v0, s[40:43], s56 offen offset:128 lds\n" \
  "s_mov_b32 s56, 7077888\n" \
  "s_add_u32 m0, s48, 90112\n" \
  "s_nop 0\n" \
  "buffer_load_dwordx4 v0, s[40:43], s56 offen lds\n" \
  "s_add_u32 m0, s48, 91008\n" \
  "s_nop 0\n" \
  "buffer_load_dwordx4 v0, s[40:43], s56 offen offset:128 lds\n" \
  "s_waitcnt vmcnt(25)\n" \
  "s_waitcnt lgkmcnt(0)\n" \
  "s_barrier\n" \
  "ds_read_b64_tr_b16 a[112:113], v2 offset:49152\n" \
  "ds_read_b64_tr_b16 a[114:115], v2 offset:51200\n" \
  "ds_read_b64_tr_b16 a[116:117], v4 offset:49152\n" \
  "ds_read_b64_tr_b16 a[118:119], v4 offset:51200\n" \
  "ds_read_b64_tr_b16 a[120:121], v2 offset:57344\n" \
  "ds_read_b64_tr_b16 a[122:123], v2 offset:59392\n" \
  "ds_read_b64_tr_b16 a[124:125], v4 offset:57344\n" \
  "ds_read_b64_tr_b16 a[126:127], v4 offset:59392\n" \
  "s_mov_b32 s56, 7340032\n" \
  "s_add_u32 m0, s48, 98304\n" \
  "s_nop 0\n" \
  "buffer_load_dwordx4 v0, s[40:43], s56 offen lds\n" \
  "s_add_u32 m0, s48, 99200\n" \
  "s_nop 0\n" \
  "buffer_load_dwordx4 v0, s[40:43], s56 offen offset:128 lds\n" \
  "s_mov_b32 s56, 7602176\n" \
  "s_add_u32 m0, s48, 106496\n" \
  "s_nop 0\n" \
  "buffer_load_dwordx4 v0, s[40:43], s56 offen lds\n" \
  "s_add_u32 m0, s48, 107392\n" \
  "s_nop 0\n" \
  "buffer_load_dwordx4 v0, s[40:43], s56 offen offset:128 lds\n" \
  "s_waitcnt vmcnt(25)\n" \
  "s_waitcnt lgkmcnt(0)\n" \
  "s_barrier\n" \
  "ds_read_b64_tr_b16 a[128:129], v1 offset:0\n" \
  "ds_read_b64_tr_b16 a[130:131], v1 offset:2048\n" \
  "ds_read_b64_tr_b16 a[132:133], v3 offset:0\n" \
  "ds_read_b64_tr_b16 a[134:135], v3 offset:2048\n" \
  "ds_read_b64_tr_b16 a[136:137], v1 offset:8192\n" \
  "ds_read_b64_tr_b16 a[138:139], v1 offset:10240\n" \
  "ds_read_b64_tr_b16 a[140:141], v3 offset:8192\n" \
  "ds_read_b64_tr_b16 a[142:143], v3 offset:10240\n" \
  "s_mov_b32 s56, 7864320\n" \
  "s_add_u32 m0, s48, 114688\n" \
  "s_nop 0\n" \
  "buffer_load_dwordx4 v0, s[40:43], s56 offen lds\n" \
  "s_add_u32 m0, s48, 115584\n" \
  "s_nop 0\n" \
  "buffer_load_dwordx4 v0, s[40:43], s56 offen offset:128 lds\n" \
  "s_mov_b32 s56, 8126464\n" \
  "s_add_u32 m0, s48, 122880\n" \
  "s_nop 0\n" \
  "buffer_load_dwordx4 v0, s[40:43], s56 offen lds\n" \
  "s_add_u32 m0, s48, 123776\n" \
  "s_nop 0\n" \
  "buffer_load_dwordx4 v0, s[40:43], s56 offen offset:128 lds\n" \
  "s_waitcnt vmcnt(25)\n" \
  "s_waitcnt lgkmcnt(0)\n" \
  "s_barrier\n" \
  "ds_read_b64_tr_b16 a[144:145], v1 offset:16384\n" \
  "ds_read_b64_tr_b16 a[146:147], v1 offset:18432\n" \
  "ds_read_b64_tr_b16 a[148:149], v3 offset:16384\n" \
  "ds_read_b64_tr_b16 a[150:151], v3 offset:18432\n" \
  "ds_read_b64_tr_b16 a[152:153], v1 offset:24576\n" \
  "ds_read_b64_tr_b16 a[154:155], v1 offset:26624\n" \
  "ds_read_b64_tr_b16 a[156:157], v3 offset:24576\n" \
  "ds_read_b64_tr_b16 a[158:159], v3 offset:26624\n" \
  "s_waitcnt vmcnt(21)\n" \
  "s_waitcnt lgkmcnt(0)\n" \
  "s_barrier\n" \
  "ds_read_b64_tr_b16 a[160:161], v1 offset:32768\n" \
  "ds_read_b64_tr_b16 a[162:163], v1 offset:34816\n" \
  "ds_read_b64_tr_b16 a[164:165], v3 offset:32768\n" \
  "ds_read_b64_tr_b16 a[166:167], v3 offset:34816\n" \
  "ds_read_b64_tr_b16 a[168:169], v1 offset:40960\n" \
  "ds_read_b64_tr_b16 a[170:171], v1 offset:43008\n" \
  "ds_read_b64_tr_b16 a[172:173], v3 offset:40960\n" \
  "ds_read_b64_tr_b16 a[174:175], v3 offset:43008\n" \
  "s_waitcnt vmcnt(17)\n" \
  "s_waitcnt lgkmcnt(0)\n" \
  "s_barrier\n" \
  "ds_read_b64_tr_b16 a[176:177], v1 offset:49152\n" \
  "ds_read_b64_tr_b16 a[178:179], v1 offset:51200\n" \
  "ds_read_b64_tr_b16 a[180:181], v3 offset:49152\n" \
  "ds_read_b64_tr_b16 a[182:183], v3 offset:51200\n" \
  "ds_read_b64_tr_b16 a[184:185], v1 offset:57344\n" \
  "ds_read_b64_tr_b16 a[186:187], v1 offset:59392\n" \
  "ds_read_b64_tr_b16 a[188:189], v3 offset:57344\n" \
  "ds_read_b64_tr_b16 a[190:191], v3 offset:59392\n" \
  "s_waitcnt vmcnt(13)\n" \
  "s_waitcnt lgkmcnt(0)\n" \
  "s_barrier\n" \
  "ds_read_b64_tr_b16 a[192:193], v2 offset:0\n" \
  "ds_read_b64_tr_b16 a[194:195], v2 offset:2048\n" \
  "ds_read_b64_tr_b16 a[196:197], v4 offset:0\n" \
  "ds_read_b64_tr_b16 a[198:199], v4 offset:2048\n" \
  "ds_read_b64_tr_b16 a[200:201], v2 offset:8192\n" \
  "ds_read_b64_tr_b16 a[202:203], v2 offset:10240\n" \
  "ds_read_b64_tr_b16 a[204:205], v4 offset:8192\n" \
  "ds_read_b64_tr_b16 a[206:207], v4 offset:10240\n" \
  "s_waitcnt vmcnt(8)\n" \
  "s_waitcnt lgkmcnt(0)\n" \
  "s_barrier\n" \
  "ds_read_b64_tr_b16 a[208:209], v2 offset:16384\n" \
  "ds_read_b64_tr_b16 a[210:211], v2 offset:18432\n" \
  "ds_read_b64_tr_b16 a[212:213], v4 offset:16384\n" \
  "ds_read_b64_tr_b16 a[214:215], v4 offset:18432\n" \
  "ds_read_b64_tr_b16 a[216:217], v2 offset:24576\n" \
  "ds_read_b64_tr_b16 a[218:219], v2 offset:26624\n" \
  "ds_read_b64_tr_b16 a[220:221], v4 offset:24576\n" \
  "ds_read_b64_tr_b16 a[222:223], v4 offset:26624\n" \
  "s_and_b32 s56, s59, 34\n" \
  "s_cbranch_scc1 SQ_ye_%=\n" \
  "SQ_yc_%=:\n" \
  "v_readfirstlane_b32 s56, v13\n" \
  "v_readfirstlane_b32 s57, v14\n" \
  "s_cmp_eq_u32 s56, s57\n" \
  "s_cbranch_scc1 SQ_ys_%=\n" \
  "s_xor_b32 s56, s57, s59\n" \
  "s_and_b32 s56, s56, 28\n" \
  "s_cbranch_scc1 SQ_yb_%=\n" \
  "SQ_ye_%=:\n" \
  "v_mbcnt_lo_u32_b32 v13, -1, 0\n" \
  "v_mbcnt_hi_u32_b32 v13, -1, v13\n" \
  "v_lshlrev_b32 v13, 2, v13\n" \
  "s_lshr_b32 s56, s59, 16\n" \
  "s_and_b32 s56, s56, 0x1f00\n" \
  "s_add_u32 s56, s56, 252190720\n" \
  "buffer_load_dword v14, v13, s[36:39], s56 offen sc1\n" \
  "s_waitcnt vmcnt(5)\n" \
  "s_waitcnt lgkmcnt(0)\n" \
  "s_barrier\n" \
  "ds_read_b64_tr_b16 a[224:225], v2 offset:32768\n" \
  "ds_read_b64_tr_b16 a[226:227], v2 offset:34816\n" \
  "ds_read_b64_tr_b16 a[228:229], v4 offset:32768\n" \
  "ds_read_b64_tr_b16 a[230:231], v4 offset:34816\n" \
  "ds_read_b64_tr_b16 a[232:233], v2 offset:40960\n" \
  "ds_read_b64_tr_b16 a[234:235], v2 offset:43008\n" \
  "ds_read_b64_tr_b16 a[236:237], v4 offset:40960\n" \
  "ds_read_b64_tr_b16 a[238:239], v4 offset:43008\n" \
  "s_waitcnt vmcnt(1)\n" \
  "s_waitcnt lgkmcnt(0)\n" \
  "s_barrier\n" \
  "ds_read_b64_tr_b16 a[240:241], v2 offset:49152\n" \
  "ds_read_b64_tr_b16 a[242:243], v2 offset:51200\n" \
  "ds_read_b64_tr_b16 a[244:245], v4 offset:49152\n" \
  "ds_read_b64_tr_b16 a[246:247], v4 offset:51200\n" \
  "ds_read_b64_tr_b16 a[248:249], v2 offset:57344\n" \
  "ds_read_b64_tr_b16 a[250:251], v2 offset:59392\n" \
  "ds_read_b64_tr_b16 a[252:253], v4 offset:57344\n" \
  "ds_read_b64_tr_b16 a[254:255], v4 offset:59392\n" \
  "s_waitcnt vmcnt(0)\n" \
  "s_bitcmp1_b32 s59, 1\n" \
  "s_cbranch_scc1 SQ_yd_%=\n" \
  "SQ_yf_%=:\n" \
  "v_cmp_gt_i32 vcc, 0, v14\n" \
  "s_bcnt1_i32_b64 s57, vcc\n" \
  "v_and_b32 v14, 0x10001, v14\n" \
  "v_cmp_ne_u32 vcc, 0x10001, v14\n" \
  "s_cbranch_vccnz SQ_yt_%=\n" \
  "SQ_yg_%=:\n" \
  "s_lshr_b32 s56, s59, 17\n" \
  "s_and_b32 s56, s56, 0xf80\n" \
  "s_add_u32 s56, s56, 252186624\n" \
  "v_mov_b32 v14, 1\n" \
  "buffer_atomic_add v14, v9, s[36:39], s56 offen\n" \
  "s_cmp_gt_u32 s57, 32\n" \
  "s_cselect_b32 s56, 0x200000, 0\n" \
  "s_or_b32 s59, s59, s56\n" \
  "s_or_b32 s59, s59, 2\n" \
  "s_add_u32 s36, s36, s56\n" \
  "s_addc_u32 s37, s37, 0\n" \
  "SQ_yd_%=:\n" \
  "s_add_u32 m0, s49, 0\n" \
  "s_nop 0\n" \
  "buffer_load_dwordx4 v5, s[44:47], s50 offen sc1 lds\n" \
  "buffer_load_dwordx4 v5, s[44:47], s50 offen offset:1024 sc1 lds\n" \
  "buffer_load_dwordx4 v5, s[44:47], s50 offen offset:2048 sc1 lds\n" \
  "buffer_load_dwordx4 v5, s[44:47], s50 offen offset:3072 sc1 lds\n" \
  "s_add_u32 s50, s50, 4096\n" \
  "s_add_u32 m0, s49, 4096\n" \
  "s_nop 0\n" \
  "buffer_load_dwordx4 v5, s[44:47], s50 offen sc1 lds\n" \
  "buffer_load_dwordx4 v5, s[44:47], s50 offen offset:1024 sc1 lds\n" \
  "buffer_load_dwordx4 v5, s[44:47], s50 offen offset:2048 sc1 lds\n" \
  "buffer_load_dwordx4 v5, s[44:47], s50 offen offset:3072 sc1 lds\n" \
  "s_add_u32 s50, s50, 28672\n" \
  "s_add_u32 m0, s49, 32768\n" \
  "s_nop 0\n" \
  "buffer_load_dwordx4 v5, s[44:47], s50 offen sc1 lds\n" \
  "buffer_load_dwordx4 v5, s[44:47], s50 offen offset:1024 sc1 lds\n" \
  "buffer_load_dwordx4 v5, s[44:47], s50 offen offset:2048 sc1 lds\n" \
  "buffer_load_dwordx4 v5, s[44:47], s50 offen offset:3072 sc1 lds\n" \
  "s_add_u32 s50, s50, 4096\n" \
  "s_add_u32 m0, s49, 36864\n" \
  "s_nop 0\n" \
  "buffer_load_dwordx4 v5, s[44:47], s50 offen sc1 lds\n" \
  "buffer_load_dwordx4 v5, s[44:47], s50 offen offset:1024 sc1 lds\n" \
  "buffer_load_dwordx4 v5, s[44:47], s50 offen offset:2048 sc1 lds\n" \
  "buffer_load_dwordx4 v5, s[44:47], s50 offen offset:3072 sc1 lds\n" \
  "s_add_u32 s50, s50, 28672\n" \
  "s_waitcnt vmcnt(8)\n" \
  "s_waitcnt lgkmcnt(0)\n" \
  "s_barrier\n" \
  "ds_read_b128 v[16:19], v6 offset:0\n" \
  "ds_read_b128 v[20:23], v6 offset:256\n" \
  "ds_read_b128 v[24:27], v6 offset:2048\n" \
  "ds_read_b128 v[28:31], v6 offset:2304\n" \
  "ds_read_b128 v[32:35], v6 offset:4096\n" \
  "ds_read_b128 v[36:39], v6 offset:4352\n" \
  ".p2align 4\n" \
  "SQ_top_%=:\n" \
  "s_waitcnt lgkmcnt(4)\n" \
  "v_mfma_f32_16x16x32_bf16 v[48:51], a[0:3], v[16:19], 0\n" \
  "ds_read_b128 v[40:43], v6 offset:6144\n" \
  "v_mfma_f32_16x16x32_bf16 v[52:55], a[4:7], v[16:19], 0\n" \
  "s_lshr_b32 s56, s51, 8\n" \
  "ds_read_b128 v[44:47], v6 offset:6400\n" \
  "v_mfma_f32_16x16x32_bf16 v[60:63], a[4:7], v[20:23], 0\n" \
  "s_and_b32 s56, s56, 0x2000\n" \
  "s_xor_b32 s56, s56, s51\n" \
  "v_mfma_f32_16x16x32_bf16 v[56:59], a[0:3], v[20:23], 0\n" \
  "s_add_u32 s57, s56, s52\n" \
  "s_waitcnt lgkmcnt(4)\n" \
  "v_mfma_f32_16x16x32_bf16 v[48:51], a[8:11], v[24:27], v[48:51]\n" \
  "ds_read_b128 v[16:19], v6 offset:8192\n" \
  "v_mfma_f32_16x16x32_bf16 v[52:55], a[12:15], v[24:27], v[52:55]\n" \
  "s_add_u32 s51, s51, 0x80000\n" \
  "v_cvt_pk_bf16_f32 v80, v64, v65\n" \
  "ds_read_b128 v[20:23], v6 offset:8448\n" \
  "v_mfma_f32_16x16x32_bf16 v[60:63], a[12:15], v[28:31], v[60:63]\n" \
  "v_cvt_pk_bf16_f32 v81, v66, v67\n" \
  "v_cvt_pk_bf16_f32 v82, v68, v69\n" \
  "v_mfma_f32_16x16x32_bf16 v[56:59], a[8:11], v[28:31], v[56:59]\n" \
  "v_cvt_pk_bf16_f32 v83, v70, v71\n" \
  "s_waitcnt lgkmcnt(4)\n" \
  "v_mfma_f32_16x16x32_bf16 v[48:51], a[16:19], v[32:35], v[48:51]\n" \
  "ds_read_b128 v[24:27], v6 offset:10240\n" \
  "v_mfma_f32_16x16x32_bf16 v[52:55], a[20:23], v[32:35], v[52:55]\n" \
  "v_cvt_pk_bf16_f32 v84, v72, v73\n" \
  "buffer_store_dwordx4 v[80:83], v11, s[36:39], s56 offen sc1\n" \
  "ds_read_b128 v[28:31], v6 offset:10496\n" \
  "v_mfma_f32_16x16x32_bf16 v[60:63], a[20:23], v[36:39], v[60:63]\n" \
  "v_cvt_pk_bf16_f32 v85, v74, v75\n" \
  "v_cvt_pk_bf16_f32 v86, v76, v77\n" \
  "v_mfma_f32_16x16x32_bf16 v[56:59], a[16:19], v[36:39], v[56:59]\n" \
  "v_cvt_pk_bf16_f32 v87, v78, v79\n" \
  "s_waitcnt lgkmcnt(4)\n" \
  "v_mfma_f32_16x16x32_bf16 v[48:51], a[24:27], v[40:43], v[48:51]\n" \
  "ds_read_b128 v[32:35], v6 offset:12288\n" \
  "v_mfma_f32_16x16x32_bf16 v[52:55], a[28:31], v[40:43], v[52:55]\n" \
  "buffer_store_dwordx4 v[84:87], v11, s[36:39], s57 offen sc1\n" \
  "ds_read_b128 v[36:39], v6 offset:12544\n" \
  "v_mfma_f32_16x16x32_bf16 v[60:63], a[28:31], v[44:47], v[60:63]\n" \
  "v_mov_b32 v11, v8\n" \
  "v_mfma_f32_16x16x32_bf16 v[56:59], a[24:27], v[44:47], v[56:59]\n" \
  "s_waitcnt lgkmcnt(4)\n" \
  "v_mfma_f32_16x16x32_bf16 v[48:51], a[32:35], v[16:19], v[48:51]\n" \
  "ds_read_b128 v[40:43], v6 offset:14336\n" \
  "v_mfma_f32_16x16x32_bf16 v[52:55], a[36:39], v[16:19], v[52:55]\n" \
  "s_add_u32 m0, s49, 65536\n" \
  "ds_read_b128 v[44:47], v6 offset:14592\n" \
  "v_mfma_f32_16x16x32_bf16 v[60:63], a[36:39], v[20:23], v[60:63]\n" \
  "buffer_load_dwordx4 v5, s[44:47], s50 offen sc1 lds\n" \
  "v_mfma_f32_16x16x32_bf16 v[56:59], a[32:35], v[20:23], v[56:59]\n" \
  "s_waitcnt lgkmcnt(4)\n" \
  "v_mfma_f32_16x16x32_bf16 v[48:51], a[40:43], v[24:27], v[48:51]\n" \
  "ds_read_b128 v[16:19], v6 offset:16384\n" \
  "v_mfma_f32_16x16x32_bf16 v[52:55], a[44:47], v[24:27], v[52:55]\n" \
  "ds_read_b128 v[20:23], v6 offset:16640\n" \
  "v_mfma_f32_16x16x32_bf16 v[60:63], a[44:47], v[28:31], v[60:63]\n" \
  "buffer_load_dwordx4 v5, s[44:47], s50 offen offset:1024 sc1 lds\n" \
  "v_mfma_f32_16x16x32_bf16 v[56:59], a[40:43], v[28:31], v[56:59]\n" \
  "s_waitcnt lgkmcnt(4)\n" \
  "v_mfma_f32_16x16x32_bf16 v[48:51], a[48:51], v[32:35], v[48:51]\n" \
  "ds_read_b128 v[24:27], v6 offset:18432\n" \
  "v_mfma_f32_16x16x32_bf16 v[52:55], a[52:55], v[32:35], v[52:55]\n" \
  "ds_read_b128 v[28:31], v6 offset:18688\n" \
  "v_mfma_f32_16x16x32_bf16 v[60:63], a[52:55], v[36:39], v[60:63]\n" \
  "buffer_load_dwordx4 v5, s[44:47], s50 offen offset:2048 sc1 lds\n" \
  "v_mfma_f32_16x16x32_bf16 v[56:59], a[48:51], v[36:39], v[56:59]\n" \
  "s_waitcnt lgkmcnt(4)\n" \
  "v_mfma_f32_16x16x32_bf16 v[48:51], a[56:59], v[40:43], v[48:51]\n" \
  "ds_read_b128 v[32:35], v6 offset:20480\n" \
  "v_mfma_f32_16x16x32_bf16 v[52:55], a[60:63], v[40:43], v[52:55]\n" \
  "ds_read_b128 v[36:39], v6 offset:20736\n" \
  "v_mfma_f32_16x16x32_bf16 v[60:63], a[60:63], v[44:47], v[60:63]\n" \
  "buffer_load_dwordx4 v5, s[44:47], s50 offen offset:3072 sc1 lds\n" \
  "s_add_u32 s50, s50, 4096\n" \
  "v_mfma_f32_16x16x32_bf16 v[56:59], a[56:59], v[44:47], v[56:59]\n" \
  "s_waitcnt lgkmcnt(4)\n" \
  "v_mfma_f32_16x16x32_bf16 v[48:51], a[64:67], v[16:19], v[48:51]\n" \
  "ds_read_b128 v[40:43], v6 offset:22528\n" \
  "v_mfma_f32_16x16x32_bf16 v[52:55], a[68:71], v[16:19], v[52:55]\n" \
  "s_add_u32 m0, s49, 69632\n" \
  "ds_read_b128 v[44:47], v6 offset:22784\n" \
  "v_mfma_f32_16x16x32_bf16 v[60:63], a[68:71], v[20:23], v[60:63]\n" \
  "buffer_load_dwordx4 v5, s[44:47], s50 offen sc1 lds\n" \
  "v_mfma_f32_16x16x32_bf16 v[56:59], a[64:67], v[20:23], v[56:59]\n" \
  "s_waitcnt lgkmcnt(4)\n" \
  "v_mfma_f32_16x16x32_bf16 v[48:51], a[72:75], v[24:27], v[48:51]\n" \
  "ds_read_b128 v[16:19], v6 offset:24576\n" \
  "v_mfma_f32_16x16x32_bf16 v[52:55], a[76:79], v[24:27], v[52:55]\n" \
  "ds_read_b128 v[20:23], v6 offset:24832\n" \
  "v_mfma_f32_16x16x32_bf16 v[60:63], a[76:79], v[28:31], v[60:63]\n" \
  "buffer_load_dwordx4 v5, s[44:47], s50 offen offset:1024 sc1 lds\n" \
  "v_mfma_f32_16x16x32_bf16 v[56:59], a[72:75], v[28:31], v[56:59]\n" \
  "s_waitcnt lgkmcnt(4)\n" \
  "v_mfma_f32_16x16x32_bf16 v[48:51], a[80:83], v[32:35], v[48:51]\n" \
  "ds_read_b128 v[24:27], v6 offset:26624\n" \
  "v_mfma_f32_16x16x32_bf16 v[52:55], a[84:87], v[32:35], v[52:55]\n" \
  "ds_read_b128 v[28:31], v6 offset:26880\n" \
  "v_mfma_f32_16x16x32_bf16 v[60:63], a[84:87], v[36:39], v[60:63]\n" \
  "buffer_load_dwordx4 v5, s[44:47], s50 offen offset:2048 sc1 lds\n" \
  "v_mfma_f32_16x16x32_bf16 v[56:59], a[80:83], v[36:39], v[56:59]\n" \
  "s_waitcnt lgkmcnt(4)\n" \
  "v_mfma_f32_16x16x32_bf16 v[48:51], a[88:91], v[40:43], v[48:51]\n" \
  "ds_read_b128 v[32:35], v6 offset:28672\n" \
  "v_mfma_f32_16x16x32_bf16 v[52:55], a[92:95], v[40:43], v[52:55]\n" \
  "ds_read_b128 v[36:39], v6 offset:28928\n" \
  "v_mfma_f32_16x16x32_bf16 v[60:63], a[92:95], v[44:47], v[60:63]\n" \
  "buffer_load_dwordx4 v5, s[44:47], s50 offen offset:3072 sc1 lds\n" \
  "s_add_u32 s50, s50, 28672\n" \
  "v_mfma_f32_16x16x32_bf16 v[56:59], a[88:91], v[44:47], v[56:59]\n" \
  "s_waitcnt lgkmcnt(4)\n" \
  "v_mfma_f32_16x16x32_bf16 v[48:51], a[96:99], v[16:19], v[48:51]\n" \
  "ds_read_b128 v[40:43], v6 offset:30720\n" \
  "v_mfma_f32_16x16x32_bf16 v[52:55], a[100:103], v[16:19], v[52:55]\n" \
  "ds_read_b128 v[44:47], v6 offset:30976\n" \
  "v_mfma_f32_16x16x32_bf16 v[60:63], a[100:103], v[20:23], v[60:63]\n" \
  "v_mfma_f32_16x16x32_bf16 v[56:59], a[96:99], v[20:23], v[56:59]\n" \
  "s_waitcnt lgkmcnt(4)\n" \
  "v_mfma_f32_16x16x32_bf16 v[48:51], a[104:107], v[24:27], v[48:51]\n" \
  "s_waitcnt vmcnt(10)\n" \
  "s_barrier\n" \
  "ds_read_b128 v[16:19], v6 offset:32768\n" \
  "v_mfma_f32_16x16x32_bf16 v[52:55], a[108:111], v[24:27], v[52:55]\n" \
  "ds_read_b128 v[20:23], v6 offset:33024\n" \
  "v_mfma_f32_16x16x32_bf16 v[60:63], a[108:111], v[28:31], v[60:63]\n" \
  "v_mfma_f32_16x16x32_bf16 v[56:59], a[104:107], v[28:31], v[56:59]\n" \
  "s_waitcnt lgkmcnt(4)\n" \
  "v_mfma_f32_16x16x32_bf16 v[48:51], a[112:115], v[32:35], v[48:51]\n" \
  "ds_read_b128 v[24:27], v6 offset:34816\n" \
  "v_mfma_f32_16x16x32_bf16 v[52:55], a[116:119], v[32:35], v[52:55]\n" \
  "ds_read_b128 v[28:31], v6 offset:35072\n" \
  "v_mfma_f32_16x16x32_bf16 v[60:63], a[116:119], v[36:39], v[60:63]\n" \
  "v_mfma_f32_16x16x32_bf16 v[56:59], a[112:115], v[36:39], v[56:59]\n" \
  "s_waitcnt lgkmcnt(4)\n" \
  "v_mfma_f32_16x16x32_bf16 v[48:51], a[120:123], v[40:43], v[48:51]\n" \
  "ds_read_b128 v[32:35], v6 offset:36864\n" \
  "v_mfma_f32_16x16x32_bf16 v[52:55], a[124:127], v[40:43], v[52:55]\n" \
  "ds_read_b128 v[36:39], v6 offset:37120\n" \
  "v_mfma_f32_16x16x32_bf16 v[60:63], a[124:127], v[44:47], v[60:63]\n" \
  "v_mfma_f32_16x16x32_bf16 v[56:59], a[120:123], v[44:47], v[56:59]\n" \
  "s_waitcnt lgkmcnt(4)\n" \
  "v_mfma_f32_16x16x32_bf16 v[48:51], a[128:131], v[16:19], v[48:51]\n" \
  "ds_read_b128 v[40:43], v6 offset:38912\n" \
  "v_mfma_f32_16x16x32_bf16 v[52:55], a[132:135], v[16:19], v[52:55]\n" \
  "ds_read_b128 v[44:47], v6 offset:39168\n" \
  "v_mfma_f32_16x16x32_bf16 v[60:63], a[132:135], v[20:23], v[60:63]\n" \
  "v_mfma_f32_16x16x32_bf16 v[56:59], a[128:131], v[20:23], v[56:59]\n" \
  "s_waitcnt lgkmcnt(4)\n" \
  "v_mfma_f32_16x16x32_bf16 v[48:51], a[136:139], v[24:27], v[48:51]\n" \
  "ds_read_b128 v[16:19], v6 offset:40960\n" \
  "v_mfma_f32_16x16x32_bf16 v[52:55], a[140:143], v[24:27], v[52:55]\n" \
  "s_add_u32 m0, s49, 98304\n" \
  "ds_read_b128 v[20:23], v6 offset:41216\n" \
  "v_mfma_f32_16x16x32_bf16 v[60:63], a[140:143], v[28:31], v[60:63]\n" \
  "buffer_load_dwordx4 v5, s[44:47], s50 offen sc1 lds\n" \
  "v_mfma_f32_16x16x32_bf16 v[56:59], a[136:139], v[28:31], v[56:59]\n" \
  "s_waitcnt lgkmcnt(4)\n" \
  "v_mfma_f32_16x16x32_bf16 v[48:51], a[144:147], v[32:35], v[48:51]\n" \
  "ds_read_b128 v[24:27], v6 offset:43008\n" \
  "v_mfma_f32_16x16x32_bf16 v[52:55], a[148:151], v[32:35], v[52:55]\n" \
  "ds_read_b128 v[28:31], v6 offset:43264\n" \
  "v_mfma_f32_16x16x32_bf16 v[60:63], a[148:151], v[36:39], v[60:63]\n" \
  "buffer_load_dwordx4 v5, s[44:47], s50 offen offset:1024 sc1 lds\n" \
  "v_mfma_f32_16x16x32_bf16 v[56:59], a[144:147], v[36:39], v[56:59]\n" \
  "s_waitcnt lgkmcnt(4)\n" \
  "v_mfma_f32_16x16x32_bf16 v[48:51], a[152:155], v[40:43], v[48:51]\n" \
  "ds_read_b128 v[32:35], v6 offset:45056\n" \
  "v_mfma_f32_16x16x32_bf16 v[52:55], a[156:159], v[40:43], v[52:55]\n" \
  "ds_read_b128 v[36:39], v6 offset:45312\n" \
  "v_mfma_f32_16x16x32_bf16 v[60:63], a[156:159], v[44:47], v[60:63]\n" \
  "buffer_load_dwordx4 v5, s[44:47], s50 offen offset:2048 sc1 lds\n" \
  "v_mfma_f32_16x16x32_bf16 v[56:59], a[152:155], v[44:47], v[56:59]\n" \
  "s_waitcnt lgkmcnt(4)\n" \
  "v_mfma_f32_16x16x32_bf16 v[48:51], a[160:163], v[16:19], v[48:51]\n" \
  "ds_read_b128 v[40:43], v6 offset:47104\n" \
  "v_mfma_f32_16x16x32_bf16 v[52:55], a[164:167], v[16:19], v[52:55]\n" \
  "ds_read_b128 v[44:47], v6 offset:47360\n" \
  "v_mfma_f32_16x16x32_bf16 v[60:63], a[164:167], v[20:23], v[60:63]\n" \
  "buffer_load_dwordx4 v5, s[44:47], s50 offen offset:3072 sc1 lds\n" \
  "s_add_u32 s50, s50, 4096\n" \
  "v_mfma_f32_16x16x32_bf16 v[56:59], a[160:163], v[20:23], v[56:59]\n" \
  "s_waitcnt lgkmcnt(4)\n" \
  "v_mfma_f32_16x16x32_bf16 v[48:51], a[168:171], v[24:27], v[48:51]\n" \
  "ds_read_b128 v[16:19], v6 offset:49152\n" \
  "v_mfma_f32_16x16x32_bf16 v[52:55], a[172:175], v[24:27], v[52:55]\n" \
  "s_add_u32 m0, s49, 102400\n" \
  "ds_read_b128 v[20:23], v6 offset:49408\n" \
  "v_mfma_f32_16x16x32_bf16 v[60:63], a[172:175], v[28:31], v[60:63]\n" \
  "buffer_load_dwordx4 v5, s[44:47], s50 offen sc1 lds\n" \
  "v_mfma_f32_16x16x32_bf16 v[56:59], a[168:171], v[28:31], v[56:59]\n" \
  "s_waitcnt lgkmcnt(4)\n" \
  "v_mfma_f32_16x16x32_bf16 v[48:51], a[176:179], v[32:35], v[48:51]\n" \
  "ds_read_b128 v[24:27], v6 offset:51200\n" \
  "v_mfma_f32_16x16x32_bf16 v[52:55], a[180:183], v[32:35], v[52:55]\n" \
  "ds_read_b128 v[28:31], v6 offset:51456\n" \
  "v_mfma_f32_16x16x32_bf16 v[60:63], a[180:183], v[36:39], v[60:63]\n" \
  "buffer_load_dwordx4 v5, s[44:47], s50 offen offset:1024 sc1 lds\n" \
  "v_mfma_f32_16x16x32_bf16 v[56:59], a[176:179], v[36:39], v[56:59]\n" \
  "s_waitcnt lgkmcnt(4)\n" \
  "v_mfma_f32_16x16x32_bf16 v[48:51], a[184:187], v[40:43], v[48:51]\n" \
  "ds_read_b128 v[32:35], v6 offset:53248\n" \
  "v_mfma_f32_16x16x32_bf16 v[52:55], a[188:191], v[40:43], v[52:55]\n" \
  "ds_read_b128 v[36:39], v6 offset:53504\n" \
  "v_mfma_f32_16x16x32_bf16 v[60:63], a[188:191], v[44:47], v[60:63]\n" \
  "buffer_load_dwordx4 v5, s[44:47], s50 offen offset:2048 sc1 lds\n" \
  "v_mfma_f32_16x16x32_bf16 v[56:59], a[184:187], v[44:47], v[56:59]\n" \
  "s_waitcnt lgkmcnt(4)\n" \
  "v_mfma_f32_16x16x32_bf16 v[48:51], a[192:195], v[16:19], v[48:51]\n" \
  "ds_read_b128 v[40:43], v6 offset:55296\n" \
  "v_mfma_f32_16x16x32_bf16 v[52:55], a[196:199], v[16:19], v[52:55]\n" \
  "ds_read_b128 v[44:47], v6 offset:55552\n" \
  "v_mfma_f32_16x16x32_bf16 v[60:63], a[196:199], v[20:23], v[60:63]\n" \
  "buffer_load_dwordx4 v5, s[44:47], s50 offen offset:3072 sc1 lds\n" \
  "s_add_u32 s50, s50, 28672\n" \
  "v_mfma_f32_16x16x32_bf16 v[56:59], a[192:195], v[20:23], v[56:59]\n" \
  "s_waitcnt lgkmcnt(4)\n" \
  "v_mfma_f32_16x16x32_bf16 v[48:51], a[200:203], v[24:27], v[48:51]\n" \
  "ds_read_b128 v[16:19], v6 offset:57344\n" \
  "v_mfma_f32_16x16x32_bf16 v[52:55], a[204:207], v[24:27], v[52:55]\n" \
  "ds_read_b128 v[20:23], v6 offset:57600\n" \
  "v_mfma_f32_16x16x32_bf16 v[60:63], a[204:207], v[28:31], v[60:63]\n" \
  "v_mfma_f32_16x16x32_bf16 v[56:59], a[200:203], v[28:31], v[56:59]\n" \
  "s_waitcnt lgkmcnt(4)\n" \
  "v_mfma_f32_16x16x32_bf16 v[48:51], a[208:211], v[32:35], v[48:51]\n" \
  "ds_read_b128 v[24:27], v6 offset:59392\n" \
  "v_mfma_f32_16x16x32_bf16 v[52:55], a[212:215], v[32:35], v[52:55]\n" \
  "ds_read_b128 v[28:31], v6 offset:59648\n" \
  "v_mfma_f32_16x16x32_bf16 v[60:63], a[212:215], v[36:39], v[60:63]\n" \
  "v_mfma_f32_16x16x32_bf16 v[56:59], a[208:211], v[36:39], v[56:59]\n" \
  "s_waitcnt lgkmcnt(4)\n" \
  "v_mfma_f32_16x16x32_bf16 v[48:51], a[216:219], v[40:43], v[48:51]\n" \
  "ds_read_b128 v[32:35], v6 offset:61440\n" \
  "v_mfma_f32_16x16x32_bf16 v[52:55], a[220:223], v[40:43], v[52:55]\n" \
  "ds_read_b128 v[36:39], v6 offset:61696\n" \
  "v_mfma_f32_16x16x32_bf16 v[60:63], a[220:223], v[44:47], v[60:63]\n" \
  "v_mfma_f32_16x16x32_bf16 v[56:59], a[216:219], v[44:47], v[56:59]\n" \
  "s_waitcnt lgkmcnt(4)\n" \
  "v_mfma_f32_16x16x32_bf16 v[48:51], a[224:227], v[16:19], v[48:51]\n" \
  "ds_read_b128 v[40:43], v6 offset:63488\n" \
  "v_mfma_f32_16x16x32_bf16 v[52:55], a[228:231], v[16:19], v[52:55]\n" \
  "ds_read_b128 v[44:47], v6 offset:63744\n" \
  "v_mfma_f32_16x16x32_bf16 v[60:63], a[228:231], v[20:23], v[60:63]\n" \
  "s_max_u32 s57, s53, s58\n" \
  "v_mfma_f32_16x16x32_bf16 v[56:59], a[224:227], v[20:23], v[56:59]\n" \
  "s_waitcnt lgkmcnt(4)\n" \
  "v_mfma_f32_16x16x32_bf16 v[48:51], a[232:235], v[24:27], v[48:51]\n" \
  "s_waitcnt vmcnt(8)\n" \
  "s_barrier\n" \
  "ds_read_b128 v[16:19], v7 offset:0\n" \
  "v_mfma_f32_16x16x32_bf16 v[52:55], a[236:239], v[24:27], v[52:55]\n" \
  "ds_read_b128 v[20:23], v7 offset:256\n" \
  "v_mfma_f32_16x16x32_bf16 v[60:63], a[236:239], v[28:31], v[60:63]\n" \
  "buffer_atomic_add v10, v12, s[36:39], s57 offen\n" \
  "s_add_u32 s53, s53, s54\n" \
  "v_mfma_f32_16x16x32_bf16 v[56:59], a[232:235], v[28:31], v[56:59]\n" \
  "s_waitcnt lgkmcnt(4)\n" \
  "v_mfma_f32_16x16x32_bf16 v[48:51], a[240:243], v[32:35], v[48:51]\n" \
  "ds_read_b128 v[24:27], v7 offset:2048\n" \
  "v_mfma_f32_16x16x32_bf16 v[52:55], a[244:247], v[32:35], v[52:55]\n" \
  "ds_read_b128 v[28:31], v7 offset:2304\n" \
  "v_mfma_f32_16x16x32_bf16 v[60:63], a[244:247], v[36:39], v[60:63]\n" \
  "v_mov_b32 v12, v9\n" \
  "v_mfma_f32_16x16x32_bf16 v[56:59], a[240:243], v[36:39], v[56:59]\n" \
  "s_waitcnt lgkmcnt(4)\n" \
  "v_mfma_f32_16x16x32_bf16 v[48:51], a[248:251], v[40:43], v[48:51]\n" \
  "ds_read_b128 v[32:35], v7 offset:4096\n" \
  "v_mfma_f32_16x16x32_bf16 v[52:55], a[252:255], v[40:43], v[52:55]\n" \
  "ds_read_b128 v[36:39], v7 offset:4352\n" \
  "v_mfma_f32_16x16x32_bf16 v[60:63], a[252:255], v[44:47], v[60:63]\n" \
  "v_mfma_f32_16x16x32_bf16 v[56:59], a[248:251], v[44:47], v[56:59]\n" \
  "s_waitcnt lgkmcnt(4)\n" \
  "v_mfma_f32_16x16x32_bf16 v[64:67], a[0:3], v[16:19], 0\n" \
  "ds_read_b128 v[40:43], v7 offset:6144\n" \
  "v_mfma_f32_16x16x32_bf16 v[68:71], a[4:7], v[16:19], 0\n" \
  "s_lshr_b32 s56, s51, 8\n" \
  "ds_read_b128 v[44:47], v7 offset:6400\n" \
  "v_mfma_f32_16x16x32_bf16 v[76:79], a[4:7], v[20:23], 0\n" \
  "s_and_b32 s56, s56, 0x2000\n" \
  "s_xor_b32 s56, s56, s51\n" \
  "v_mfma_f32_16x16x32_bf16 v[72:75], a[0:3], v[20:23], 0\n" \
  "s_add_u32 s57, s56, s52\n" \
  "s_waitcnt lgkmcnt(4)\n" \
  "v_mfma_f32_16x16x32_bf16 v[64:67], a[8:11], v[24:27], v[64:67]\n" \
  "ds_read_b128 v[16:19], v7 offset:8192\n" \
  "v_mfma_f32_16x16x32_bf16 v[68:71], a[12:15], v[24:27], v[68:71]\n" \
  "s_add_u32 s51, s51, 0x80000\n" \
  "v_cvt_pk_bf16_f32 v80, v48, v49\n" \
  "ds_read_b128 v[20:23], v7 offset:8448\n" \
  "v_mfma_f32_16x16x32_bf16 v[76:79], a[12:15], v[28:31], v[76:79]\n" \
  "v_cvt_pk_bf16_f32 v81, v50, v51\n" \
  "v_cvt_pk_bf16_f32 v82, v52, v53\n" \
  "v_mfma_f32_16x16x32_bf16 v[72:75], a[8:11], v[28:31], v[72:75]\n" \
  "v_cvt_pk_bf16_f32 v83, v54, v55\n" \
  "s_waitcnt lgkmcnt(4)\n" \
  "v_mfma_f32_16x16x32_bf16 v[64:67], a[16:19], v[32:35], v[64:67]\n" \
  "ds_read_b128 v[24:27], v7 offset:10240\n" \
  "v_mfma_f32_16x16x32_bf16 v[68:71], a[20:23], v[32:35], v[68:71]\n" \
  "v_cvt_pk_bf16_f32 v84, v56, v57\n" \
  "buffer_store_dwordx4 v[80:83], v11, s[36:39], s56 offen sc1\n" \
  "ds_read_b128 v[28:31], v7 offset:10496\n" \
  "v_mfma_f32_16x16x32_bf16 v[76:79], a[20:23], v[36:39], v[76:79]\n" \
  "v_cvt_pk_bf16_f32 v85, v58, v59\n" \
  "v_cvt_pk_bf16_f32 v86, v60, v61\n" \
  "v_mfma_f32_16x16x32_bf16 v[72:75], a[16:19], v[36:39], v[72:75]\n" \
  "v_cvt_pk_bf16_f32 v87, v62, v63\n" \
  "s_waitcnt lgkmcnt(4)\n" \
  "v_mfma_f32_16x16x32_bf16 v[64:67], a[24:27], v[40:43], v[64:67]\n" \
  "ds_read_b128 v[32:35], v7 offset:12288\n" \
  "v_mfma_f32_16x16x32_bf16 v[68:71], a[28:31], v[40:43], v[68:71]\n" \
  "buffer_store_dwordx4 v[84:87], v11, s[36:39], s57 offen sc1\n" \
  "ds_read_b128 v[36:39], v7 offset:12544\n" \
  "v_mfma_f32_16x16x32_bf16 v[76:79], a[28:31], v[44:47], v[76:79]\n" \
  "v_mfma_f32_16x16x32_bf16 v[72:75], a[24:27], v[44:47], v[72:75]\n" \
  "s_waitcnt lgkmcnt(4)\n" \
  "v_mfma_f32_16x16x32_bf16 v[64:67], a[32:35], v[16:19], v[64:67]\n" \
  "ds_read_b128 v[40:43], v7 offset:14336\n" \
  "v_mfma_f32_16x16x32_bf16 v[68:71], a[36:39], v[16:19], v[68:71]\n" \
  "s_add_u32 m0, s49, 0\n" \
  "ds_read_b128 v[44:47], v7 offset:14592\n" \
  "v_mfma_f32_16x16x32_bf16 v[76:79], a[36:39], v[20:23], v[76:79]\n" \
  "buffer_load_dwordx4 v5, s[44:47], s50 offen sc1 lds\n" \
  "v_mfma_f32_16x16x32_bf16 v[72:75], a[32:35], v[20:23], v[72:75]\n" \
  "s_waitcnt lgkmcnt(4)\n" \
  "v_mfma_f32_16x16x32_bf16 v[64:67], a[40:43], v[24:27], v[64:67]\n" \
  "ds_read_b128 v[16:19], v7 offset:16384\n" \
  "v_mfma_f32_16x16x32_bf16 v[68:71], a[44:47], v[24:27], v[68:71]\n" \
  "ds_read_b128 v[20:23], v7 offset:16640\n" \
  "v_mfma_f32_16x16x32_bf16 v[76:79], a[44:47], v[28:31], v[76:79]\n" \
  "buffer_load_dwordx4 v5, s[44:47], s50 offen offset:1024 sc1 lds\n" \
  "v_mfma_f32_16x16x32_bf16 v[72:75], a[40:43], v[28:31], v[72:75]\n" \
  "s_waitcnt lgkmcnt(4)\n" \
  "v_mfma_f32_16x16x32_bf16 v[64:67], a[48:51], v[32:35], v[64:67]\n" \
  "ds_read_b128 v[24:27], v7 offset:18432\n" \
  "v_mfma_f32_16x16x32_bf16 v[68:71], a[52:55], v[32:35], v[68:71]\n" \
  "ds_read_b128 v[28:31], v7 offset:18688\n" \
  "v_mfma_f32_16x16x32_bf16 v[76:79], a[52:55], v[36:39], v[76:79]\n" \
  "buffer_load_dwordx4 v5, s[44:47], s50 offen offset:2048 sc1 lds\n" \
  "v_mfma_f32_16x16x32_bf16 v[72:75], a[48:51], v[36:39], v[72:75]\n" \
  "s_waitcnt lgkmcnt(4)\n" \
  "v_mfma_f32_16x16x32_bf16 v[64:67], a[56:59], v[40:43], v[64:67]\n" \
  "ds_read_b128 v[32:35], v7 offset:20480\n" \
  "v_mfma_f32_16x16x32_bf16 v[68:71], a[60:63], v[40:43], v[68:71]\n" \
  "ds_read_b128 v[36:39], v7 offset:20736\n" \
  "v_mfma_f32_16x16x32_bf16 v[76:79], a[60:63], v[44:47], v[76:79]\n" \
  "buffer_load_dwordx4 v5, s[44:47], s50 offen offset:3072 sc1 lds\n" \
  "s_add_u32 s50, s50, 4096\n" \
  "v_mfma_f32_16x16x32_bf16 v[72:75], a[56:59], v[44:47], v[72:75]\n" \
  "s_waitcnt lgkmcnt(4)\n" \
  "v_mfma_f32_16x16x32_bf16 v[64:67], a[64:67], v[16:19], v[64:67]\n" \
  "ds_read_b128 v[40:43], v7 offset:22528\n" \
  "v_mfma_f32_16x16x32_bf16 v[68:71], a[68:71], v[16:19], v[68:71]\n" \
  "s_add_u32 m0, s49, 4096\n" \
  "ds_read_b128 v[44:47], v7 offset:22784\n" \
  "v_mfma_f32_16x16x32_bf16 v[76:79], a[68:71], v[20:23], v[76:79]\n" \
  "buffer_load_dwordx4 v5, s[44:47], s50 offen sc1 lds\n" \
  "v_mfma_f32_16x16x32_bf16 v[72:75], a[64:67], v[20:23], v[72:75]\n" \
  "s_waitcnt lgkmcnt(4)\n" \
  "v_mfma_f32_16x16x32_bf16 v[64:67], a[72:75], v[24:27], v[64:67]\n" \
  "ds_read_b128 v[16:19], v7 offset:24576\n" \
  "v_mfma_f32_16x16x32_bf16 v[68:71], a[76:79], v[24:27], v[68:71]\n" \
  "ds_read_b128 v[20:23], v7 offset:24832\n" \
  "v_mfma_f32_16x16x32_bf16 v[76:79], a[76:79], v[28:31], v[76:79]\n" \
  "buffer_load_dwordx4 v5, s[44:47], s50 offen offset:1024 sc1 lds\n" \
  "v_mfma_f32_16x16x32_bf16 v[72:75], a[72:75], v[28:31], v[72:75]\n" \
  "s_waitcnt lgkmcnt(4)\n" \
  "v_mfma_f32_16x16x32_bf16 v[64:67], a[80:83], v[32:35], v[64:67]\n" \
  "ds_read_b128 v[24:27], v7 offset:26624\n" \
  "v_mfma_f32_16x16x32_bf16 v[68:71], a[84:87], v[32:35], v[68:71]\n" \
  "ds_read_b128 v[28:31], v7 offset:26880\n" \
  "v_mfma_f32_16x16x32_bf16 v[76:79], a[84:87], v[36:39], v[76:79]\n" \
  "buffer_load_dwordx4 v5, s[44:47], s50 offen offset:2048 sc1 lds\n" \
  "v_mfma_f32_16x16x32_bf16 v[72:75], a[80:83], v[36:39], v[72:75]\n" \
  "s_waitcnt lgkmcnt(4)\n" \
  "v_mfma_f32_16x16x32_bf16 v[64:67], a[88:91], v[40:43], v[64:67]\n" \
  "ds_read_b128 v[32:35], v7 offset:28672\n" \
  "v_mfma_f32_16x16x32_bf16 v[68:71], a[92:95], v[40:43], v[68:71]\n" \
  "ds_read_b128 v[36:39], v7 offset:28928\n" \
  "v_mfma_f32_16x16x32_bf16 v[76:79], a[92:95], v[44:47], v[76:79]\n" \
  "buffer_load_dwordx4 v5, s[44:47], s50 offen offset:3072 sc1 lds\n" \
  "s_add_u32 s50, s50, 28672\n" \
  "v_mfma_f32_16x16x32_bf16 v[72:75], a[88:91], v[44:47], v[72:75]\n" \
  "s_waitcnt lgkmcnt(4)\n" \
  "v_mfma_f32_16x16x32_bf16 v[64:67], a[96:99], v[16:19], v[64:67]\n" \
  "ds_read_b128 v[40:43], v7 offset:30720\n" \
  "v_mfma_f32_16x16x32_bf16 v[68:71], a[100:103], v[16:19], v[68:71]\n" \
  "ds_read_b128 v[44:47], v7 offset:30976\n" \
  "v_mfma_f32_16x16x32_bf16 v[76:79], a[100:103], v[20:23], v[76:79]\n" \
  "v_mfma_f32_16x16x32_bf16 v[72:75], a[96:99], v[20:23], v[72:75]\n" \
  "s_waitcnt lgkmcnt(4)\n" \
  "v_mfma_f32_16x16x32_bf16 v[64:67], a[104:107], v[24:27], v[64:67]\n" \
  "s_waitcnt vmcnt(10)\n" \
  "s_barrier\n" \
  "ds_read_b128 v[16:19], v7 offset:32768\n" \
  "v_mfma_f32_16x16x32_bf16 v[68:71], a[108:111], v[24:27], v[68:71]\n" \
  "ds_read_b128 v[20:23], v7 offset:33024\n" \
  "v_mfma_f32_16x16x32_bf16 v[76:79], a[108:111], v[28:31], v[76:79]\n" \
  "v_mfma_f32_16x16x32_bf16 v[72:75], a[104:107], v[28:31], v[72:75]\n" \
  "s_waitcnt lgkmcnt(4)\n" \
  "v_mfma_f32_16x16x32_bf16 v[64:67], a[112:115], v[32:35], v[64:67]\n" \
  "ds_read_b128 v[24:27], v7 offset:34816\n" \
  "v_mfma_f32_16x16x32_bf16 v[68:71], a[116:119], v[32:35], v[68:71]\n" \
  "ds_read_b128 v[28:31], v7 offset:35072\n" \
  "v_mfma_f32_16x16x32_bf16 v[76:79], a[116:119], v[36:39], v[76:79]\n" \
  "v_mfma_f32_16x16x32_bf16 v[72:75], a[112:115], v[36:39], v[72:75]\n" \
  "s_waitcnt lgkmcnt(4)\n" \
  "v_mfma_f32_16x16x32_bf16 v[64:67], a[120:123], v[40:43], v[64:67]\n" \
  "ds_read_b128 v[32:35], v7 offset:36864\n" \
  "v_mfma_f32_16x16x32_bf16 v[68:71], a[124:127], v[40:43], v[68:71]\n" \
  "ds_read_b128 v[36:39], v7 offset:37120\n" \
  "v_mfma_f32_16x16x32_bf16 v[76:79], a[124:127], v[44:47], v[76:79]\n" \
  "v_mfma_f32_16x16x32_bf16 v[72:75], a[120:123], v[44:47], v[72:75]\n" \
  "s_waitcnt lgkmcnt(4)\n" \
  "v_mfma_f32_16x16x32_bf16 v[64:67], a[128:131], v[16:19], v[64:67]\n" \
  "ds_read_b128 v[40:43], v7 offset:38912\n" \
  "v_mfma_f32_16x16x32_bf16 v[68:71], a[132:135], v[16:19], v[68:71]\n" \
  "ds_read_b128 v[44:47], v7 offset:39168\n" \
  "v_mfma_f32_16x16x32_bf16 v[76:79], a[132:135], v[20:23], v[76:79]\n" \
  "v_mfma_f32_16x16x32_bf16 v[72:75], a[128:131], v[20:23], v[72:75]\n" \
  "s_waitcnt lgkmcnt(4)\n" \
  "v_mfma_f32_16x16x32_bf16 v[64:67], a[136:139], v[24:27], v[64:67]\n" \
  "ds_read_b128 v[16:19], v7 offset:40960\n" \
  "v_mfma_f32_16x16x32_bf16 v[68:71], a[140:143], v[24:27], v[68:71]\n" \
  "s_add_u32 m0, s49, 32768\n" \
  "ds_read_b128 v[20:23], v7 offset:41216\n" \
  "v_mfma_f32_16x16x32_bf16 v[76:79], a[140:143], v[28:31], v[76:79]\n" \
  "buffer_load_dwordx4 v5, s[44:47], s50 offen sc1 lds\n" \
  "v_mfma_f32_16x16x32_bf16 v[72:75], a[136:139], v[28:31], v[72:75]\n" \
  "s_waitcnt lgkmcnt(4)\n" \
  "v_mfma_f32_16x16x32_bf16 v[64:67], a[144:147], v[32:35], v[64:67]\n" \
  "ds_read_b128 v[24:27], v7 offset:43008\n" \
  "v_mfma_f32_16x16x32_bf16 v[68:71], a[148:151], v[32:35], v[68:71]\n" \
  "ds_read_b128 v[28:31], v7 offset:43264\n" \
  "v_mfma_f32_16x16x32_bf16 v[76:79], a[148:151], v[36:39], v[76:79]\n" \
  "buffer_load_dwordx4 v5, s[44:47], s50 offen offset:1024 sc1 lds\n" \
  "v_mfma_f32_16x16x32_bf16 v[72:75], a[144:147], v[36:39], v[72:75]\n" \
  "s_waitcnt lgkmcnt(4)\n" \
  "v_mfma_f32_16x16x32_bf16 v[64:67], a[152:155], v[40:43], v[64:67]\n" \
  "ds_read_b128 v[32:35], v7 offset:45056\n" \
  "v_mfma_f32_16x16x32_bf16 v[68:71], a[156:159], v[40:43], v[68:71]\n" \
  "ds_read_b128 v[36:39], v7 offset:45312\n" \
  "v_mfma_f32_16x16x32_bf16 v[76:79], a[156:159], v[44:47], v[76:79]\n" \
  "buffer_load_dwordx4 v5, s[44:47], s50 offen offset:2048 sc1 lds\n" \
  "v_mfma_f32_16x16x32_bf16 v[72:75], a[152:155], v[44:47], v[72:75]\n" \
  "s_waitcnt lgkmcnt(4)\n" \
  "v_mfma_f32_16x16x32_bf16 v[64:67], a[160:163], v[16:19], v[64:67]\n" \
  "ds_read_b128 v[40:43], v7 offset:47104\n" \
  "v_mfma_f32_16x16x32_bf16 v[68:71], a[164:167], v[16:19], v[68:71]\n" \
  "ds_read_b128 v[44:47], v7 offset:47360\n" \
  "v_mfma_f32_16x16x32_bf16 v[76:79], a[164:167], v[20:23], v[76:79]\n" \
  "buffer_load_dwordx4 v5, s[44:47], s50 offen offset:3072 sc1 lds\n" \
  "s_add_u32 s50, s50, 4096\n" \
  "v_mfma_f32_16x16x32_bf16 v[72:75], a[160:163], v[20:23], v[72:75]\n" \
  "s_waitcnt lgkmcnt(4)\n" \
  "v_mfma_f32_16x16x32_bf16 v[64:67], a[168:171], v[24:27], v[64:67]\n" \
  "ds_read_b128 v[16:19], v7 offset:49152\n" \
  "v_mfma_f32_16x16x32_bf16 v[68:71], a[172:175], v[24:27], v[68:71]\n" \
  "s_add_u32 m0, s49, 36864\n" \
  "ds_read_b128 v[20:23], v7 offset:49408\n" \
  "v_mfma_f32_16x16x32_bf16 v[76:79], a[172:175], v[28:31], v[76:79]\n" \
  "buffer_load_dwordx4 v5, s[44:47], s50 offen sc1 lds\n" \
  "v_mfma_f32_16x16x32_bf16 v[72:75], a[168:171], v[28:31], v[72:75]\n" \
  "s_waitcnt lgkmcnt(4)\n" \
  "v_mfma_f32_16x16x32_bf16 v[64:67], a[176:179], v[32:35], v[64:67]\n" \
  "ds_read_b128 v[24:27], v7 offset:51200\n" \
  "v_mfma_f32_16x16x32_bf16 v[68:71], a[180:183], v[32:35], v[68:71]\n" \
  "ds_read_b128 v[28:31], v7 offset:51456\n" \
  "v_mfma_f32_16x16x32_bf16 v[76:79], a[180:183], v[36:39], v[76:79]\n" \
  "buffer_load_dwordx4 v5, s[44:47], s50 offen offset:1024 sc1 lds\n" \
  "v_mfma_f32_16x16x32_bf16 v[72:75], a[176:179], v[36:39], v[72:75]\n" \
  "s_waitcnt lgkmcnt(4)\n" \
  "v_mfma_f32_16x16x32_bf16 v[64:67], a[184:187], v[40:43], v[64:67]\n" \
  "ds_read_b128 v[32:35], v7 offset:53248\n" \
  "v_mfma_f32_16x16x32_bf16 v[68:71], a[188:191], v[40:43], v[68:71]\n" \
  "ds_read_b128 v[36:39], v7 offset:53504\n" \
  "v_mfma_f32_16x16x32_bf16 v[76:79], a[188:191], v[44:47], v[76:79]\n" \
  "buffer_load_dwordx4 v5, s[44:47], s50 offen offset:2048 sc1 lds\n" \
  "v_mfma_f32_16x16x32_bf16 v[72:75], a[184:187], v[44:47], v[72:75]\n" \
  "s_waitcnt lgkmcnt(4)\n" \
  "v_mfma_f32_16x16x32_bf16 v[64:67], a[192:195], v[16:19], v[64:67]\n" \
  "ds_read_b128 v[40:43], v7 offset:55296\n" \
  "v_mfma_f32_16x16x32_bf16 v[68:71], a[196:199], v[16:19], v[68:71]\n" \
  "ds_read_b128 v[44:47], v7 offset:55552\n" \
  "v_mfma_f32_16x16x32_bf16 v[76:79], a[196:199], v[20:23], v[76:79]\n" \
  "buffer_load_dwordx4 v5, s[44:47], s50 offen offset:3072 sc1 lds\n" \
  "s_add_u32 s50, s50, 28672\n" \
  "v_mfma_f32_16x16x32_bf16 v[72:75], a[192:195], v[20:23], v[72:75]\n" \
  "s_waitcnt lgkmcnt(4)\n" \
  "v_mfma_f32_16x16x32_bf16 v[64:67], a[200:203], v[24:27], v[64:67]\n" \
  "ds_read_b128 v[16:19], v7 offset:57344\n" \
  "v_mfma_f32_16x16x32_bf16 v[68:71], a[204:207], v[24:27], v[68:71]\n" \
  "ds_read_b128 v[20:23], v7 offset:57600\n" \
  "v_mfma_f32_16x16x32_bf16 v[76:79], a[204:207], v[28:31], v[76:79]\n" \
  "v_mfma_f32_16x16x32_bf16 v[72:75], a[200:203], v[28:31], v[72:75]\n" \
  "s_waitcnt lgkmcnt(4)\n" \
  "v_mfma_f32_16x16x32_bf16 v[64:67], a[208:211], v[32:35], v[64:67]\n" \
  "ds_read_b128 v[24:27], v7 offset:59392\n" \
  "v_mfma_f32_16x16x32_bf16 v[68:71], a[212:215], v[32:35], v[68:71]\n" \
  "ds_read_b128 v[28:31], v7 offset:59648\n" \
  "v_mfma_f32_16x16x32_bf16 v[76:79], a[212:215], v[36:39], v[76:79]\n" \
  "v_mfma_f32_16x16x32_bf16 v[72:75], a[208:211], v[36:39], v[72:75]\n" \
  "s_waitcnt lgkmcnt(4)\n" \
  "v_mfma_f32_16x16x32_bf16 v[64:67], a[216:219], v[40:43], v[64:67]\n" \
  "ds_read_b128 v[32:35], v7 offset:61440\n" \
  "v_mfma_f32_16x16x32_bf16 v[68:71], a[220:223], v[40:43], v[68:71]\n" \
  "ds_read_b128 v[36:39], v7 offset:61696\n" \
  "v_mfma_f32_16x16x32_bf16 v[76:79], a[220:223], v[44:47], v[76:79]\n" \
  "v_mfma_f32_16x16x32_bf16 v[72:75], a[216:219], v[44:47], v[72:75]\n" \
  "s_waitcnt lgkmcnt(4)\n" \
  "v_mfma_f32_16x16x32_bf16 v[64:67], a[224:227], v[16:19], v[64:67]\n" \
  "ds_read_b128 v[40:43], v7 offset:63488\n" \
  "v_mfma_f32_16x16x32_bf16 v[68:71], a[228:231], v[16:19], v[68:71]\n" \
  "ds_read_b128 v[44:47], v7 offset:63744\n" \
  "v_mfma_f32_16x16x32_bf16 v[76:79], a[228:231], v[20:23], v[76:79]\n" \
  "s_max_u32 s57, s53, s58\n" \
  "v_mfma_f32_16x16x32_bf16 v[72:75], a[224:227], v[20:23], v[72:75]\n" \
  "s_waitcnt lgkmcnt(4)\n" \
  "v_mfma_f32_16x16x32_bf16 v[64:67], a[232:235], v[24:27], v[64:67]\n" \
  "s_waitcnt vmcnt(8)\n" \
  "s_barrier\n" \
  "ds_read_b128 v[16:19], v6 offset:0\n" \
  "v_mfma_f32_16x16x32_bf16 v[68:71], a[236:239], v[24:27], v[68:71]\n" \
  "ds_read_b128 v[20:23], v6 offset:256\n" \
  "v_mfma_f32_16x16x32_bf16 v[76:79], a[236:239], v[28:31], v[76:79]\n" \
  "buffer_atomic_add v10, v12, s[36:39], s57 offen\n" \
  "s_add_u32 s53, s53, s54\n" \
  "v_mfma_f32_16x16x32_bf16 v[72:75], a[232:235], v[28:31], v[72:75]\n" \
  "s_waitcnt lgkmcnt(4)\n" \
  "v_mfma_f32_16x16x32_bf16 v[64:67], a[240:243], v[32:35], v[64:67]\n" \
  "ds_read_b128 v[24:27], v6 offset:2048\n" \
  "v_mfma_f32_16x16x32_bf16 v[68:71], a[244:247], v[32:35], v[68:71]\n" \
  "ds_read_b128 v[28:31], v6 offset:2304\n" \
  "v_mfma_f32_16x16x32_bf16 v[76:79], a[244:247], v[36:39], v[76:79]\n" \
  "v_mfma_f32_16x16x32_bf16 v[72:75], a[240:243], v[36:39], v[72:75]\n" \
  "s_waitcnt lgkmcnt(4)\n" \
  "v_mfma_f32_16x16x32_bf16 v[64:67], a[248:251], v[40:43], v[64:67]\n" \
  "ds_read_b128 v[32:35], v6 offset:4096\n" \
  "v_mfma_f32_16x16x32_bf16 v[68:71], a[252:255], v[40:43], v[68:71]\n" \
  "ds_read_b128 v[36:39], v6 offset:4352\n" \
  "v_mfma_f32_16x16x32_bf16 v[76:79], a[252:255], v[44:47], v[76:79]\n" \
  "s_sub_u32 s55, s55, 1\n" \
  "s_cmp_lg_u32 s55, 0\n" \
  "v_mfma_f32_16x16x32_bf16 v[72:75], a[248:251], v[44:47], v[72:75]\n" \
  "s_cbranch_scc1 SQ_top_%=\n" \
  "s_lshr_b32 s56, s51, 8\n" \
  "s_and_b32 s56, s56, 0x2000\n" \
  "s_xor_b32 s56, s56, s51\n" \
  "s_add_u32 s57, s56, s52\n" \
  "s_add_u32 s51, s51, 0x80000\n" \
  "v_cvt_pk_bf16_f32 v80, v64, v65\n" \
  "v_cvt_pk_bf16_f32 v81, v66, v67\n" \
  "v_cvt_pk_bf16_f32 v82, v68, v69\n" \
  "v_cvt_pk_bf16_f32 v83, v70, v71\n" \
  "v_cvt_pk_bf16_f32 v84, v72, v73\n" \
  "v_cvt_pk_bf16_f32 v85, v74, v75\n" \
  "v_cvt_pk_bf16_f32 v86, v76, v77\n" \
  "v_cvt_pk_bf16_f32 v87, v78, v79\n" \
  "buffer_store_dwordx4 v[80:83], v8, s[36:39], s56 offen sc1\n" \
  "buffer_store_dwordx4 v[84:87], v8, s[36:39], s57 offen sc1\n" \
  "s_waitcnt lgkmcnt(0)\n" \
  "s_waitcnt lgkmcnt(0)\n" \
  "s_branch SQ_end_%=\n" \
  "SQ_ys_%=:\n" \
  "s_sub_u32 s60, s60, 1\n" \
  "s_cbranch_scc1 SQ_ysx_%=\n" \
  "s_sleep 2\n" \
  "s_lshr_b32 s56, s59, 17\n" \
  "s_and_b32 s56, s56, 0xf80\n" \
  "s_add_u32 s56, s56, 252182528\n" \
  "buffer_load_dword v14, off, s[36:39], s56 sc1\n" \
  "s_waitcnt vmcnt(0)\n" \
  "s_branch SQ_yc_%=\n" \
  "SQ_ysx_%=:\n" \
  "s_mov_b32 s60, 0\n" \
  "s_branch SQ_ye_%=\n" \
  "SQ_yt_%=:\n" \
  "s_sub_u32 s60, s60, 1\n" \
  "s_cbranch_scc1 SQ_ytx_%=\n" \
  "s_sleep 2\n" \
  "s_lshr_b32 s56, s59, 16\n" \
  "s_and_b32 s56, s56, 0x1f00\n" \
  "s_add_u32 s56, s56, 252190720\n" \
  "buffer_load_dword v14, v13, s[36:39], s56 offen sc1\n" \
  "s_waitcnt vmcnt(0)\n" \
  "s_branch SQ_yf_%=\n" \
  "SQ_ytx_%=:\n" \
  "s_mov_b32 s60, 0\n" \
  "s_branch SQ_yg_%=\n" \
  "SQ_yb_%=:\n" \
  "s_and_b32 s57, s57, 28\n" \
  "s_lshl_b32 s57, s57, 6\n" \
  "s_or_b32 s59, s59, s57\n" \
  "s_or_b32 s59, s59, 1\n" \
  "s_waitcnt vmcnt(0) lgkmcnt(0)\n" \
  "s_branch SQ_end_%=\n" \
  "SQ_end_%=:\n" \
  "s_nop 1\n"
#define SQ_CLOB "v2", "v3", "v4", "v7", "v11", "v12", "v13", "v14", "v15", "v16", "v17", "v18", "v19", "v20", "v21", "v22", "v23", "v24", "v25", "v26", "v27", "v28", "v29", "v30", "v31", "v32", "v33", "v34", "v35", "v36", "v37", "v38", "v39", "v40", "v41", "v42", "v43", "v44", "v45", "v46", "v47", "v48", "v49", "v50", "v51", "v52", "v53", "v54", "v55", "v56", "v57", "v58", "v59", "v60", "v61", "v62", "v63", "v64", "v65", "v66", "v67", "v68", "v69", "v70", "v71", "v72", "v73", "v74", "v75", "v76", "v77", "v78", "v79", "v80", "v81", "v82", "v83", "v84", "v85", "v86", "v87", "s56", "s57", "vcc", "a0", "a1", "a2", "a3", "a4", "a5", "a6", "a7", "a8", "a9", "a10", "a11", "a12", "a13", "a14", "a15", "a16", "a17", "a18", "a19", "a20", "a21", "a22", "a23", "a24", "a25", "a26", "a27", "a28", "a29", "a30", "a31", "a32", "a33", "a34", "a35", "a36", "a37", "a38", "a39", "a40", "a41", "a42", "a43", "a44", "a45", "a46", "a47", "a48", "a49", "a50", "a51", "a52", "a53", "a54", "a55", "a56", "a57", "a58", "a59", "a60", "a61", "a62", "a63", "a64", "a65", "a66", "a67", "a68", "a69", "a70", "a71", "a72", "a73", "a74", "a75", "a76", "a77", "a78", "a79", "a80", "a81", "a82", "a83", "a84", "a85", "a86", "a87", "a88", "a89", "a90", "a91", "a92", "a93", "a94", "a95", "a96", "a97", "a98", "a99", "a100", "a101", "a102", "a103", "a104", "a105", "a106", "a107", "a108", "a109", "a110", "a111", "a112", "a113", "a114", "a115", "a116", "a117", "a118", "a119", "a120", "a121", "a122", "a123", "a124", "a125", "a126", "a127", "a128", "a129", "a130", "a131", "a132", "a133", "a134", "a135", "a136", "a137", "a138", "a139", "a140", "a141", "a142", "a143", "a144", "a145", "a146", "a147", "a148", "a149", "a150", "a151", "a152", "a153", "a154", "a155", "a156", "a157", "a158", "a159", "a160", "a161", "a162", "a163", "a164", "a165", "a166", "a167", "a168", "a169", "a170", "a171", "a172", "a173", "a174", "a175", "a176", "a177", "a178", "a179", "a180", "a181", "a182", "a183", "a184", "a185", "a186", "a187", "a188", "a189", "a190", "a191", "a192", "a193", "a194", "a195", "a196", "a197", "a198", "a199", "a200", "a201", "a202", "a203", "a204", "a205", "a206", "a207", "a208", "a209", "a210", "a211", "a212", "a213", "a214", "a215", "a216", "a217", "a218", "a219", "a220", "a221", "a222", "a223", "a224", "a225", "a226", "a227", "a228", "a229", "a230", "a231", "a232", "a233", "a234", "a235", "a236", "a237", "a238", "a239", "a240", "a241", "a242", "a243", "a244", "a245", "a246", "a247", "a248", "a249", "a250", "a251", "a252", "a253", "a254", "a255", "scc", "memory"
#define SU_ASM \
  "v_xor_b32 v2, 32, v1\n" \
  "v_xor_b32 v3, 64, v1\n" \
  "v_xor_b32 v4, 96, v1\n" \
  "v_mov_b32 v11, 0x40000000\n" \
  "v_mov_b32 v12, 0x40000000\n" \
  "v_mov_b32 v10, 2\n" \
  "v_mov_b32 v13, 0\n" \
  "s_lshr_b32 s56, s59, 17\n" \
  "s_and_b32 s56, s56, 0xf80\n" \
  "s_add_u32 s56, s56, 252182528\n" \
  "buffer_load_dword v8, off, s[36:39], s56 sc1\n" \
  "s_add_u32 m0, s48, 0\n" \
  "s_mov_b32 s56, 0\n" \
  "buffer_load_dwordx4 v0, s[40:43], s56 offen lds\n" \
  "s_add_u32 m0, s48, 1024\n" \
  "s_mov_b32 s56, 16384\n" \
  "buffer_load_dwordx4 v0, s[40:43], s56 offen lds\n" \
  "s_add_u32 m0, s48, 2048\n" \
  "s_mov_b32 s56, 32768\n" \
  "buffer_load_dwordx4 v0, s[40:43], s56 offen lds\n" \
  "s_add_u32 m0, s48, 3072\n" \
  "s_mov_b32 s56, 49152\n" \
  "buffer_load_dwordx4 v0, s[40:43], s56 offen lds\n" \
  "s_add_u32 m0, s48, 4096\n" \
  "s_mov_b32 s56, 65536\n" \
  "buffer_load_dwordx4 v0, s[40:43], s56 offen lds\n" \
  "s_add_u32 m0, s48, 5120\n" \
  "s_mov_b32 s56, 81920\n" \
  "buffer_load_dwordx4 v0, s[40:43], s56 offen lds\n" \
  "s_add_u32 m0, s48, 6144\n" \
  "s_mov_b32 s56, 98304\n" \
  "buffer_load_dwordx4 v0, s[40:43], s56 offen lds\n" \
  "s_add_u32 m0, s48, 7168\n" \
  "s_mov_b32 s56, 114688\n" \
  "buffer_load_dwordx4 v0, s[40:43], s56 offen lds\n" \
  "s_add_u32 m0, s48, 8192\n" \
  "s_mov_b32 s56, 131072\n" \
  "buffer_load_dwordx4 v0, s[40:43], s56 offen lds\n" \
  "s_add_u32 m0, s48, 9216\n" \
  "s_mov_b32 s56, 147456\n" \
  "buffer_load_dwordx4 v0, s[40:43], s56 offen lds\n" \
  "s_add_u32 m0, s48, 10240\n" \
  "s_mov_b32 s56, 163840\n" \
  "buffer_load_dwordx4 v0, s[40:43], s56 offen lds\n" \
  "s_add_u32 m0, s48, 11264\n" \
  "s_mov_b32 s56, 180224\n" \
  "buffer_load_dwordx4 v0, s[40:43], s56 offen lds\n" \
  "s_add_u32 m0, s48, 12288\n" \
  "s_mov_b32 s56, 196608\n" \
  "buffer_load_dwordx4 v0, s[40:43], s56 offen lds\n" \
  "s_add_u32 m0, s48, 13312\n" \
  "s_mov_b32 s56, 212992\n" \
  "buffer_load_dwordx4 v0, s[40:43], s56 offen lds\n" \
  "s_add_u32 m0, s48, 14336\n" \
  "s_mov_b32 s56, 229376\n" \
  "buffer_load_dwordx4 v0, s[40:43], s56 offen lds\n" \
  "s_add_u32 m0, s48, 15360\n" \
  "s_mov_b32 s56, 245760\n" \
  "buffer_load_dwordx4 v0, s[40:43], s56 offen lds\n" \
  "s_add_u32 m0, s48, 16384\n" \
  "s_mov_b32 s56, 128\n" \
  "buffer_load_dwordx4 v0, s[40:43], s56 offen lds\n" \
  "s_add_u32 m0, s48, 17408\n" \
  "s_mov_b32 s56, 16512\n" \
  "buffer_load_dwordx4 v0, s[40:43], s56 offen lds\n" \
  "s_add_u32 m0, s48, 18432\n" \
  "s_mov_b32 s56, 32896\n" \
  "buffer_load_dwordx4 v0, s[40:43], s56 offen lds\n" \
  "s_add_u32 m0, s48, 19456\n" \
  "s_mov_b32 s56, 49280\n" \
  "buffer_load_dwordx4 v0, s[40:43], s56 offen lds\n" \
  "s_add_u32 m0, s48, 20480\n" \
  "s_mov_b32 s56, 65664\n" \
  "buffer_load_dwordx4 v0, s[40:43], s56 offen lds\n" \
  "s_add_u32 m0, s48, 21504\n" \
  "s_mov_b32 s56, 82048\n" \
  "buffer_load_dwordx4 v0, s[40:43], s56 offen lds\n" \
  "s_add_u32 m0, s48, 22528\n" \
  "s_mov_b32 s56, 98432\n" \
  "buffer_load_dwordx4 v0, s[40:43], s56 offen lds\n" \
  "s_add_u32 m0, s48, 23552\n" \
  "s_mov_b32 s56, 114816\n" \
  "buffer_load_dwordx4 v0, s[40:43], s56 offen lds\n" \
  "s_lshr_b32 s56, s59, 17\n" \
  "s_and_b32 s56, s56, 0xf80\n" \
  "s_add_u32 s56, s56, 252182528\n" \
  "buffer_load_dword v14, off, s[36:39], s56 sc1\n" \
  "s_add_u32 m0, s48, 24576\n" \
  "s_mov_b32 s56, 131200\n" \
  "buffer_load_dwordx4 v0, s[40:43], s56 offen lds\n" \
  "s_add_u32 m0, s48, 25600\n" \
  "s_mov_b32 s56, 147584\n" \
  "buffer_load_dwordx4 v0, s[40:43], s56 offen lds\n" \
  "s_add_u32 m0, s48, 26624\n" \
  "s_mov_b32 s56, 163968\n" \
  "buffer_load_dwordx4 v0, s[40:43], s56 offen lds\n" \
  "s_add_u32 m0, s48, 27648\n" \
  "s_mov_b32 s56, 180352\n" \
  "buffer_load_dwordx4 v0, s[40:43], s56 offen lds\n" \
  "s_add_u32 m0, s48, 28672\n" \
  "s_mov_b32 s56, 196736\n" \
  "buffer_load_dwordx4 v0, s[40:43], s56 offen lds\n" \
  "s_add_u32 m0, s48, 29696\n" \
  "s_mov_b32 s56, 213120\n" \
  "buffer_load_dwordx4 v0, s[40:43], s56 offen lds\n" \
  "s_add_u32 m0, s48, 30720\n" \
  "s_mov_b32 s56, 229504\n" \
  "buffer_load_dwordx4 v0, s[40:43], s56 offen lds\n" \
  "s_add_u32 m0, s48, 31744\n" \
  "s_mov_b32 s56, 245888\n" \
  "buffer_load_dwordx4 v0, s[40:43], s56 offen lds\n" \
  "s_waitcnt vmcnt(25)\n" \
  "ds_read_b64_tr_b16 a[0:1], v1 offset:0\n" \
  "ds_read_b64_tr_b16 a[2:3], v1 offset:2048\n" \
  "ds_read_b64_tr_b16 a[4:5], v1 offset:4096\n" \
  "ds_read_b64_tr_b16 a[6:7], v1 offset:6144\n" \
  "ds_read_b64_tr_b16 a[16:17], v2 offset:0\n" \
  "ds_read_b64_tr_b16 a[18:19], v2 offset:2048\n" \
  "ds_read_b64_tr_b16 a[20:21], v2 offset:4096\n" \
  "ds_read_b64_tr_b16 a[22:23], v2 offset:6144\n" \
  "ds_read_b64_tr_b16 a[32:33], v3 offset:0\n" \
  "ds_read_b64_tr_b16 a[34:35], v3 offset:2048\n" \
  "ds_read_b64_tr_b16 a[36:37], v3 offset:4096\n" \
  "ds_read_b64_tr_b16 a[38:39], v3 offset:6144\n" \
  "ds_read_b64_tr_b16 a[48:49], v4 offset:0\n" \
  "ds_read_b64_tr_b16 a[50:51], v4 offset:2048\n" \
  "ds_read_b64_tr_b16 a[52:53], v4 offset:4096\n" \
  "ds_read_b64_tr_b16 a[54:55], v4 offset:6144\n" \
  "s_waitcnt lgkmcnt(0)\n" \
  "s_add_u32 m0, s48, 0\n" \
  "s_mov_b32 s56, 256\n" \
  "buffer_load_dwordx4 v0, s[40:43], s56 offen lds\n" \
  "s_add_u32 m0, s48, 1024\n" \
  "s_mov_b32 s56, 16640\n" \
  "buffer_load_dwordx4 v0, s[40:43], s56 offen lds\n" \
  "s_add_u32 m0, s48, 2048\n" \
  "s_mov_b32 s56, 33024\n" \
  "buffer_load_dwordx4 v0, s[40:43], s56 offen lds\n" \
  "s_add_u32 m0, s48, 3072\n" \
  "s_mov_b32 s56, 49408\n" \
  "buffer_load_dwordx4 v0, s[40:43], s56 offen lds\n" \
  "s_add_u32 m0, s48, 4096\n" \
  "s_mov_b32 s56, 65792\n" \
  "buffer_load_dwordx4 v0, s[40:43], s56 offen lds\n" \
  "s_add_u32 m0, s48, 5120\n" \
  "s_mov_b32 s56, 82176\n" \
  "buffer_load_dwordx4 v0, s[40:43], s56 offen lds\n" \
  "s_add_u32 m0, s48, 6144\n" \
  "s_mov_b32 s56, 98560\n" \
  "buffer_load_dwordx4 v0, s[40:43], s56 offen lds\n" \
  "s_add_u32 m0, s48, 7168\n" \
  "s_mov_b32 s56, 114944\n" \
  "buffer_load_dwordx4 v0, s[40:43], s56 offen lds\n" \
  "s_waitcnt vmcnt(25)\n" \
  "ds_read_b64_tr_b16 a[8:9], v1 offset:8192\n" \
  "ds_read_b64_tr_b16 a[10:11], v1 offset:10240\n" \
  "ds_read_b64_tr_b16 a[12:13], v1 offset:12288\n" \
  "ds_read_b64_tr_b16 a[14:15], v1 offset:14336\n" \
  "ds_read_b64_tr_b16 a[24:25], v2 offset:8192\n" \
  "ds_read_b64_tr_b16 a[26:27], v2 offset:10240\n" \
  "ds_read_b64_tr_b16 a[28:29], v2 offset:12288\n" \
  "ds_read_b64_tr_b16 a[30:31], v2 offset:14336\n" \
  "ds_read_b64_tr_b16 a[40:41], v3 offset:8192\n" \
  "ds_read_b64_tr_b16 a[42:43], v3 offset:10240\n" \
  "ds_read_b64_tr_b16 a[44:45], v3 offset:12288\n" \
  "ds_read_b64_tr_b16 a[46:47], v3 offset:14336\n" \
  "ds_read_b64_tr_b16 a[56:57], v4 offset:8192\n" \
  "ds_read_b64_tr_b16 a[58:59], v4 offset:10240\n" \
  "ds_read_b64_tr_b16 a[60:61], v4 offset:12288\n" \
  "ds_read_b64_tr_b16 a[62:63], v4 offset:14336\n" \
  "s_waitcnt lgkmcnt(0)\n" \
  "s_add_u32 m0, s48, 8192\n" \
  "s_mov_b32 s56, 131328\n" \
  "buffer_load_dwordx4 v0, s[40:43], s56 offen lds\n" \
  "s_add_u32 m0, s48, 9216\n" \
  "s_mov_b32 s56, 147712\n" \
  "buffer_load_dwordx4 v0, s[40:43], s56 offen lds\n" \
  "s_add_u32 m0, s48, 10240\n" \
  "s_mov_b32 s56, 164096\n" \
  "buffer_load_dwordx4 v0, s[40:43], s56 offen lds\n" \
  "s_add_u32 m0, s48, 11264\n" \
  "s_mov_b32 s56, 180480\n" \
  "buffer_load_dwordx4 v0, s[40:43], s56 offen lds\n" \
  "s_add_u32 m0, s48, 12288\n" \
  "s_mov_b32 s56, 196864\n" \
  "buffer_load_dwordx4 v0, s[40:43], s56 offen lds\n" \
  "s_add_u32 m0, s48, 13312\n" \
  "s_mov_b32 s56, 213248\n" \
  "buffer_load_dwordx4 v0, s[40:43], s56 offen lds\n" \
  "s_add_u32 m0, s48, 14336\n" \
  "s_mov_b32 s56, 229632\n" \
  "buffer_load_dwordx4 v0, s[40:43], s56 offen lds\n" \
  "s_add_u32 m0, s48, 15360\n" \
  "s_mov_b32 s56, 246016\n" \
  "buffer_load_dwordx4 v0, s[40:43], s56 offen lds\n" \
  "s_waitcnt vmcnt(25)\n" \
  "ds_read_b64_tr_b16 a[64:65], v1 offset:16384\n" \
  "ds_read_b64_tr_b16 a[66:67], v1 offset:18432\n" \
  "ds_read_b64_tr_b16 a[68:69], v1 offset:20480\n" \
  "ds_read_b64_tr_b16 a[70:71], v1 offset:22528\n" \
  "ds_read_b64_tr_b16 a[80:81], v2 offset:16384\n" \
  "ds_read_b64_tr_b16 a[82:83], v2 offset:18432\n" \
  "ds_read_b64_tr_b16 a[84:85], v2 offset:20480\n" \
  "ds_read_b64_tr_b16 a[86:87], v2 offset:22528\n" \
  "ds_read_b64_tr_b16 a[96:97], v3 offset:16384\n" \
  "ds_read_b64_tr_b16 a[98:99], v3 offset:18432\n" \
  "ds_read_b64_tr_b16 a[100:101], v3 offset:20480\n" \
  "ds_read_b64_tr_b16 a[102:103], v3 offset:22528\n" \
  "ds_read_b64_tr_b16 a[112:113], v4 offset:16384\n" \
  "ds_read_b64_tr_b16 a[114:115], v4 offset:18432\n" \
  "ds_read_b64_tr_b16 a[116:117], v4 offset:20480\n" \
  "ds_read_b64_tr_b16 a[118:119], v4 offset:22528\n" \
  "s_waitcnt lgkmcnt(0)\n" \
  "s_waitcnt vmcnt(24)\n" \
  "s_and_b32 s56, s59, 34\n" \
  "s_cbranch_scc1 SU_ye_%=\n" \
  "SU_yc_%=:\n" \
  "v_readfirstlane_b32 s56, v8\n" \
  "v_readfirstlane_b32 s57, v14\n" \
  "s_cmp_eq_u32 s56, s57\n" \
  "s_cbranch_scc1 SU_ys_%=\n" \
  "s_xor_b32 s56, s57, s59\n" \
  "s_and_b32 s56, s56, 28\n" \
  "s_cbranch_scc1 SU_yb_%=\n" \
  "SU_ye_%=:\n" \
  "v_mbcnt_lo_u32_b32 v8, -1, 0\n" \
  "v_mbcnt_hi_u32_b32 v8, -1, v8\n" \
  "v_lshlrev_b32 v8, 2, v8\n" \
  "s_lshr_b32 s56, s59, 16\n" \
  "s_and_b32 s56, s56, 0x1f00\n" \
  "s_add_u32 s56, s56, 252190720\n" \
  "buffer_load_dword v14, v8, s[36:39], s56 offen sc1\n" \
  "s_add_u32 m0, s48, 16384\n" \
  "s_mov_b32 s56, 384\n" \
  "buffer_load_dwordx4 v0, s[40:43], s56 offen lds\n" \
  "s_add_u32 m0, s48, 17408\n" \
  "s_mov_b32 s56, 16768\n" \
  "buffer_load_dwordx4 v0, s[40:43], s56 offen lds\n" \
  "s_add_u32 m0, s48, 18432\n" \
  "s_mov_b32 s56, 33152\n" \
  "buffer_load_dwordx4 v0, s[40:43], s56 offen lds\n" \
  "s_add_u32 m0, s48, 19456\n" \
  "s_mov_b32 s56, 49536\n" \
  "buffer_load_dwordx4 v0, s[40:43], s56 offen lds\n" \
  "s_add_u32 m0, s48, 20480\n" \
  "s_mov_b32 s56, 65920\n" \
  "buffer_load_dwordx4 v0, s[40:43], s56 offen lds\n" \
  "s_add_u32 m0, s48, 21504\n" \
  "s_mov_b32 s56, 82304\n" \
  "buffer_load_dwordx4 v0, s[40:43], s56 offen lds\n" \
  "s_add_u32 m0, s48, 22528\n" \
  "s_mov_b32 s56, 98688\n" \
  "buffer_load_dwordx4 v0, s[40:43], s56 offen lds\n" \
  "s_add_u32 m0, s48, 23552\n" \
  "s_mov_b32 s56, 115072\n" \
  "buffer_load_dwordx4 v0, s[40:43], s56 offen lds\n" \
  "s_waitcnt vmcnt(25)\n" \
  "ds_read_b64_tr_b16 a[72:73], v1 offset:24576\n" \
  "ds_read_b64_tr_b16 a[74:75], v1 offset:26624\n" \
  "ds_read_b64_tr_b16 a[76:77], v1 offset:28672\n" \
  "ds_read_b64_tr_b16 a[78:79], v1 offset:30720\n" \
  "ds_read_b64_tr_b16 a[88:89], v2 offset:24576\n" \
  "ds_read_b64_tr_b16 a[90:91], v2 offset:26624\n" \
  "ds_read_b64_tr_b16 a[92:93], v2 offset:28672\n" \
  "ds_read_b64_tr_b16 a[94:95], v2 offset:30720\n" \
  "ds_read_b64_tr_b16 a[104:105], v3 offset:24576\n" \
  "ds_read_b64_tr_b16 a[106:107], v3 offset:26624\n" \
  "ds_read_b64_tr_b16 a[108:109], v3 offset:28672\n" \
  "ds_read_b64_tr_b16 a[110:111], v3 offset:30720\n" \
  "ds_read_b64_tr_b16 a[120:121], v4 offset:24576\n" \
  "ds_read_b64_tr_b16 a[122:123], v4 offset:26624\n" \
  "ds_read_b64_tr_b16 a[124:125], v4 offset:28672\n" \
  "ds_read_b64_tr_b16 a[126:127], v4 offset:30720\n" \
  "s_waitcnt lgkmcnt(0)\n" \
  "s_add_u32 m0, s48, 24576\n" \
  "s_mov_b32 s56, 131456\n" \
  "buffer_load_dwordx4 v0, s[40:43], s56 offen lds\n" \
  "s_add_u32 m0, s48, 25600\n" \
  "s_mov_b32 s56, 147840\n" \
  "buffer_load_dwordx4 v0, s[40:43], s56 offen lds\n" \
  "s_add_u32 m0, s48, 26624\n" \
  "s_mov_b32 s56, 164224\n" \
  "buffer_load_dwordx4 v0, s[40:43], s56 offen lds\n" \
  "s_add_u32 m0, s48, 27648\n" \
  "s_mov_b32 s56, 180608\n" \
  "buffer_load_dwordx4 v0, s[40:43], s56 offen lds\n" \
  "s_add_u32 m0, s48, 28672\n" \
  "s_mov_b32 s56, 196992\n" \
  "buffer_load_dwordx4 v0, s[40:43], s56 offen lds\n" \
  "s_add_u32 m0, s48, 29696\n" \
  "s_mov_b32 s56, 213376\n" \
  "buffer_load_dwordx4 v0, s[40:43], s56 offen lds\n" \
  "s_add_u32 m0, s48, 30720\n" \
  "s_mov_b32 s56, 229760\n" \
  "buffer_load_dwordx4 v0, s[40:43], s56 offen lds\n" \
  "s_add_u32 m0, s48, 31744\n" \
  "s_mov_b32 s56, 246144\n" \
  "buffer_load_dwordx4 v0, s[40:43], s56 offen lds\n" \
  "s_waitcnt vmcnt(25)\n" \
  "ds_read_b64_tr_b16 a[128:129], v1 offset:0\n" \
  "ds_read_b64_tr_b16 a[130:131], v1 offset:2048\n" \
  "ds_read_b64_tr_b16 a[132:133], v1 offset:4096\n" \
  "ds_read_b64_tr_b16 a[134:135], v1 offset:6144\n" \
  "ds_read_b64_tr_b16 a[144:145], v2 offset:0\n" \
  "ds_read_b64_tr_b16 a[146:147], v2 offset:2048\n" \
  "ds_read_b64_tr_b16 a[148:149], v2 offset:4096\n" \
  "ds_read_b64_tr_b16 a[150:151], v2 offset:6144\n" \
  "ds_read_b64_tr_b16 a[160:161], v3 offset:0\n" \
  "ds_read_b64_tr_b16 a[162:163], v3 offset:2048\n" \
  "ds_read_b64_tr_b16 a[164:165], v3 offset:4096\n" \
  "ds_read_b64_tr_b16 a[166:167], v3 offset:6144\n" \
  "ds_read_b64_tr_b16 a[176:177], v4 offset:0\n" \
  "ds_read_b64_tr_b16 a[178:179], v4 offset:2048\n" \
  "ds_read_b64_tr_b16 a[180:181], v4 offset:4096\n" \
  "ds_read_b64_tr_b16 a[182:183], v4 offset:6144\n" \
  "s_waitcnt lgkmcnt(0)\n" \
  "s_waitcnt vmcnt(17)\n" \
  "ds_read_b64_tr_b16 a[136:137], v1 offset:8192\n" \
  "ds_read_b64_tr_b16 a[138:139], v1 offset:10240\n" \
  "ds_read_b64_tr_b16 a[140:141], v1 offset:12288\n" \
  "ds_read_b64_tr_b16 a[142:143], v1 offset:14336\n" \
  "ds_read_b64_tr_b16 a[152:153], v2 offset:8192\n" \
  "ds_read_b64_tr_b16 a[154:155], v2 offset:10240\n" \
  "ds_read_b64_tr_b16 a[156:157], v2 offset:12288\n" \
  "ds_read_b64_tr_b16 a[158:159], v2 offset:14336\n" \
  "ds_read_b64_tr_b16 a[168:169], v3 offset:8192\n" \
  "ds_read_b64_tr_b16 a[170:171], v3 offset:10240\n" \
  "ds_read_b64_tr_b16 a[172:173], v3 offset:12288\n" \
  "ds_read_b64_tr_b16 a[174:175], v3 offset:14336\n" \
  "ds_read_b64_tr_b16 a[184:185], v4 offset:8192\n" \
  "ds_read_b64_tr_b16 a[186:187], v4 offset:10240\n" \
  "ds_read_b64_tr_b16 a[188:189], v4 offset:12288\n" \
  "ds_read_b64_tr_b16 a[190:191], v4 offset:14336\n" \
  "s_waitcnt lgkmcnt(0)\n" \
  "s_waitcnt vmcnt(16)\n" \
  "s_bitcmp1_b32 s59, 1\n" \
  "s_cbranch_scc1 SU_yd_%=\n" \
  "SU_yf_%=:\n" \
  "v_cmp_gt_i32 vcc, 0, v14\n" \
  "s_bcnt1_i32_b64 s57, vcc\n" \
  "v_and_b32 v14, 0x10001, v14\n" \
  "v_cmp_ne_u32 vcc, 0x10001, v14\n" \
  "s_cbranch_vccnz SU_yt_%=\n" \
  "SU_yg_%=:\n" \
  "s_lshr_b32 s56, s59, 17\n" \
  "s_and_b32 s56, s56, 0xf80\n" \
  "s_add_u32 s56, s56, 252186624\n" \
  "v_mov_b32 v14, 1\n" \
  "buffer_atomic_add v14, v9, s[36:39], s56 offen\n" \
  "s_cmp_gt_u32 s57, 32\n" \
  "s_cselect_b32 s56, 0x200000, 0\n" \
  "s_or_b32 s59, s59, s56\n" \
  "s_or_b32 s59, s59, 2\n" \
  "s_add_u32 s36, s36, s56\n" \
  "s_addc_u32 s37, s37, 0\n" \
  "SU_yd_%=:\n" \
  "buffer_load_dword v8, v6, s[36:39], 0 offen sc1\n" \
  "s_waitcnt vmcnt(9)\n" \
  "ds_read_b64_tr_b16 a[192:193], v1 offset:16384\n" \
  "ds_read_b64_tr_b16 a[194:195], v1 offset:18432\n" \
  "ds_read_b64_tr_b16 a[196:197], v1 offset:20480\n" \
  "ds_read_b64_tr_b16 a[198:199], v1 offset:22528\n" \
  "ds_read_b64_tr_b16 a[208:209], v2 offset:16384\n" \
  "ds_read_b64_tr_b16 a[210:211], v2 offset:18432\n" \
  "ds_read_b64_tr_b16 a[212:213], v2 offset:20480\n" \
  "ds_read_b64_tr_b16 a[214:215], v2 offset:22528\n" \
  "ds_read_b64_tr_b16 a[224:225], v3 offset:16384\n" \
  "ds_read_b64_tr_b16 a[226:227], v3 offset:18432\n" \
  "ds_read_b64_tr_b16 a[228:229], v3 offset:20480\n" \
  "ds_read_b64_tr_b16 a[230:231], v3 offset:22528\n" \
  "ds_read_b64_tr_b16 a[240:241], v4 offset:16384\n" \
  "ds_read_b64_tr_b16 a[242:243], v4 offset:18432\n" \
  "ds_read_b64_tr_b16 a[244:245], v4 offset:20480\n" \
  "ds_read_b64_tr_b16 a[246:247], v4 offset:22528\n" \
  "s_waitcnt lgkmcnt(0)\n" \
  "s_waitcnt vmcnt(1)\n" \
  "ds_read_b64_tr_b16 a[200:201], v1 offset:24576\n" \
  "ds_read_b64_tr_b16 a[202:203], v1 offset:26624\n" \
  "ds_read_b64_tr_b16 a[204:205], v1 offset:28672\n" \
  "ds_read_b64_tr_b16 a[206:207], v1 offset:30720\n" \
  "ds_read_b64_tr_b16 a[216:217], v2 offset:24576\n" \
  "ds_read_b64_tr_b16 a[218:219], v2 offset:26624\n" \
  "ds_read_b64_tr_b16 a[220:221], v2 offset:28672\n" \
  "ds_read_b64_tr_b16 a[222:223], v2 offset:30720\n" \
  "ds_read_b64_tr_b16 a[232:233], v3 offset:24576\n" \
  "ds_read_b64_tr_b16 a[234:235], v3 offset:26624\n" \
  "ds_read_b64_tr_b16 a[236:237], v3 offset:28672\n" \
  "ds_read_b64_tr_b16 a[238:239], v3 offset:30720\n" \
  "ds_read_b64_tr_b16 a[248:249], v4 offset:24576\n" \
  "ds_read_b64_tr_b16 a[250:251], v4 offset:26624\n" \
  "ds_read_b64_tr_b16 a[252:253], v4 offset:28672\n" \
  "ds_read_b64_tr_b16 a[254:255], v4 offset:30720\n" \
  "s_waitcnt lgkmcnt(0)\n" \
  "s_add_u32 s57, s52, 1\n" \
  "s_min_u32 s57, s57, s54\n" \
  "v_writelane_b32 v13, s57, 0\n" \
  "s_cmp_ge_u32 s52, 24\n" \
  "s_cselect_b32 s56, 32, 0\n" \
  "s_cmp_lt_u32 s52, s54\n" \
  "s_cselect_b32 s56, s56, 0\n" \
  "v_writelane_b32 v13, s56, 1\n" \
  "s_lshr_b32 s56, s49, 8\n" \
  "s_and_b32 s56, s56, 0x2000\n" \
  "s_xor_b32 s56, s56, s49\n" \
  "s_add_u32 s57, s56, 4096\n" \
  "s_waitcnt vmcnt(0)\n" \
  "v_cmp_gt_u32 vcc, v13, v8\n" \
  "s_cbranch_vccnz SU_r0_%=\n" \
  "SU_k0_%=:\n" \
  "v_add_u32 v6, v6, v7\n" \
  "buffer_load_dwordx4 v[48:51], v5, s[36:39], s56 offen sc1\n" \
  "buffer_load_dwordx4 v[52:55], v5, s[36:39], s56 offen offset:1024 sc1\n" \
  "buffer_load_dwordx4 v[56:59], v5, s[36:39], s56 offen offset:2048 sc1\n" \
  "buffer_load_dwordx4 v[60:63], v5, s[36:39], s56 offen offset:3072 sc1\n" \
  "buffer_load_dwordx4 v[64:67], v5, s[36:39], s57 offen sc1\n" \
  "buffer_load_dwordx4 v[68:71], v5, s[36:39], s57 offen offset:1024 sc1\n" \
  "buffer_load_dwordx4 v[72:75], v5, s[36:39], s57 offen offset:2048 sc1\n" \
  "buffer_load_dwordx4 v[76:79], v5, s[36:39], s57 offen offset:3072 sc1\n" \
  "buffer_load_dword v8, v6, s[36:39], 0 offen sc1\n" \
  "s_add_u32 s49, s49, 0x80000\n" \
  "s_add_u32 s52, s52, 1\n" \
  "s_add_u32 s57, s52, 1\n" \
  "s_min_u32 s57, s57, s54\n" \
  "v_writelane_b32 v13, s57, 0\n" \
  "s_cmp_ge_u32 s52, 24\n" \
  "s_cselect_b32 s56, 32, 0\n" \
  "s_cmp_lt_u32 s52, s54\n" \
  "s_cselect_b32 s56, s56, 0\n" \
  "v_writelane_b32 v13, s56, 1\n" \
  "s_lshr_b32 s56, s49, 8\n" \
  "s_and_b32 s56, s56, 0x2000\n" \
  "s_xor_b32 s56, s56, s49\n" \
  "s_add_u32 s57, s56, 4096\n" \
  "s_waitcnt vmcnt(0)\n" \
  "v_mfma_f32_16x16x32_bf16 v[80:83], v[48:51], a[0:3], 0\n" \
  "v_mfma_f32_16x16x32_bf16 v[84:87], v[52:55], a[0:3], 0\n" \
  "v_mfma_f32_16x16x32_bf16 v[80:83], v[56:59], a[4:7], v[80:83]\n" \
  "v_mfma_f32_16x16x32_bf16 v[84:87], v[60:63], a[4:7], v[84:87]\n" \
  "v_mfma_f32_16x16x32_bf16 v[80:83], v[64:67], a[8:11], v[80:83]\n" \
  "v_mfma_f32_16x16x32_bf16 v[84:87], v[68:71], a[8:11], v[84:87]\n" \
  "v_mfma_f32_16x16x32_bf16 v[80:83], v[72:75], a[12:15], v[80:83]\n" \
  "v_mfma_f32_16x16x32_bf16 v[84:87], v[76:79], a[12:15], v[84:87]\n" \
  "v_mfma_f32_16x16x32_bf16 v[88:91], v[48:51], a[16:19], 0\n" \
  "v_mfma_f32_16x16x32_bf16 v[92:95], v[52:55], a[16:19], 0\n" \
  "v_mfma_f32_16x16x32_bf16 v[88:91], v[56:59], a[20:23], v[88:91]\n" \
  "v_mfma_f32_16x16x32_bf16 v[92:95], v[60:63], a[20:23], v[92:95]\n" \
  "v_mfma_f32_16x16x32_bf16 v[88:91], v[64:67], a[24:27], v[88:91]\n" \
  "s_nop 1\n" \
  "v_cvt_pk_bf16_f32 v80, v80, v81\n" \
  "v_cvt_pk_bf16_f32 v81, v82, v83\n" \
  "v_mfma_f32_16x16x32_bf16 v[92:95], v[68:71], a[24:27], v[92:95]\n" \
  "v_cvt_pk_bf16_f32 v82, v84, v85\n" \
  "v_cvt_pk_bf16_f32 v83, v86, v87\n" \
  "v_mfma_f32_16x16x32_bf16 v[88:91], v[72:75], a[28:31], v[88:91]\n" \
  "buffer_store_dwordx4 v[80:83], v5, s[36:39], s50 offen sc1\n" \
  "s_add_u32 s50, s50, 65536\n" \
  "v_mfma_f32_16x16x32_bf16 v[92:95], v[76:79], a[28:31], v[92:95]\n" \
  "v_mfma_f32_16x16x32_bf16 v[96:99], v[48:51], a[32:35], 0\n" \
  "v_mfma_f32_16x16x32_bf16 v[100:103], v[52:55], a[32:35], 0\n" \
  "v_mfma_f32_16x16x32_bf16 v[96:99], v[56:59], a[36:39], v[96:99]\n" \
  "v_mfma_f32_16x16x32_bf16 v[100:103], v[60:63], a[36:39], v[100:103]\n" \
  "v_mfma_f32_16x16x32_bf16 v[96:99], v[64:67], a[40:43], v[96:99]\n" \
  "v_cvt_pk_bf16_f32 v88, v88, v89\n" \
  "v_cvt_pk_bf16_f32 v89, v90, v91\n" \
  "v_mfma_f32_16x16x32_bf16 v[100:103], v[68:71], a[40:43], v[100:103]\n" \
  "v_cvt_pk_bf16_f32 v90, v92, v93\n" \
  "v_cvt_pk_bf16_f32 v91, v94, v95\n" \
  "v_mfma_f32_16x16x32_bf16 v[96:99], v[72:75], a[44:47], v[96:99]\n" \
  "buffer_store_dwordx4 v[88:91], v5, s[36:39], s50 offen sc1\n" \
  "s_add_u32 s50, s50, 65536\n" \
  "v_mfma_f32_16x16x32_bf16 v[100:103], v[76:79], a[44:47], v[100:103]\n" \
  "v_mfma_f32_16x16x32_bf16 v[104:107], v[48:51], a[48:51], 0\n" \
  "v_mfma_f32_16x16x32_bf16 v[108:111], v[52:55], a[48:51], 0\n" \
  "v_mfma_f32_16x16x32_bf16 v[104:107], v[56:59], a[52:55], v[104:107]\n" \
  "v_mfma_f32_16x16x32_bf16 v[108:111], v[60:63], a[52:55], v[108:111]\n" \
  "v_mfma_f32_16x16x32_bf16 v[104:107], v[64:67], a[56:59], v[104:107]\n" \
  "v_cvt_pk_bf16_f32 v96, v96, v97\n" \
  "v_cvt_pk_bf16_f32 v97, v98, v99\n" \
  "v_mfma_f32_16x16x32_bf16 v[108:111], v[68:71], a[56:59], v[108:111]\n" \
  "v_cvt_pk_bf16_f32 v98, v100, v101\n" \
  "v_cvt_pk_bf16_f32 v99, v102, v103\n" \
  "v_mfma_f32_16x16x32_bf16 v[104:107], v[72:75], a[60:63], v[104:107]\n" \
  "buffer_store_dwordx4 v[96:99], v5, s[36:39], s50 offen sc1\n" \
  "s_add_u32 s50, s50, 65536\n" \
  "v_mfma_f32_16x16x32_bf16 v[108:111], v[76:79], a[60:63], v[108:111]\n" \
  "v_mfma_f32_16x16x32_bf16 v[80:83], v[48:51], a[64:67], 0\n" \
  "v_mfma_f32_16x16x32_bf16 v[84:87], v[52:55], a[64:67], 0\n" \
  "v_mfma_f32_16x16x32_bf16 v[80:83], v[56:59], a[68:71], v[80:83]\n" \
  "v_mfma_f32_16x16x32_bf16 v[84:87], v[60:63], a[68:71], v[84:87]\n" \
  "v_mfma_f32_16x16x32_bf16 v[80:83], v[64:67], a[72:75], v[80:83]\n" \
  "v_cvt_pk_bf16_f32 v104, v104, v105\n" \
  "v_cvt_pk_bf16_f32 v105, v106, v107\n" \
  "v_mfma_f32_16x16x32_bf16 v[84:87], v[68:71], a[72:75], v[84:87]\n" \
  "v_cvt_pk_bf16_f32 v106, v108, v109\n" \
  "v_cvt_pk_bf16_f32 v107, v110, v111\n" \
  "v_mfma_f32_16x16x32_bf16 v[80:83], v[72:75], a[76:79], v[80:83]\n" \
  "buffer_store_dwordx4 v[104:107], v5, s[36:39], s50 offen sc1\n" \
  "s_add_u32 s50, s50, 65536\n" \
  "v_mfma_f32_16x16x32_bf16 v[84:87], v[76:79], a[76:79], v[84:87]\n" \
  "v_mfma_f32_16x16x32_bf16 v[88:91], v[48:51], a[80:83], 0\n" \
  "v_mfma_f32_16x16x32_bf16 v[92:95], v[52:55], a[80:83], 0\n" \
  "v_mfma_f32_16x16x32_bf16 v[88:91], v[56:59], a[84:87], v[88:91]\n" \
  "v_mfma_f32_16x16x32_bf16 v[92:95], v[60:63], a[84:87], v[92:95]\n" \
  "v_mfma_f32_16x16x32_bf16 v[88:91], v[64:67], a[88:91], v[88:91]\n" \
  "v_cvt_pk_bf16_f32 v80, v80, v81\n" \
  "v_cvt_pk_bf16_f32 v81, v82, v83\n" \
  "v_mfma_f32_16x16x32_bf16 v[92:95], v[68:71], a[88:91], v[92:95]\n" \
  "v_cvt_pk_bf16_f32 v82, v84, v85\n" \
  "v_cvt_pk_bf16_f32 v83, v86, v87\n" \
  "v_mfma_f32_16x16x32_bf16 v[88:91], v[72:75], a[92:95], v[88:91]\n" \
  "buffer_store_dwordx4 v[80:83], v5, s[36:39], s50 offen sc1\n" \
  "s_add_u32 s50, s50, 65536\n" \
  "v_mfma_f32_16x16x32_bf16 v[92:95], v[76:79], a[92:95], v[92:95]\n" \
  "v_mfma_f32_16x16x32_bf16 v[96:99], v[48:51], a[96:99], 0\n" \
  "v_mfma_f32_16x16x32_bf16 v[100:103], v[52:55], a[96:99], 0\n" \
  "v_mfma_f32_16x16x32_bf16 v[96:99], v[56:59], a[100:103], v[96:99]\n" \
  "v_mfma_f32_16x16x32_bf16 v[100:103], v[60:63], a[100:103], v[100:103]\n" \
  "v_mfma_f32_16x16x32_bf16 v[96:99], v[64:67], a[104:107], v[96:99]\n" \
  "v_cvt_pk_bf16_f32 v88, v88, v89\n" \
  "v_cvt_pk_bf16_f32 v89, v90, v91\n" \
  "v_mfma_f32_16x16x32_bf16 v[100:103], v[68:71], a[104:107], v[100:103]\n" \
  "v_cvt_pk_bf16_f32 v90, v92, v93\n" \
  "v_cvt_pk_bf16_f32 v91, v94, v95\n" \
  "v_mfma_f32_16x16x32_bf16 v[96:99], v[72:75], a[108:111], v[96:99]\n" \
  "buffer_store_dwordx4 v[88:91], v5, s[36:39], s50 offen sc1\n" \
  "s_add_u32 s50, s50, 65536\n" \
  "v_mfma_f32_16x16x32_bf16 v[100:103], v[76:79], a[108:111], v[100:103]\n" \
  "v_mfma_f32_16x16x32_bf16 v[104:107], v[48:51], a[112:115], 0\n" \
  "v_mfma_f32_16x16x32_bf16 v[108:111], v[52:55], a[112:115], 0\n" \
  "buffer_load_dword v8, v6, s[36:39], 0 offen sc1\n" \
  "v_mfma_f32_16x16x32_bf16 v[104:107], v[56:59], a[116:119], v[104:107]\n" \
  "v_mfma_f32_16x16x32_bf16 v[108:111], v[60:63], a[116:119], v[108:111]\n" \
  "v_mfma_f32_16x16x32_bf16 v[104:107], v[64:67], a[120:123], v[104:107]\n" \
  "v_cvt_pk_bf16_f32 v96, v96, v97\n" \
  "v_cvt_pk_bf16_f32 v97, v98, v99\n" \
  "v_mfma_f32_16x16x32_bf16 v[108:111], v[68:71], a[120:123], v[108:111]\n" \
  "v_cvt_pk_bf16_f32 v98, v100, v101\n" \
  "v_cvt_pk_bf16_f32 v99, v102, v103\n" \
  "v_mfma_f32_16x16x32_bf16 v[104:107], v[72:75], a[124:127], v[104:107]\n" \
  "buffer_store_dwordx4 v[96:99], v5, s[36:39], s50 offen sc1\n" \
  "s_add_u32 s50, s50, 65536\n" \
  "v_mfma_f32_16x16x32_bf16 v[108:111], v[76:79], a[124:127], v[108:111]\n" \
  "v_mfma_f32_16x16x32_bf16 v[80:83], v[48:51], a[128:131], 0\n" \
  "v_mfma_f32_16x16x32_bf16 v[84:87], v[52:55], a[128:131], 0\n" \
  "v_mfma_f32_16x16x32_bf16 v[80:83], v[56:59], a[132:135], v[80:83]\n" \
  "v_mfma_f32_16x16x32_bf16 v[84:87], v[60:63], a[132:135], v[84:87]\n" \
  "v_mfma_f32_16x16x32_bf16 v[80:83], v[64:67], a[136:139], v[80:83]\n" \
  "v_cvt_pk_bf16_f32 v104, v104, v105\n" \
  "v_cvt_pk_bf16_f32 v105, v106, v107\n" \
  "v_mfma_f32_16x16x32_bf16 v[84:87], v[68:71], a[136:139], v[84:87]\n" \
  "v_cvt_pk_bf16_f32 v106, v108, v109\n" \
  "v_cvt_pk_bf16_f32 v107, v110, v111\n" \
  "v_mfma_f32_16x16x32_bf16 v[80:83], v[72:75], a[140:143], v[80:83]\n" \
  "buffer_store_dwordx4 v[104:107], v5, s[36:39], s50 offen sc1\n" \
  "s_add_u32 s50, s50, 65536\n" \
  "v_mfma_f32_16x16x32_bf16 v[84:87], v[76:79], a[140:143], v[84:87]\n" \
  "v_mfma_f32_16x16x32_bf16 v[88:91], v[48:51], a[144:147], 0\n" \
  "v_mfma_f32_16x16x32_bf16 v[92:95], v[52:55], a[144:147], 0\n" \
  "v_mfma_f32_16x16x32_bf16 v[88:91], v[56:59], a[148:151], v[88:91]\n" \
  "v_mfma_f32_16x16x32_bf16 v[92:95], v[60:63], a[148:151], v[92:95]\n" \
  "v_mfma_f32_16x16x32_bf16 v[88:91], v[64:67], a[152:155], v[88:91]\n" \
  "v_cvt_pk_bf16_f32 v80, v80, v81\n" \
  "v_cvt_pk_bf16_f32 v81, v82, v83\n" \
  "v_mfma_f32_16x16x32_bf16 v[92:95], v[68:71], a[152:155], v[92:95]\n" \
  "v_cvt_pk_bf16_f32 v82, v84, v85\n" \
  "v_cvt_pk_bf16_f32 v83, v86, v87\n" \
  "v_mfma_f32_16x16x32_bf16 v[88:91], v[72:75], a[156:159], v[88:91]\n" \
  "buffer_store_dwordx4 v[80:83], v5, s[36:39], s50 offen sc1\n" \
  "s_add_u32 s50, s50, 65536\n" \
  "v_mfma_f32_16x16x32_bf16 v[92:95], v[76:79], a[156:159], v[92:95]\n" \
  "v_mfma_f32_16x16x32_bf16 v[96:99], v[48:51], a[160:163], 0\n" \
  "v_mfma_f32_16x16x32_bf16 v[100:103], v[52:55], a[160:163], 0\n" \
  "v_mfma_f32_16x16x32_bf16 v[96:99], v[56:59], a[164:167], v[96:99]\n" \
  "v_mfma_f32_16x16x32_bf16 v[100:103], v[60:63], a[164:167], v[100:103]\n" \
  "v_mfma_f32_16x16x32_bf16 v[96:99], v[64:67], a[168:171], v[96:99]\n" \
  "v_cvt_pk_bf16_f32 v88, v88, v89\n" \
  "v_cvt_pk_bf16_f32 v89, v90, v91\n" \
  "v_mfma_f32_16x16x32_bf16 v[100:103], v[68:71], a[168:171], v[100:103]\n" \
  "v_cvt_pk_bf16_f32 v90, v92, v93\n" \
  "v_cvt_pk_bf16_f32 v91, v94, v95\n" \
  "v_mfma_f32_16x16x32_bf16 v[96:99], v[72:75], a[172:175], v[96:99]\n" \
  "buffer_store_dwordx4 v[88:91], v5, s[36:39], s50 offen sc1\n" \
  "s_add_u32 s50, s50, 65536\n" \
  "v_mfma_f32_16x16x32_bf16 v[100:103], v[76:79], a[172:175], v[100:103]\n" \
  "v_mfma_f32_16x16x32_bf16 v[104:107], v[48:51], a[176:179], 0\n" \
  "v_mfma_f32_16x16x32_bf16 v[108:111], v[52:55], a[176:179], 0\n" \
  "v_mfma_f32_16x16x32_bf16 v[104:107], v[56:59], a[180:183], v[104:107]\n" \
  "v_mfma_f32_16x16x32_bf16 v[108:111], v[60:63], a[180:183], v[108:111]\n" \
  "v_mfma_f32_16x16x32_bf16 v[104:107], v[64:67], a[184:187], v[104:107]\n" \
  "v_cvt_pk_bf16_f32 v96, v96, v97\n" \
  "v_cvt_pk_bf16_f32 v97, v98, v99\n" \
  "v_mfma_f32_16x16x32_bf16 v[108:111], v[68:71], a[184:187], v[108:111]\n" \
  "v_cvt_pk_bf16_f32 v98, v100, v101\n" \
  "v_cvt_pk_bf16_f32 v99, v102, v103\n" \
  "v_mfma_f32_16x16x32_bf16 v[104:107], v[72:75], a[188:191], v[104:107]\n" \
  "buffer_store_dwordx4 v[96:99], v5, s[36:39], s50 offen sc1\n" \
  "s_add_u32 s50, s50, 65536\n" \
  "v_mfma_f32_16x16x32_bf16 v[108:111], v[76:79], a[188:191], v[108:111]\n" \
  "v_mfma_f32_16x16x32_bf16 v[80:83], v[48:51], a[192:195], 0\n" \
  "v_mfma_f32_16x16x32_bf16 v[84:87], v[52:55], a[192:195], 0\n" \
  "v_mfma_f32_16x16x32_bf16 v[80:83], v[56:59], a[196:199], v[80:83]\n" \
  "v_mfma_f32_16x16x32_bf16 v[84:87], v[60:63], a[196:199], v[84:87]\n" \
  "v_mfma_f32_16x16x32_bf16 v[80:83], v[64:67], a[200:203], v[80:83]\n" \
  "v_cvt_pk_bf16_f32 v104, v104, v105\n" \
  "v_cvt_pk_bf16_f32 v105, v106, v107\n" \
  "v_mfma_f32_16x16x32_bf16 v[84:87], v[68:71], a[200:203], v[84:87]\n" \
  "v_cvt_pk_bf16_f32 v106, v108, v109\n" \
  "v_cvt_pk_bf16_f32 v107, v110, v111\n" \
  "v_mfma_f32_16x16x32_bf16 v[80:83], v[72:75], a[204:207], v[80:83]\n" \
  "buffer_store_dwordx4 v[104:107], v5, s[36:39], s50 offen sc1\n" \
  "s_add_u32 s50, s50, 65536\n" \
  "v_mfma_f32_16x16x32_bf16 v[84:87], v[76:79], a[204:207], v[84:87]\n" \
  "v_mfma_f32_16x16x32_bf16 v[88:91], v[48:51], a[208:211], 0\n" \
  "v_mfma_f32_16x16x32_bf16 v[92:95], v[52:55], a[208:211], 0\n" \
  "v_mfma_f32_16x16x32_bf16 v[88:91], v[56:59], a[212:215], v[88:91]\n" \
  "v_mfma_f32_16x16x32_bf16 v[92:95], v[60:63], a[212:215], v[92:95]\n" \
  "v_mfma_f32_16x16x32_bf16 v[88:91], v[64:67], a[216:219], v[88:91]\n" \
  "v_cvt_pk_bf16_f32 v80, v80, v81\n" \
  "v_cvt_pk_bf16_f32 v81, v82, v83\n" \
  "v_mfma_f32_16x16x32_bf16 v[92:95], v[68:71], a[216:219], v[92:95]\n" \
  "v_cvt_pk_bf16_f32 v82, v84, v85\n" \
  "v_cvt_pk_bf16_f32 v83, v86, v87\n" \
  "v_mfma_f32_16x16x32_bf16 v[88:91], v[72:75], a[220:223], v[88:91]\n" \
  "buffer_store_dwordx4 v[80:83], v5, s[36:39], s50 offen sc1\n" \
  "s_add_u32 s50, s50, 65536\n" \
  "v_mfma_f32_16x16x32_bf16 v[92:95], v[76:79], a[220:223], v[92:95]\n" \
  "v_mfma_f32_16x16x32_bf16 v[96:99], v[48:51], a[224:227], 0\n" \
  "v_mfma_f32_16x16x32_bf16 v[100:103], v[52:55], a[224:227], 0\n" \
  "v_mfma_f32_16x16x32_bf16 v[96:99], v[56:59], a[228:231], v[96:99]\n" \
  "v_mfma_f32_16x16x32_bf16 v[100:103], v[60:63], a[228:231], v[100:103]\n" \
  "v_mfma_f32_16x16x32_bf16 v[96:99], v[64:67], a[232:235], v[96:99]\n" \
  "v_cvt_pk_bf16_f32 v88, v88, v89\n" \
  "v_cvt_pk_bf16_f32 v89, v90, v91\n" \
  "v_mfma_f32_16x16x32_bf16 v[100:103], v[68:71], a[232:235], v[100:103]\n" \
  "v_cvt_pk_bf16_f32 v90, v92, v93\n" \
  "v_cvt_pk_bf16_f32 v91, v94, v95\n" \
  "v_mfma_f32_16x16x32_bf16 v[96:99], v[72:75], a[236:239], v[96:99]\n" \
  "buffer_store_dwordx4 v[88:91], v5, s[36:39], s50 offen sc1\n" \
  "s_add_u32 s50, s50, 65536\n" \
  "v_mfma_f32_16x16x32_bf16 v[100:103], v[76:79], a[236:239], v[100:103]\n" \
  "v_mfma_f32_16x16x32_bf16 v[104:107], v[48:51], a[240:243], 0\n" \
  "v_mfma_f32_16x16x32_bf16 v[108:111], v[52:55], a[240:243], 0\n" \
  "v_mfma_f32_16x16x32_bf16 v[104:107], v[56:59], a[244:247], v[104:107]\n" \
  "v_mfma_f32_16x16x32_bf16 v[108:111], v[60:63], a[244:247], v[108:111]\n" \
  "v_mfma_f32_16x16x32_bf16 v[104:107], v[64:67], a[248:251], v[104:107]\n" \
  "v_cvt_pk_bf16_f32 v96, v96, v97\n" \
  "v_cvt_pk_bf16_f32 v97, v98, v99\n" \
  "v_mfma_f32_16x16x32_bf16 v[108:111], v[68:71], a[248:251], v[108:111]\n" \
  "v_cvt_pk_bf16_f32 v98, v100, v101\n" \
  "v_cvt_pk_bf16_f32 v99, v102, v103\n" \
  "v_mfma_f32_16x16x32_bf16 v[104:107], v[72:75], a[252:255], v[104:107]\n" \
  "buffer_store_dwordx4 v[96:99], v5, s[36:39], s50 offen sc1\n" \
  "s_add_u32 s50, s50, 65536\n" \
  "v_mfma_f32_16x16x32_bf16 v[108:111], v[76:79], a[252:255], v[108:111]\n" \
  "s_nop 4\n" \
  "v_cvt_pk_bf16_f32 v104, v104, v105\n" \
  "v_cvt_pk_bf16_f32 v105, v106, v107\n" \
  "s_nop 0\n" \
  "v_cvt_pk_bf16_f32 v106, v108, v109\n" \
  "v_cvt_pk_bf16_f32 v107, v110, v111\n" \
  "buffer_store_dwordx4 v[104:107], v5, s[36:39], s50 offen sc1\n" \
  "s_waitcnt vmcnt(10)\n" \
  "v_cmp_gt_u32 vcc, v13, v8\n" \
  "s_cbranch_vccnz SU_f1_%=\n" \
  "v_add_u32 v6, v6, v7\n" \
  "buffer_load_dwordx4 v[16:19], v5, s[36:39], s56 offen sc1\n" \
  "buffer_load_dwordx4 v[20:23], v5, s[36:39], s56 offen offset:1024 sc1\n" \
  "buffer_load_dwordx4 v[24:27], v5, s[36:39], s56 offen offset:2048 sc1\n" \
  "buffer_load_dwordx4 v[28:31], v5, s[36:39], s56 offen offset:3072 sc1\n" \
  "buffer_load_dwordx4 v[32:35], v5, s[36:39], s57 offen sc1\n" \
  "buffer_load_dwordx4 v[36:39], v5, s[36:39], s57 offen offset:1024 sc1\n" \
  "buffer_load_dwordx4 v[40:43], v5, s[36:39], s57 offen offset:2048 sc1\n" \
  "buffer_load_dwordx4 v[44:47], v5, s[36:39], s57 offen offset:3072 sc1\n" \
  "buffer_load_dword v8, v6, s[36:39], 0 offen sc1\n" \
  "s_add_u32 s49, s49, 0x80000\n" \
  "s_add_u32 s52, s52, 1\n" \
  "s_add_u32 s57, s52, 1\n" \
  "s_min_u32 s57, s57, s54\n" \
  "v_writelane_b32 v13, s57, 0\n" \
  "s_cmp_ge_u32 s52, 24\n" \
  "s_cselect_b32 s56, 32, 0\n" \
  "s_cmp_lt_u32 s52, s54\n" \
  "s_cselect_b32 s56, s56, 0\n" \
  "v_writelane_b32 v13, s56, 1\n" \
  "s_lshr_b32 s56, s49, 8\n" \
  "s_and_b32 s56, s56, 0x2000\n" \
  "s_xor_b32 s56, s56, s49\n" \
  "s_add_u32 s57, s56, 4096\n" \
  "s_waitcnt vmcnt(0)\n" \
  "s_barrier\n" \
  "buffer_atomic_add v10, v9, s[36:39], s53 offen\n" \
  "s_branch SU_top_%=\n" \
  "SU_f1_%=:\n" \
  "buffer_load_dword v8, v6, s[36:39], 0 offen sc1\n" \
  "s_waitcnt vmcnt(0)\n" \
  "s_barrier\n" \
  "buffer_atomic_add v10, v9, s[36:39], s53 offen\n" \
  "v_cmp_gt_u32 vcc, v13, v8\n" \
  "s_cbranch_vccnz SU_r1_%=\n" \
  "SU_k1_%=:\n" \
  "v_add_u32 v6, v6, v7\n" \
  "buffer_load_dwordx4 v[16:19], v5, s[36:39], s56 offen sc1\n" \
  "buffer_load_dwordx4 v[20:23], v5, s[36:39], s56 offen offset:1024 sc1\n" \
  "buffer_load_dwordx4 v[24:27], v5, s[36:39], s56 offen offset:2048 sc1\n" \
  "buffer_load_dwordx4 v[28:31], v5, s[36:39], s56 offen offset:3072 sc1\n" \
  "buffer_load_dwordx4 v[32:35], v5, s[36:39], s57 offen sc1\n" \
  "buffer_load_dwordx4 v[36:39], v5, s[36:39], s57 offen offset:1024 sc1\n" \
  "buffer_load_dwordx4 v[40:43], v5, s[36:39], s57 offen offset:2048 sc1\n" \
  "buffer_load_dwordx4 v[44:47], v5, s[36:39], s57 offen offset:3072 sc1\n" \
  "buffer_load_dword v8, v6, s[36:39], 0 offen sc1\n" \
  "s_add_u32 s49, s49, 0x80000\n" \
  "s_add_u32 s52, s52, 1\n" \
  "s_add_u32 s57, s52, 1\n" \
  "s_min_u32 s57, s57, s54\n" \
  "v_writelane_b32 v13, s57, 0\n" \
  "s_cmp_ge_u32 s52, 24\n" \
  "s_cselect_b32 s56, 32, 0\n" \
  "s_cmp_lt_u32 s52, s54\n" \
  "s_cselect_b32 s56, s56, 0\n" \
  "v_writelane_b32 v13, s56, 1\n" \
  "s_lshr_b32 s56, s49, 8\n" \
  "s_and_b32 s56, s56, 0x2000\n" \
  "s_xor_b32 s56, s56, s49\n" \
  "s_add_u32 s57, s56, 4096\n" \
  "s_waitcnt vmcnt(0)\n" \
  ".p2align 4\n" \
  "SU_top_%=:\n" \
  "v_mfma_f32_16x16x32_bf16 v[80:83], v[16:19], a[0:3], 0\n" \
  "v_mfma_f32_16x16x32_bf16 v[84:87], v[20:23], a[0:3], 0\n" \
  "v_mfma_f32_16x16x32_bf16 v[80:83], v[24:27], a[4:7], v[80:83]\n" \
  "v_mfma_f32_16x16x32_bf16 v[84:87], v[28:31], a[4:7], v[84:87]\n" \
  "v_mfma_f32_16x16x32_bf16 v[80:83], v[32:35], a[8:11], v[80:83]\n" \
  "v_cvt_pk_bf16_f32 v104, v104, v105\n" \
  "v_cvt_pk_bf16_f32 v105, v106, v107\n" \
  "v_mfma_f32_16x16x32_bf16 v[84:87], v[36:39], a[8:11], v[84:87]\n" \
  "v_cvt_pk_bf16_f32 v106, v108, v109\n" \
  "v_cvt_pk_bf16_f32 v107, v110, v111\n" \
  "v_mfma_f32_16x16x32_bf16 v[80:83], v[40:43], a[12:15], v[80:83]\n" \
  "buffer_store_dwordx4 v[104:107], v11, s[36:39], s50 offen sc1\n" \
  "s_add_u32 s50, s50, 3211264\n" \
  "v_mfma_f32_16x16x32_bf16 v[84:87], v[44:47], a[12:15], v[84:87]\n" \
  "s_cmp_eq_u32 s50, s51\n" \
  "s_cselect_b32 s58, 100663296, 0\n" \
  "v_mfma_f32_16x16x32_bf16 v[88:91], v[16:19], a[16:19], 0\n" \
  "s_sub_u32 s50, s50, s58\n" \
  "v_mov_b32 v11, v5\n" \
  "v_mfma_f32_16x16x32_bf16 v[92:95], v[20:23], a[16:19], 0\n" \
  "v_cmp_gt_u32 vcc, v13, v8\n" \
  "s_cbranch_vccnz SU_r2_%=\n" \
  "SU_k2_%=:\n" \
  "v_mfma_f32_16x16x32_bf16 v[88:91], v[24:27], a[20:23], v[88:91]\n" \
  "v_add_u32 v6, v6, v7\n" \
  "buffer_load_dwordx4 v[48:51], v5, s[36:39], s56 offen sc1\n" \
  "v_mfma_f32_16x16x32_bf16 v[92:95], v[28:31], a[20:23], v[92:95]\n" \
  "buffer_load_dwordx4 v[52:55], v5, s[36:39], s56 offen offset:1024 sc1\n" \
  "buffer_load_dwordx4 v[56:59], v5, s[36:39], s56 offen offset:2048 sc1\n" \
  "v_mfma_f32_16x16x32_bf16 v[88:91], v[32:35], a[24:27], v[88:91]\n" \
  "v_cvt_pk_bf16_f32 v80, v80, v81\n" \
  "v_cvt_pk_bf16_f32 v81, v82, v83\n" \
  "v_mfma_f32_16x16x32_bf16 v[92:95], v[36:39], a[24:27], v[92:95]\n" \
  "v_cvt_pk_bf16_f32 v82, v84, v85\n" \
  "v_cvt_pk_bf16_f32 v83, v86, v87\n" \
  "v_mfma_f32_16x16x32_bf16 v[88:91], v[40:43], a[28:31], v[88:91]\n" \
  "buffer_store_dwordx4 v[80:83], v5, s[36:39], s50 offen sc1\n" \
  "s_add_u32 s50, s50, 65536\n" \
  "v_mfma_f32_16x16x32_bf16 v[92:95], v[44:47], a[28:31], v[92:95]\n" \
  "buffer_load_dwordx4 v[60:63], v5, s[36:39], s56 offen offset:3072 sc1\n" \
  "buffer_load_dwordx4 v[64:67], v5, s[36:39], s57 offen sc1\n" \
  "v_mfma_f32_16x16x32_bf16 v[96:99], v[16:19], a[32:35], 0\n" \
  "buffer_load_dwordx4 v[68:71], v5, s[36:39], s57 offen offset:1024 sc1\n" \
  "buffer_load_dwordx4 v[72:75], v5, s[36:39], s57 offen offset:2048 sc1\n" \
  "v_mfma_f32_16x16x32_bf16 v[100:103], v[20:23], a[32:35], 0\n" \
  "buffer_load_dwordx4 v[76:79], v5, s[36:39], s57 offen offset:3072 sc1\n" \
  "v_mfma_f32_16x16x32_bf16 v[96:99], v[24:27], a[36:39], v[96:99]\n" \
  "buffer_load_dword v8, v6, s[36:39], 0 offen sc1\n" \
  "v_mfma_f32_16x16x32_bf16 v[100:103], v[28:31], a[36:39], v[100:103]\n" \
  "s_add_u32 s49, s49, 0x80000\n" \
  "s_add_u32 s52, s52, 1\n" \
  "v_mfma_f32_16x16x32_bf16 v[96:99], v[32:35], a[40:43], v[96:99]\n" \
  "v_cvt_pk_bf16_f32 v88, v88, v89\n" \
  "v_cvt_pk_bf16_f32 v89, v90, v91\n" \
  "v_mfma_f32_16x16x32_bf16 v[100:103], v[36:39], a[40:43], v[100:103]\n" \
  "v_cvt_pk_bf16_f32 v90, v92, v93\n" \
  "v_cvt_pk_bf16_f32 v91, v94, v95\n" \
  "v_mfma_f32_16x16x32_bf16 v[96:99], v[40:43], a[44:47], v[96:99]\n" \
  "buffer_store_dwordx4 v[88:91], v5, s[36:39], s50 offen sc1\n" \
  "s_add_u32 s50, s50, 65536\n" \
  "v_mfma_f32_16x16x32_bf16 v[100:103], v[44:47], a[44:47], v[100:103]\n" \
  "s_add_u32 s57, s52, 1\n" \
  "s_min_u32 s57, s57, s54\n" \
  "v_mfma_f32_16x16x32_bf16 v[104:107], v[16:19], a[48:51], 0\n" \
  "v_writelane_b32 v13, s57, 0\n" \
  "v_mfma_f32_16x16x32_bf16 v[108:111], v[20:23], a[48:51], 0\n" \
  "s_cmp_ge_u32 s52, 24\n" \
  "s_cselect_b32 s56, 32, 0\n" \
  "v_mfma_f32_16x16x32_bf16 v[104:107], v[24:27], a[52:55], v[104:107]\n" \
  "s_cmp_lt_u32 s52, s54\n" \
  "s_cselect_b32 s56, s56, 0\n" \
  "v_mfma_f32_16x16x32_bf16 v[108:111], v[28:31], a[52:55], v[108:111]\n" \
  "v_writelane_b32 v13, s56, 1\n" \
  "v_mfma_f32_16x16x32_bf16 v[104:107], v[32:35], a[56:59], v[104:107]\n" \
  "v_cvt_pk_bf16_f32 v96, v96, v97\n" \
  "v_cvt_pk_bf16_f32 v97, v98, v99\n" \
  "v_mfma_f32_16x16x32_bf16 v[108:111], v[36:39], a[56:59], v[108:111]\n" \
  "v_cvt_pk_bf16_f32 v98, v100, v101\n" \
  "v_cvt_pk_bf16_f32 v99, v102, v103\n" \
  "v_mfma_f32_16x16x32_bf16 v[104:107], v[40:43], a[60:63], v[104:107]\n" \
  "buffer_store_dwordx4 v[96:99], v5, s[36:39], s50 offen sc1\n" \
  "s_add_u32 s50, s50, 65536\n" \
  "v_mfma_f32_16x16x32_bf16 v[108:111], v[44:47], a[60:63], v[108:111]\n" \
  "s_lshr_b32 s56, s49, 8\n" \
  "s_and_b32 s56, s56, 0x2000\n" \
  "v_mfma_f32_16x16x32_bf16 v[80:83], v[16:19], a[64:67], 0\n" \
  "s_xor_b32 s56, s56, s49\n" \
  "s_add_u32 s57, s56, 4096\n" \
  "v_mfma_f32_16x16x32_bf16 v[84:87], v[20:23], a[64:67], 0\n" \
  "v_mfma_f32_16x16x32_bf16 v[80:83], v[24:27], a[68:71], v[80:83]\n" \
  "v_mfma_f32_16x16x32_bf16 v[84:87], v[28:31], a[68:71], v[84:87]\n" \
  "v_mfma_f32_16x16x32_bf16 v[80:83], v[32:35], a[72:75], v[80:83]\n" \
  "v_cvt_pk_bf16_f32 v104, v104, v105\n" \
  "v_cvt_pk_bf16_f32 v105, v106, v107\n" \
  "v_mfma_f32_16x16x32_bf16 v[84:87], v[36:39], a[72:75], v[84:87]\n" \
  "v_cvt_pk_bf16_f32 v106, v108, v109\n" \
  "v_cvt_pk_bf16_f32 v107, v110, v111\n" \
  "v_mfma_f32_16x16x32_bf16 v[80:83], v[40:43], a[76:79], v[80:83]\n" \
  "buffer_store_dwordx4 v[104:107], v5, s[36:39], s50 offen sc1\n" \
  "s_add_u32 s50, s50, 65536\n" \
  "v_mfma_f32_16x16x32_bf16 v[84:87], v[44:47], a[76:79], v[84:87]\n" \
  "v_mfma_f32_16x16x32_bf16 v[88:91], v[16:19], a[80:83], 0\n" \
  "v_mfma_f32_16x16x32_bf16 v[92:95], v[20:23], a[80:83], 0\n" \
  "v_mfma_f32_16x16x32_bf16 v[88:91], v[24:27], a[84:87], v[88:91]\n" \
  "v_mfma_f32_16x16x32_bf16 v[92:95], v[28:31], a[84:87], v[92:95]\n" \
  "v_mfma_f32_16x16x32_bf16 v[88:91], v[32:35], a[88:91], v[88:91]\n" \
  "v_cvt_pk_bf16_f32 v80, v80, v81\n" \
  "v_cvt_pk_bf16_f32 v81, v82, v83\n" \
  "v_mfma_f32_16x16x32_bf16 v[92:95], v[36:39], a[88:91], v[92:95]\n" \
  "v_cvt_pk_bf16_f32 v82, v84, v85\n" \
  "v_cvt_pk_bf16_f32 v83, v86, v87\n" \
  "v_mfma_f32_16x16x32_bf16 v[88:91], v[40:43], a[92:95], v[88:91]\n" \
  "buffer_store_dwordx4 v[80:83], v5, s[36:39], s50 offen sc1\n" \
  "s_add_u32 s50, s50, 65536\n" \
  "v_mfma_f32_16x16x32_bf16 v[92:95], v[44:47], a[92:95], v[92:95]\n" \
  "v_mfma_f32_16x16x32_bf16 v[96:99], v[16:19], a[96:99], 0\n" \
  "v_mfma_f32_16x16x32_bf16 v[100:103], v[20:23], a[96:99], 0\n" \
  "v_mfma_f32_16x16x32_bf16 v[96:99], v[24:27], a[100:103], v[96:99]\n" \
  "v_mfma_f32_16x16x32_bf16 v[100:103], v[28:31], a[100:103], v[100:103]\n" \
  "v_mfma_f32_16x16x32_bf16 v[96:99], v[32:35], a[104:107], v[96:99]\n" \
  "v_cvt_pk_bf16_f32 v88, v88, v89\n" \
  "v_cvt_pk_bf16_f32 v89, v90, v91\n" \
  "v_mfma_f32_16x16x32_bf16 v[100:103], v[36:39], a[104:107], v[100:103]\n" \
  "v_cvt_pk_bf16_f32 v90, v92, v93\n" \
  "v_cvt_pk_bf16_f32 v91, v94, v95\n" \
  "v_mfma_f32_16x16x32_bf16 v[96:99], v[40:43], a[108:111], v[96:99]\n" \
  "buffer_store_dwordx4 v[88:91], v5, s[36:39], s50 offen sc1\n" \
  "s_add_u32 s50, s50, 65536\n" \
  "v_mfma_f32_16x16x32_bf16 v[100:103], v[44:47], a[108:111], v[100:103]\n" \
  "v_mfma_f32_16x16x32_bf16 v[104:107], v[16:19], a[112:115], 0\n" \
  "v_mfma_f32_16x16x32_bf16 v[108:111], v[20:23], a[112:115], 0\n" \
  "v_mfma_f32_16x16x32_bf16 v[104:107], v[24:27], a[116:119], v[104:107]\n" \
  "v_mfma_f32_16x16x32_bf16 v[108:111], v[28:31], a[116:119], v[108:111]\n" \
  "v_mfma_f32_16x16x32_bf16 v[104:107], v[32:35], a[120:123], v[104:107]\n" \
  "v_cvt_pk_bf16_f32 v96, v96, v97\n" \
  "v_cvt_pk_bf16_f32 v97, v98, v99\n" \
  "v_mfma_f32_16x16x32_bf16 v[108:111], v[36:39], a[120:123], v[108:111]\n" \
  "v_cvt_pk_bf16_f32 v98, v100, v101\n" \
  "v_cvt_pk_bf16_f32 v99, v102, v103\n" \
  "v_mfma_f32_16x16x32_bf16 v[104:107], v[40:43], a[124:127], v[104:107]\n" \
  "buffer_store_dwordx4 v[96:99], v5, s[36:39], s50 offen sc1\n" \
  "s_add_u32 s50, s50, 65536\n" \
  "v_mfma_f32_16x16x32_bf16 v[108:111], v[44:47], a[124:127], v[108:111]\n" \
  "v_mfma_f32_16x16x32_bf16 v[80:83], v[16:19], a[128:131], 0\n" \
  "s_waitcnt vmcnt(16)\n" \
  "s_barrier\n" \
  "v_mfma_f32_16x16x32_bf16 v[84:87], v[20:23], a[128:131], 0\n" \
  "buffer_atomic_add v10, v12, s[36:39], s53 offen\n" \
  "s_add_u32 s53, s53, 128\n" \
  "v_mfma_f32_16x16x32_bf16 v[80:83], v[24:27], a[132:135], v[80:83]\n" \
  "v_mfma_f32_16x16x32_bf16 v[84:87], v[28:31], a[132:135], v[84:87]\n" \
  "v_mfma_f32_16x16x32_bf16 v[80:83], v[32:35], a[136:139], v[80:83]\n" \
  "v_cvt_pk_bf16_f32 v104, v104, v105\n" \
  "v_cvt_pk_bf16_f32 v105, v106, v107\n" \
  "v_mfma_f32_16x16x32_bf16 v[84:87], v[36:39], a[136:139], v[84:87]\n" \
  "v_cvt_pk_bf16_f32 v106, v108, v109\n" \
  "v_cvt_pk_bf16_f32 v107, v110, v111\n" \
  "v_mfma_f32_16x16x32_bf16 v[80:83], v[40:43], a[140:143], v[80:83]\n" \
  "buffer_store_dwordx4 v[104:107], v5, s[36:39], s50 offen sc1\n" \
  "s_add_u32 s50, s50, 65536\n" \
  "v_mfma_f32_16x16x32_bf16 v[84:87], v[44:47], a[140:143], v[84:87]\n" \
  "v_mov_b32 v12, v9\n" \
  "v_mfma_f32_16x16x32_bf16 v[88:91], v[16:19], a[144:147], 0\n" \
  "v_mfma_f32_16x16x32_bf16 v[92:95], v[20:23], a[144:147], 0\n" \
  "v_mfma_f32_16x16x32_bf16 v[88:91], v[24:27], a[148:151], v[88:91]\n" \
  "v_mfma_f32_16x16x32_bf16 v[92:95], v[28:31], a[148:151], v[92:95]\n" \
  "v_mfma_f32_16x16x32_bf16 v[88:91], v[32:35], a[152:155], v[88:91]\n" \
  "v_cvt_pk_bf16_f32 v80, v80, v81\n" \
  "v_cvt_pk_bf16_f32 v81, v82, v83\n" \
  "v_mfma_f32_16x16x32_bf16 v[92:95], v[36:39], a[152:155], v[92:95]\n" \
  "v_cvt_pk_bf16_f32 v82, v84, v85\n" \
  "v_cvt_pk_bf16_f32 v83, v86, v87\n" \
  "v_mfma_f32_16x16x32_bf16 v[88:91], v[40:43], a[156:159], v[88:91]\n" \
  "buffer_store_dwordx4 v[80:83], v5, s[36:39], s50 offen sc1\n" \
  "s_add_u32 s50, s50, 65536\n" \
  "v_mfma_f32_16x16x32_bf16 v[92:95], v[44:47], a[156:159], v[92:95]\n" \
  "v_mfma_f32_16x16x32_bf16 v[96:99], v[16:19], a[160:163], 0\n" \
  "v_mfma_f32_16x16x32_bf16 v[100:103], v[20:23], a[160:163], 0\n" \
  "v_mfma_f32_16x16x32_bf16 v[96:99], v[24:27], a[164:167], v[96:99]\n" \
  "v_mfma_f32_16x16x32_bf16 v[100:103], v[28:31], a[164:167], v[100:103]\n" \
  "v_mfma_f32_16x16x32_bf16 v[96:99], v[32:35], a[168:171], v[96:99]\n" \
  "v_cvt_pk_bf16_f32 v88, v88, v89\n" \
  "v_cvt_pk_bf16_f32 v89, v90, v91\n" \
  "v_mfma_f32_16x16x32_bf16 v[100:103], v[36:39], a[168:171], v[100:103]\n" \
  "v_cvt_pk_bf16_f32 v90, v92, v93\n" \
  "v_cvt_pk_bf16_f32 v91, v94, v95\n" \
  "v_mfma_f32_16x16x32_bf16 v[96:99], v[40:43], a[172:175], v[96:99]\n" \
  "buffer_store_dwordx4 v[88:91], v5, s[36:39], s50 offen sc1\n" \
  "s_add_u32 s50, s50, 65536\n" \
  "v_mfma_f32_16x16x32_bf16 v[100:103], v[44:47], a[172:175], v[100:103]\n" \
  "v_mfma_f32_16x16x32_bf16 v[104:107], v[16:19], a[176:179], 0\n" \
  "v_mfma_f32_16x16x32_bf16 v[108:111], v[20:23], a[176:179], 0\n" \
  "v_mfma_f32_16x16x32_bf16 v[104:107], v[24:27], a[180:183], v[104:107]\n" \
  "v_mfma_f32_16x16x32_bf16 v[108:111], v[28:31], a[180:183], v[108:111]\n" \
  "v_mfma_f32_16x16x32_bf16 v[104:107], v[32:35], a[184:187], v[104:107]\n" \
  "v_cvt_pk_bf16_f32 v96, v96, v97\n" \
  "v_cvt_pk_bf16_f32 v97, v98, v99\n" \
  "v_mfma_f32_16x16x32_bf16 v[108:111], v[36:39], a[184:187], v[108:111]\n" \
  "v_cvt_pk_bf16_f32 v98, v100, v101\n" \
  "v_cvt_pk_bf16_f32 v99, v102, v103\n" \
  "v_mfma_f32_16x16x32_bf16 v[104:107], v[40:43], a[188:191], v[104:107]\n" \
  "buffer_store_dwordx4 v[96:99], v5, s[36:39], s50 offen sc1\n" \
  "s_add_u32 s50, s50, 65536\n" \
  "v_mfma_f32_16x16x32_bf16 v[108:111], v[44:47], a[188:191], v[108:111]\n" \
  "v_mfma_f32_16x16x32_bf16 v[80:83], v[16:19], a[192:195], 0\n" \
  "v_mfma_f32_16x16x32_bf16 v[84:87], v[20:23], a[192:195], 0\n" \
  "v_mfma_f32_16x16x32_bf16 v[80:83], v[24:27], a[196:199], v[80:83]\n" \
  "v_mfma_f32_16x16x32_bf16 v[84:87], v[28:31], a[196:199], v[84:87]\n" \
  "v_mfma_f32_16x16x32_bf16 v[80:83], v[32:35], a[200:203], v[80:83]\n" \
  "v_cvt_pk_bf16_f32 v104, v104, v105\n" \
  "v_cvt_pk_bf16_f32 v105, v106, v107\n" \
  "v_mfma_f32_16x16x32_bf16 v[84:87], v[36:39], a[200:203], v[84:87]\n" \
  "v_cvt_pk_bf16_f32 v106, v108, v109\n" \
  "v_cvt_pk_bf16_f32 v107, v110, v111\n" \
  "v_mfma_f32_16x16x32_bf16 v[80:83], v[40:43], a[204:207], v[80:83]\n" \
  "buffer_store_dwordx4 v[104:107], v5, s[36:39], s50 offen sc1\n" \
  "s_add_u32 s50, s50, 65536\n" \
  "v_mfma_f32_16x16x32_bf16 v[84:87], v[44:47], a[204:207], v[84:87]\n" \
  "v_mfma_f32_16x16x32_bf16 v[88:91], v[16:19], a[208:211], 0\n" \
  "v_mfma_f32_16x16x32_bf16 v[92:95], v[20:23], a[208:211], 0\n" \
  "v_mfma_f32_16x16x32_bf16 v[88:91], v[24:27], a[212:215], v[88:91]\n" \
  "v_mfma_f32_16x16x32_bf16 v[92:95], v[28:31], a[212:215], v[92:95]\n" \
  "v_mfma_f32_16x16x32_bf16 v[88:91], v[32:35], a[216:219], v[88:91]\n" \
  "v_cvt_pk_bf16_f32 v80, v80, v81\n" \
  "v_cvt_pk_bf16_f32 v81, v82, v83\n" \
  "v_mfma_f32_16x16x32_bf16 v[92:95], v[36:39], a[216:219], v[92:95]\n" \
  "v_cvt_pk_bf16_f32 v82, v84, v85\n" \
  "v_cvt_pk_bf16_f32 v83, v86, v87\n" \
  "v_mfma_f32_16x16x32_bf16 v[88:91], v[40:43], a[220:223], v[88:91]\n" \
  "buffer_store_dwordx4 v[80:83], v5, s[36:39], s50 offen sc1\n" \
  "s_add_u32 s50, s50, 65536\n" \
  "v_mfma_f32_16x16x32_bf16 v[92:95], v[44:47], a[220:223], v[92:95]\n" \
  "v_mfma_f32_16x16x32_bf16 v[96:99], v[16:19], a[224:227], 0\n" \
  "v_mfma_f32_16x16x32_bf16 v[100:103], v[20:23], a[224:227], 0\n" \
  "v_mfma_f32_16x16x32_bf16 v[96:99], v[24:27], a[228:231], v[96:99]\n" \
  "v_mfma_f32_16x16x32_bf16 v[100:103], v[28:31], a[228:231], v[100:103]\n" \
  "v_mfma_f32_16x16x32_bf16 v[96:99], v[32:35], a[232:235], v[96:99]\n" \
  "v_cvt_pk_bf16_f32 v88, v88, v89\n" \
  "v_cvt_pk_bf16_f32 v89, v90, v91\n" \
  "v_mfma_f32_16x16x32_bf16 v[100:103], v[36:39], a[232:235], v[100:103]\n" \
  "v_cvt_pk_bf16_f32 v90, v92, v93\n" \
  "v_cvt_pk_bf16_f32 v91, v94, v95\n" \
  "v_mfma_f32_16x16x32_bf16 v[96:99], v[40:43], a[236:239], v[96:99]\n" \
  "buffer_store_dwordx4 v[88:91], v5, s[36:39], s50 offen sc1\n" \
  "s_add_u32 s50, s50, 65536\n" \
  "v_mfma_f32_16x16x32_bf16 v[100:103], v[44:47], a[236:239], v[100:103]\n" \
  "v_mfma_f32_16x16x32_bf16 v[104:107], v[16:19], a[240:243], 0\n" \
  "v_mfma_f32_16x16x32_bf16 v[108:111], v[20:23], a[240:243], 0\n" \
  "v_mfma_f32_16x16x32_bf16 v[104:107], v[24:27], a[244:247], v[104:107]\n" \
  "v_mfma_f32_16x16x32_bf16 v[108:111], v[28:31], a[244:247], v[108:111]\n" \
  "v_mfma_f32_16x16x32_bf16 v[104:107], v[32:35], a[248:251], v[104:107]\n" \
  "v_cvt_pk_bf16_f32 v96, v96, v97\n" \
  "v_cvt_pk_bf16_f32 v97, v98, v99\n" \
  "v_mfma_f32_16x16x32_bf16 v[108:111], v[36:39], a[248:251], v[108:111]\n" \
  "v_cvt_pk_bf16_f32 v98, v100, v101\n" \
  "v_cvt_pk_bf16_f32 v99, v102, v103\n" \
  "v_mfma_f32_16x16x32_bf16 v[104:107], v[40:43], a[252:255], v[104:107]\n" \
  "buffer_store_dwordx4 v[96:99], v5, s[36:39], s50 offen sc1\n" \
  "s_add_u32 s50, s50, 65536\n" \
  "v_mfma_f32_16x16x32_bf16 v[108:111], v[44:47], a[252:255], v[108:111]\n" \
  "s_cmp_le_u32 s52, s54\n" \
  "s_waitcnt vmcnt(15)\n" \
  "s_cbranch_scc0 SU_out_%=\n" \
  "v_mfma_f32_16x16x32_bf16 v[80:83], v[48:51], a[0:3], 0\n" \
  "v_mfma_f32_16x16x32_bf16 v[84:87], v[52:55], a[0:3], 0\n" \
  "v_mfma_f32_16x16x32_bf16 v[80:83], v[56:59], a[4:7], v[80:83]\n" \
  "v_mfma_f32_16x16x32_bf16 v[84:87], v[60:63], a[4:7], v[84:87]\n" \
  "v_mfma_f32_16x16x32_bf16 v[80:83], v[64:67], a[8:11], v[80:83]\n" \
  "v_cvt_pk_bf16_f32 v104, v104, v105\n" \
  "v_cvt_pk_bf16_f32 v105, v106, v107\n" \
  "v_mfma_f32_16x16x32_bf16 v[84:87], v[68:71], a[8:11], v[84:87]\n" \
  "v_cvt_pk_bf16_f32 v106, v108, v109\n" \
  "v_cvt_pk_bf16_f32 v107, v110, v111\n" \
  "v_mfma_f32_16x16x32_bf16 v[80:83], v[72:75], a[12:15], v[80:83]\n" \
  "buffer_store_dwordx4 v[104:107], v5, s[36:39], s50 offen sc1\n" \
  "s_add_u32 s50, s50, 3211264\n" \
  "v_mfma_f32_16x16x32_bf16 v[84:87], v[76:79], a[12:15], v[84:87]\n" \
  "s_cmp_eq_u32 s50, s51\n" \
  "s_cselect_b32 s58, 100663296, 0\n" \
  "v_mfma_f32_16x16x32_bf16 v[88:91], v[48:51], a[16:19], 0\n" \
  "s_sub_u32 s50, s50, s58\n" \
  "v_mfma_f32_16x16x32_bf16 v[92:95], v[52:55], a[16:19], 0\n" \
  "v_cmp_gt_u32 vcc, v13, v8\n" \
  "s_cbranch_vccnz SU_r3_%=\n" \
  "SU_k3_%=:\n" \
  "v_mfma_f32_16x16x32_bf16 v[88:91], v[56:59], a[20:23], v[88:91]\n" \
  "v_add_u32 v6, v6, v7\n" \
  "buffer_load_dwordx4 v[16:19], v5, s[36:39], s56 offen sc1\n" \
  "v_mfma_f32_16x16x32_bf16 v[92:95], v[60:63], a[20:23], v[92:95]\n" \
  "buffer_load_dwordx4 v[20:23], v5, s[36:39], s56 offen offset:1024 sc1\n" \
  "buffer_load_dwordx4 v[24:27], v5, s[36:39], s56 offen offset:2048 sc1\n" \
  "v_mfma_f32_16x16x32_bf16 v[88:91], v[64:67], a[24:27], v[88:91]\n" \
  "v_cvt_pk_bf16_f32 v80, v80, v81\n" \
  "v_cvt_pk_bf16_f32 v81, v82, v83\n" \
  "v_mfma_f32_16x16x32_bf16 v[92:95], v[68:71], a[24:27], v[92:95]\n" \
  "v_cvt_pk_bf16_f32 v82, v84, v85\n" \
  "v_cvt_pk_bf16_f32 v83, v86, v87\n" \
  "v_mfma_f32_16x16x32_bf16 v[88:91], v[72:75], a[28:31], v[88:91]\n" \
  "buffer_store_dwordx4 v[80:83], v5, s[36:39], s50 offen sc1\n" \
  "s_add_u32 s50, s50, 65536\n" \
  "v_mfma_f32_16x16x32_bf16 v[92:95], v[76:79], a[28:31], v[92:95]\n" \
  "buffer_load_dwordx4 v[28:31], v5, s[36:39], s56 offen offset:3072 sc1\n" \
  "buffer_load_dwordx4 v[32:35], v5, s[36:39], s57 offen sc1\n" \
  "v_mfma_f32_16x16x32_bf16 v[96:99], v[48:51], a[32:35], 0\n" \
  "buffer_load_dwordx4 v[36:39], v5, s[36:39], s57 offen offset:1024 sc1\n" \
  "buffer_load_dwordx4 v[40:43], v5, s[36:39], s57 offen offset:2048 sc1\n" \
  "v_mfma_f32_16x16x32_bf16 v[100:103], v[52:55], a[32:35], 0\n" \
  "buffer_load_dwordx4 v[44:47], v5, s[36:39], s57 offen offset:3072 sc1\n" \
  "v_mfma_f32_16x16x32_bf16 v[96:99], v[56:59], a[36:39], v[96:99]\n" \
  "buffer_load_dword v8, v6, s[36:39], 0 offen sc1\n" \
  "v_mfma_f32_16x16x32_bf16 v[100:103], v[60:63], a[36:39], v[100:103]\n" \
  "s_add_u32 s49, s49, 0x80000\n" \
  "s_add_u32 s52, s52, 1\n" \
  "v_mfma_f32_16x16x32_bf16 v[96:99], v[64:67], a[40:43], v[96:99]\n" \
  "v_cvt_pk_bf16_f32 v88, v88, v89\n" \
  "v_cvt_pk_bf16_f32 v89, v90, v91\n" \
  "v_mfma_f32_16x16x32_bf16 v[100:103], v[68:71], a[40:43], v[100:103]\n" \
  "v_cvt_pk_bf16_f32 v90, v92, v93\n" \
  "v_cvt_pk_bf16_f32 v91, v94, v95\n" \
  "v_mfma_f32_16x16x32_bf16 v[96:99], v[72:75], a[44:47], v[96:99]\n" \
  "buffer_store_dwordx4 v[88:91], v5, s[36:39], s50 offen sc1\n" \
  "s_add_u32 s50, s50, 65536\n" \
  "v_mfma_f32_16x16x32_bf16 v[100:103], v[76:79], a[44:47], v[100:103]\n" \
  "s_add_u32 s57, s52, 1\n" \
  "s_min_u32 s57, s57, s54\n" \
  "v_mfma_f32_16x16x32_bf16 v[104:107], v[48:51], a[48:51], 0\n" \
  "v_writelane_b32 v13, s57, 0\n" \
  "v_mfma_f32_16x16x32_bf16 v[108:111], v[52:55], a[48:51], 0\n" \
  "s_cmp_ge_u32 s52, 24\n" \
  "s_cselect_b32 s56, 32, 0\n" \
  "v_mfma_f32_16x16x32_bf16 v[104:107], v[56:59], a[52:55], v[104:107]\n" \
  "s_cmp_lt_u32 s52, s54\n" \
  "s_cselect_b32 s56, s56, 0\n" \
  "v_mfma_f32_16x16x32_bf16 v[108:111], v[60:63], a[52:55], v[108:111]\n" \
  "v_writelane_b32 v13, s56, 1\n" \
  "v_mfma_f32_16x16x32_bf16 v[104:107], v[64:67], a[56:59], v[104:107]\n" \
  "v_cvt_pk_bf16_f32 v96, v96, v97\n" \
  "v_cvt_pk_bf16_f32 v97, v98, v99\n" \
  "v_mfma_f32_16x16x32_bf16 v[108:111], v[68:71], a[56:59], v[108:111]\n" \
  "v_cvt_pk_bf16_f32 v98, v100, v101\n" \
  "v_cvt_pk_bf16_f32 v99, v102, v103\n" \
  "v_mfma_f32_16x16x32_bf16 v[104:107], v[72:75], a[60:63], v[104:107]\n" \
  "buffer_store_dwordx4 v[96:99], v5, s[36:39], s50 offen sc1\n" \
  "s_add_u32 s50, s50, 65536\n" \
  "v_mfma_f32_16x16x32_bf16 v[108:111], v[76:79], a[60:63], v[108:111]\n" \
  "s_lshr_b32 s56, s49, 8\n" \
  "s_and_b32 s56, s56, 0x2000\n" \
  "v_mfma_f32_16x16x32_bf16 v[80:83], v[48:51], a[64:67], 0\n" \
  "s_xor_b32 s56, s56, s49\n" \
  "s_add_u32 s57, s56, 4096\n" \
  "v_mfma_f32_16x16x32_bf16 v[84:87], v[52:55], a[64:67], 0\n" \
  "v_mfma_f32_16x16x32_bf16 v[80:83], v[56:59], a[68:71], v[80:83]\n" \
  "v_mfma_f32_16x16x32_bf16 v[84:87], v[60:63], a[68:71], v[84:87]\n" \
  "v_mfma_f32_16x16x32_bf16 v[80:83], v[64:67], a[72:75], v[80:83]\n" \
  "v_cvt_pk_bf16_f32 v104, v104, v105\n" \
  "v_cvt_pk_bf16_f32 v105, v106, v107\n" \
  "v_mfma_f32_16x16x32_bf16 v[84:87], v[68:71], a[72:75], v[84:87]\n" \
  "v_cvt_pk_bf16_f32 v106, v108, v109\n" \
  "v_cvt_pk_bf16_f32 v107, v110, v111\n" \
  "v_mfma_f32_16x16x32_bf16 v[80:83], v[72:75], a[76:79], v[80:83]\n" \
  "buffer_store_dwordx4 v[104:107], v5, s[36:39], s50 offen sc1\n" \
  "s_add_u32 s50, s50, 65536\n" \
  "v_mfma_f32_16x16x32_bf16 v[84:87], v[76:79], a[76:79], v[84:87]\n" \
  "v_mfma_f32_16x16x32_bf16 v[88:91], v[48:51], a[80:83], 0\n" \
  "v_mfma_f32_16x16x32_bf16 v[92:95], v[52:55], a[80:83], 0\n" \
  "v_mfma_f32_16x16x32_bf16 v[88:91], v[56:59], a[84:87], v[88:91]\n" \
  "v_mfma_f32_16x16x32_bf16 v[92:95], v[60:63], a[84:87], v[92:95]\n" \
  "v_mfma_f32_16x16x32_bf16 v[88:91], v[64:67], a[88:91], v[88:91]\n" \
  "v_cvt_pk_bf16_f32 v80, v80, v81\n" \
  "v_cvt_pk_bf16_f32 v81, v82, v83\n" \
  "v_mfma_f32_16x16x32_bf16 v[92:95], v[68:71], a[88:91], v[92:95]\n" \
  "v_cvt_pk_bf16_f32 v82, v84, v85\n" \
  "v_cvt_pk_bf16_f32 v83, v86, v87\n" \
  "v_mfma_f32_16x16x32_bf16 v[88:91], v[72:75], a[92:95], v[88:91]\n" \
  "buffer_store_dwordx4 v[80:83], v5, s[36:39], s50 offen sc1\n" \
  "s_add_u32 s50, s50, 65536\n" \
  "v_mfma_f32_16x16x32_bf16 v[92:95], v[76:79], a[92:95], v[92:95]\n" \
  "v_mfma_f32_16x16x32_bf16 v[96:99], v[48:51], a[96:99], 0\n" \
  "v_mfma_f32_16x16x32_bf16 v[100:103], v[52:55], a[96:99], 0\n" \
  "v_mfma_f32_16x16x32_bf16 v[96:99], v[56:59], a[100:103], v[96:99]\n" \
  "v_mfma_f32_16x16x32_bf16 v[100:103], v[60:63], a[100:103], v[100:103]\n" \
  "v_mfma_f32_16x16x32_bf16 v[96:99], v[64:67], a[104:107], v[96:99]\n" \
  "v_cvt_pk_bf16_f32 v88, v88, v89\n" \
  "v_cvt_pk_bf16_f32 v89, v90, v91\n" \
  "v_mfma_f32_16x16x32_bf16 v[100:103], v[68:71], a[104:107], v[100:103]\n" \
  "v_cvt_pk_bf16_f32 v90, v92, v93\n" \
  "v_cvt_pk_bf16_f32 v91, v94, v95\n" \
  "v_mfma_f32_16x16x32_bf16 v[96:99], v[72:75], a[108:111], v[96:99]\n" \
  "buffer_store_dwordx4 v[88:91], v5, s[36:39], s50 offen sc1\n" \
  "s_add_u32 s50, s50, 65536\n" \
  "v_mfma_f32_16x16x32_bf16 v[100:103], v[76:79], a[108:111], v[100:103]\n" \
  "v_mfma_f32_16x16x32_bf16 v[104:107], v[48:51], a[112:115], 0\n" \
  "v_mfma_f32_16x16x32_bf16 v[108:111], v[52:55], a[112:115], 0\n" \
  "v_mfma_f32_16x16x32_bf16 v[104:107], v[56:59], a[116:119], v[104:107]\n" \
  "v_mfma_f32_16x16x32_bf16 v[108:111], v[60:63], a[116:119], v[108:111]\n" \
  "v_mfma_f32_16x16x32_bf16 v[104:107], v[64:67], a[120:123], v[104:107]\n" \
  "v_cvt_pk_bf16_f32 v96, v96, v97\n" \
  "v_cvt_pk_bf16_f32 v97, v98, v99\n" \
  "v_mfma_f32_16x16x32_bf16 v[108:111], v[68:71], a[120:123], v[108:111]\n" \
  "v_cvt_pk_bf16_f32 v98, v100, v101\n" \
  "v_cvt_pk_bf16_f32 v99, v102, v103\n" \
  "v_mfma_f32_16x16x32_bf16 v[104:107], v[72:75], a[124:127], v[104:107]\n" \
  "buffer_store_dwordx4 v[96:99], v5, s[36:39], s50 offen sc1\n" \
  "s_add_u32 s50, s50, 65536\n" \
  "v_mfma_f32_16x16x32_bf16 v[108:111], v[76:79], a[124:127], v[108:111]\n" \
  "v_mfma_f32_16x16x32_bf16 v[80:83], v[48:51], a[128:131], 0\n" \
  "s_waitcnt vmcnt(16)\n" \
  "s_barrier\n" \
  "v_mfma_f32_16x16x32_bf16 v[84:87], v[52:55], a[128:131], 0\n" \
  "buffer_atomic_add v10, v12, s[36:39], s53 offen\n" \
  "s_add_u32 s53, s53, 128\n" \
  "v_mfma_f32_16x16x32_bf16 v[80:83], v[56:59], a[132:135], v[80:83]\n" \
  "v_mfma_f32_16x16x32_bf16 v[84:87], v[60:63], a[132:135], v[84:87]\n" \
  "v_mfma_f32_16x16x32_bf16 v[80:83], v[64:67], a[136:139], v[80:83]\n" \
  "v_cvt_pk_bf16_f32 v104, v104, v105\n" \
  "v_cvt_pk_bf16_f32 v105, v106, v107\n" \
  "v_mfma_f32_16x16x32_bf16 v[84:87], v[68:71], a[136:139], v[84:87]\n" \
  "v_cvt_pk_bf16_f32 v106, v108, v109\n" \
  "v_cvt_pk_bf16_f32 v107, v110, v111\n" \
  "v_mfma_f32_16x16x32_bf16 v[80:83], v[72:75], a[140:143], v[80:83]\n" \
  "buffer_store_dwordx4 v[104:107], v5, s[36:39], s50 offen sc1\n" \
  "s_add_u32 s50, s50, 65536\n" \
  "v_mfma_f32_16x16x32_bf16 v[84:87], v[76:79], a[140:143], v[84:87]\n" \
  "v_mfma_f32_16x16x32_bf16 v[88:91], v[48:51], a[144:147], 0\n" \
  "v_mfma_f32_16x16x32_bf16 v[92:95], v[52:55], a[144:147], 0\n" \
  "v_mfma_f32_16x16x32_bf16 v[88:91], v[56:59], a[148:151], v[88:91]\n" \
  "v_mfma_f32_16x16x32_bf16 v[92:95], v[60:63], a[148:151], v[92:95]\n" \
  "v_mfma_f32_16x16x32_bf16 v[88:91], v[64:67], a[152:155], v[88:91]\n" \
  "v_cvt_pk_bf16_f32 v80, v80, v81\n" \
  "v_cvt_pk_bf16_f32 v81, v82, v83\n" \
  "v_mfma_f32_16x16x32_bf16 v[92:95], v[68:71], a[152:155], v[92:95]\n" \
  "v_cvt_pk_bf16_f32 v82, v84, v85\n" \
  "v_cvt_pk_bf16_f32 v83, v86, v87\n" \
  "v_mfma_f32_16x16x32_bf16 v[88:91], v[72:75], a[156:159], v[88:91]\n" \
  "buffer_store_dwordx4 v[80:83], v5, s[36:39], s50 offen sc1\n" \
  "s_add_u32 s50, s50, 65536\n" \
  "v_mfma_f32_16x16x32_bf16 v[92:95], v[76:79], a[156:159], v[92:95]\n" \
  "v_mfma_f32_16x16x32_bf16 v[96:99], v[48:51], a[160:163], 0\n" \
  "v_mfma_f32_16x16x32_bf16 v[100:103], v[52:55], a[160:163], 0\n" \
  "v_mfma_f32_16x16x32_bf16 v[96:99], v[56:59], a[164:167], v[96:99]\n" \
  "v_mfma_f32_16x16x32_bf16 v[100:103], v[60:63], a[164:167], v[100:103]\n" \
  "v_mfma_f32_16x16x32_bf16 v[96:99], v[64:67], a[168:171], v[96:99]\n" \
  "v_cvt_pk_bf16_f32 v88, v88, v89\n" \
  "v_cvt_pk_bf16_f32 v89, v90, v91\n" \
  "v_mfma_f32_16x16x32_bf16 v[100:103], v[68:71], a[168:171], v[100:103]\n" \
  "v_cvt_pk_bf16_f32 v90, v92, v93\n" \
  "v_cvt_pk_bf16_f32 v91, v94, v95\n" \
  "v_mfma_f32_16x16x32_bf16 v[96:99], v[72:75], a[172:175], v[96:99]\n" \
  "buffer_store_dwordx4 v[88:91], v5, s[36:39], s50 offen sc1\n" \
  "s_add_u32 s50, s50, 65536\n" \
  "v_mfma_f32_16x16x32_bf16 v[100:103], v[76:79], a[172:175], v[100:103]\n" \
  "v_mfma_f32_16x16x32_bf16 v[104:107], v[48:51], a[176:179], 0\n" \
  "v_mfma_f32_16x16x32_bf16 v[108:111], v[52:55], a[176:179], 0\n" \
  "v_mfma_f32_16x16x32_bf16 v[104:107], v[56:59], a[180:183], v[104:107]\n" \
  "v_mfma_f32_16x16x32_bf16 v[108:111], v[60:63], a[180:183], v[108:111]\n" \
  "v_mfma_f32_16x16x32_bf16 v[104:107], v[64:67], a[184:187], v[104:107]\n" \
  "v_cvt_pk_bf16_f32 v96, v96, v97\n" \
  "v_cvt_pk_bf16_f32 v97, v98, v99\n" \
  "v_mfma_f32_16x16x32_bf16 v[108:111], v[68:71], a[184:187], v[108:111]\n" \
  "v_cvt_pk_bf16_f32 v98, v100, v101\n" \
  "v_cvt_pk_bf16_f32 v99, v102, v103\n" \
  "v_mfma_f32_16x16x32_bf16 v[104:107], v[72:75], a[188:191], v[104:107]\n" \
  "buffer_store_dwordx4 v[96:99], v5, s[36:39], s50 offen sc1\n" \
  "s_add_u32 s50, s50, 65536\n" \
  "v_mfma_f32_16x16x32_bf16 v[108:111], v[76:79], a[188:191], v[108:111]\n" \
  "v_mfma_f32_16x16x32_bf16 v[80:83], v[48:51], a[192:195], 0\n" \
  "v_mfma_f32_16x16x32_bf16 v[84:87], v[52:55], a[192:195], 0\n" \
  "v_mfma_f32_16x16x32_bf16 v[80:83], v[56:59], a[196:199], v[80:83]\n" \
  "v_mfma_f32_16x16x32_bf16 v[84:87], v[60:63], a[196:199], v[84:87]\n" \
  "v_mfma_f32_16x16x32_bf16 v[80:83], v[64:67], a[200:203], v[80:83]\n" \
  "v_cvt_pk_bf16_f32 v104, v104, v105\n" \
  "v_cvt_pk_bf16_f32 v105, v106, v107\n" \
  "v_mfma_f32_16x16x32_bf16 v[84:87], v[68:71], a[200:203], v[84:87]\n" \
  "v_cvt_pk_bf16_f32 v106, v108, v109\n" \
  "v_cvt_pk_bf16_f32 v107, v110, v111\n" \
  "v_mfma_f32_16x16x32_bf16 v[80:83], v[72:75], a[204:207], v[80:83]\n" \
  "buffer_store_dwordx4 v[104:107], v5, s[36:39], s50 offen sc1\n" \
  "s_add_u32 s50, s50, 65536\n" \
  "v_mfma_f32_16x16x32_bf16 v[84:87], v[76:79], a[204:207], v[84:87]\n" \
  "v_mfma_f32_16x16x32_bf16 v[88:91], v[48:51], a[208:211], 0\n" \
  "v_mfma_f32_16x16x32_bf16 v[92:95], v[52:55], a[208:211], 0\n" \
  "v_mfma_f32_16x16x32_bf16 v[88:91], v[56:59], a[212:215], v[88:91]\n" \
  "v_mfma_f32_16x16x32_bf16 v[92:95], v[60:63], a[212:215], v[92:95]\n" \
  "v_mfma_f32_16x16x32_bf16 v[88:91], v[64:67], a[216:219], v[88:91]\n" \
  "v_cvt_pk_bf16_f32 v80, v80, v81\n" \
  "v_cvt_pk_bf16_f32 v81, v82, v83\n" \
  "v_mfma_f32_16x16x32_bf16 v[92:95], v[68:71], a[216:219], v[92:95]\n" \
  "v_cvt_pk_bf16_f32 v82, v84, v85\n" \
  "v_cvt_pk_bf16_f32 v83, v86, v87\n" \
  "v_mfma_f32_16x16x32_bf16 v[88:91], v[72:75], a[220:223], v[88:91]\n" \
  "buffer_store_dwordx4 v[80:83], v5, s[36:39], s50 offen sc1\n" \
  "s_add_u32 s50, s50, 65536\n" \
  "v_mfma_f32_16x16x32_bf16 v[92:95], v[76:79], a[220:223], v[92:95]\n" \
  "v_mfma_f32_16x16x32_bf16 v[96:99], v[48:51], a[224:227], 0\n" \
  "v_mfma_f32_16x16x32_bf16 v[100:103], v[52:55], a[224:227], 0\n" \
  "v_mfma_f32_16x16x32_bf16 v[96:99], v[56:59], a[228:231], v[96:99]\n" \
  "v_mfma_f32_16x16x32_bf16 v[100:103], v[60:63], a[228:231], v[100:103]\n" \
  "v_mfma_f32_16x16x32_bf16 v[96:99], v[64:67], a[232:235], v[96:99]\n" \
  "v_cvt_pk_bf16_f32 v88, v88, v89\n" \
  "v_cvt_pk_bf16_f32 v89, v90, v91\n" \
  "v_mfma_f32_16x16x32_bf16 v[100:103], v[68:71], a[232:235], v[100:103]\n" \
  "v_cvt_pk_bf16_f32 v90, v92, v93\n" \
  "v_cvt_pk_bf16_f32 v91, v94, v95\n" \
  "v_mfma_f32_16x16x32_bf16 v[96:99], v[72:75], a[236:239], v[96:99]\n" \
  "buffer_store_dwordx4 v[88:91], v5, s[36:39], s50 offen sc1\n" \
  "s_add_u32 s50, s50, 65536\n" \
  "v_mfma_f32_16x16x32_bf16 v[100:103], v[76:79], a[236:239], v[100:103]\n" \
  "v_mfma_f32_16x16x32_bf16 v[104:107], v[48:51], a[240:243], 0\n" \
  "v_mfma_f32_16x16x32_bf16 v[108:111], v[52:55], a[240:243], 0\n" \
  "v_mfma_f32_16x16x32_bf16 v[104:107], v[56:59], a[244:247], v[104:107]\n" \
  "v_mfma_f32_16x16x32_bf16 v[108:111], v[60:63], a[244:247], v[108:111]\n" \
  "v_mfma_f32_16x16x32_bf16 v[104:107], v[64:67], a[248:251], v[104:107]\n" \
  "v_cvt_pk_bf16_f32 v96, v96, v97\n" \
  "v_cvt_pk_bf16_f32 v97, v98, v99\n" \
  "v_mfma_f32_16x16x32_bf16 v[108:111], v[68:71], a[248:251], v[108:111]\n" \
  "v_cvt_pk_bf16_f32 v98, v100, v101\n" \
  "v_cvt_pk_bf16_f32 v99, v102, v103\n" \
  "v_mfma_f32_16x16x32_bf16 v[104:107], v[72:75], a[252:255], v[104:107]\n" \
  "buffer_store_dwordx4 v[96:99], v5, s[36:39], s50 offen sc1\n" \
  "s_add_u32 s50, s50, 65536\n" \
  "v_mfma_f32_16x16x32_bf16 v[108:111], v[76:79], a[252:255], v[108:111]\n" \
  "s_cmp_le_u32 s52, s54\n" \
  "s_waitcnt vmcnt(15)\n" \
  "s_cbranch_scc1 SU_top_%=\n" \
  "SU_out_%=:\n" \
  "s_nop 7\n" \
  "v_cvt_pk_bf16_f32 v104, v104, v105\n" \
  "v_cvt_pk_bf16_f32 v105, v106, v107\n" \
  "v_cvt_pk_bf16_f32 v106, v108, v109\n" \
  "v_cvt_pk_bf16_f32 v107, v110, v111\n" \
  "buffer_store_dwordx4 v[104:107], v5, s[36:39], s50 offen sc1\n" \
  "s_branch SU_end_%=\n" \
  "SU_r0_%=:\n" \
  "s_sub_u32 s55, s55, 1\n" \
  "s_cbranch_scc1 SU_x0_%=\n" \
  "s_sleep 1\n" \
  "buffer_load_dword v8, v6, s[36:39], 0 offen sc1\n" \
  "s_waitcnt vmcnt(0)\n" \
  "v_cmp_gt_u32 vcc, v13, v8\n" \
  "s_cbranch_vccnz SU_r0_%=\n" \
  "s_branch SU_k0_%=\n" \
  "SU_x0_%=:\n" \
  "s_mov_b32 s55, 0\n" \
  "s_branch SU_k0_%=\n" \
  "SU_r1_%=:\n" \
  "s_sub_u32 s55, s55, 1\n" \
  "s_cbranch_scc1 SU_x1_%=\n" \
  "s_sleep 1\n" \
  "buffer_load_dword v8, v6, s[36:39], 0 offen sc1\n" \
  "s_waitcnt vmcnt(0)\n" \
  "v_cmp_gt_u32 vcc, v13, v8\n" \
  "s_cbranch_vccnz SU_r1_%=\n" \
  "s_branch SU_k1_%=\n" \
  "SU_x1_%=:\n" \
  "s_mov_b32 s55, 0\n" \
  "s_branch SU_k1_%=\n" \
  "SU_r2_%=:\n" \
  "s_sub_u32 s55, s55, 1\n" \
  "s_cbranch_scc1 SU_x2_%=\n" \
  "s_sleep 1\n" \
  "buffer_load_dword v8, v6, s[36:39], 0 offen sc1\n" \
  "s_waitcnt vmcnt(0)\n" \
  "v_cmp_gt_u32 vcc, v13, v8\n" \
  "s_cbranch_vccnz SU_r2_%=\n" \
  "s_branch SU_k2_%=\n" \
  "SU_x2_%=:\n" \
  "s_mov_b32 s55, 0\n" \
  "s_branch SU_k2_%=\n" \
  "SU_r3_%=:\n" \
  "s_sub_u32 s55, s55, 1\n" \
  "s_cbranch_scc1 SU_x3_%=\n" \
  "s_sleep 1\n" \
  "buffer_load_dword v8, v6, s[36:39], 0 offen sc1\n" \
  "s_waitcnt vmcnt(0)\n" \
  "v_cmp_gt_u32 vcc, v13, v8\n" \
  "s_cbranch_vccnz SU_r3_%=\n" \
  "s_branch SU_k3_%=\n" \
  "SU_x3_%=:\n" \
  "s_mov_b32 s55, 0\n" \
  "s_branch SU_k3_%=\n" \
  "SU_ys_%=:\n" \
  "s_sub_u32 s55, s55, 1\n" \
  "s_cbranch_scc1 SU_ysx_%=\n" \
  "s_sleep 2\n" \
  "s_lshr_b32 s56, s59, 17\n" \
  "s_and_b32 s56, s56, 0xf80\n" \
  "s_add_u32 s56, s56, 252182528\n" \
  "buffer_load_dword v14, off, s[36:39], s56 sc1\n" \
  "s_waitcnt vmcnt(0)\n" \
  "s_branch SU_yc_%=\n" \
  "SU_ysx_%=:\n" \
  "s_mov_b32 s55, 0\n" \
  "s_branch SU_ye_%=\n" \
  "SU_yt_%=:\n" \
  "s_sub_u32 s55, s55, 1\n" \
  "s_cbranch_scc1 SU_ytx_%=\n" \
  "s_sleep 2\n" \
  "s_lshr_b32 s56, s59, 16\n" \
  "s_and_b32 s56, s56, 0x1f00\n" \
  "s_add_u32 s56, s56, 252190720\n" \
  "buffer_load_dword v14, v8, s[36:39], s56 offen sc1\n" \
  "s_waitcnt vmcnt(0)\n" \
  "s_branch SU_yf_%=\n" \
  "SU_ytx_%=:\n" \
  "s_mov_b32 s55, 0\n" \
  "s_branch SU_yg_%=\n" \
  "SU_yb_%=:\n" \
  "s_and_b32 s57, s57, 28\n" \
  "s_lshl_b32 s57, s57, 6\n" \
  "s_or_b32 s59, s59, s57\n" \
  "s_or_b32 s59, s59, 1\n" \
  "s_waitcnt vmcnt(0) lgkmcnt(0)\n" \
  "s_branch SU_end_%=\n" \
  "SU_end_%=:\n" \
  "s_nop 1\n"
#define SU_CLOB "v2", "v3", "v4", "v8", "v10", "v11", "v12", "v13", "v14", "v15", "v16", "v17", "v18", "v19", "v20", "v21", "v22", "v23", "v24", "v25", "v26", "v27", "v28", "v29", "v30", "v31", "v32", "v33", "v34", "v35", "v36", "v37", "v38", "v39", "v40", "v41", "v42", "v43", "v44", "v45", "v46", "v47", "v48", "v49", "v50", "v51", "v52", "v53", "v54", "v55", "v56", "v57", "v58", "v59", "v60", "v61", "v62", "v63", "v64", "v65", "v66", "v67", "v68", "v69", "v70", "v71", "v72", "v73", "v74", "v75", "v76", "v77", "v78", "v79", "v80", "v81", "v82", "v83", "v84", "v85", "v86", "v87", "v88", "v89", "v90", "v91", "v92", "v93", "v94", "v95", "v96", "v97", "v98", "v99", "v100", "v101", "v102", "v103", "v104", "v105", "v106", "v107", "v108", "v109", "v110", "v111", "s56", "s57", "s58", "vcc", "a0", "a1", "a2", "a3", "a4", "a5", "a6", "a7", "a8", "a9", "a10", "a11", "a12", "a13", "a14", "a15", "a16", "a17", "a18", "a19", "a20", "a21", "a22", "a23", "a24", "a25", "a26", "a27", "a28", "a29", "a30", "a31", "a32", "a33", "a34", "a35", "a36", "a37", "a38", "a39", "a40", "a41", "a42", "a43", "a44", "a45", "a46", "a47", "a48", "a49", "a50", "a51", "a52", "a53", "a54", "a55", "a56", "a57", "a58", "a59", "a60", "a61", "a62", "a63", "a64", "a65", "a66", "a67", "a68", "a69", "a70", "a71", "a72", "a73", "a74", "a75", "a76", "a77", "a78", "a79", "a80", "a81", "a82", "a83", "a84", "a85", "a86", "a87", "a88", "a89", "a90", "a91", "a92", "a93", "a94", "a95", "a96", "a97", "a98", "a99", "a100", "a101", "a102", "a103", "a104", "a105", "a106", "a107", "a108", "a109", "a110", "a111", "a112", "a113", "a114", "a115", "a116", "a117", "a118", "a119", "a120", "a121", "a122", "a123", "a124", "a125", "a126", "a127", "a128", "a129", "a130", "a131", "a132", "a133", "a134", "a135", "a136", "a137", "a138", "a139", "a140", "a141", "a142", "a143", "a144", "a145", "a146", "a147", "a148", "a149", "a150", "a151", "a152", "a153", "a154", "a155", "a156", "a157", "a158", "a159", "a160", "a161", "a162", "a163", "a164", "a165", "a166", "a167", "a168", "a169", "a170", "a171", "a172", "a173", "a174", "a175", "a176", "a177", "a178", "a179", "a180", "a181", "a182", "a183", "a184", "a185", "a186", "a187", "a188", "a189", "a190", "a191", "a192", "a193", "a194", "a195", "a196", "a197", "a198", "a199", "a200", "a201", "a202", "a203", "a204", "a205", "a206", "a207", "a208", "a209", "a210", "a211", "a212", "a213", "a214", "a215", "a216", "a217", "a218", "a219", "a220", "a221", "a222", "a223", "a224", "a225", "a226", "a227", "a228", "a229", "a230", "a231", "a232", "a233", "a234", "a235", "a236", "a237", "a238", "a239", "a240", "a241", "a242", "a243", "a244", "a245", "a246", "a247", "a248", "a249", "a250", "a251", "a252", "a253", "a254", "a255", "scc", "memory"
constexpr unsigned SCHED_LEN = 12, QLEN = 512, NCAL = 64;
#define SCHED_TYPES T(1, 15) T(2, 15) T(3, 15) T(4, 1) T(4, 2) T(4, 3) T(4, 4) T(4, 15)
__constant__ const unsigned sched[256 * SCHED_LEN + 4 * QLEN] = {
36864,2147760640u,0,0,0,0,0,0,0,0,0,0,36864,2147760644u,0,0,0,0,0,0,0,0,0,0,36864,2147760648u,0,0,0,0,0,0,
0,0,0,0,36864,2147760652u,0,0,0,0,0,0,0,0,0,0,2215112704u,0,0,0,0,0,0,0,0,0,0,0,2215112720u,0,0,0,
0,0,0,0,0,0,0,0,2215112736u,0,0,0,0,0,0,0,0,0,0,0,2215112752u,0,0,0,0,0,0,0,0,0,0,0,
36864,2147760641u,0,0,0,0,0,0,0,0,0,0,36864,2147760645u,0,0,0,0,0,0,0,0,0,0,36864,2147760649u,0,0,0,0,0,0,
0,0,0,0,36864,2147760653u,0,0,0,0,0,0,0,0,0,0,47104,2215178240u,0,0,0,0,0,0,0,0,0,0,47616,2215178256u,0,0,
0,0,0,0,0,0,0,0,48128,2215178272u,0,0,0,0,0,0,0,0,0,0,48640,2215178288u,0,0,0,0,0,0,0,0,0,0,
36864,2147760642u,0,0,0,0,0,0,0,0,0,0,36864,2147760646u,0,0,0,0,0,0,0,0,0,0,36864,2147760650u,0,0,0,0,0,0,
0,0,0,0,36864,2147760654u,0,0,0,0,0,0,0,0,0,0,67563520,2215049730u,0,0,0,0,0,0,0,0,0,0,67563536,2215049746u,0,0,
0,0,0,0,0,0,0,0,67563552,2215049762u,0,0,0,0,0,0,0,0,0,0,67563568,2215049778u,0,0,0,0,0,0,0,0,0,0,
36864,2147760643u,0,0,0,0,0,0,0,0,0,0,36864,2147760647u,0,0,0,0,0,0,0,0,0,0,36864,2147760651u,0,0,0,0,0,0,
0,0,0,0,36864,2147760655u,0,0,0,0,0,0,0,0,0,0,45056,24832,2214981632u,0,0,0,0,0,0,0,0,0,45568,24832,2214981648u,0,
0,0,0,0,0,0,0,0,46080,24832,2214981664u,0,0,0,0,0,0,0,0,0,46592,24832,2214981680u,0,0,0,0,0,0,0,0,0,
36864,2147760896u,0,0,0,0,0,0,0,0,0,0,36864,2147760900u,0,0,0,0,0,0,0,0,0,0,36864,2147760904u,0,0,0,0,0,0,
0,0,0,0,36864,2147760908u,0,0,0,0,0,0,0,0,0,0,2215112706u,0,0,0,0,0,0,0,0,0,0,0,2215112722u,0,0,0,
0,0,0,0,0,0,0,0,2215112738u,0,0,0,0,0,0,0,0,0,0,0,2215112754u,0,0,0,0,0,0,0,0,0,0,0,
36864,2147760897u,0,0,0,0,0,0,0,0,0,0,36864,2147760901u,0,0,0,0,0,0,0,0,0,0,36864,2147760905u,0,0,0,0,0,0,
0,0,0,0,36864,2147760909u,0,0,0,0,0,0,0,0,0,0,47168,2215178242u,0,0,0,0,0,0,0,0,0,0,47680,2215178258u,0,0,
0,0,0,0,0,0,0,0,48192,2215178274u,0,0,0,0,0,0,0,0,0,0,48704,2215178290u,0,0,0,0,0,0,0,0,0,0,
36864,2147760898u,0,0,0,0,0,0,0,0,0,0,36864,2147760902u,0,0,0,0,0,0,0,0,0,0,36864,2147760906u,0,0,0,0,0,0,
0,0,0,0,36864,2147760910u,0,0,0,0,0,0,0,0,0,0,2189881346u,0,0,0,0,0,0,0,0,0,0,0,2189881362u,0,0,0,
0,0,0,0,0,0,0,0,2189881378u,0,0,0,0,0,0,0,0,0,0,0,2189881394u,0,0,0,0,0,0,0,0,0,0,0,
36864,2147760899u,0,0,0,0,0,0,0,0,0,0,36864,2147760903u,0,0,0,0,0,0,0,0,0,0,36864,2147760907u,0,0,0,0,0,0,
0,0,0,0,36864,2147760911u,0,0,0,0,0,0,0,0,0,0,45120,24832,2214981634u,0,0,0,0,0,0,0,0,0,45632,24832,2214981650u,0,
0,0,0,0,0,0,0,0,46144,24832,2214981666u,0,0,0,0,0,0,0,0,0,46656,24832,2214981682u,0,0,0,0,0,0,0,0,0,
36864,2147761152u,0,0,0,0,0,0,0,0,0,0,36864,2147761156u,0,0,0,0,0,0,0,0,0,0,36864,2147761160u,0,0,0,0,0,0,
0,0,0,0,36864,2147761164u,0,0,0,0,0,0,0,0,0,0,2215112708u,0,0,0,0,0,0,0,0,0,0,0,2215112724u,0,0,0,
0,0,0,0,0,0,0,0,2215112740u,0,0,0,0,0,0,0,0,0,0,0,2215112756u,0,0,0,0,0,0,0,0,0,0,0,
36864,2147761153u,0,0,0,0,0,0,0,0,0,0,36864,2147761157u,0,0,0,0,0,0,0,0,0,0,36864,2147761161u,0,0,0,0,0,0,
0,0,0,0,36864,2147761165u,0,0,0,0,0,0,0,0,0,0,47232,2215178244u,0,0,0,0,0,0,0,0,0,0,47744,2215178260u,0,0,
0,0,0,0,0,0,0,0,48256,2215178276u,0,0,0,0,0,0,0,0,0,0,48768,2215178292u,0,0,0,0,0,0,0,0,0,0,
36864,2147761154u,0,0,0,0,0,0,0,0,0,0,36864,2147761158u,0,0,0,0,0,0,0,0,0,0,36864,2147761162u,0,0,0,0,0,0,
0,0,0,0,36864,2147761166u,0,0,0,0,0,0,0,0,0,0,67563524,2215049734u,0,0,0,0,0,0,0,0,0,0,67563540,2215049750u,0,0,
0,0,0,0,0,0,0,0,67563556,2215049766u,0,0,0,0,0,0,0,0,0,0,67563572,2215049782u,0,0,0,0,0,0,0,0,0,0,
36864,2147761155u,0,0,0,0,0,0,0,0,0,0,36864,2147761159u,0,0,0,0,0,0,0,0,0,0,36864,2147761163u,0,0,0,0,0,0,
0,0,0,0,36864,2147761167u,0,0,0,0,0,0,0,0,0,0,45184,24832,2214981636u,0,0,0,0,0,0,0,0,0,45696,24832,2214981652u,0,
0,0,0,0,0,0,0,0,46208,24832,2214981668u,0,0,0,0,0,0,0,0,0,46720,24832,2214981684u,0,0,0,0,0,0,0,0,0,
36864,2147761408u,0,0,0,0,0,0,0,0,0,0,36864,2147761412u,0,0,0,0,0,0,0,0,0,0,36864,2147761416u,0,0,0,0,0,0,
0,0,0,0,36864,2147761420u,0,0,0,0,0,0,0,0,0,0,2215112710u,0,0,0,0,0,0,0,0,0,0,0,2215112726u,0,0,0,
0,0,0,0,0,0,0,0,2215112742u,0,0,0,0,0,0,0,0,0,0,0,2215112758u,0,0,0,0,0,0,0,0,0,0,0,
36864,2147761409u,0,0,0,0,0,0,0,0,0,0,36864,2147761413u,0,0,0,0,0,0,0,0,0,0,36864,2147761417u,0,0,0,0,0,0,
0,0,0,0,36864,2147761421u,0,0,0,0,0,0,0,0,0,0,47296,2215178246u,0,0,0,0,0,0,0,0,0,0,47808,2215178262u,0,0,
0,0,0,0,0,0,0,0,48320,2215178278u,0,0,0,0,0,0,0,0,0,0,48832,2215178294u,0,0,0,0,0,0,0,0,0,0,
36864,2147761410u,0,0,0,0,0,0,0,0,0,0,36864,2147761414u,0,0,0,0,0,0,0,0,0,0,36864,2147761418u,0,0,0,0,0,0,
0,0,0,0,36864,2147761422u,0,0,0,0,0,0,0,0,0,0,2189881350u,0,0,0,0,0,0,0,0,0,0,0,2189881366u,0,0,0,
0,0,0,0,0,0,0,0,2189881382u,0,0,0,0,0,0,0,0,0,0,0,2189881398u,0,0,0,0,0,0,0,0,0,0,0,
36864,2147761411u,0,0,0,0,0,0,0,0,0,0,36864,2147761415u,0,0,0,0,0,0,0,0,0,0,36864,2147761419u,0,0,0,0,0,0,
0,0,0,0,36864,2147761423u,0,0,0,0,0,0,0,0,0,0,45248,24832,2214981638u,0,0,0,0,0,0,0,0,0,45760,24832,2214981654u,0,
0,0,0,0,0,0,0,0,46272,24832,2214981670u,0,0,0,0,0,0,0,0,0,46784,24832,2214981686u,0,0,0,0,0,0,0,0,0,
16384,49152,4096,2214919936u,0,0,0,0,0,0,0,0,16448,49152,4096,2214919944u,0,0,0,0,0,0,0,0,16512,49152,4096,2214919952u,0,0,0,0,
0,0,0,0,16576,49152,4096,2214919960u,0,0,0,0,0,0,0,0,2215112712u,0,0,0,0,0,0,0,0,0,0,0,2215112728u,0,0,0,
0,0,0,0,0,0,0,0,2215112744u,0,0,0,0,0,0,0,0,0,0,0,2215112760u,0,0,0,0,0,0,0,0,0,0,0,
16640,49152,24896,12696,4096,2214919937u,0,0,0,0,0,0,16704,49152,24896,12697,4096,2214919945u,0,0,0,0,0,0,16768,49152,24896,12698,4096,2214919953u,0,0,
0,0,0,0,16832,49152,24896,12699,4096,2214919961u,0,0,0,0,0,0,47360,2215178248u,0,0,0,0,0,0,0,0,0,0,47872,2215178264u,0,0,
0,0,0,0,0,0,0,0,48384,2215178280u,0,0,0,0,0,0,0,0,0,0,48896,2215178296u,0,0,0,0,0,0,0,0,0,0,
16896,49152,24896,12692,4096,2214919938u,0,0,0,0,0,0,16960,49152,24896,12693,4096,2214919946u,0,0,0,0,0,0,17024,49152,24896,12694,4096,2214919954u,0,0,
0,0,0,0,17088,49152,24896,12695,4096,2214919962u,0,0,0,0,0,0,67563528,2215049738u,0,0,0,0,0,0,0,0,0,0,67563544,2215049754u,0,0,
0,0,0,0,0,0,0,0,67563560,2215049770u,0,0,0,0,0,0,0,0,0,0,67563576,2215049786u,0,0,0,0,0,0,0,0,0,0,
17152,49152,24896,12688,4096,2214919939u,0,0,0,0,0,0,17216,49152,24896,12689,4096,2214919947u,0,0,0,0,0,0,17280,49152,24896,12690,4096,2214919955u,0,0,
0,0,0,0,17344,49152,24896,12691,4096,2214919963u,0,0,0,0,0,0,45312,24832,2214981640u,0,0,0,0,0,0,0,0,0,45824,24832,2214981656u,0,
0,0,0,0,0,0,0,0,46336,24832,2214981672u,0,0,0,0,0,0,0,0,0,46848,24832,2214981688u,0,0,0,0,0,0,0,0,0,
17408,49152,24896,12684,12752,4096,2214919940u,0,0,0,0,0,17472,49152,24896,12685,12753,4096,2214919948u,0,0,0,0,0,17536,49152,24896,12686,12754,4096,2214919956u,0,
0,0,0,0,17600,49152,24896,12687,12755,4096,2214919964u,0,0,0,0,0,2215112714u,0,0,0,0,0,0,0,0,0,0,0,2215112730u,0,0,0,
0,0,0,0,0,0,0,0,2215112746u,0,0,0,0,0,0,0,0,0,0,0,2215112762u,0,0,0,0,0,0,0,0,0,0,0,
17664,49152,24896,12680,12748,4096,2214919941u,0,0,0,0,0,17728,49152,24896,12681,12749,4096,2214919949u,0,0,0,0,0,17792,49152,24896,12682,12750,4096,2214919957u,0,
0,0,0,0,17856,49152,24896,12683,12751,4096,2214919965u,0,0,0,0,0,47424,2215178250u,0,0,0,0,0,0,0,0,0,0,47936,2215178266u,0,0,
0,0,0,0,0,0,0,0,48448,2215178282u,0,0,0,0,0,0,0,0,0,0,48960,2215178298u,0,0,0,0,0,0,0,0,0,0,
17920,49152,24896,12676,12744,4096,2214919942u,0,0,0,0,0,17984,49152,24896,12677,12745,4096,2214919950u,0,0,0,0,0,18048,49152,24896,12678,12746,4096,2214919958u,0,
0,0,0,0,18112,49152,24896,12679,12747,4096,2214919966u,0,0,0,0,0,2189881354u,0,0,0,0,0,0,0,0,0,0,0,2189881370u,0,0,0,
0,0,0,0,0,0,0,0,2189881386u,0,0,0,0,0,0,0,0,0,0,0,2189881402u,0,0,0,0,0,0,0,0,0,0,0,
18176,49152,24896,12672,12740,12788,4096,2214919943u,0,0,0,0,18240,49152,24896,12673,12741,12789,4096,2214919951u,0,0,0,0,18304,49152,24896,12674,12742,12790,4096,2214919959u,
0,0,0,0,18368,49152,24896,12675,12743,12791,4096,2214919967u,0,0,0,0,45376,24832,2214981642u,0,0,0,0,0,0,0,0,0,45888,24832,2214981658u,0,
0,0,0,0,0,0,0,0,46400,24832,2214981674u,0,0,0,0,0,0,0,0,0,46912,24832,2214981690u,0,0,0,0,0,0,0,0,0,
18432,49152,24896,12620,12736,12784,4096,2214920000u,0,0,0,0,18496,49152,24896,12621,12737,12785,4096,2214920008u,0,0,0,0,18560,49152,24896,12622,12738,12786,4096,2214920016u,
0,0,0,0,18624,49152,24896,12623,12739,12787,4096,2214920024u,0,0,0,0,2215112716u,0,0,0,0,0,0,0,0,0,0,0,2215112732u,0,0,0,
0,0,0,0,0,0,0,0,2215112748u,0,0,0,0,0,0,0,0,0,0,0,2215112764u,0,0,0,0,0,0,0,0,0,0,0,
18688,49152,24896,12616,12724,12780,4096,2214920001u,0,0,0,0,18752,49152,24896,12617,12725,12781,4096,2214920009u,0,0,0,0,18816,49152,24896,12618,12726,12782,4096,2214920017u,
0,0,0,0,18880,49152,24896,12619,12727,12783,4096,2214920025u,0,0,0,0,47488,2215178252u,0,0,0,0,0,0,0,0,0,0,48000,2215178268u,0,0,
0,0,0,0,0,0,0,0,48512,2215178284u,0,0,0,0,0,0,0,0,0,0,49024,2215178300u,0,0,0,0,0,0,0,0,0,0,
18944,49152,24896,12612,12720,12776,4096,2214920002u,0,0,0,0,19008,49152,24896,12613,12721,12777,4096,2214920010u,0,0,0,0,19072,49152,24896,12614,12722,12778,4096,2214920018u,
0,0,0,0,19136,49152,24896,12615,12723,12779,4096,2214920026u,0,0,0,0,67563532,2215049742u,0,0,0,0,0,0,0,0,0,0,67563548,2215049758u,0,0,
0,0,0,0,0,0,0,0,67563564,2215049774u,0,0,0,0,0,0,0,0,0,0,67563580,2215049790u,0,0,0,0,0,0,0,0,0,0,
19200,49152,24896,12608,12716,12772,4096,2214920003u,0,0,0,0,19264,49152,24896,12609,12717,12773,4096,2214920011u,0,0,0,0,19328,49152,24896,12610,12718,12774,4096,2214920019u,
0,0,0,0,19392,49152,24896,12611,12719,12775,4096,2214920027u,0,0,0,0,45440,24832,2214981644u,0,0,0,0,0,0,0,0,0,45952,24832,2214981660u,0,
0,0,0,0,0,0,0,0,46464,24832,2214981676u,0,0,0,0,0,0,0,0,0,46976,24832,2214981692u,0,0,0,0,0,0,0,0,0,
19456,49152,24896,12556,12712,12768,4096,2214920004u,0,0,0,0,19520,49152,24896,12557,12713,12769,4096,2214920012u,0,0,0,0,19584,49152,24896,12558,12714,12770,4096,2214920020u,
0,0,0,0,19648,49152,24896,12559,12715,12771,4096,2214920028u,0,0,0,0,2215112718u,0,0,0,0,0,0,0,0,0,0,0,2215112734u,0,0,0,
0,0,0,0,0,0,0,0,2215112750u,0,0,0,0,0,0,0,0,0,0,0,2215112766u,0,0,0,0,0,0,0,0,0,0,0,
19712,49152,24896,12552,12708,12764,4096,2214920005u,0,0,0,0,19776,49152,24896,12553,12709,12765,4096,2214920013u,0,0,0,0,19840,49152,24896,12554,12710,12766,4096,2214920021u,
0,0,0,0,19904,49152,24896,12555,12711,12767,4096,2214920029u,0,0,0,0,47552,2215178254u,0,0,0,0,0,0,0,0,0,0,48064,2215178270u,0,0,
0,0,0,0,0,0,0,0,48576,2215178286u,0,0,0,0,0,0,0,0,0,0,49088,2215178302u,0,0,0,0,0,0,0,0,0,0,
19968,49152,24896,12548,12704,12760,4096,2214920006u,0,0,0,0,20032,49152,24896,12549,12705,12761,4096,2214920014u,0,0,0,0,20096,49152,24896,12550,12706,12762,4096,2214920022u,
0,0,0,0,20160,49152,24896,12551,12707,12763,4096,2214920030u,0,0,0,0,2189881358u,0,0,0,0,0,0,0,0,0,0,0,2189881374u,0,0,0,
0,0,0,0,0,0,0,0,2189881390u,0,0,0,0,0,0,0,0,0,0,0,2189881406u,0,0,0,0,0,0,0,0,0,0,0,
20224,49152,24896,12544,12700,12756,4096,2214920007u,0,0,0,0,20288,49152,24896,12545,12701,12757,4096,2214920015u,0,0,0,0,20352,49152,24896,12546,12702,12758,4096,2214920023u,
0,0,0,0,20416,49152,24896,12547,12703,12759,4096,2214920031u,0,0,0,0,45504,24832,2214981646u,0,0,0,0,0,0,0,0,0,46016,24832,2214981662u,0,
0,0,0,0,0,0,0,0,46528,24832,2214981678u,0,0,0,0,0,0,0,0,0,47040,24832,2214981694u,0,0,0,0,0,0,0,0,0,
3556897408u,4171362304u,3556897410u,4171362306u,2349002752u,2349002754u,2214785344u,2214785346u,2214850560u,2214850624u,2214850562u,2214850626u,2214850564u,2214850628u,2214850566u,2214850630u,2214850568u,2214850632u,2214850570u,2214850634u,2214850572u,2214850636u,2214722112u,2214722114u,2147753984u,2147753986u,2147754496u,2147754498u,2147750912u,2147750914u,2147751168u,2147751170u,
2147751424u,2147751426u,2147751680u,2147751682u,2147751936u,2147751938u,2147752192u,2147752194u,2147765760u,2147765761u,2147765762u,2147765763u,2147765888u,2147765889u,2147765890u,2147765891u,0,0,0,0,0,0,0,0,0,0,0,0,0,0,0,0,
0,0,0,0,0,0,0,0,0,0,0,0,0,0,0,0,0,0,0,0,0,0,0,0,0,0,0,0,0,0,0,0,
0,0,0,0,0,0,0,0,0,0,0,0,0,0,0,0,0,0,0,0,0,0,0,0,0,0,0,0,0,0,0,0,
0,0,0,0,0,0,0,0,0,0,0,0,0,0,0,0,0,0,0,0,0,0,0,0,0,0,0,0,0,0,0,0,
0,0,0,0,0,0,0,0,0,0,0,0,0,0,0,0,0,0,0,0,0,0,0,0,0,0,0,0,0,0,0,0,
0,0,0,0,0,0,0,0,0,0,0,0,0,0,0,0,0,0,0,0,0,0,0,0,0,0,0,0,0,0,0,0,
0,0,0,0,0,0,0,0,0,0,0,0,0,0,0,0,0,0,0,0,0,0,0,0,0,0,0,0,0,0,0,0,
0,0,0,0,0,0,0,0,0,0,0,0,0,0,0,0,0,0,0,0,0,0,0,0,0,0,0,0,0,0,0,0,
0,0,0,0,0,0,0,0,0,0,0,0,0,0,0,0,0,0,0,0,0,0,0,0,0,0,0,0,0,0,0,0,
0,0,0,0,0,0,0,0,0,0,0,0,0,0,0,0,0,0,0,0,0,0,0,0,0,0,0,0,0,0,0,0,
0,0,0,0,0,0,0,0,0,0,0,0,0,0,0,0,0,0,0,0,0,0,0,0,0,0,0,0,0,0,0,0,
0,0,0,0,0,0,0,0,0,0,0,0,0,0,0,0,0,0,0,0,0,0,0,0,0,0,0,0,0,0,0,0,
0,0,0,0,0,0,0,0,0,0,0,0,0,0,0,0,0,0,0,0,0,0,0,0,0,0,0,0,0,0,0,0,
0,0,0,0,0,0,0,0,0,0,0,0,0,0,0,0,0,0,0,0,0,0,0,0,0,0,0,0,0,0,0,0,
0,0,0,0,0,0,0,0,0,0,0,0,0,0,0,0,0,0,0,0,0,0,0,0,0,0,0,0,0,0,0,0,
3556897412u,4171362308u,3556897414u,4171362310u,2349002756u,2349002758u,2214785348u,2214785350u,2214850574u,2214850638u,2214850576u,2214850640u,2214850578u,2214850642u,2214850580u,2214850644u,2214850582u,2214850646u,2214850584u,2214850648u,2214850586u,2214850650u,2214722116u,2214722118u,2147753988u,2147753990u,2147754500u,2147754502u,2147750916u,2147750918u,2147751172u,2147751174u,
2147751428u,2147751430u,2147751684u,2147751686u,2147751940u,2147751942u,2147752196u,2147752198u,2147765764u,2147765765u,2147765766u,2147765767u,2147765892u,2147765893u,2147765894u,2147765895u,0,0,0,0,0,0,0,0,0,0,0,0,0,0,0,0,
0,0,0,0,0,0,0,0,0,0,0,0,0,0,0,0,0,0,0,0,0,0,0,0,0,0,0,0,0,0,0,0,
0,0,0,0,0,0,0,0,0,0,0,0,0,0,0,0,0,0,0,0,0,0,0,0,0,0,0,0,0,0,0,0,
0,0,0,0,0,0,0,0,0,0,0,0,0,0,0,0,0,0,0,0,0,0,0,0,0,0,0,0,0,0,0,0,
0,0,0,0,0,0,0,0,0,0,0,0,0,0,0,0,0,0,0,0,0,0,0,0,0,0,0,0,0,0,0,0,
0,0,0,0,0,0,0,0,0,0,0,0,0,0,0,0,0,0,0,0,0,0,0,0,0,0,0,0,0,0,0,0,
0,0,0,0,0,0,0,0,0,0,0,0,0,0,0,0,0,0,0,0,0,0,0,0,0,0,0,0,0,0,0,0,
0,0,0,0,0,0,0,0,0,0,0,0,0,0,0,0,0,0,0,0,0,0,0,0,0,0,0,0,0,0,0,0,
0,0,0,0,0,0,0,0,0,0,0,0,0,0,0,0,0,0,0,0,0,0,0,0,0,0,0,0,0,0,0,0,
0,0,0,0,0,0,0,0,0,0,0,0,0,0,0,0,0,0,0,0,0,0,0,0,0,0,0,0,0,0,0,0,
0,0,0,0,0,0,0,0,0,0,0,0,0,0,0,0,0,0,0,0,0,0,0,0,0,0,0,0,0,0,0,0,
0,0,0,0,0,0,0,0,0,0,0,0,0,0,0,0,0,0,0,0,0,0,0,0,0,0,0,0,0,0,0,0,
0,0,0,0,0,0,0,0,0,0,0,0,0,0,0,0,0,0,0,0,0,0,0,0,0,0,0,0,0,0,0,0,
0,0,0,0,0,0,0,0,0,0,0,0,0,0,0,0,0,0,0,0,0,0,0,0,0,0,0,0,0,0,0,0,
0,0,0,0,0,0,0,0,0,0,0,0,0,0,0,0,0,0,0,0,0,0,0,0,0,0,0,0,0,0,0,0,
3556897416u,4171362312u,3556897418u,4171362314u,2349002760u,2349002762u,2214785352u,2214785354u,2214850588u,2214850652u,2214850590u,2214850654u,2214850592u,2214850656u,2214850594u,2214850658u,2214850596u,2214850660u,2214850598u,2214850662u,2214850600u,2214850664u,2214722120u,2214722122u,2147753992u,2147753994u,2147754504u,2147754506u,2147750920u,2147750922u,2147751176u,2147751178u,
2147751432u,2147751434u,2147751688u,2147751690u,2147751944u,2147751946u,2147752200u,2147752202u,2147765768u,2147765769u,2147765770u,2147765771u,2147765896u,2147765897u,2147765898u,2147765899u,0,0,0,0,0,0,0,0,0,0,0,0,0,0,0,0,
0,0,0,0,0,0,0,0,0,0,0,0,0,0,0,0,0,0,0,0,0,0,0,0,0,0,0,0,0,0,0,0,
0,0,0,0,0,0,0,0,0,0,0,0,0,0,0,0,0,0,0,0,0,0,0,0,0,0,0,0,0,0,0,0,
0,0,0,0,0,0,0,0,0,0,0,0,0,0,0,0,0,0,0,0,0,0,0,0,0,0,0,0,0,0,0,0,
0,0,0,0,0,0,0,0,0,0,0,0,0,0,0,0,0,0,0,0,0,0,0,0,0,0,0,0,0,0,0,0,
0,0,0,0,0,0,0,0,0,0,0,0,0,0,0,0,0,0,0,0,0,0,0,0,0,0,0,0,0,0,0,0,
0,0,0,0,0,0,0,0,0,0,0,0,0,0,0,0,0,0,0,0,0,0,0,0,0,0,0,0,0,0,0,0,
0,0,0,0,0,0,0,0,0,0,0,0,0,0,0,0,0,0,0,0,0,0,0,0,0,0,0,0,0,0,0,0,
0,0,0,0,0,0,0,0,0,0,0,0,0,0,0,0,0,0,0,0,0,0,0,0,0,0,0,0,0,0,0,0,
0,0,0,0,0,0,0,0,0,0,0,0,0,0,0,0,0,0,0,0,0,0,0,0,0,0,0,0,0,0,0,0,
0,0,0,0,0,0,0,0,0,0,0,0,0,0,0,0,0,0,0,0,0,0,0,0,0,0,0,0,0,0,0,0,
0,0,0,0,0,0,0,0,0,0,0,0,0,0,0,0,0,0,0,0,0,0,0,0,0,0,0,0,0,0,0,0,
0,0,0,0,0,0,0,0,0,0,0,0,0,0,0,0,0,0,0,0,0,0,0,0,0,0,0,0,0,0,0,0,
0,0,0,0,0,0,0,0,0,0,0,0,0,0,0,0,0,0,0,0,0,0,0,0,0,0,0,0,0,0,0,0,
0,0,0,0,0,0,0,0,0,0,0,0,0,0,0,0,0,0,0,0,0,0,0,0,0,0,0,0,0,0,0,0,
3556897420u,4171362316u,3556897422u,4171362318u,2349002764u,2349002766u,2214785356u,2214785358u,2214850602u,2214850666u,2214850604u,2214850668u,2214850606u,2214850670u,2214850608u,2214850672u,2214850610u,2214850674u,2214850612u,2214850676u,2214850614u,2214850678u,2214722124u,2214722126u,2147753996u,2147753998u,2147754508u,2147754510u,2147750924u,2147750926u,2147751180u,2147751182u,
2147751436u,2147751438u,2147751692u,2147751694u,2147751948u,2147751950u,2147752204u,2147752206u,2147765772u,2147765773u,2147765774u,2147765775u,2147765900u,2147765901u,2147765902u,2147765903u,0,0,0,0,0,0,0,0,0,0,0,0,0,0,0,0,
0,0,0,0,0,0,0,0,0,0,0,0,0,0,0,0,0,0,0,0,0,0,0,0,0,0,0,0,0,0,0,0,
0,0,0,0,0,0,0,0,0,0,0,0,0,0,0,0,0,0,0,0,0,0,0,0,0,0,0,0,0,0,0,0,
0,0,0,0,0,0,0,0,0,0,0,0,0,0,0,0,0,0,0,0,0,0,0,0,0,0,0,0,0,0,0,0,
0,0,0,0,0,0,0,0,0,0,0,0,0,0,0,0,0,0,0,0,0,0,0,0,0,0,0,0,0,0,0,0,
0,0,0,0,0,0,0,0,0,0,0,0,0,0,0,0,0,0,0,0,0,0,0,0,0,0,0,0,0,0,0,0,
0,0,0,0,0,0,0,0,0,0,0,0,0,0,0,0,0,0,0,0,0,0,0,0,0,0,0,0,0,0,0,0,
0,0,0,0,0,0,0,0,0,0,0,0,0,0,0,0,0,0,0,0,0,0,0,0,0,0,0,0,0,0,0,0,
0,0,0,0,0,0,0,0,0,0,0,0,0,0,0,0,0,0,0,0,0,0,0,0,0,0,0,0,0,0,0,0,
0,0,0,0,0,0,0,0,0,0,0,0,0,0,0,0,0,0,0,0,0,0,0,0,0,0,0,0,0,0,0,0,
0,0,0,0,0,0,0,0,0,0,0,0,0,0,0,0,0,0,0,0,0,0,0,0,0,0,0,0,0,0,0,0,
0,0,0,0,0,0,0,0,0,0,0,0,0,0,0,0,0,0,0,0,0,0,0,0,0,0,0,0,0,0,0,0,
0,0,0,0,0,0,0,0,0,0,0,0,0,0,0,0,0,0,0,0,0,0,0,0,0,0,0,0,0,0,0,0,
0,0,0,0,0,0,0,0,0,0,0,0,0,0,0,0,0,0,0,0,0,0,0,0,0,0,0,0,0,0,0,0,
0,0,0,0,0,0,0,0,0,0,0,0,0,0,0,0,0,0,0,0,0,0,0,0,0,0,0,0,0,0,0,0,
};
#define PFN 8
#define CTL 52
#define CALN 2
#define SLP 2
DEV unsigned ld1(__amdgpu_buffer_rsrc_t r, unsigned v) { return __builtin_amdgcn_raw_buffer_load_b32(r, v, 0, SC1); }
DEV void vm0() { __builtin_amdgcn_s_waitcnt(0x0f70); }
DEV void bar() { asm volatile("s_barrier" ::: "memory"); }
DEV void waitc(char* ws, unsigned w, unsigned& budget, unsigned c0, unsigned n0, unsigned t0, unsigned c1 = 0, unsigned t1 = 0, unsigned c2 = 0, unsigned t2 = 0) {
  if (w == 0) {
    const auto rs = mkrs(ws + OFF_CNT, C_END * 128);
    const unsigned l = lane_id(), t = l < n0 ? t0 : l == 62 ? t1 : l == 63 ? t2 : 0, o = !t ? OOB : (l < n0 ? c0 + l : l == 62 ? c1 : c2) * 128;
    while (__builtin_amdgcn_ballot_w64(ld1(rs, o) < t) && budget) asm volatile("s_sub_u32 %0, %0, 1\n\ts_sleep %1" : "+s"(budget) : "n"(SLP) : "memory", "scc");
  }
  bar();
}
DEV void signal(char* ws, unsigned w, unsigned c, unsigned n, unsigned v, unsigned c1 = 0, unsigned v1 = 0) {
  asm volatile("s_waitcnt vmcnt(0) lgkmcnt(0)\n\ts_barrier" ::: "memory");
  if (w == 1) {
    const unsigned l = lane_id(), a = l < n ? v : l == 63 ? v1 : 0;
    unsigned o = OFF_CNT + (l < n ? c + l : c1) * 128;
    asm volatile("" : "+v"(o));
    if (a) __hip_atomic_fetch_add((GAS unsigned*)((GAS char*)ws + o), a, __ATOMIC_RELAXED, __HIP_MEMORY_SCOPE_AGENT);
  }
}
constexpr unsigned L_Q = 0x26000, L_TK = L_TOTAL - 4;
DEV void ticket(char* ws, unsigned w, unsigned e, unsigned b, unsigned i) {
  if ((int)e >= 0) return;
  const unsigned l = lane_id();
  asm volatile("s_mov_b32 m0, %2\n\ts_nop 0\n\tbuffer_load_dword %0, %1, 0 offen lds\n\tbuffer_load_dword %0, %1, 0 offen offset:1024 lds"
    ::"v"(l * 4 + w * 256), "s"(mksrd(sched + 256 * SCHED_LEN + (b & 3) * QLEN, QLEN * 4)), "s"(L_Q + w * 256) : "memory");
  if (w == 0 && l == 0) *(LAS unsigned*)(L_TK - (i & 1) * 4) = __hip_atomic_fetch_add((GAS unsigned*)((GAS char*)ws + (OFF_CNT + (C_TK + (b & 3)) * 128)), 1, __ATOMIC_RELAXED, __HIP_MEMORY_SCOPE_AGENT);
}
#define NS 4
DEV void seg_loop(KPtrs& p, char* ws, unsigned w, unsigned& budget, unsigned seg, unsigned x, unsigned jj) {
  const unsigned c0 = x + 16 * jj, h = (x >> 1) + 2 * (x & 1) + 8 * jj + 4 * (w & 1), cpr = C_PROG + ((x + 1) & 3);
  const auto rs = mkrs(ws, WSB);
  u32x4 q[8], k[8];
  auto ldqk = [&](unsigned sg) {
    if (w >= 2) {
      const unsigned l = lane_id(), kl = kf_lane(l);
#pragma unroll
      for (int i = 0; i < 8; ++i) q[i] = ld4(rs, l * 16 + (i & 3) * 1024, qoff(sg, h) + (i >> 2) * 4096);
#pragma unroll
      for (int i = 0; i < 8; ++i) k[i] = ld4(rs, kl + i * 512, koff(sg, h));
    }
  };
  u32x4 ux[2][16];
  auto ldux = [&](unsigned sg) {
    const unsigned l = lane_id(), vu = (l >> 5) * 256 + (l & 15) * 16 + (l & 16) * 16384;
#pragma unroll
    for (int t = 0; t < 2; ++t)
#pragma unroll
      for (int i = 0; i < 16; ++i) ux[t][i] = ld4(rs, vu + (i & 7) * 512, OFF_UX + (c0 + 8 * t) * 65536 + (jj & 2) * 4096 + (sg % UX_SEGS) * 4 * MB + w * 16384 + (i >> 3) * 4096);
  };
  waitc(ws, w, budget, C_RDY, seg + 1, RDY_T, C_Q + seg, 32);
  ldqk(seg);
  ldux(seg);
  for (; seg < 64; seg += NS) {
    const unsigned l = lane_id(), hi = l >> 5, tok = l & 31, vu = hi * 256 + (l & 15) * 16, cs = (l >> 4) & 1;
    u32x4 km[8], um[2][4];
    u32x2 xf[4];
#pragma unroll
    for (int u = 0; u < 4; ++u) xf[u] = __builtin_amdgcn_raw_buffer_load_b64(mkrs(p.x, 4 * MB), w < 2 ? tok * 2048 + hi * 8 + (u >> 1) * 128 + (u & 1) * 16 : OOB, seg * 65536 + (c0 + 8 * w) * 32, 0);
    f32x16 s = {};
    float mx = -3e38f;
    if (w >= 2) {
#pragma unroll
      for (int i = 0; i < 8; ++i) s = mfma32(k[i], q[i], s);
#pragma unroll
      for (int r = 0; r < 16; ++r) { if (8 * (r >> 2) + 4 * hi + (r & 3) > tok) s[r] = -3e38f; mx = fmaxf(mx, s[r]); }
    }
#pragma unroll
    for (int i = 0; i < 32; ++i) asm volatile("" : "+a"(ux[i >> 4][i & 15]));
    waitc(ws, w, budget, cpr, 1, seg + (seg < FLAG_S));
    if (seg >= FLAG_S) waitc(ws + 64, w, budget, C_RDY, 64, 200 + seg);
    if (w >= 2) {
#pragma unroll
      for (int i = 0; i < 8; ++i) km[i] = ld4(rs, (l & 7) * 256 + hi * 16 + i * 32, OFF_KM + (seg & 15) * 131072 + (h >> 2) * 16384 + (h & 3) * 2048);
    }
    const unsigned ub = OFF_RING + (seg % 3) * R_PAGE + ((seg / 3) & 1) * MB + c0 * BS + w * 2048, pb = OFF_P + (seg % (2 * NS)) * 262144;
#pragma unroll
    for (int i = 0; i < 8; ++i) um[i >> 2][i & 3] = ld4(rs, vu + cs * 4 * BS + (i & 3) * 512, ub + (i >> 2) * 8 * BS);
    if (seg < 58) {
#pragma unroll
      for (int i = 0; i < 8; ++i) st4(rs, vu + cs * 4 * BS + (i & 3) * 512, ub + (i >> 2) * 8 * BS, fill4(~0u));
    }
    if (w >= 2) {
      f32x16 sm = {};
#pragma unroll
      for (int i = 0; i < 8; ++i) sm = mfma32(km[i], q[i], sm);
      mx = xhalf_max(fmaxf(mx, fmaxf(fmaxf(sm[0], sm[1]), fmaxf(sm[2], sm[3]))));
      const float c = 0.08838834764831845f * 1.4426950408889634f, mc = mx * c;
      float sum = 0;
#pragma unroll
      for (int r = 0; r < 16; ++r) { s[r] = __builtin_amdgcn_exp2f(__builtin_fmaf(s[r], c, -mc)); sum += s[r]; }
#pragma unroll
      for (int r = 0; r < 4; ++r) { sm[r] = __builtin_amdgcn_exp2f(__builtin_fmaf(sm[r], c, -mc)); sum += sm[r]; }
      const float inv = __builtin_amdgcn_rcpf(xhalf_add(sum));
      s *= inv;
      const unsigned a = pk2(sm[0] * inv, sm[1] * inv), b = pk2(sm[2] * inv, sm[3] * inv);
      const auto a2 = __builtin_amdgcn_permlane32_swap(a, a, false, false), b2 = __builtin_amdgcn_permlane32_swap(b, b, false, false);
      st4(rs, l * 16, pb + (h >> 2) * 16384 + (h & 3) * 2048, pk8(s, 0, 8));
      st4(rs, l * 16 + 1024, pb + (h >> 2) * 16384 + (h & 3) * 2048, pk8(s, 4, 12));
      st4(rs, hi ? OOB : l * 16, pb + 131072 + (h >> 4) * 16384 + (h & 15) * 512, u32x4{a2[0], a2[1], b2[0], b2[1]});
    }
    signal(ws, w, C_P + seg, 1, 2, C_RDY + seg + 6, seg < 58);
#pragma unroll
    for (int i = 0; i < 8; ++i) asm volatile("" : "+a"(um[i >> 2][i & 3]));
    waitc(ws, w, budget, C_RDY + seg + NS - 1, seg < 64 - NS ? 2 : 0, RDY_T, C_P + seg, 32, C_Q + seg + NS, seg < 64 - NS ? 32 : 0);
    u32x4 px[16], pm[4];
#pragma unroll
    for (int i = 0; i < 16; ++i) px[i] = ld4(rs, l * 16 + (i & 3) * 1024, pb + w * 32768 + (i >> 3) * 16384 + ((i >> 2) & 1) * 4096);
#pragma unroll
    for (int i = 0; i < 4; ++i) pm[i] = ld4(rs, l * 16 + i * 1024, pb + 131072 + (w >> 1) * 16384 + (w & 1) * 4096);
    if (seg < 64 - NS) ldqk(seg + NS);
    f32x16 o[2] = {};
#pragma unroll
    for (int i = 0; i < 16; ++i)
#pragma unroll
      for (int t = 0; t < 2; ++t) o[t] = mfma32(ux[t][i], px[i], o[t]);
#pragma unroll
    for (int i = 0; i < 4; ++i)
#pragma unroll
      for (int t = 0; t < 2; ++t) o[t] = mfma32(um[t][i], pm[i], o[t]);
#pragma unroll
    for (int t = 0; t < 2; ++t)
#pragma unroll
      for (int i = 0; i < 4; ++i) *(LAS f32x4*)(t * 16384 + w * 4096 + i * 1024 + l * 16) = f32x4{o[t][4 * i], o[t][4 * i + 1], o[t][4 * i + 2], o[t][4 * i + 3]};
    if (seg < 64 - NS) ldux(seg + NS);
    lds_bar();
    if (w < 2) {
      f32x16 t;
#pragma unroll
      for (int u = 0; u < 2; ++u) { float g[8]; unpk8(cat4(xf[2 * u], xf[2 * u + 1]), g);
#pragma unroll
        for (int r = 0; r < 8; ++r) t[8 * u + r] = g[r]; }
#pragma unroll
      for (int j = 0; j < 4; ++j)
#pragma unroll
        for (int i = 0; i < 4; ++i) { const f32x4 d = *(LAS f32x4*)(w * 16384 + j * 4096 + i * 1024 + l * 16);
#pragma unroll
          for (int r = 0; r < 4; ++r) t[4 * i + r] += d[r]; }
      store_stats(rs, l, t, OFF_H + rowoff<OFF_H>(seg) + (2 * jj + w) * 16384 + x * 1024, 4096, OFF_ST1 + seg * 16384 + (x * 8 + jj * 2 + w) * 256);
    }
    signal(ws, w, C_H + seg, 1, 2);
    if (PFN && w == 1 && (seg >> 1) == 6) {
      KPtrs* q = &p;
      asm volatile("" : "+s"(q));
#pragma unroll
      for (int i = 0; i < 8; ++i) asm volatile("s_mov_b32 m0, 0x27000\n\ts_nop 0\n\tglobal_load_lds_dword %0, %1" ::"v"(l * 128 + (OFF_XN + ((seg - 10) * 16 + jj * 4 + x) * 65536 + i * 8192)), "s"(q->ws) : "memory");
    }
    stamp(p.ws, w, 100 + (seg >> 2), 128 + (seg & 3) * 32 + jj * 8 + x);
  }
}
typedef float f32x32 __attribute__((ext_vector_type(32)));
#define AW_OUT(c) "={a[0:31]}"(c[0]), "={a[32:63]}"(c[1]), "={a[64:95]}"(c[2]), "={a[96:127]}"(c[3]), "={a[128:159]}"(c[4]), "={a[160:191]}"(c[5]), "={a[192:223]}"(c[6])
#define AW_IN(c) "{a[0:31]}"(c[0]), "{a[32:63]}"(c[1]), "{a[64:95]}"(c[2]), "{a[96:127]}"(c[3]), "{a[128:159]}"(c[4]), "{a[160:191]}"(c[5]), "{a[192:223]}"(c[6])
template <int KIND>
DEV void task_sp(KPtrs& p, char* ws, unsigned w, unsigned& budget, unsigned s, unsigned s1, unsigned sx, unsigned n0) {
  constexpr unsigned ldw = KIND == 3 ? 7168 : 2048, XO = KIND == 1 ? OFF_H : KIND == 2 ? OFF_G : OFF_X1, CI = KIND == 1 ? C_H : KIND == 2 ? C_G : C_X1, CO = KIND == 1 ? C_G : KIND == 2 ? C_X1 : C_H3;
  const unsigned l = lane_id(), hi = l >> 5, q = (l & 15) >> 2, nb = (n0 >> 5) + w, co = coloff(nb);
  f32x32 wa[8]; f32x16 cs;
  stampz(w);
  asm volatile(SPW_ASM : AW_OUT(wa), "={a[224:255]}"(wa[7]), "={v[16:31]}"(cs)
    : "{s[40:43]}"(mksrd((const char*)(KIND == 1 ? p.w1 : KIND == 2 ? p.w2 : p.w3) + n0 * 2, 1024 * ldw - n0 * 2)), "{s44}"(64 * ldw), "{s45}"(32 * ldw), "{s46}"(w * 2048),
      "{v0}"((w * 8 + (l >> 3)) * ldw + ((l & 7) ^ ((l >> 2) & 4)) * 16), "{v1}"((((4 * hi + q) * 128 + (q >> 1) * 64 + ((l >> 4) & 1) * 32 + (l & 3) * 8) ^ ((w & 1) * 64)) + (w >> 1) * 1024) : SPW_CLOB);
  const u32x4 wsr = mksrd(ws, WSB);
  const unsigned vx = l * 16 + (w >> 1) * 16384 + (w & 1) * 4096, vst = l * 16 + w * 2048, vr = l * 16 + co, vs = w == 1 && l == 0 ? OFF_CNT + CO * 128 : OOB;
  unsigned rdy = 0, par = 0, sp = 0, sq = OOB / 128, vp = OOB, op = 0;
  f32x16 xb = {};
  __builtin_amdgcn_s_waitcnt(0);
  auto nx = [&](unsigned s) { return s + (s < sx ? 1 : 2); };
  auto src = [&](unsigned s, unsigned par) { return u32x4{XO + rowoff<XO>(s), (KIND == 3 ? OFF_ST2 : OFF_ST1) + s * 16384, OFF_H + rowoff<OFF_H>(s), par * 65536 + w * 4096}; };
  auto poll = [&](unsigned s) { return w ? OOB : OFF_CNT + (CI + min(s + (KIND == 2 ? min(l, 2u) - 1 : 0), 63u)) * 128; };
#pragma unroll 1
  for (unsigned n; s < s1; s = n, par ^= 1) {
    n = nx(s);
    u32x4 dr;
    unsigned vo = l * 16, so = KIND == 2 ? OFF_X1 + rowoff<OFF_X1>(s) : OFF_H3 + rowoff<OFF_H3>(s);
    if (KIND == 1) {
      const unsigned cg = nb >> 3, row = s * 32 + (l & 31) + (cg == 0 ? 1 : cg == 1 ? -1 : cg == 2 ? 2 : -2);
      vo = row < 2048 ? rowoff<OFF_G>(row >> 5) + (hi * 32 + (row & 31)) * 16 : OOB; so = OFF_G;
    }
#define SPO
#define SPM(L, M) asm volatile("s_cmp_lg_u32 s55, 0\ns_cbranch_scc1 0f\n" L "s_branch 1f\n0:\n" M "1:\n" : "={s[40:43]}"(dr), "+{v[144:159]}"(xb) SPO : AW_IN(wa), "{a[224:255]}"(wa[7]), "{v[16:31]}"(cs), "{s[36:39]}"(wsr), "{v0}"(vx), "{v1}"(vst), "{v2}"(vr), "{v3}"(l * 16 + par * 65536), \
      "{v[4:5]}"(u32x2{LB + l * 8, LB + 8192 + vst}), "{v6}"(LB + 16384 + par * 1024 + l * 4), "{v7}"(poll(nx(n))), "{v8}"(vs + sq * 128), "{v15}"(vo), "{v140}"(hi ? OOB : l * 8), "{v142}"(vp), "{v143}"(KIND == 2 ? (hi ? OOB : l * 8) + (OFF_ST2 + sp * 16384 + nb * 256) : vr), "{s54}"(op), "{s[44:47]}"(src(n, par ^ 1)), \
      "{s48}"(LB + w * 2048), "{s49}"(LB + 17408 - par * 1024 + w * 256), "{s50}"(n < s1 ? 32 : ~0u), "{s[52:53]}"(u32x2{so + co, OFF_ST2 + s * 16384 + nb * 256}), "{v9}"(poll(n)), "{s[56:59]}"(src(s, par)), "{s55}"(rdy) : SPM_CLOB)
    if (!rdy) {
      if (KIND == 2) waitc(ws, w, budget, C_G + s, 1, 32, C_G + s - 1, s ? 32 : 0, C_G + s + 1, s < 63 ? 32 : 0);
      else waitc(ws, w, budget, CI + s, 1, 32);
    }
    stampl(w, s, rdy != 0);
    if (KIND == 1) SPM(SPL1_ASM, SPM1_ASM);
    if (KIND == 2) SPM(SPL2_ASM, SPM2_ASM);
    if (KIND == 3) SPM(SPL3_ASM, SPM3_ASM);
    sq = rdy ? sp : OOB / 128;
    rdy = __builtin_amdgcn_readfirstlane(dr[2]);
    if (!rdy) { signal(ws, w, CO + s, 1, 4, CO + sq, sq < 64 ? 4 : 0); sq = OOB / 128; }
    sp = s; vp = vo; op = so + co;
    stampl(w, 64 + s, 0);
  }
  stampf(p.ws, w, 2048 + KIND * 32 + (n0 >> 7));
}
DEV void task_s4(KPtrs& p, char* ws, unsigned w, unsigned& budget, unsigned s, unsigned s1, unsigned nb) {
  const unsigned l = lane_id(), hi = l >> 5;
  f32x32 wa[7];
  asm volatile(S4W_ASM : AW_OUT(wa) : "{s[40:43]}"(mksrd((const char*)p.w4 + nb * 64, 3584 * 2048 - nb * 64)), "{s44}"(w * 896 * 2048), "{s46}"(w * 32768), "{v0}"((l >> 2) * 2048 + (l & 3) * 16),
    "{v1}"(w * 32768 + (4 * hi + ((l & 15) >> 2)) * 64 + ((l >> 4) & 1) * 32 + (l & 3) * 8) : "s45", "scc", "memory");
  const auto rs = mkrs(ws, WSB);
  const unsigned vr = l * 16 + (w & 1) * 8 + (w >> 1) * 1024 + coloff(nb);
#pragma unroll 1
  for (; s < s1; s += 2) {
    waitc(ws, w, budget, C_H3 + s, 1, 112);
    stamp(p.ws, w, s, 2048 + 128 + nb);
    const u32x2 r0 = ld2(rs, vr, OFF_H + rowoff<OFF_H>(s)), r1 = ld2(rs, vr, OFF_X1 + rowoff<OFF_X1>(s));
    f32x16 acc;
    asm volatile(S4M_ASM : "={v[32:47]}"(acc) : AW_IN(wa), "{s[36:39]}"(mksrd(ws, WSB)), "{s44}"(OFF_H3 + rowoff<OFF_H3>(s) + w * 7 * 16384), "{v0}"(l * 16) : S4M_CLOB);
#pragma unroll
    for (int i = 0; i < 4; ++i) *(LAS f32x4*)(i * 4096 + w * 1024 + l * 16) = f32x4{acc[4 * i], acc[4 * i + 1], acc[4 * i + 2], acc[4 * i + 3]};
    lds_bar();
    f32x4 t = {bf_lo(r0[0]) + bf_lo(r1[0]), bf_hi(r0[0]) + bf_hi(r1[0]), bf_lo(r0[1]) + bf_lo(r1[1]), bf_hi(r0[1]) + bf_hi(r1[1])};
#pragma unroll
    for (int i = 0; i < 4; ++i) t += *(LAS f32x4*)(w * 4096 + i * 1024 + l * 16);
    __builtin_amdgcn_raw_buffer_store_b64(u32x2{pk2(t[0], t[1]), pk2(t[2], t[3])}, mkrs(p.out, 4u << 20), (l & 31) * 2048 + hi * 8 + w * 16, s * 65536 + nb * 64, 0);
    stamp(p.ws, w, 64 + s, 2048 + 128 + nb);
  }
}
DEV void task_sq(KPtrs& p, char* ws, unsigned w, unsigned& budget, unsigned& st, unsigned kind, unsigned s, unsigned s1, unsigned h) {
  const unsigned l = lane_id(), g = l >> 4, r = (l >> 2) & 7, t = l & 15;
  const unsigned vo = kind == 5 ? w * 2048 + g * 512 + t * 16 : w * (kind == 6 ? 1024 : 2048) + l * 16;
  const unsigned lx = kind == 6 ? (l & 43) + (l & 16) / 4 + (l & 4) * 4 : l, tx = kind == 6 ? (t & 9) + (t & 4) / 2 + (t & 2) * 2 : t;
  unsigned sx = s * 65536, so = (kind == 5 ? OFF_Q : kind == 6 ? OFF_K : OFF_V) + h * 16384 + (s - 1) * 524288, sg = OFF_CNT + (kind == 7 ? C_V + h : (kind == 5 ? C_Q : C_RDY - 1) + s - 1) * 128, n = (s1 - s) >> 1;
  u32x4 wsr = mksrd(ws, WSB);
  stampz(w);
  asm volatile(SQ_ASM : "+{s50}"(sx), "+{s51}"(so), "+{s53}"(sg), "+{s55}"(n), "+{s59}"(st), "+{s60}"(budget), "+{s[36:39]}"(wsr)
    : "{s58}"(OFF_CNT + C_RDY * 128 - 64), "{s[40:43]}"(mksrd((&p.Wq)[kind - 5] + h * 128, 8 * MB - h * 256)), "{s[44:47]}"(mksrd(p.ws + OFF_XN, s1 * 65536)), "{s48}"(w * 2048), "{s49}"(w * 8192),
      "{s52}"(kind == 5 ? 256 : kind == 6 ? 4096 : 1024), "{s54}"(kind == 7 ? 0 : 128), "{v0}"((w * 8 + (l >> 3)) * 8192 + ((l & 7) ^ (g * 2)) * 16),
      "{v1}"((g >> 1) * 4096 + r * 128 + (((w & 1) * 2) ^ (r >> 1)) * 32 + (l & 3) * 8 + (w >> 1) * 1024), "{v5}"(lx * 16 + w * 8192), "{v6}"(g * 512 + tx * 16), "{v8}"(vo),
      "{v9}"(w == 1 && l == 0 ? 0 : OOB), "{v10}"(kind == 6 ? 2 : 1) : SQ_CLOB);
  stampf(p.ws, w, 2048 + kind * 32 + h);
}
DEV void task_su(KPtrs& p, char* ws, unsigned w, unsigned& budget, unsigned& st, unsigned s, unsigned s1, unsigned h) {
  const unsigned l = lane_id(), g = l >> 4, r = (l >> 2) & 7, ub = OFF_UX + (h >> 3) * 16384 + (h & 7) * 1024 + w * MB + (w >> 1) * 8192;
  unsigned sv = OFF_V + h * 16384 + s * 524288, so = ub + (s % UX_SEGS) * 4 * MB, sg = OFF_CNT + (C_RDY + s) * 128, vp = l > 1 ? OOB : OFF_CNT + (l ? C_P + s - UX_SEGS : C_V + h) * 128;
  u32x4 wsr = mksrd(ws, WSB);
  stampz(w);
  asm volatile(SU_ASM : "+{s49}"(sv), "+{s50}"(so), "+{s52}"(s), "+{s53}"(sg), "+{s55}"(budget), "+{v6}"(vp), "+{s59}"(st), "+{s[36:39]}"(wsr)
    : "{s[40:43]}"(mksrd(p.Wo + h * 131072, 262144)), "{s48}"(w * 32768), "{s51}"(ub + UX_SEGS * 4 * MB), "{s54}"(s1),
      "{v0}"((l >> 3) * 2048 + ((l & 7) ^ (g * 2)) * 16 + w * 512), "{v1}"(w * 32768 + (g >> 1) * 1024 + r * 128 + (r >> 1) * 32 + (l & 3) * 8), "{v5}"(l * 16), "{v7}"(l == 1 ? 128 : 0),
      "{v9}"(w == 1 && l == 0 ? 0 : OOB) : SU_CLOB);
  stampf(p.ws, w, 2048 + 8 * 32 + h);
}
DEV unsigned hb_off(unsigned st) { return OFF_HB + ((st >> 17) & 0xf80); }
DEV unsigned hb_ld(__amdgpu_buffer_rsrc_t rs, unsigned st) { return __builtin_amdgcn_raw_buffer_load_b32(rs, 0, hb_off(st), SC1); }
DEV void beat(__amdgpu_buffer_rsrc_t rs, unsigned v, unsigned n, unsigned o = 0) { const unsigned l = lane_id(); __builtin_amdgcn_raw_buffer_store_b32(v, rs, l < n ? l * 128 : OOB, OFF_HB + o, SC1); }
DEV bool agreed(__amdgpu_buffer_rsrc_t rs, unsigned& budget, unsigned& st, unsigned h0, unsigned h) {
  h0 = __builtin_amdgcn_readfirstlane(h0); h = __builtin_amdgcn_readfirstlane(h);
  while (h == h0 && budget) { asm volatile("s_sub_u32 %0, %0, 1\n\ts_sleep 2" : "+s"(budget) :: "memory", "scc"); h = __builtin_amdgcn_readfirstlane(hb_ld(rs, st)); }
  if (h != h0) st |= (h ^ st) & 28 ? (h & 28) * 64 + 1 : 32;
  return !(st & 1);
}
DEV void flag(__amdgpu_buffer_rsrc_t rs, unsigned w, unsigned k, unsigned v, unsigned n) { const unsigned l = lane_id(); __builtin_amdgcn_raw_buffer_store_b16(v, rs, w == 1 && l < n ? (l & 31) * 256 + (l & 32) * 4 : OOB, OFF_HB + 8192 + k * 2, SC1); }
DEV void clear(__amdgpu_buffer_rsrc_t rs) {
  const u32x4 z = fill4(0);
  beat(rs, 0, 32, 4096);
#pragma unroll
  for (int i = 0; i < 8; ++i) st4(rs, lane_id() * 16, OFF_HB + 8192 + i * 1024, z);
}
DEV void task_init(KPtrs& p, char* wa, unsigned w, unsigned& budget, unsigned& st, unsigned j) {
  const auto rs = mkrs(wa, WSB), rn = mkrs(p.ws + OFF_XN, 4 * MB);
  const u32x4 rx = mksrd(p.x, 4 * MB);
  const unsigned l = lane_id(), r = w * 8 + (l & 7), c = l >> 3;
  stampz(w); stampl(w, 4, 0);
  asm volatile("s_mov_b32 m0, %2\n\ts_nop 0\n\tbuffer_load_dword off, %0, %1 sc1 lds" ::"s"(mksrd(wa, WSB)), "s"(hb_off(st)), "s"(L_Q + w * 256) : "memory");
#pragma unroll
  for (int i = 0; i < 16; ++i)
    asm volatile("s_mov_b32 m0, %3\n\ts_nop 0\n\tbuffer_load_dwordx4 %0, %1, %2 offen lds" ::"v"((l ^ ((i >> 1) & 1) ^ (i & 4) * 2) * 16), "s"(rx), "s"(j * 65536 + w * 16384 + i * 1024), "s"(w * 16384 + i * 1024) : "memory");
  {
    asm volatile("s_waitcnt vmcnt(0)" ::: "memory");
    u32x4 d[16];
#pragma unroll
    for (int i = 0; i < 16; ++i) d[i] = ((LAS u32x4*)(r * 2048 + ((c * 2) ^ (i & 1) ^ (l & 1) ^ (l & 2) * 4) * 16))[(i >> 1) * 16];
    __builtin_amdgcn_sched_barrier(0);
    f32x2 s = {}, q = {};
#pragma unroll
    for (int i = 0; i < 64; ++i) {
      const unsigned v = d[i >> 2][i & 3];
      const bf16x2 a = __builtin_bit_cast(bf16x2, v);
      s[i & 1] = __builtin_amdgcn_fdot2_f32_bf16(a, __builtin_bit_cast(bf16x2, 0x3f803f80), s[i & 1], false); q[i & 1] = __builtin_amdgcn_fdot2_f32_bf16(a, a, q[i & 1], false);
    }
    const float m = sum8(s[0] + s[1]) * (1.f / 1024), a = __builtin_amdgcn_rsqf(fmaxf(sum8(q[0] + q[1]) * (1.f / 1024) - m * m, 0.f) + 1e-5f);
    const f32x2 a2 = {a, a}, b2 = {-m * a, -m * a};
#pragma unroll
    for (int i = 0; i < 16; ++i) {
      u32x4 o;
#pragma unroll
      for (int k = 0; k < 4; ++k) { const unsigned u = d[(i & 14) + (k >> 1)][(i & 1) * 2 + (k & 1)]; const f32x2 y = __builtin_elementwise_fma(f32x2{bf_lo(u), bf_hi(u)}, a2, b2); o[k] = pk2(y[0], y[1]); }
      st4(rn, c * 1024 + r * 16 + (i & 1) * 512, j * 65536 + (i >> 1) * 8192, o);
    }
  }
  stampl(w, 1, 0);
  if (!agreed(rs, budget, st, *(LAS unsigned*)(L_Q + w * 256), hb_ld(rs, st))) return;
  asm volatile("s_waitcnt vmcnt(0)\n\ts_barrier" ::: "memory");
  flag(rs, w, 2 * j, 1, 32);
  stampl(w, 3, 0); stampf(p.ws, w, 256 + j);
}
DEV void task_calib(KPtrs& p, char* wa, unsigned w, unsigned& budget, unsigned& st, unsigned j, bool lead, bool gemm_die) {
  const auto rs = mkrs(wa, WSB + 2 * MB);
  const unsigned l = lane_id(), t = w * 64 + l, vo = l < 8 ? l * 16 : OOB, so = OFF_CAL + ((j & 31) * 4 + w) * BS + (j >> 5) * 128 + (w + j) % 32 * 256;
  unsigned best[2] = {~0u, ~0u};
  stampz(w); stampl(w, 4, 0);
  if (lead) {
    if (w == 0) { clear(rs); vm0(); beat(rs, 64 + (st & 28), 32); }
  } else {
    const unsigned h0 = hb_ld(rs, st);
    st4(rs, vo, so, fill4(0)); st4(rs, vo, so + 8192, fill4(0));
    asm volatile("s_waitcnt vmcnt(0)" ::: "memory");
    stampl(w, 0, 0);
#pragma unroll 1
    for (int i = 0; i < CALN; ++i)
#pragma unroll
      for (int h = 0; h < 2; ++h) {
        const unsigned t0 = __builtin_amdgcn_s_memtime();
        asm volatile("s_waitcnt lgkmcnt(0)" ::: "memory");
        st4(rs, vo, so + h * 8192, fill4(0));
        asm volatile("s_waitcnt vmcnt(0)" ::: "memory");
        best[h] = min(best[h], (unsigned)__builtin_amdgcn_s_memtime() - t0);
      }
    stampl(w, 1, 0);
    if (!agreed(rs, budget, st, h0, hb_ld(rs, st))) return;
  }
  *(LAS u32x2*)(L_Q + w * 8) = u32x2{best[0], best[1]};
  const unsigned u = t >> 5, cg = u < 2 ? u : 2 + ((u - 2) >> 1), rw = u == 0 ? 0 : u == 1 ? 2047 : u < 4 ? u - 2 : 2042 + u;
#pragma unroll 1
  for (unsigned i = j; i < 64; i += NCAL) {
    const unsigned o = i < 36 ? OFF_RING + 3 * R_PAGE + (i >> 1) * BS + (i & 1) * 4096 + t * 16
      : i >= 48 ? OFF_CNT + (i - 48) * 4096 + t * 16
      : i == 36 && t < 192 ? OFF_G + rowoff<OFF_G>(rw >> 5) + (cg * 2 + ((t >> 4) & 1)) * 16384 + ((t & 15) >> 1) * 1024 + ((t & 1) * 32 + (rw & 31)) * 16 : OOB;
    const unsigned k = (i - 48) * 32 + (t >> 3) - C_RDY, y = i < 36 ? ~0u : i < 48 || k > 63 ? 0 : (k < 6 ? 80 : k < 63 ? 64 : 128);
    st4(rs, o, 0, fill4(y)); st4(rs, o, 2 * MB, fill4(y));
  }
  asm volatile("s_waitcnt vmcnt(0) lgkmcnt(0)\n\ts_barrier" ::: "memory");
  const u32x4 v = __builtin_elementwise_min(lds_r128(L_Q), lds_r128(L_Q + 16));
  flag(rs, w, 2 * j + 1, (min(v[0], v[2]) < min(v[1], v[3])) == gemm_die ? 0x8001 : 1, 2048 / NCAL);
  stampl(w, 3, 0); stampf(p.ws, w, 320 + j);
}
DEV void task_sync(char* wa, unsigned w, unsigned& budget, unsigned& st, bool lead) {
  const auto rs = mkrs(wa, WSB);
  const unsigned l = lane_id();
  if (lead && w == 0)
    for (unsigned c = 0, h = 128 + (st & 28); c < 192 && budget; h += 32) {
      asm volatile("s_sub_u32 %0, %0, 1" : "+s"(budget) :: "memory", "scc");
      beat(rs, h, 32);
      c = ld1(rs, l < 32 ? OFF_HB + 4096 + l * 128 : OOB);
#pragma unroll
      for (int i = 0; i < 4; ++i) c += i == 0 ? __builtin_amdgcn_update_dpp(0u, c, 0x128, 15, 15, true) : i == 1 ? __builtin_amdgcn_update_dpp(0u, c, 0x124, 15, 15, true) : i == 2 ? __builtin_amdgcn_update_dpp(0u, c, 0x122, 15, 15, true) : __builtin_amdgcn_update_dpp(0u, c, 0x121, 15, 15, true);
      c = __builtin_amdgcn_readlane(c, 0) + __builtin_amdgcn_readlane(c, 16);
    }
  unsigned f;
  while (f = ld1(rs, OFF_HB + 8192 + ((st >> 16) & 0x1f00) + l * 4), __builtin_amdgcn_ballot_w64(~f & 0x10001) && budget) asm volatile("s_sub_u32 %0, %0, 1\n\ts_sleep 16" : "+s"(budget) :: "memory", "scc");
  st |= (__builtin_popcountll(__builtin_amdgcn_ballot_w64((int)f < 0)) > 32 ? 2 * MB : 0) + 2;
}
struct RecTune { unsigned par, xm, d1w0, d1, d2a0, d3a0, d2a, d3a, d2b, d3b; };
#define TUNE_ARG
#define TUNE_PASS
constexpr RecTune TUNE = {0,0,9,18,10,4,13,2,16,0};
#define XIT 1
#define FLIP 0
template <int KIND, int C>
DEV void run_task(KPtrs& p, char* ws, unsigned w, unsigned& budget, unsigned tb, unsigned n0, unsigned i, unsigned b, unsigned e) {
  if constexpr (C == 15) {
    const unsigned s1 = (e >> 20) & 127;
    stamp(p.ws, w, 3 * i + 1, b);
    if constexpr (KIND == 4) task_s4(p, ws, w, budget, tb, s1, n0 >> 6);
    else task_sp<KIND>(p, ws, w, budget, tb, s1, (e >> 25) & 60, n0);
  } else {
    waitc(ws, w, budget, C_H3 + tb, Cfg<C>::MI * Cfg<C>::WM, 112);
    stamp(p.ws, w, 3 * i + 1, b);
    task_w4<C>(p, ws, w, tb, n0);
  }
}
DEV unsigned role(unsigned g, unsigned r) { const unsigned x = ((g + r) & 7) ^ ((r + FLIP) & 4); return ((g & ~7u) + x) ^ (x < 4 ? 128 : 0); }
__global__ __launch_bounds__(256, 1) void main_kernel(Ptrs p0 TUNE_ARG) {
  KPtrs* kp = (KPtrs*)__builtin_amdgcn_kernarg_segment_ptr();
  unsigned w = wave_id(), budget = 1u << 20, st = (xcc_id() - blockIdx.x) & 7;
  unsigned b = role(blockIdx.x, st), e;
  {
    unsigned long long pc;
    asm volatile("s_load_dword %0, %2, 0\n\ts_load_dword vcc_lo, %3, 0\n\ts_load_dword vcc_hi, %3, 64\n\ts_bitcmp1_b32 %5, 7\n\ts_cbranch_scc0 0f\n\ts_getpc_b64 %1\n\ts_mov_b32 m0, 0x27000\n\ts_nop 0\n\tglobal_load_lds_dword %4, %1\n0:\ts_waitcnt lgkmcnt(0)"
      : "=&s"(e), "=&s"(pc) : "s"(sched + b * SCHED_LEN), "s"(kp), "v"((((b >> 3) & 15) * CTL + min(lane_id(), CTL - 1u)) * 128), "s"(w == 3 && CTL > 1 ? b : 0) : "vcc", "scc", "memory");
  }
  st = st * 4 + (blockIdx.x << 24);
  stamp(p0.ws, w, 126, b); stamp(p0.ws, w, 127, b); stampc(p0.ws, w, 125, b);
#pragma unroll 1
  for (unsigned i = 0; e; ++i) {
    asm volatile("" : "+s"(kp), "+s"(b), "+s"(w), "+s"(st));
    stamp(kp->ws, w, 3 * i, b);
    if (e == 9 << 12) {
      unsigned c = (b >> 3) * 4 + (b & 7);
      const bool w0 = w == 0, isA = ((c ^ (c >> 2)) & 1) == 0;
      rec_role(kp, st, w, isA, c >> 1, c, kp->mem0, isA ? kp->Wq : kp->Wv, isA ? kp->Wk : kp->Wo, kp->ws + ws_align(kp->ws), w0 ? TUNE.d1w0 : TUNE.d1, !isA ? TUNE.d2b : w0 ? TUNE.d2a0 : TUNE.d2a, !isA ? TUNE.d3b : w0 ? TUNE.d3a0 : TUNE.d3a);
      b = (c >> 2) * 8 + (c & 3);
    }
    asm volatile("" : "+s"(kp), "+s"(b), "+s"(w), "+s"(budget), "+s"(st), "+s"(e));
    __builtin_assume(w < 4);
    const unsigned tb = (e >> 6) & 63, n0 = (e & 63) * 64, s1 = (e >> 20) & 127;
    KPtrs& p = *kp;
    char* ws = p.ws + (ws_align(p.ws) + (st & 2 * MB));
    unsigned sc = 0, sn = 0, sv = 0;
    switch ((e >> 12) & 255) {
#define T(K, C) case K * 16 + C: run_task<K, C>(p, ws, w, budget, tb, n0, i, b, e); break;
      SCHED_TYPES
#undef T
      case 95: case 111: case 127: {
        const unsigned k = (e >> 16) & 15;
        stamp(p.ws, w, 3 * i + 1, b);
        task_sq(p, ws, w, budget, st, k, tb, s1, n0 >> 7);
        sc = k == 7 ? C_V + (n0 >> 7) : (k == 5 ? C_Q : C_RDY - 1) + s1 - 1; sn = 1; sv = k == 6 ? 2 : 1;
        break; }
      case 143:
        stamp(p.ws, w, 3 * i + 1, b);
        task_su(p, ws, w, budget, st, tb, s1, n0 >> 7);
        sc = C_RDY + s1 - 1; sn = 1; sv = 2;
        break;
      case 1: seg_loop(p, ws, w, budget, (b >> 5) & 3, b & 7, (b >> 3) & 3); break;
      case 2: task_touch(w, p.w4, 8, 32768, n0 * 2048); break;
      case 3: task_touch(w, (&p.Wq)[tb], tb < 3 ? 1 : tb == 3 ? 3 : 8, tb < 3 ? 8192 : tb == 3 ? 2048 : 32768, tb < 3 ? (n0 >> 7) * 256 + (n0 & 64) * 65536 : tb == 3 ? (n0 >> 7) * 262144 + (n0 & 64) * 16 : n0 * 2048); break;
      case 6: for (unsigned k = 0; k < tb; ++k) __builtin_amdgcn_s_sleep(16); break;
      case 11: task_init(p, ws, w, budget, st, tb); break;
      case 4: task_calib(p, ws, w, budget, st, tb, st >> 24 == FLIP, b & 4); break;
      case 12: task_sync(ws, w, budget, st, st >> 24 == FLIP); break;
      case 9: sn = 1; break;
    }
    if (XIT && ((e >> 12) & 255) == 79) { stamp(kp->ws, w, 3 * i + 2, b); break; }
    if (st & 1) {
      bar();
      b = role(st >> 24, (st >> 8) & 7); st = (st & 0xff000000u) + ((st >> 6) & 28); e = sched[b * SCHED_LEN]; i = -1;
      continue;
    }
    if ((int)e < 0 || (e & 0xf0000) || sn) {
      ws = kp->ws + (ws_align(kp->ws) + (st & 2 * MB));
      ticket(ws, w, e, b, i);
      signal(ws, w, sc, sn, sv);
    }
    stamp(kp->ws, w, 3 * i + 2, b);
    e = (int)e < 0 ? __builtin_amdgcn_readfirstlane(*(LAS unsigned*)(L_Q + min(*(LAS unsigned*)(L_TK - (i & 1) * 4), QLEN - 1) * 4)) : sched[b * SCHED_LEN + i + 1];
  }
  stamp(kp->ws, w, 123, b); stampc(kp->ws, w, 124, b);
}
extern "C" void kernel_launch(void* const* d_in, const int* in_sizes, int n_in, void* d_out, int out_size, void* d_ws, size_t ws_size, hipStream_t stream) {
  Ptrs p = {(const bf16*)d_in[0], (const bf16*)d_in[1], (const bf16*)d_in[2], (const bf16*)d_in[3], (const bf16*)d_in[4], (const bf16*)d_in[5],
            (const bf16*)d_in[8], (const bf16*)d_in[9], (const bf16*)d_in[10], (const bf16*)d_in[11], (bf16*)d_out, (char*)d_ws};
  (void)hipFuncSetAttribute((const void*)main_kernel, hipFuncAttributeMaxDynamicSharedMemorySize, L_TOTAL);
  main_kernel<<<256, 256, L_TOTAL, stream>>>(p TUNE_PASS);
}
```
